```python
import torch

def _softmax(x, axis=-1):
    return torch.softmax(x, dim=axis)

def _gelu(x):
    return torch.nn.functional.gelu(x, approximate='tanh')

def _silu(x):
    return torch.nn.functional.silu(x)

def _sigmoid(x):
    return torch.sigmoid(x)

def _layernorm(x, axis=-1, eps=1e-05):
    m = x.mean(dim=axis, keepdim=True)
    v = x.var(dim=axis, keepdim=True, unbiased=False)
    return (x - m) / torch.sqrt(v + eps)

def _scatter_add(tgt, idx, src, axis=0):
    return tgt.clone().index_add_(axis, idx, src)

def _segment_sum(src, seg, n):
    out = torch.zeros((n,) + src.shape[1:], dtype=src.dtype)
    return out.index_add_(0, seg, src)

def _pad(x, pw):
    flat = tuple((v for pair in reversed(pw) for v in pair))
    return torch.nn.functional.pad(x, flat)
kernel_name = 'adaLN_cond__memory_aug_recurrent_mem_tr__dense_activation_gelu__prompt'

def setup_inputs():

    _w = lambda a: (lambda t: t.to(torch.bfloat16) if t.is_floating_point() else t)(torch.as_tensor(a))

    def _A_setup():
        B, S, Dm = (1, 2048, 1024)
        H, Dh = (32, 128)
        M, seg = (8, 32)
        seg = min(seg, S)
        S = S // seg * seg
        return {'x': _w((lambda *s: torch.randn(*s, dtype=torch.bfloat16) * 0.05)(B, S, Dm)), 'mem0': _w((lambda *s: torch.randn(*s, dtype=torch.bfloat16) * 0.8)(B, M, Dm)), 'Wq': _w((lambda *s: torch.randn(*s, dtype=torch.bfloat16) * 1.6)(Dm, H, Dh) * Dm ** (-0.5)), 'Wk': _w((lambda *s: torch.randn(*s, dtype=torch.bfloat16) * 1.6)(Dm, H, Dh) * Dm ** (-0.5)), 'Wv': _w((lambda *s: torch.randn(*s, dtype=torch.bfloat16) * 0.8)(Dm, H, Dh) * Dm ** (-0.5)), 'Wo': _w((lambda *s: torch.randn(*s, dtype=torch.bfloat16) * 0.8)(H, Dh, Dm) * (H * Dh) ** (-0.5)), 'seg_len': seg}
    _A_in = _A_setup()

    def _B_setup():
        B, S, D, F = (1, 2048, 1024, 3584)
        return {'x': _w((lambda *s: torch.randn(*s, dtype=torch.bfloat16) * 0.05)(B, S, D)), 'w1': _w((lambda *s: torch.randn(*s, dtype=torch.bfloat16) * 1.6)(D, F) * D ** (-0.5)), 'b1': _w((lambda *s: torch.randn(*s, dtype=torch.bfloat16) * 1.6)(F) * 0.01), 'w2': _w((lambda *s: torch.randn(*s, dtype=torch.bfloat16) * 0.2)(F, D) * F ** (-0.5)), 'b2': _w((lambda *s: torch.randn(*s, dtype=torch.bfloat16) * 0.2)(D) * 0.01)}
    _B_in = _B_setup()
    return {'A_x': _A_in['x'], 'A_mem0': _A_in['mem0'], 'A_Wq': _A_in['Wq'], 'A_Wk': _A_in['Wk'], 'A_Wv': _A_in['Wv'], 'A_Wo': _A_in['Wo'], 'A_seg_len': _A_in['seg_len'], 'B_x': _B_in['x'], 'B_w1': _B_in['w1'], 'B_b1': _B_in['b1'], 'B_w2': _B_in['w2'], 'B_b2': _B_in['b2'], 'c': _w((lambda *s: torch.randn(*s, dtype=torch.bfloat16) * 0.05)(1, 1024) * 8.0), 'w_mod': _w((lambda *s: torch.randn(*s, dtype=torch.bfloat16) * 0.05)(1024, 6 * 1024) * 1024 ** (-0.5) * 8.0)}

def reference(A_x, A_mem0, A_Wq, A_Wk, A_Wv, A_Wo, A_seg_len, B_x, B_w1, B_b1, B_w2, B_b2, c, w_mod):

    def _A_ref(x, mem0, Wq, Wk, Wv, Wo, seg_len):
        B, S, Dm = x.shape
        H, Dh = (Wq.shape[1], Wq.shape[2])
        M = mem0.shape[1]
        scale = Dh ** (-0.5)
        n_seg = S // seg_len
        L = M + seg_len
        idx = torch.arange(L)
        mask = (idx[:, None] < M) | (idx[:, None] >= idx[None, :])
        mem = mem0
        outs = []
        for s in range(n_seg):
            xs = x[:, s * seg_len:(s + 1) * seg_len, :].clone()
            h = torch.concatenate([mem, xs], axis=1)
            q = torch.einsum('btd,dhe->bhte', h, Wq)
            k = torch.einsum('btd,dhe->bhte', h, Wk)
            v = torch.einsum('btd,dhe->bhte', h, Wv)
            scores = torch.einsum('bhqe,bhke->bhqk', q, k) * scale
            scores = torch.where(mask[None, None], scores, -1000000000.0)
            p = _softmax(scores, axis=-1)
            o = torch.einsum('bhqk,bhke->bhqe', p, v)
            o = torch.einsum('bhte,hed->btd', o, Wo)
            mem = o[:, :M, :].clone()
            outs.append(o[:, M:, :])
        return torch.concatenate(outs, axis=1)

    def _B_ref(x, w1, b1, w2, b2):
        h = torch.einsum('bsd,df->bsf', x, w1) + b1
        h = _gelu(h)
        return torch.einsum('bsf,fd->bsd', h, w2) + b2
    x = A_x
    mod = (_silu(c) @ w_mod).reshape(c.shape[0], 6, 1, -1)
    sa, ca, ga, sb, cb, gb = (mod[:, 0].clone(), mod[:, 1].clone(), mod[:, 2].clone(), mod[:, 3].clone(), mod[:, 4].clone(), mod[:, 5].clone())
    h = _layernorm(x, axis=-1) * (1 + ca) + sa
    x = x + ga * _A_ref(h, A_mem0, A_Wq, A_Wk, A_Wv, A_Wo, A_seg_len)
    h = _layernorm(x, axis=-1) * (1 + cb) + sb
    x = x + gb * _B_ref(h, B_w1, B_b1, B_w2, B_b2)
    return x
```

```cpp
#include <hip/hip_runtime.h>
#include <hip/hip_bf16.h>
#include <stdint.h>

typedef unsigned short u16;
typedef unsigned long long u64;
typedef __attribute__((ext_vector_type(8))) short bf16x8;
typedef __attribute__((ext_vector_type(4))) short bf16x4;
typedef __attribute__((ext_vector_type(4))) __bf16 bf16x4_t;
typedef __attribute__((ext_vector_type(8))) __bf16 v8bf;
typedef __attribute__((ext_vector_type(16))) __bf16 v16bf;
typedef __attribute__((ext_vector_type(4))) float f32x4;
typedef __attribute__((ext_vector_type(2))) float f32x2;
typedef unsigned v8u __attribute__((ext_vector_type(8)));
typedef unsigned v4u __attribute__((ext_vector_type(4)));
typedef unsigned v2u __attribute__((ext_vector_type(2)));
#define GAS __attribute__((address_space(1)))
#define LAS __attribute__((address_space(3)))
typedef GAS unsigned gu32;
#define RLX_AGENT __ATOMIC_RELAXED, __HIP_MEMORY_SCOPE_AGENT

constexpr int S_TOK = 2048, DM = 1024, NH = 32, DH = 128, MEMR = 8, SEG = 32, NSEG = 64;
constexpr int HD = NH * DH;
constexpr int QKN = 2 * HD;
constexpr int FF = 3584;

#define NTW 12
#define HXM 0
#define RG 16
#define RI(s) ((unsigned)(s) & (RG - 1))
#define PLACE 9
constexpr unsigned MiB = 1u << 20;
constexpr unsigned O_CTL = 0;
constexpr unsigned CTL_BYTES = 65536;
constexpr unsigned O_MOD = 65536;
constexpr unsigned O_POL = O_MOD + 32768;
constexpr unsigned O_MODP = 131072;
constexpr unsigned O_EP = 4 * MiB, EP_SLOT = 524288;
constexpr unsigned O_UM = O_EP + RG * EP_SLOT;
constexpr unsigned UM_SLOT = 64 * 16384;
constexpr unsigned O_E1 = O_UM + RG * UM_SLOT;
constexpr unsigned E1_SLOT = 262144;
constexpr unsigned O_KV = O_E1 + RG * E1_SLOT, KV_SLOT = 524288;
constexpr unsigned O_D = O_KV + RG * KV_SLOT, D_SLOT = MiB, D_SL = 28 * 16384, D_H1 = 52 * 16384, D_X2 = 10 * 16384, D_ST = 18 * 16384;
constexpr unsigned WS_END = O_D + 64 * D_SLOT;
#define WSB 0
static_assert(WSB * MiB + WS_END <= (RG == 64 ? 512 : 256) * MiB && O_D % (2 * MiB) == 0, "ws");
__device__ __forceinline__ unsigned nearg(const char* ws, unsigned pol, unsigned off, unsigned h) {
    unsigned r;
    asm("s_add_u32 %0, %1, %2\n\ts_lshr_b32 %0, %0, 8\n\ts_xor_b32 %0, %0, %3\n\ts_and_b32 %0, %0, 0x2000\n\ts_add_u32 %0, %0, %2" : "=&s"(r) : "s"((unsigned)(size_t)ws), "s"(__builtin_amdgcn_readfirstlane(off)), "s"(pol ^ h << 13) : "scc");
    return r;
}
__device__ __forceinline__ unsigned kmof(const char* ws, unsigned pol, int seg, int h) { return nearg(ws, pol, O_EP + RI(seg) * EP_SLOT, 1) + (h >> 2) * 16384 + (h & 3) * 2048; }
constexpr int C_ABORT = 0;
constexpr int C_X = 64;
#define PW_V(h) (ctl + C_X + 32 * (h))
#define PW_U(i) (ctl + C_X + 32 * (32 + (i)))
#define PW_Q(h) (ctl + C_X + 32 * (64 + (h)))
constexpr int C_H = C_X + 5120;
constexpr int C_R = 12288, C_P = C_R + 256;
constexpr int C_C = C_H + 2048;
constexpr int C_N = C_C + 288;
constexpr int C_G = C_N + 512;

__device__ __forceinline__ float bf2f(u16 v) { return __uint_as_float(((unsigned)v) << 16); }
__device__ __forceinline__ u16 f2bf(float f) {
    __hip_bfloat16 h = __float2bfloat16(f);
    return __builtin_bit_cast(u16, h);
}
__device__ __forceinline__ float rbf(float f) { return bf2f(f2bf(f)); }

#define NPR 6
#define CTC 3
__device__ __forceinline__ void code_touch(unsigned char* lds, int part) {
    u64 pc; const unsigned sz = 104 * 1024;
    asm volatile("s_getpc_b64 %0" : "=s"(pc));
    const v4u r = {(unsigned)pc, (unsigned)(pc >> 32) & 0xFFFFu, sz, 0x00020000u};
    asm volatile("s_mov_b32 m0, %2\n\ts_nop 4\n\tbuffer_load_dword %0, %1, 0 offen lds" :: "v"(((threadIdx.x & 63) + 64 * part) * 128), "s"(r), "s"((unsigned)(size_t)(LAS unsigned char*)lds) : "m0");
}
__global__ __launch_bounds__(256) void k_pre(const u16* __restrict__ c, const u16* __restrict__ w_mod, const u16* __restrict__ mem0, char* __restrict__ ws) {
    __shared__ float red[32][65];
    const int tid = threadIdx.x;
    const unsigned bid = blockIdx.x;
    if (bid >= 24) {
        const int cg = (bid - 24) & 31, kq = (bid - 24) >> 5, c8 = tid & 7, kr = 128 * kq + (tid >> 3);
        v4u wv[4];
#pragma unroll
        for (int i = 0; i < 4; ++i) wv[i] = NTW & 4 ? __builtin_nontemporal_load((const v4u*)(w_mod + (size_t)(kr + 32 * i) * 6144 + cg * 64 + 8 * c8)) : *(const v4u*)(w_mod + (size_t)(kr + 32 * i) * 6144 + cg * 64 + 8 * c8);
        float acc[8] = {0.f, 0.f, 0.f, 0.f, 0.f, 0.f, 0.f, 0.f};
#pragma unroll
        for (int i = 0; i < 4; ++i) {
            const float v = bf2f(c[kr + 32 * i]), sv = rbf(v / (1.f + __expf(-v)));
#pragma unroll
            for (int j = 0; j < 4; ++j) { acc[2 * j] += sv * __uint_as_float(wv[i][j] << 16); acc[2 * j + 1] += sv * __uint_as_float(wv[i][j] & 0xFFFF0000u); }
        }
#pragma unroll
        for (int j = 0; j < 8; ++j) red[tid >> 3][8 * c8 + j] = acc[j];
        __syncthreads();
        if (tid < 64) {
            float v = 0.f;
#pragma unroll
            for (int i = 0; i < 32; ++i) v += red[i][tid];
            ((float*)(ws + O_MODP))[kq * 2048 + cg * 64 + tid] = v;
        }
        return;
    }
    if (bid >= 16) {
        if (bid < 22) *(uint4*)(ws + O_MOD + (bid - 16) * 4096 + tid * 16) = make_uint4(~0u, ~0u, ~0u, ~0u);
        if (bid == 23) { for (int i = 0; i < 4; ++i) code_touch((unsigned char*)red, i * 4 + (tid >> 6)); asm volatile("s_waitcnt vmcnt(0)"); }
        if (bid != 22 || tid) return;
        unsigned best[2] = {~0u, ~0u};
        for (int r = 0; r < NPR; ++r) {
            const u64 t0 = __builtin_readcyclecounter();
            asm volatile("global_store_dword %0, %1, %2 sc1\n\ts_waitcnt vmcnt(0)" :: "v"(O_POL + 128 + (r & 1) * 8192 + (r & 2) * 2048), "v"(0u), "s"(ws) : "memory");
            best[r & 1] = min(best[r & 1], (unsigned)(__builtin_readcyclecounter() - t0));
        }
        const unsigned xcc = __builtin_amdgcn_s_getreg((3 << 11) | 20);
        *(unsigned*)(ws + O_POL) = (((best[1] < best[0]) ^ (xcc >> 2) ^ (((unsigned)(size_t)ws + O_POL) >> 21)) & 1) << 13;
        return;
    }
    ((uint4*)(ws + O_CTL))[bid * 256 + tid] = make_uint4(0u, 0u, 0u, 0u);
    const unsigned u = (tid >> 6) * 256 + (bid & 3) * 64 + (tid & 63);
    uint4 m = *(const uint4*)(mem0 + ((u >> 1) & 7) * DM + (u >> 4) * 16 + (u & 1) * 8);
    m.x = min(m.x, 0xFFFFFFFEu); m.y = min(m.y, 0xFFFFFFFEu); m.z = min(m.z, 0xFFFFFFFEu); m.w = min(m.w, 0xFFFFFFFEu);
    for (int i = 0; i < 4; ++i) *(uint4*)(ws + O_E1 + bid * 16384 + i * 4096 + tid * 16) = m;
}

struct Args {
    const u16* Wq; const u16* Wk; const u16* Wv; const u16* Wo; const u16* mem0;
    const u16* c; const u16* w_mod;
    const u16* x; const u16* w1; const u16* b1; const u16* w2; const u16* b2;
    u16* out; char* ws;
    unsigned mode, pad0;
};
#define RSTAMP(sg, i) do { } while (0)
#define STAMP(i) do { } while (0)
#define XSTAMP(i) do { } while (0)
#define CSTAMP(i, v) do { } while (0)
#define STAMP_WAVE 0
constexpr unsigned SPIN_CAP = 1u << 22;
constexpr unsigned SENT = 0xFFFFFFFFu;

__device__ __forceinline__ void lds_barrier() { asm volatile("s_waitcnt lgkmcnt(0)\n\ts_barrier" ::: "memory"); }
__device__ __forceinline__ void wait_vm0() { asm volatile("s_waitcnt vmcnt(0)" ::: "memory"); }
typedef __bf16 bf16x2_t __attribute__((ext_vector_type(2)));
__device__ __forceinline__ unsigned pack2(float a, float b) { const f32x2 v = {a, b}; return __builtin_bit_cast(unsigned, __builtin_convertvector(v, bf16x2_t)); }
__device__ __forceinline__ float lo16f(unsigned u) { return __uint_as_float(u << 16); }
__device__ __forceinline__ float hi16f(unsigned u) { return __uint_as_float(u & 0xFFFF0000u); }
__device__ __forceinline__ unsigned umax4(v4u x) { return max(max(x.x, x.y), max(x.z, x.w)); }
__device__ __forceinline__ unsigned unsent(unsigned x) { return min(x, 0xFFFFFFFEu); }

__device__ __forceinline__ unsigned ldg4_sc1(const void* sb, unsigned vo) {
    unsigned r;
    asm volatile("global_load_dword %0, %1, %2 sc1\n\ts_waitcnt vmcnt(0)" : "=&v"(r) : "v"(vo), "s"(sb) : "memory");
    return r;
}
__device__ __forceinline__ bool spin_fail(unsigned& spins, unsigned* abortw) {
    if (++spins > SPIN_CAP) { __hip_atomic_store((gu32*)abortw, 1u, RLX_AGENT); return true; }
    if ((spins & 63u) == 0 && __builtin_amdgcn_readfirstlane(__hip_atomic_load((gu32*)abortw, RLX_AGENT)) == 1u) return true;
    return false;
}
__device__ __forceinline__ float red16_add(float x) { auto r = __builtin_amdgcn_permlane16_swap(__float_as_uint(x), __float_as_uint(x), false, false); return __uint_as_float(r[0]) + __uint_as_float(r[1]); }
__device__ __forceinline__ float red32_add(float x) { auto r = __builtin_amdgcn_permlane32_swap(__float_as_uint(x), __float_as_uint(x), false, false); return __uint_as_float(r[0]) + __uint_as_float(r[1]); }
__device__ __forceinline__ v2u tr_rd(const unsigned char* lds_base, unsigned off) {
    return __builtin_bit_cast(v2u, __builtin_amdgcn_ds_read_tr16_b64_v4bf16((LAS bf16x4_t*)(lds_base + off)));
}
__device__ __forceinline__ v4u lds16(const unsigned char* p) { return *(const LAS v4u*)p; }
__device__ __forceinline__ void dma16(const void* src, unsigned char* dst, int aux) {
    if (aux) __builtin_amdgcn_global_load_lds((const unsigned*)src, (unsigned*)dst, 16, 0, 2);
    else __builtin_amdgcn_global_load_lds((const unsigned*)src, (unsigned*)dst, 16, 0, 0);
}

__device__ __forceinline__ v4u mkrs(const void* p, unsigned bytes) { return (v4u){(unsigned)(u64)p, (unsigned)((u64)p >> 32) & 0xFFFFu, bytes, 0x00020000u}; }
template <int AUX> __device__ __forceinline__ void bdma(v4u r, unsigned vo, unsigned so, unsigned ldsaddr) {
    if (AUX == 16) asm volatile("s_mov_b32 m0, %0\n\ts_nop 0\n\tbuffer_load_dwordx4 %1, %2, %3 offen sc1 lds" :: "s"(ldsaddr), "v"(vo), "s"(r), "s"(so) : "m0");
    else if (AUX) asm volatile("s_mov_b32 m0, %0\n\ts_nop 0\n\tbuffer_load_dwordx4 %1, %2, %3 offen nt lds" :: "s"(ldsaddr), "v"(vo), "s"(r), "s"(so) : "m0");
    else asm volatile("s_mov_b32 m0, %0\n\ts_nop 0\n\tbuffer_load_dwordx4 %1, %2, %3 offen lds" :: "s"(ldsaddr), "v"(vo), "s"(r), "s"(so) : "m0");
}

constexpr int L_W = 0;
constexpr int L_KX = 131072;
constexpr int L_MEM = L_KX + 8192;
constexpr int L_T = L_MEM + 16384;
constexpr int L_RED = L_T + 4096;
constexpr int LDS_BYTES = 163840;

#define CH_A 143
#define CH_B 83
#define CH_EU 14
#define CH_EV 24
#define CH_DMIN 0
#define CH_R 56
#define CH_S1 32
#define CH_A1 0
#define CH_B1 0
#define CH_S2 54
#define CH_A2 0
#define CH_B2 0
#define FINE_PAR
constexpr unsigned CH_PAR = CH_A | CH_B << 8 | CH_EU << 16 | CH_DMIN << 21 | (unsigned)CH_R << 26;

constexpr int chord(bool isV, int n) { return !isV ? 4 * (n & 3) + (n >> 2) : n < 8 ? 2 * (n & 3) + (n >> 2) : n; }
constexpr int flat(bool isV, int j) { int u = 0; while (chord(isV, u) != j) ++u; return u > 11 + (j >> 2) ? u : 11 + (j >> 2); }
template <int I> struct IC { static constexpr int v = I; };
template <int B, int E, class F> __device__ __forceinline__ void sfor(F&& f) { if constexpr (B < E) { f(IC<B>{}); sfor<B + 1, E>(f); } }
#define SB() __builtin_amdgcn_sched_barrier(0)

__device__ __forceinline__ int lane_id() { int l; asm volatile("v_mbcnt_lo_u32_b32 %0, -1, 0\n\tv_mbcnt_hi_u32_b32 %0, -1, %0" : "=v"(l)); return l; }
#define CHAIN_QK \
    "s_mov_b32 m0, s58\n.Lchq1_%=:\nds_read_b128 v[220:223], v7 offset:0\nds_read_b128 v[224:227], v7 offset:1024\nds_read_b128 v[228:231], v7 offset:2048\n" \
    "ds_read_b128 v[232:235], v7 offset:3072\nds_read_b128 v[236:239], v7 offset:4096\nds_read_b128 v[240:243], v7 offset:5120\nds_read_b128 v[244:247], v7 offset:6144\n" \
    "ds_read_b128 v[248:251], v7 offset:7168\ns_waitcnt lgkmcnt(0)\nds_read_b128 v[84:87], v7 offset:8192\nds_read_b128 v[88:91], v7 offset:9216\n" \
    "ds_read_b128 v[60:63], v7 offset:10240\nds_read_b128 v[64:67], v7 offset:11264\nds_read_b128 v[68:71], v7 offset:12288\nds_read_b128 v[72:75], v7 offset:13312\n" \
    "ds_read_b128 v[76:79], v7 offset:14336\nds_read_b128 v[80:83], v7 offset:15360\ns_mov_b32 s68, 0\n.Lchq3_%=:\n.Lchq9_%=:\ns_memrealtime s[72:73]\ns_waitcnt lgkmcnt(0)\n" \
    ".Lchq10_%=:\ns_sub_u32 s72, s72, s63\ns_cmp_lt_i32 s72, -3\ns_cbranch_scc1 .Lchq9_%=\ns_cmp_ge_i32 s72, 0\ns_cbranch_scc1 .Lchq11_%=\ns_nop 2\ns_cmp_ge_i32 s72, -1\n" \
    "s_cbranch_scc1 .Lchq11_%=\ns_nop 2\ns_cmp_ge_i32 s72, -2\ns_cbranch_scc1 .Lchq11_%=\ns_nop 2\n.Lchq11_%=:\n" \
    "buffer_load_dwordx4 v[40:43], v2, s[64:67], s48 offen offset:0 sc1\nbuffer_load_dwordx4 v[44:47], v2, s[64:67], s48 offen offset:1024 sc1\n" \
    "buffer_load_dwordx4 v[48:51], v2, s[64:67], s48 offen offset:2048 sc1\nbuffer_load_dwordx4 v[52:55], v2, s[64:67], s48 offen offset:3072 sc1\nv_mov_b32 v24, 0\n" \
    "v_mov_b32 v25, 0\nv_mov_b32 v26, 0\nv_mov_b32 v27, 0\nv_mov_b32 v28, 0\nv_mov_b32 v29, 0\nv_mov_b32 v30, 0\nv_mov_b32 v31, 0\nv_mov_b32 v32, 0\nv_mov_b32 v33, 0\n" \
    "v_mov_b32 v34, 0\nv_mov_b32 v35, 0\nv_mov_b32 v36, 0\nv_mov_b32 v37, 0\nv_mov_b32 v38, 0\nv_mov_b32 v39, 0\ns_waitcnt vmcnt(3)\n" \
    "v_smfmac_f32_16x16x64_bf16 v[24:27], v[40:43], a[0:7], v0\nds_write_b128 v3, v[40:43] offset:0\nv_smfmac_f32_16x16x64_bf16 v[28:31], v[40:43], a[8:15], v0\n" \
    "v_max3_u32 v252, v40, v41, v42\ns_waitcnt vmcnt(2)\nds_write_b128 v3, v[44:47] offset:1024\nv_smfmac_f32_16x16x64_bf16 v[32:35], v[40:43], a[16:23], v0\n" \
    "v_max3_u32 v252, v252, v43, v44\nv_max3_u32 v253, v45, v46, v47\ns_waitcnt vmcnt(1)\nds_write_b128 v3, v[48:51] offset:2048\n" \
    "v_smfmac_f32_16x16x64_bf16 v[36:39], v[40:43], v[220:227], v0\nv_max3_u32 v252, v252, v48, v49\nv_max3_u32 v253, v253, v50, v51\ns_waitcnt vmcnt(0)\n" \
    "ds_write_b128 v3, v[52:55] offset:3072\nv_smfmac_f32_16x16x64_bf16 v[24:27], v[44:47], a[24:31], v0\nv_max3_u32 v252, v252, v52, v53\nv_max3_u32 v253, v253, v54, v55\n" \
    "v_smfmac_f32_16x16x64_bf16 v[28:31], v[44:47], a[32:39], v0\nv_max_u32 v252, v252, v253\nv_cmp_eq_u32 vcc, -1, v252\n" \
    "v_smfmac_f32_16x16x64_bf16 v[32:35], v[44:47], a[40:47], v0\nv_smfmac_f32_16x16x64_bf16 v[36:39], v[44:47], v[228:235], v0\ns_cbranch_vccnz .Lchq4_%=\n" \
    "s_waitcnt lgkmcnt(0)\ns_barrier\nv_smfmac_f32_16x16x64_bf16 v[36:39], v[48:51], v[236:243], v0\nbuffer_load_dword v21, v15, s[64:67], s49 offen sc1\n" \
    "v_smfmac_f32_16x16x64_bf16 v[32:35], v[48:51], a[64:71], v0\nds_read_b128 v[56:59], v4 offset:0\nv_smfmac_f32_16x16x64_bf16 v[28:31], v[48:51], a[56:63], v0\n" \
    "ds_read_b128 v[40:43], v4 offset:1024\nv_smfmac_f32_16x16x64_bf16 v[24:27], v[48:51], a[48:55], v0\nds_read_b128 v[44:47], v4 offset:2048\n" \
    "v_smfmac_f32_16x16x64_bf16 v[36:39], v[52:55], v[244:251], v0\nds_read_b128 v[48:51], v4 offset:3072\nv_smfmac_f32_16x16x64_bf16 v[32:35], v[52:55], a[88:95], v0\n" \
    "ds_read_b128 v[220:223], v7 offset:16384\nv_smfmac_f32_16x16x64_bf16 v[28:31], v[52:55], a[80:87], v0\nds_read_b128 v[224:227], v7 offset:17408\n" \
    "v_smfmac_f32_16x16x64_bf16 v[24:27], v[52:55], a[72:79], v0\nds_read_b128 v[52:55], v5 offset:0\ns_waitcnt lgkmcnt(6)\n" \
    "v_smfmac_f32_16x16x64_bf16 v[36:39], v[56:59], v[84:91], v0\nds_read_b128 v[84:87], v5 offset:1024\nv_smfmac_f32_16x16x64_bf16 v[32:35], v[56:59], a[112:119], v0\n" \
    "ds_read_b128 v[228:231], v7 offset:18432\ns_waitcnt lgkmcnt(7)\nv_smfmac_f32_16x16x64_bf16 v[36:39], v[40:43], v[60:67], v0\nds_read_b128 v[232:235], v7 offset:19456\n" \
    "v_smfmac_f32_16x16x64_bf16 v[32:35], v[40:43], a[136:143], v0\nds_read_b128 v[88:91], v5 offset:2048\ns_waitcnt lgkmcnt(8)\n" \
    "v_smfmac_f32_16x16x64_bf16 v[36:39], v[44:47], v[68:75], v0\nds_read_b128 v[236:239], v7 offset:20480\nv_smfmac_f32_16x16x64_bf16 v[32:35], v[44:47], a[160:167], v0\n" \
    "ds_read_b128 v[240:243], v7 offset:21504\ns_waitcnt lgkmcnt(9)\nv_smfmac_f32_16x16x64_bf16 v[36:39], v[48:51], v[76:83], v0\nds_read_b128 v[56:59], v5 offset:3072\n" \
    "v_smfmac_f32_16x16x64_bf16 v[32:35], v[48:51], a[184:191], v0\nds_read_b128 v[244:247], v7 offset:22528\ns_waitcnt lgkmcnt(8)\n" \
    "v_smfmac_f32_16x16x64_bf16 v[36:39], v[52:55], v[220:227], v0\nds_read_b128 v[248:251], v7 offset:23552\nv_smfmac_f32_16x16x64_bf16 v[32:35], v[52:55], a[208:215], v0\n" \
    "ds_read_b128 v[40:43], v6 offset:0\ns_waitcnt lgkmcnt(7)\nv_smfmac_f32_16x16x64_bf16 v[36:39], v[84:87], v[228:235], v0\nds_read_b128 v[60:63], v7 offset:24576\n" \
    "ds_read_b128 v[64:67], v7 offset:25600\nv_smfmac_f32_16x16x64_bf16 v[32:35], v[84:87], a[232:239], v0\nds_read_b128 v[44:47], v6 offset:1024\ns_waitcnt lgkmcnt(7)\n" \
    "v_smfmac_f32_16x16x64_bf16 v[36:39], v[88:91], v[236:243], v0\nds_read_b128 v[68:71], v7 offset:26624\nds_read_b128 v[72:75], v7 offset:27648\n" \
    "v_smfmac_f32_16x16x64_bf16 v[32:35], v[88:91], v[92:99], v0\nds_read_b128 v[48:51], v6 offset:2048\ns_waitcnt lgkmcnt(7)\n" \
    "v_smfmac_f32_16x16x64_bf16 v[36:39], v[56:59], v[244:251], v0\nds_read_b128 v[76:79], v7 offset:28672\nds_read_b128 v[80:83], v7 offset:29696\n" \
    "v_smfmac_f32_16x16x64_bf16 v[32:35], v[56:59], v[116:123], v0\nds_read_b128 v[52:55], v6 offset:3072\ns_waitcnt lgkmcnt(7)\n" \
    "v_smfmac_f32_16x16x64_bf16 v[36:39], v[40:43], v[60:67], v0\nds_read_b128 v[220:223], v7 offset:30720\nds_read_b128 v[224:227], v7 offset:31744\n" \
    "v_smfmac_f32_16x16x64_bf16 v[32:35], v[40:43], v[140:147], v0\ns_waitcnt lgkmcnt(6)\nv_smfmac_f32_16x16x64_bf16 v[36:39], v[44:47], v[68:75], v0\n" \
    "v_smfmac_f32_16x16x64_bf16 v[32:35], v[44:47], v[164:171], v0\ns_waitcnt lgkmcnt(3)\nv_smfmac_f32_16x16x64_bf16 v[36:39], v[48:51], v[76:83], v0\n" \
    "ds_read_b128 v[76:79], v8 offset:0\nv_smfmac_f32_16x16x64_bf16 v[32:35], v[48:51], v[188:195], v0\nds_read_b128 v[80:83], v8 offset:256\ns_waitcnt lgkmcnt(2)\n" \
    "v_smfmac_f32_16x16x64_bf16 v[36:39], v[52:55], v[220:227], v0\nv_smfmac_f32_16x16x64_bf16 v[32:35], v[52:55], v[212:219], v0\n" \
    "v_smfmac_f32_16x16x64_bf16 v[24:27], v[84:87], a[216:223], v0\ns_nop 0\nv_smfmac_f32_16x16x64_bf16 v[28:31], v[84:87], a[224:231], v0\nds_read_b128 v[84:87], v4 offset:0\n" \
    "s_nop 0\nv_smfmac_f32_16x16x64_bf16 v[24:27], v[88:91], a[240:247], v0\ns_nop 1\nv_permlane32_swap_b32 v32, v36\n" \
    "v_smfmac_f32_16x16x64_bf16 v[28:31], v[88:91], a[248:255], v0\nds_read_b128 v[88:91], v4 offset:1024\nv_permlane32_swap_b32 v33, v37\n" \
    "v_smfmac_f32_16x16x64_bf16 v[24:27], v[56:59], v[100:107], v0\nv_add_f32 v32, v32, v36\nv_smfmac_f32_16x16x64_bf16 v[28:31], v[56:59], v[108:115], v0\n" \
    "ds_read_b128 v[56:59], v4 offset:2048\nv_add_f32 v33, v33, v37\nv_smfmac_f32_16x16x64_bf16 v[24:27], v[40:43], v[124:131], v0\nv_permlane32_swap_b32 v34, v38\n" \
    "v_smfmac_f32_16x16x64_bf16 v[28:31], v[40:43], v[132:139], v0\nds_read_b128 v[40:43], v4 offset:3072\nv_permlane32_swap_b32 v35, v39\n" \
    "v_smfmac_f32_16x16x64_bf16 v[24:27], v[44:47], v[148:155], v0\nv_add_f32 v34, v34, v38\nv_smfmac_f32_16x16x64_bf16 v[28:31], v[44:47], v[156:163], v0\n" \
    "ds_read_b128 v[44:47], v5 offset:0\nv_add_f32 v35, v35, v39\nv_smfmac_f32_16x16x64_bf16 v[24:27], v[48:51], v[172:179], v0\nv_cvt_pk_bf16_f32 v32, v32, v33\n" \
    "v_smfmac_f32_16x16x64_bf16 v[28:31], v[48:51], v[180:187], v0\nds_read_b128 v[48:51], v8 offset:2048\nv_cvt_pk_bf16_f32 v33, v34, v35\n" \
    "v_smfmac_f32_16x16x64_bf16 v[24:27], v[52:55], v[196:203], v0\nds_write_b64 v9, v[32:33]\nv_smfmac_f32_16x16x64_bf16 v[28:31], v[52:55], v[204:211], v0\n" \
    "ds_read_b128 v[52:55], v8 offset:2304\ns_waitcnt lgkmcnt(6)\nv_smfmac_f32_16x16x64_bf16 v[24:27], v[84:87], a[96:103], v0\n" \
    "v_smfmac_f32_16x16x64_bf16 v[28:31], v[84:87], a[104:111], v0\nds_read_b128 v[84:87], v8 offset:4096\nv_smfmac_f32_16x16x64_bf16 v[24:27], v[88:91], a[120:127], v0\n" \
    "s_waitcnt lgkmcnt(2)\ns_barrier\nv_smfmac_f32_16x16x64_bf16 v[28:31], v[88:91], a[128:135], v0\nds_read_b64_tr_b16 v[60:61], v10 offset:0\n" \
    "v_smfmac_f32_16x16x64_bf16 v[24:27], v[56:59], a[144:151], v0\nds_read_b64_tr_b16 v[62:63], v10 offset:64\nv_smfmac_f32_16x16x64_bf16 v[28:31], v[56:59], a[152:159], v0\n" \
    "ds_read_b64_tr_b16 v[64:65], v10 offset:512\nv_smfmac_f32_16x16x64_bf16 v[24:27], v[40:43], a[168:175], v0\nds_read_b64_tr_b16 v[66:67], v10 offset:576\n" \
    "v_smfmac_f32_16x16x64_bf16 v[28:31], v[40:43], a[176:183], v0\nds_read_b64_tr_b16 v[68:69], v10 offset:1024\nv_smfmac_f32_16x16x64_bf16 v[24:27], v[44:47], a[192:199], v0\n" \
    "ds_read_b64_tr_b16 v[70:71], v10 offset:1088\nv_smfmac_f32_16x16x64_bf16 v[28:31], v[44:47], a[200:207], v0\nds_read_b64_tr_b16 v[72:73], v10 offset:1536\n" \
    "s_waitcnt lgkmcnt(5)\nv_mfma_f32_16x16x32_bf16 v[32:35], v[76:79], v[60:63], 0\nds_read_b64_tr_b16 v[74:75], v10 offset:1600\ns_nop 0\n" \
    "v_mfma_f32_16x16x32_bf16 v[36:39], v[80:83], v[60:63], 0\nds_read_b128 v[88:91], v8 offset:4352\ns_nop 0\nv_permlane32_swap_b32 v24, v28\nv_permlane32_swap_b32 v25, v29\n" \
    "v_permlane32_swap_b32 v26, v30\nv_permlane32_swap_b32 v27, v31\nv_pk_add_f32 v[24:25], v[24:25], v[28:29]\nv_pk_add_f32 v[26:27], v[26:27], v[30:31]\n" \
    "v_cvt_pk_bf16_f32 v24, v24, v25\nv_cvt_pk_bf16_f32 v25, v26, v27\nds_write_b64 v9, v[24:25] offset:2048\ns_waitcnt lgkmcnt(6)\n" \
    "v_mfma_f32_16x16x32_bf16 v[32:35], v[48:51], v[64:67], v[32:35]\nds_read_b128 v[56:59], v8 offset:6144\nv_mfma_f32_16x16x32_bf16 v[36:39], v[52:55], v[64:67], v[36:39]\n" \
    "ds_read_b128 v[40:43], v8 offset:6400\ns_waitcnt lgkmcnt(6)\nv_mfma_f32_16x16x32_bf16 v[32:35], v[84:87], v[68:71], v[32:35]\ns_waitcnt lgkmcnt(3)\n" \
    "v_mfma_f32_16x16x32_bf16 v[36:39], v[88:91], v[68:71], v[36:39]\ns_xor_b32 s74, s50, 0x1000\ns_cmp_eq_u32 s61, 63\ns_waitcnt lgkmcnt(0)\ns_barrier\n" \
    "s_cbranch_scc1 .Lchq7_%=\nds_read_b64_tr_b16 v[76:77], v10 offset:2048\nds_read_b64_tr_b16 v[78:79], v10 offset:2112\nds_read_b64_tr_b16 v[80:81], v10 offset:2560\n" \
    "v_mfma_f32_16x16x32_bf16 v[32:35], v[56:59], v[72:75], v[32:35]\nds_read_b64_tr_b16 v[82:83], v10 offset:2624\nds_read_b64_tr_b16 v[44:45], v10 offset:3072\n" \
    "ds_read_b64_tr_b16 v[46:47], v10 offset:3136\nv_mfma_f32_16x16x32_bf16 v[36:39], v[40:43], v[72:75], v[36:39]\nds_read_b64_tr_b16 v[48:49], v10 offset:3584\n" \
    "ds_read_b64_tr_b16 v[50:51], v10 offset:3648\ns_waitcnt lgkmcnt(6)\nv_mfma_f32_16x16x32_bf16 v[28:31], v[76:79], v[60:63], 0\ns_waitcnt lgkmcnt(4)\n" \
    "v_mfma_f32_16x16x32_bf16 v[28:31], v[80:83], v[64:67], v[28:31]\ns_waitcnt lgkmcnt(2)\ns_nop 0\nv_mov_b32_dpp v32, v36 quad_perm:[0,1,2,3] row_mask:0xf bank_mask:0xc\n" \
    "v_mov_b32_dpp v33, v37 quad_perm:[0,1,2,3] row_mask:0xf bank_mask:0xc\nv_mfma_f32_16x16x32_bf16 v[28:31], v[44:47], v[68:71], v[28:31]\ns_waitcnt lgkmcnt(0)\n" \
    "v_mov_b32_dpp v34, v38 quad_perm:[0,1,2,3] row_mask:0xf bank_mask:0xc\nv_mov_b32_dpp v35, v39 quad_perm:[0,1,2,3] row_mask:0xf bank_mask:0xc\n" \
    "v_mfma_f32_16x16x32_bf16 v[28:31], v[48:51], v[72:75], v[28:31]\nv_max3_f32 v36, v32, v33, v34\nv_max_f32 v36, v36, v35\ns_nop 1\n" \
    "v_max_f32_dpp v37, v36, v36 row_ror:8 row_mask:0xf bank_mask:0xf\nv_max_f32_dpp v38, v36, v36 row_ror:8 row_mask:0xf bank_mask:0xf\ns_nop 1\n" \
    "v_permlane32_swap_b32 v37, v38\nv_max3_f32 v36, v28, v29, v30\nv_max3_f32 v39, v31, v37, v38\nv_max_f32 v37, v36, v39\nv_max_f32 v36, v36, v39\ns_nop 1\n" \
    "v_permlane16_swap_b32 v36, v37\nv_max_f32 v36, v36, v37\nv_mul_f32_e64 v36, -s32, v36\nv_pk_fma_f32 v[32:33], v[32:33], s[32:33], v[36:37] op_sel_hi:[1,0,0]\n" \
    "v_pk_fma_f32 v[34:35], v[34:35], s[32:33], v[36:37] op_sel_hi:[1,0,0]\nv_pk_fma_f32 v[28:29], v[28:29], s[32:33], v[36:37] op_sel_hi:[1,0,0]\n" \
    "v_pk_fma_f32 v[30:31], v[30:31], s[32:33], v[36:37] op_sel_hi:[1,0,0]\nv_exp_f32 v32, v32\nv_exp_f32 v33, v33\nv_exp_f32 v34, v34\nv_exp_f32 v35, v35\nv_exp_f32 v28, v28\n" \
    "v_pk_add_f32 v[36:37], v[32:33], v[34:35]\nv_add_f32 v36, v36, v37\nv_exp_f32 v29, v29\nv_exp_f32 v30, v30\n" \
    "v_add_f32_dpp v37, v36, v36 row_ror:8 row_mask:0xf bank_mask:0xf\nv_add_f32_dpp v38, v36, v36 row_ror:8 row_mask:0xf bank_mask:0xf\nv_exp_f32 v31, v31\n" \
    "v_add_f32 v36, v28, v29\nv_permlane32_swap_b32 v37, v38\nv_add_f32 v39, v30, v31\nv_pk_add_f32 v[36:37], v[36:37], v[38:39] op_sel:[0,1] op_sel_hi:[1,0]\n" \
    "v_add_f32 v38, v36, v37\nv_add_f32 v36, v36, v37\ns_nop 1\nv_permlane16_swap_b32 v36, v38\nv_add_f32 v36, v36, v38\nv_rcp_f32 v36, v36\ns_nop 0\n" \
    "v_pk_mul_f32 v[28:29], v[28:29], v[36:37] op_sel_hi:[1,0]\nv_pk_mul_f32 v[30:31], v[30:31], v[36:37] op_sel_hi:[1,0]\nv_cvt_pk_bf16_f32 v76, v28, v29\n" \
    "v_cvt_pk_bf16_f32 v77, v30, v31\nv_pk_mul_f32 v[32:33], v[32:33], v[36:37] op_sel_hi:[1,0]\nv_pk_mul_f32 v[34:35], v[34:35], v[36:37] op_sel_hi:[1,0]\n" \
    "v_permlane16_swap_b32 v76, v78\nv_cvt_pk_bf16_f32 v32, v32, v33\nv_permlane16_swap_b32 v77, v79\nv_cvt_pk_bf16_f32 v33, v34, v35\n" \
    "v_mov_b32_dpp v76, v32 quad_perm:[0,1,2,3] row_mask:0xf bank_mask:0x3\nv_mov_b32_dpp v78, v32 row_ror:8 row_mask:0xf bank_mask:0x3\n" \
    "v_mov_b32_dpp v77, v33 quad_perm:[0,1,2,3] row_mask:0xf bank_mask:0x3\nv_mov_b32_dpp v79, v33 row_ror:8 row_mask:0xf bank_mask:0x3\ns_mov_b64 exec, s[46:47]\n" \
    "buffer_store_dwordx4 v[76:79], v11, s[64:67], s50 offen sc1\nbuffer_store_dwordx4 v[76:79], v11, s[64:67], s74 offen sc1\ns_mov_b64 exec, -1\ns_memrealtime s[72:73]\n" \
    "s_add_u32 s63, s63, s44\ns_waitcnt lgkmcnt(0)\ns_sub_u32 s69, s63, s72\ns_cmp_lt_i32 s69, s37\ns_cbranch_scc1 .Lchq12_%=\n.Lchq13_%=:\nds_read_b64_tr_b16 v[56:57], v13\n" \
    "ds_read_b64_tr_b16 v[58:59], v13 offset:64\ns_waitcnt lgkmcnt(0)\ns_mov_b32 exec_lo, 0x00ff00ff\ns_mov_b32 exec_hi, 0x00ff00ff\n" \
    "buffer_store_dwordx4 v[56:59], v14, s[64:67], s51 offen sc1\ns_mov_b64 exec, -1\ns_add_u32 s63, s63, s45\ns_mov_b32 s68, 0\ns_cmp_gt_i32 s60, s61\n" \
    "s_cbranch_scc0 .Lchq5_%=\n.Lchq6_%=:\nbuffer_load_dwordx4 v1, s[64:67], s56 offen offset:0 sc1 lds\nbuffer_load_dwordx4 v1, s[64:67], s56 offen offset:1024 sc1 lds\n" \
    "s_add_u32 s61, s61, 1\ns_and_b32 s69, s61, %[rm]\ns_cselect_b32 s78, 1, %[rw]\ns_cmp_lg_u32 s69, %[rm]\ns_cselect_b32 s79, 1, %[rw]\ns_lshl_b32 s77, s78, 18\n" \
    "s_add_u32 s74, s40, s77\ns_xor_b32 s69, s74, s40\ns_mov_b32 s40, s74\ns_lshr_b32 s69, s69, 8\ns_and_b32 s69, s69, 0x2000\ns_add_u32 s48, s48, s77\ns_xor_b32 s48, s48, s69\n" \
    "s_add_u32 s69, s61, 2\ns_and_b32 s69, s69, %[rm]\ns_lshl_b32 s69, s69, 19\ns_add_u32 s69, s69, s52\ns_add_u32 s74, s53, s69\ns_lshr_b32 s74, s74, 8\n" \
    "s_xor_b32 s74, s74, s54\ns_and_b32 s74, s74, 0x2000\ns_add_u32 s49, s74, s69\ns_lshl_b32 s77, s78, 19\ns_add_u32 s74, s41, s77\ns_xor_b32 s69, s74, s41\n" \
    "s_mov_b32 s41, s74\ns_lshr_b32 s69, s69, 8\ns_and_b32 s69, s69, 0x2000\ns_add_u32 s50, s50, s77\ns_xor_b32 s50, s50, s69\ns_add_u32 s51, s51, s77\ns_xor_b32 s51, s51, s69\n" \
    "s_lshl_b32 s77, s79, 19\ns_add_u32 s74, s35, s77\ns_xor_b32 s69, s74, s35\ns_mov_b32 s35, s74\ns_lshr_b32 s69, s69, 8\ns_and_b32 s69, s69, 0x2000\ns_add_u32 s56, s56, s77\n" \
    "s_xor_b32 s56, s56, s69\ns_cmp_eq_u32 s61, %[s1]\ns_cselect_b32 s69, %[a1], 0\ns_cselect_b32 s74, %[b1], 0\ns_sub_u32 s44, s44, s69\ns_sub_u32 s45, s45, s74\n" \
    "s_cmp_eq_u32 s61, %[s2]\ns_cselect_b32 s69, %[a2], 0\ns_cselect_b32 s74, %[b2], 0\ns_sub_u32 s44, s44, s69\ns_sub_u32 s45, s45, s74\ns_branch .Lchq1_%=\n.Lchq4_%=:\n" \
    "s_add_u32 s63, s63, s38\ns_xor_b32 s48, s48, 0x1000\ns_xor_b32 s50, s50, 0x1000\ns_add_u32 s68, s68, 1\ns_and_b32 s69, s68, 63\ns_cbranch_scc1 .Lchq3_%=\n" \
    "s_cmp_gt_u32 s68, 0x100000\ns_cbranch_scc1 .Lchq8_%=\nv_mov_b32 v252, 0\nbuffer_load_dword v253, v252, s[64:67], 0 offen sc1\ns_waitcnt vmcnt(0)\n" \
    "v_readfirstlane_b32 s69, v253\ns_cmp_eq_u32 s69, 1\ns_cbranch_scc0 .Lchq3_%=\ns_endpgm\n.Lchq5_%=:\nbuffer_load_dword v22, v12, s[64:67], 0 offen sc1\ns_waitcnt vmcnt(0)\n" \
    "v_readfirstlane_b32 s74, v22\ns_max_i32 s60, s60, s74\ns_cmp_gt_i32 s60, s61\ns_cbranch_scc1 .Lchq14_%=\ns_sleep 4\ns_add_u32 s68, s68, 1\ns_and_b32 s69, s68, 63\n" \
    "s_cbranch_scc1 .Lchq5_%=\ns_cmp_gt_u32 s68, 0x100000\ns_cbranch_scc1 .Lchq8_%=\nv_mov_b32 v252, 0\nbuffer_load_dword v253, v252, s[64:67], 0 offen sc1\ns_waitcnt vmcnt(0)\n" \
    "v_readfirstlane_b32 s69, v253\ns_cmp_eq_u32 s69, 1\ns_cbranch_scc0 .Lchq5_%=\ns_endpgm\n.Lchq14_%=:\ns_memrealtime s[72:73]\ns_waitcnt lgkmcnt(0)\n.Lchq15_%=:\n" \
    "s_sub_u32 s69, s63, s72\ns_cmp_ge_i32 s69, 20\ns_cbranch_scc1 .Lchq6_%=\ns_add_u32 s63, s63, s38\ns_xor_b32 s48, s48, 0x1000\ns_xor_b32 s50, s50, 0x1000\n" \
    "s_branch .Lchq15_%=\n.Lchq12_%=:\ns_add_u32 s63, s63, s38\ns_xor_b32 s48, s48, 0x1000\ns_xor_b32 s50, s50, 0x1000\ns_sub_u32 s69, s63, s72\ns_cmp_lt_i32 s69, s37\n" \
    "s_cbranch_scc1 .Lchq12_%=\ns_branch .Lchq13_%=\n.Lchq8_%=:\nv_mov_b32 v252, 0\nv_mov_b32 v253, 1\nbuffer_store_dword v253, v252, s[64:67], 0 offen sc1\ns_endpgm\n" \
    ".Lchq7_%=:\nds_read_b64_tr_b16 v[56:57], v13\nds_read_b64_tr_b16 v[58:59], v13 offset:64\ns_waitcnt lgkmcnt(0)\ns_mov_b32 exec_lo, 0x00ff00ff\n" \
    "s_mov_b32 exec_hi, 0x00ff00ff\nbuffer_store_dwordx4 v[56:59], v14, s[64:67], s51 offen sc1\ns_mov_b64 exec, -1\n.Lchq2_%=:\ns_waitcnt vmcnt(0)\n"
#define CHAIN_V \
    "s_mov_b32 m0, s58\n.Lchv1_%=:\nds_read_b128 v[220:223], v7 offset:0\nds_read_b128 v[224:227], v7 offset:1024\nds_read_b128 v[228:231], v7 offset:2048\n" \
    "ds_read_b128 v[232:235], v7 offset:3072\nds_read_b128 v[236:239], v7 offset:4096\nds_read_b128 v[240:243], v7 offset:5120\nds_read_b128 v[244:247], v7 offset:6144\n" \
    "ds_read_b128 v[248:251], v7 offset:7168\ns_waitcnt lgkmcnt(0)\nds_read_b128 v[60:63], v7 offset:8192\nds_read_b128 v[64:67], v7 offset:9216\n" \
    "ds_read_b128 v[68:71], v7 offset:10240\nds_read_b128 v[72:75], v7 offset:11264\nds_read_b128 v[76:79], v7 offset:12288\nds_read_b128 v[80:83], v7 offset:13312\n" \
    "ds_read_b128 v[84:87], v7 offset:14336\nds_read_b128 v[88:91], v7 offset:15360\ns_mov_b32 s68, 0\n.Lchv3_%=:\n.Lchv9_%=:\ns_memrealtime s[72:73]\ns_waitcnt lgkmcnt(0)\n" \
    ".Lchv10_%=:\ns_sub_u32 s72, s72, s63\ns_cmp_lt_i32 s72, -3\ns_cbranch_scc1 .Lchv9_%=\ns_cmp_ge_i32 s72, 0\ns_cbranch_scc1 .Lchv11_%=\ns_nop 2\ns_cmp_ge_i32 s72, -1\n" \
    "s_cbranch_scc1 .Lchv11_%=\ns_nop 2\ns_cmp_ge_i32 s72, -2\ns_cbranch_scc1 .Lchv11_%=\ns_nop 2\n.Lchv11_%=:\n" \
    "buffer_load_dwordx4 v[32:35], v2, s[64:67], s48 offen offset:0 sc1\nbuffer_load_dwordx4 v[36:39], v2, s[64:67], s48 offen offset:1024 sc1\n" \
    "buffer_load_dwordx4 v[40:43], v2, s[64:67], s48 offen offset:2048 sc1\nbuffer_load_dwordx4 v[44:47], v2, s[64:67], s48 offen offset:3072 sc1\nv_mov_b32 v24, 0\n" \
    "v_mov_b32 v25, 0\nv_mov_b32 v26, 0\nv_mov_b32 v27, 0\nv_mov_b32 v28, 0\nv_mov_b32 v29, 0\nv_mov_b32 v30, 0\nv_mov_b32 v31, 0\ns_add_u32 s77, s63, s44\n" \
    "s_lshl_b32 s77, s77, 8\ns_add_u32 s77, s77, s61\ns_sub_u32 s77, s77, 45055\ns_lshr_b32 s74, s48, 5\ns_and_b32 s74, s74, 128\ns_or_b32 s77, s77, s74\nv_mov_b32 v56, s77\n" \
    "s_waitcnt vmcnt(3)\nv_smfmac_f32_16x16x64_bf16 v[24:27], v[32:35], a[0:7], v0\nds_write_b128 v3, v[32:35] offset:0\nv_max3_u32 v252, v32, v33, v34\ns_waitcnt vmcnt(2)\n" \
    "ds_write_b128 v3, v[36:39] offset:1024\nv_smfmac_f32_16x16x64_bf16 v[28:31], v[32:35], a[8:15], v0\nv_max3_u32 v252, v252, v35, v36\nv_max3_u32 v253, v37, v38, v39\n" \
    "s_waitcnt vmcnt(1)\nds_write_b128 v3, v[40:43] offset:2048\nv_smfmac_f32_16x16x64_bf16 v[24:27], v[36:39], a[16:23], v0\nv_max3_u32 v252, v252, v40, v41\n" \
    "v_max3_u32 v253, v253, v42, v43\ns_waitcnt vmcnt(0)\nds_write_b128 v3, v[44:47] offset:3072\nv_max3_u32 v252, v252, v44, v45\nv_max3_u32 v253, v253, v46, v47\n" \
    "v_smfmac_f32_16x16x64_bf16 v[28:31], v[36:39], v[220:227], v0\nv_max_u32 v252, v252, v253\nv_cmp_eq_u32 vcc, -1, v252\ns_cbranch_vccnz .Lchv4_%=\ns_waitcnt lgkmcnt(0)\n" \
    "s_barrier\nds_read_b128 v[48:51], v4 offset:0\nv_smfmac_f32_16x16x64_bf16 v[24:27], v[40:43], a[24:31], v0\nds_read_b128 v[52:55], v4 offset:1024\n" \
    "v_smfmac_f32_16x16x64_bf16 v[28:31], v[40:43], a[32:39], v0\nds_read_b128 v[32:35], v4 offset:2048\nv_smfmac_f32_16x16x64_bf16 v[24:27], v[44:47], a[40:47], v0\n" \
    "ds_read_b128 v[36:39], v4 offset:3072\nv_smfmac_f32_16x16x64_bf16 v[28:31], v[44:47], v[228:235], v0\nds_read_b128 v[40:43], v5 offset:0\ns_waitcnt lgkmcnt(4)\n" \
    "v_smfmac_f32_16x16x64_bf16 v[24:27], v[48:51], a[48:55], v0\nbuffer_store_dword v56, v14, s[64:67], s51 offen sc1\n" \
    "v_smfmac_f32_16x16x64_bf16 v[28:31], v[48:51], a[56:63], v0\nds_read_b128 v[44:47], v5 offset:1024\ns_waitcnt lgkmcnt(4)\n" \
    "v_smfmac_f32_16x16x64_bf16 v[24:27], v[52:55], a[64:71], v0\nds_read_b128 v[48:51], v5 offset:2048\nv_smfmac_f32_16x16x64_bf16 v[28:31], v[52:55], v[236:243], v0\n" \
    "ds_read_b128 v[52:55], v5 offset:3072\ns_waitcnt lgkmcnt(5)\nv_smfmac_f32_16x16x64_bf16 v[24:27], v[32:35], a[72:79], v0\n" \
    "buffer_store_dword v56, v8, s[64:67], 0 offen sc1\nv_smfmac_f32_16x16x64_bf16 v[28:31], v[32:35], a[80:87], v0\nbuffer_load_dword v21, v15, s[64:67], s49 offen sc1\n" \
    "s_waitcnt lgkmcnt(4)\nv_smfmac_f32_16x16x64_bf16 v[24:27], v[36:39], a[88:95], v0\nds_read_b128 v[32:35], v6 offset:0\n" \
    "v_smfmac_f32_16x16x64_bf16 v[28:31], v[36:39], v[244:251], v0\nds_read_b128 v[36:39], v6 offset:1024\ns_waitcnt lgkmcnt(5)\n" \
    "v_smfmac_f32_16x16x64_bf16 v[24:27], v[40:43], a[96:103], v0\nv_smfmac_f32_16x16x64_bf16 v[28:31], v[40:43], a[104:111], v0\nds_read_b128 v[40:43], v6 offset:2048\n" \
    "s_waitcnt lgkmcnt(5)\nv_smfmac_f32_16x16x64_bf16 v[24:27], v[44:47], a[112:119], v0\nv_smfmac_f32_16x16x64_bf16 v[28:31], v[44:47], v[60:67], v0\n" \
    "ds_read_b128 v[44:47], v6 offset:3072\ns_waitcnt lgkmcnt(5)\nv_smfmac_f32_16x16x64_bf16 v[24:27], v[48:51], a[120:127], v0\nds_read_b128 v[60:63], v7 offset:16384\n" \
    "v_smfmac_f32_16x16x64_bf16 v[28:31], v[48:51], a[128:135], v0\nds_read_b128 v[64:67], v7 offset:17408\ns_waitcnt lgkmcnt(6)\n" \
    "v_smfmac_f32_16x16x64_bf16 v[24:27], v[52:55], a[136:143], v0\nv_smfmac_f32_16x16x64_bf16 v[28:31], v[52:55], v[68:75], v0\nds_read_b128 v[68:71], v7 offset:18432\n" \
    "s_waitcnt lgkmcnt(6)\nv_smfmac_f32_16x16x64_bf16 v[24:27], v[32:35], a[144:151], v0\nds_read_b128 v[72:75], v7 offset:19456\n" \
    "v_smfmac_f32_16x16x64_bf16 v[28:31], v[32:35], a[152:159], v0\ns_waitcnt lgkmcnt(6)\nv_smfmac_f32_16x16x64_bf16 v[24:27], v[36:39], a[160:167], v0\n" \
    "v_smfmac_f32_16x16x64_bf16 v[28:31], v[36:39], v[76:83], v0\nds_read_b128 v[76:79], v7 offset:20480\ns_waitcnt lgkmcnt(6)\n" \
    "v_smfmac_f32_16x16x64_bf16 v[24:27], v[40:43], a[168:175], v0\nds_read_b128 v[80:83], v7 offset:21504\nv_smfmac_f32_16x16x64_bf16 v[28:31], v[40:43], a[176:183], v0\n" \
    "s_waitcnt lgkmcnt(6)\nv_smfmac_f32_16x16x64_bf16 v[24:27], v[44:47], a[184:191], v0\nv_smfmac_f32_16x16x64_bf16 v[28:31], v[44:47], v[84:91], v0\n" \
    "ds_read_b128 v[84:87], v7 offset:22528\nds_read_b128 v[88:91], v7 offset:23552\nv_mov_b64 v[48:49], 0\nv_mov_b64 v[50:51], 0\nv_mov_b64 v[52:53], 0\nv_mov_b64 v[54:55], 0\n" \
    "s_nop 1\nv_permlane32_swap_b32 v24, v28\nv_permlane32_swap_b32 v25, v29\nv_permlane32_swap_b32 v26, v30\nv_permlane32_swap_b32 v27, v31\n" \
    "v_pk_add_f32 v[24:25], v[24:25], v[28:29]\nv_pk_add_f32 v[26:27], v[26:27], v[30:31]\nv_cvt_pk_bf16_f32 v24, v24, v25\nv_cvt_pk_bf16_f32 v25, v26, v27\n" \
    "ds_write_b64 v9, v[24:25]\ns_xor_b32 s74, s50, 0x10000\ns_xor_b32 s75, s50, 0x20000\ns_xor_b32 s76, s50, 0x30000\ns_waitcnt lgkmcnt(0)\ns_barrier\n" \
    "ds_read_b64_tr_b16 v[32:33], v10 offset:0\nds_read_b64_tr_b16 v[34:35], v10 offset:64\nds_read_b64_tr_b16 v[36:37], v10 offset:512\n" \
    "ds_read_b64_tr_b16 v[38:39], v10 offset:576\nds_read_b64_tr_b16 v[40:41], v10 offset:1024\nds_read_b64_tr_b16 v[42:43], v10 offset:1088\n" \
    "ds_read_b64_tr_b16 v[44:45], v10 offset:1536\nds_read_b64_tr_b16 v[46:47], v10 offset:1600\nv_mov_b64 v[24:25], 0\nv_mov_b64 v[26:27], 0\nv_mov_b64 v[28:29], 0\n" \
    "v_mov_b64 v[30:31], 0\ns_waitcnt lgkmcnt(6)\nv_smfmac_f32_16x16x64_bf16 v[48:51], v[32:35], a[192:199], v0\nv_smfmac_f32_16x16x64_bf16 v[52:55], v[32:35], a[208:215], v0\n" \
    "s_waitcnt lgkmcnt(4)\nv_smfmac_f32_16x16x64_bf16 v[48:51], v[36:39], a[200:207], v0\nv_smfmac_f32_16x16x64_bf16 v[52:55], v[36:39], v[60:67], v0\ns_waitcnt lgkmcnt(2)\n" \
    "v_mov_b64 v[60:61], 0\nv_smfmac_f32_16x16x64_bf16 v[48:51], v[40:43], a[216:223], v0\nv_mov_b64 v[62:63], 0\nv_smfmac_f32_16x16x64_bf16 v[52:55], v[40:43], a[232:239], v0\n" \
    "s_waitcnt lgkmcnt(0)\nv_mov_b64 v[64:65], 0\nv_smfmac_f32_16x16x64_bf16 v[48:51], v[44:47], a[224:231], v0\nv_mov_b64 v[66:67], 0\n" \
    "v_smfmac_f32_16x16x64_bf16 v[52:55], v[44:47], v[68:75], v0\nv_mov_b64 v[68:69], 0\nv_smfmac_f32_16x16x64_bf16 v[24:27], v[32:35], a[240:247], v0\ns_nop 1\n" \
    "v_mov_b64 v[70:71], 0\nv_smfmac_f32_16x16x64_bf16 v[28:31], v[32:35], v[92:99], v0\ns_nop 1\nv_mov_b64 v[72:73], 0\n" \
    "v_smfmac_f32_16x16x64_bf16 v[24:27], v[36:39], a[248:255], v0\nv_cvt_pk_bf16_f32 v56, v48, v49\nv_smfmac_f32_16x16x64_bf16 v[28:31], v[36:39], v[76:83], v0\n" \
    "ds_read_b128 v[76:79], v7 offset:24576\nv_cvt_pk_bf16_f32 v57, v52, v53\nv_smfmac_f32_16x16x64_bf16 v[24:27], v[40:43], v[100:107], v0\n" \
    "ds_read_b128 v[80:83], v7 offset:25600\nv_cvt_pk_bf16_f32 v58, v50, v51\nv_smfmac_f32_16x16x64_bf16 v[28:31], v[40:43], v[116:123], v0\nv_cvt_pk_bf16_f32 v59, v54, v55\n" \
    "v_smfmac_f32_16x16x64_bf16 v[24:27], v[44:47], v[108:115], v0\nv_permlane16_swap_b32 v56, v57\nv_smfmac_f32_16x16x64_bf16 v[28:31], v[44:47], v[84:91], v0\n" \
    "ds_read_b128 v[84:87], v7 offset:26624\nv_permlane16_swap_b32 v58, v59\nv_smfmac_f32_16x16x64_bf16 v[60:63], v[32:35], v[124:131], v0\n" \
    "buffer_store_dwordx4 v[56:59], v11, s[64:67], s50 offen sc1\nv_smfmac_f32_16x16x64_bf16 v[64:67], v[32:35], v[140:147], v0\ns_nop 1\nv_mov_b64 v[74:75], 0\n" \
    "v_smfmac_f32_16x16x64_bf16 v[60:63], v[36:39], v[132:139], v0\nds_read_b128 v[88:91], v7 offset:27648\ns_waitcnt lgkmcnt(2)\nv_cvt_pk_bf16_f32 v56, v24, v25\n" \
    "v_smfmac_f32_16x16x64_bf16 v[64:67], v[36:39], v[76:83], v0\nds_read_b128 v[76:79], v7 offset:28672\nv_cvt_pk_bf16_f32 v57, v28, v29\n" \
    "v_smfmac_f32_16x16x64_bf16 v[60:63], v[40:43], v[148:155], v0\nds_read_b128 v[80:83], v7 offset:29696\nv_cvt_pk_bf16_f32 v58, v26, v27\n" \
    "v_smfmac_f32_16x16x64_bf16 v[64:67], v[40:43], v[164:171], v0\nv_cvt_pk_bf16_f32 v59, v30, v31\nv_smfmac_f32_16x16x64_bf16 v[60:63], v[44:47], v[156:163], v0\n" \
    "s_waitcnt lgkmcnt(2)\nv_permlane16_swap_b32 v56, v57\nv_smfmac_f32_16x16x64_bf16 v[64:67], v[44:47], v[84:91], v0\nds_read_b128 v[84:87], v7 offset:30720\n" \
    "v_permlane16_swap_b32 v58, v59\nv_smfmac_f32_16x16x64_bf16 v[68:71], v[32:35], v[172:179], v0\nbuffer_store_dwordx4 v[56:59], v11, s[64:67], s74 offen sc1\n" \
    "v_smfmac_f32_16x16x64_bf16 v[72:75], v[32:35], v[188:195], v0\ns_nop 1\nv_smfmac_f32_16x16x64_bf16 v[68:71], v[36:39], v[180:187], v0\n" \
    "ds_read_b128 v[88:91], v7 offset:31744\ns_waitcnt lgkmcnt(2)\nv_cvt_pk_bf16_f32 v56, v60, v61\nv_smfmac_f32_16x16x64_bf16 v[72:75], v[36:39], v[76:83], v0\n" \
    "v_cvt_pk_bf16_f32 v57, v64, v65\nv_smfmac_f32_16x16x64_bf16 v[68:71], v[40:43], v[196:203], v0\nv_cvt_pk_bf16_f32 v58, v62, v63\n" \
    "v_smfmac_f32_16x16x64_bf16 v[72:75], v[40:43], v[212:219], v0\nv_cvt_pk_bf16_f32 v59, v66, v67\nv_smfmac_f32_16x16x64_bf16 v[68:71], v[44:47], v[204:211], v0\n" \
    "s_waitcnt lgkmcnt(0)\nv_permlane16_swap_b32 v56, v57\nv_smfmac_f32_16x16x64_bf16 v[72:75], v[44:47], v[84:91], v0\nv_permlane16_swap_b32 v58, v59\n" \
    "buffer_store_dwordx4 v[56:59], v11, s[64:67], s75 offen sc1\ns_nop 2\nv_cvt_pk_bf16_f32 v48, v68, v69\nv_cvt_pk_bf16_f32 v50, v70, v71\ns_nop 0\n" \
    "v_cvt_pk_bf16_f32 v49, v72, v73\nv_cvt_pk_bf16_f32 v51, v74, v75\ns_nop 0\nv_permlane16_swap_b32 v48, v49\nv_permlane16_swap_b32 v50, v51\n" \
    "buffer_store_dwordx4 v[48:51], v11, s[64:67], s76 offen sc1\ns_cmp_eq_u32 s61, 63\ns_cbranch_scc1 .Lchv2_%=\ns_memrealtime s[72:73]\ns_add_u32 s63, s63, s44\n" \
    "s_waitcnt lgkmcnt(0)\ns_sub_u32 s69, s63, s72\ns_cmp_lt_i32 s69, s37\ns_cbranch_scc1 .Lchv12_%=\n.Lchv13_%=:\ns_add_u32 s63, s63, s45\ns_mov_b32 s68, 0\n" \
    "s_cmp_gt_i32 s60, s61\ns_cbranch_scc0 .Lchv5_%=\n.Lchv6_%=:\ns_add_u32 s61, s61, 1\ns_and_b32 s69, s61, %[rm]\ns_cselect_b32 s78, 1, %[rw]\ns_cmp_lg_u32 s69, %[rm]\n" \
    "s_cselect_b32 s79, 1, %[rw]\ns_lshl_b32 s77, s78, 18\ns_add_u32 s74, s40, s77\ns_xor_b32 s69, s74, s40\ns_mov_b32 s40, s74\ns_lshr_b32 s69, s69, 8\n" \
    "s_and_b32 s69, s69, 0x2000\ns_add_u32 s48, s48, s77\ns_xor_b32 s48, s48, s69\ns_sub_u32 s69, s61, 2\ns_max_i32 s69, s69, 0\ns_lshl_b32 s69, s69, 20\n" \
    "s_add_u32 s69, s69, s52\ns_add_u32 s74, s53, s69\ns_lshr_b32 s74, s74, 8\ns_xor_b32 s74, s74, s54\ns_and_b32 s74, s74, 0x2000\ns_add_u32 s49, s74, s69\n" \
    "s_lshl_b32 s77, s78, 20\ns_add_u32 s74, s42, s77\ns_xor_b32 s69, s74, s42\ns_mov_b32 s42, s74\ns_lshr_b32 s69, s69, 8\ns_and_b32 s69, s69, 0x2000\ns_add_u32 s50, s50, s77\n" \
    "s_xor_b32 s50, s50, s69\ns_lshl_b32 s77, s78, 19\ns_add_u32 s74, s41, s77\ns_xor_b32 s69, s74, s41\ns_mov_b32 s41, s74\ns_lshr_b32 s69, s69, 8\ns_and_b32 s69, s69, 0x2000\n" \
    "s_add_u32 s51, s51, s77\ns_xor_b32 s51, s51, s69\ns_cmp_eq_u32 s61, %[s1]\ns_cselect_b32 s69, %[a1], 0\ns_cselect_b32 s74, %[b1], 0\ns_sub_u32 s44, s44, s69\n" \
    "s_sub_u32 s45, s45, s74\ns_cmp_eq_u32 s61, %[s2]\ns_cselect_b32 s69, %[a2], 0\ns_cselect_b32 s74, %[b2], 0\ns_sub_u32 s44, s44, s69\ns_sub_u32 s45, s45, s74\n" \
    "s_branch .Lchv1_%=\n.Lchv4_%=:\ns_add_u32 s63, s63, s38\ns_xor_b32 s48, s48, 0x1000\ns_add_u32 s68, s68, 1\ns_and_b32 s69, s68, 63\ns_cbranch_scc1 .Lchv3_%=\n" \
    "s_cmp_gt_u32 s68, 0x100000\ns_cbranch_scc1 .Lchv8_%=\nv_mov_b32 v252, 0\nbuffer_load_dword v253, v252, s[64:67], 0 offen sc1\ns_waitcnt vmcnt(0)\n" \
    "v_readfirstlane_b32 s69, v253\ns_cmp_eq_u32 s69, 1\ns_cbranch_scc0 .Lchv3_%=\ns_endpgm\n.Lchv5_%=:\nbuffer_load_dword v22, v12, s[64:67], 0 offen sc1\ns_waitcnt vmcnt(0)\n" \
    "v_readfirstlane_b32 s74, v22\ns_max_i32 s60, s60, s74\ns_cmp_gt_i32 s60, s61\ns_cbranch_scc1 .Lchv14_%=\ns_sleep 4\ns_add_u32 s68, s68, 1\ns_and_b32 s69, s68, 63\n" \
    "s_cbranch_scc1 .Lchv5_%=\ns_cmp_gt_u32 s68, 0x100000\ns_cbranch_scc1 .Lchv8_%=\nv_mov_b32 v252, 0\nbuffer_load_dword v253, v252, s[64:67], 0 offen sc1\ns_waitcnt vmcnt(0)\n" \
    "v_readfirstlane_b32 s69, v253\ns_cmp_eq_u32 s69, 1\ns_cbranch_scc0 .Lchv5_%=\ns_endpgm\n.Lchv14_%=:\ns_memrealtime s[72:73]\ns_waitcnt lgkmcnt(0)\n.Lchv15_%=:\n" \
    "s_sub_u32 s69, s63, s72\ns_cmp_ge_i32 s69, 20\ns_cbranch_scc1 .Lchv6_%=\ns_add_u32 s63, s63, s38\ns_xor_b32 s48, s48, 0x1000\ns_branch .Lchv15_%=\n.Lchv12_%=:\n" \
    "s_add_u32 s63, s63, s38\ns_xor_b32 s48, s48, 0x1000\ns_sub_u32 s69, s63, s72\ns_cmp_lt_i32 s69, s37\ns_cbranch_scc1 .Lchv12_%=\ns_branch .Lchv13_%=\n.Lchv8_%=:\n" \
    "v_mov_b32 v252, 0\nv_mov_b32 v253, 1\nbuffer_store_dword v253, v252, s[64:67], 0 offen sc1\ns_endpgm\n.Lchv2_%=:\ns_waitcnt vmcnt(0)\n"
typedef unsigned v32u __attribute__((ext_vector_type(32)));
typedef unsigned v16u __attribute__((ext_vector_type(16)));
template <bool isV>
__device__ __forceinline__ void chain_main(const Args& a, unsigned char* lds, const int xg, const int ii, int wave) {
    asm volatile("" : "+s"(wave));
    int tid = wave * 64 + lane_id(); asm volatile("" : "+v"(tid));
    const int head = xg * 4 + (ii >> 1);
    char* const ws = a.ws;
    unsigned* const ctl = (unsigned*)(ws + O_CTL);
    v16bf w[48];
    {
        const int lane = tid & 63, l15 = lane & 15, gb = lane >> 4;
        auto bufp = [&](int c) { const int b = (c + 4) % 5; return lds + (b < 4 ? L_W + wave * 32768 + b * 8192 : L_KX + wave * 8192); };
        auto kst = [&](int i) { return 4 * (i & 3) + ((wave + (i >> 2)) & 3); };
        auto issue = [&](int n) {
            const int c = chord(isV, n);
            unsigned char* buf = bufp(n);
#pragma unroll
            for (int r8 = 0; r8 < 8; ++r8) {
                const int row = 8 * r8 + (lane >> 3), ch = (lane & 7) ^ (((row >> 2) & 3) << 1);
                const u16* src;
                if (!isV) src = (ch < 4 ? a.Wk : a.Wq) + (size_t)(64 * kst(c) + row) * HD + head * DH + 32 * wave + 8 * (ch & 3);
                else if (c < 8) src = a.Wv + (size_t)(64 * kst(2 * c + (ch >> 2)) + row) * HD + head * DH + 32 * wave + 8 * (ch & 3);
                else src = a.Wo + (size_t)(head * DH + 64 * (c & 1) + row) * DM + 256 * wave + 64 * (((c - 8) >> 1) ^ (head & HXM)) + 8 * ch;
                dma16(src, buf + r8 * 1024, isV ? NTW & (c >= 8 ? 32 : 64) : NTW & 2);
            }
        };
        const unsigned q = l15 >> 2, p = lane & 3;
        const unsigned row0 = 16 * (gb >> 1) + 4 * (gb & 1) + (q & 1) + 8 * (q >> 1);
        const unsigned sw = ((gb & 1) | ((q >> 1) << 1)) << 1;
        v16bf pend[16];
        auto flush = [&](int j) {
            const v8u v = __builtin_bit_cast(v8u, pend[j]);
            unsigned char* d = lds + L_W + wave * 32768 + j * 2048 + lane * 16;
            *(LAS v4u*)d = (v4u){v[0], v[1], v[2], v[3]};
            *(LAS v4u*)(d + 1024) = (v4u){v[4], v[5], v[6], v[7]};
        };
        issue(0); issue(1); issue(2); issue(3); issue(4);
        sfor<0, 16>([&](auto cc) {
            constexpr int u = decltype(cc)::v, c = chord(isV, u);
            asm volatile("s_waitcnt vmcnt(%0)" :: "n"(u < 12 ? 32 : 8 * (15 - u)) : "memory");
            unsigned char* buf = bufp(u);
            v16bf t[4];
#pragma unroll
            for (int n = 0; n < 4; ++n) {
                constexpr bool pk = isV && c >= 8;
                const unsigned o = pk ? (32 * (n & 1) + 4 * gb + (q & 1)) * 128 + (((4 * (n >> 1) + 2 * (q >> 1) + (p >> 1)) ^ (gb << 1)) << 4) + (p & 1) * 8
                                      : row0 * 128 + (((2 * n + (p >> 1)) ^ sw) << 4) + (p & 1) * 8;
                const v2u r0 = tr_rd(buf, o), r1 = tr_rd(buf, o + 2 * 128), r2 = tr_rd(buf, o + (pk ? 16 : 32) * 128), r3 = tr_rd(buf, o + (pk ? 18 : 34) * 128);
                const v8u v = {r0.x, r0.y, r1.x, r1.y, r2.x, r2.y, r3.x, r3.y};
                t[n] = __builtin_bit_cast(v16bf, v);
            }
            asm volatile("s_waitcnt lgkmcnt(0)" ::: "memory");
            sfor<0, 4>([&](auto nn) {
                constexpr int n = decltype(nn)::v;
                if constexpr (n == 3) { pend[c] = t[n]; asm volatile("" : "+v"(pend[c])); }
                else {
                    constexpr int r = 3 * c + n;
                    w[r] = t[n];
                    if constexpr (r < 32) asm volatile("" : "+a"(w[r])); else asm volatile("" : "+v"(w[r]));
                }
            });
            if (u + 5 < 16) issue(u + 5);
            sfor<0, 16>([&](auto jj) { if constexpr (flat(isV, decltype(jj)::v) == u) flush(decltype(jj)::v); });
        });
        asm volatile("s_waitcnt vmcnt(0) lgkmcnt(0)\n\ts_barrier" ::: "memory");
    }
    { const int s = 0; STAMP(8); }
    auto olane = [&]() { int l = tid & 63; asm volatile("" : "+v"(l)); return l; };
    const unsigned pol = __builtin_amdgcn_readfirstlane(*(const unsigned*)(ws + O_POL));
    const v4u rws = mkrs(ws, WS_END);
    const unsigned lb0 = (unsigned)(size_t)(LAS unsigned char*)lds;
    auto pwo = [&](int l) { return (C_X + 32 * (l < 32 ? l : 64 + head)) * 4; };
    int rdy = 0;
    if (wave == 0) {
        const int l = olane();
        for (unsigned spins = 0;;) {
            const int c = (int)ldg4_sc1(ws, pwo(l));
            if (!__any(c < 1)) { rdy = 1; break; }
            if (spin_fail(spins, ctl + C_ABORT)) break;
            __builtin_amdgcn_s_sleep(8);
        }
        *(volatile LAS int*)(lds + L_RED) = rdy;
    }
    lds_barrier();
    rdy = __builtin_amdgcn_readfirstlane(*(volatile LAS int*)(lds + L_RED));
    if (!rdy) asm volatile("s_endpgm");
    { const int s = 0; STAMP(9); }
    const unsigned kx0 = O_KV + head * 16384;
    if (!isV) { const int l = olane(); for (int i = 0; i < 2; ++i) bdma<16>(rws, l * 16, nearg(ws, pol, kx0, 1) + wave * 2048 + i * 1024, lb0 + L_KX + wave * 2048 + i * 1024); }
    {
        const unsigned lane = olane(), l15 = lane & 15, g = lane >> 4, q = l15 >> 2, p = lane & 3, l16 = lane * 16, to = (8 * g + q) * 16 + (p & 1) * 8;
        const v16u c0 = {(lane & 8) ? 0xEEEEu : 0x4444u, l16, g * 256 + (l15 & 7) * 32 + (l15 >> 3) * 16, lb0 + L_MEM + wave * 4096 + l16,
                         lb0 + L_MEM + ((wave + 1) & 3) * 4096 + l16, lb0 + L_MEM + ((wave + 2) & 3) * 4096 + l16, lb0 + L_MEM + ((wave + 3) & 3) * 4096 + l16,
                         lb0 + L_W + wave * 32768 + l16, !isV ? lb0 + L_KX + g * 512 + l15 * 16 : ii == 1 && wave == 0 && lane == 0 ? (C_C + 32 * xg) * 4 : 0x7F000000u, lb0 + L_T + (32 * wave + 16 * (g >> 1) + l15) * 16 + (g & 1) * 8,
                         lb0 + L_T + to,
                         isV ? (((g & 1) << 1) | (g >> 1)) * 16384 + l15 * 16 : l15 < 8 ? (4 * wave + (head >> 3)) * 16384 + (head & 7) * 512 + g * 128 + l15 * 16 : (16 + wave) * 16384 + head * 128 + (l15 & 7) * 16,
                         nearg(ws, pol, (C_R + 32 * xg) * 4, 0), lb0 + L_T + 2048 + to + wave * 512, !isV ? l15 * 64 + g * 16 : ii == 1 && wave == 1 && lane == 0 ? xg * 128 : 0x7F000000u, isV && ((head & 7) * 4 + wave >= 20 || lane >= 32) ? 0x7F000000u : lane * 128};
        const unsigned par = a.pad0, R = par >> 26, blo = (unsigned)(size_t)ws;
        const unsigned n0 = __builtin_amdgcn_readfirstlane((unsigned)__builtin_amdgcn_s_memrealtime() / R), tc = n0 & 1, cr = xg >> 1;
        v16u sv = {nearg(ws, pol, O_E1 + (cr * 4 + wave) * 16384, 0) + tc * 4096, MiB,
                   isV ? nearg(ws, pol, O_UM + wave * 262144, 0) + head * 256 : nearg(ws, pol, O_EP, 0) + tc * 4096,
                   isV ? nearg(ws, pol, O_EP + 20 * 16384, 0) : kmof(ws, pol, 0, head) + wave * 512, isV ? O_D + ((head & 7) * 2 + (wave >> 1)) * 16384 + (wave & 1) * 4096 : O_KV + ((head & 7) * 4 + wave) * 16384, blo, pol, 384 * MiB + ((xg * 8 + ii) * 4 + wave) * 262144u,
                   nearg(ws, pol, kx0 + KV_SLOT, 1) + wave * 2048, 0u, lb0 + L_KX + wave * 2048,
                   (unsigned)wave, 0u, 0u, lb0 + L_RED + 2560 + (wave == STAMP_WAVE ? 0 : MiB), n0 * R};
        v4u sa = {0u, (par >> 21) & 31, R, 1u};
        unsigned sk = blo + O_KV + KV_SLOT;
        v8u sb = {blo + O_E1, blo + O_EP, blo + O_UM, 0u, par & 255, (par >> 8) & 255, 0x00FFFFFFu, 0x00FF00FFu};
        v32u ww[12];
#pragma unroll
        for (int i = 0; i < 12; ++i) {
            const v8u x0 = __builtin_bit_cast(v8u, w[4 * i]), x1 = __builtin_bit_cast(v8u, w[4 * i + 1]), x2 = __builtin_bit_cast(v8u, w[4 * i + 2]), x3 = __builtin_bit_cast(v8u, w[4 * i + 3]);
            ww[i] = __builtin_shufflevector(__builtin_shufflevector(x0, x1, 0, 1, 2, 3, 4, 5, 6, 7, 8, 9, 10, 11, 12, 13, 14, 15), __builtin_shufflevector(x2, x3, 0, 1, 2, 3, 4, 5, 6, 7, 8, 9, 10, 11, 12, 13, 14, 15),
                                            0, 1, 2, 3, 4, 5, 6, 7, 8, 9, 10, 11, 12, 13, 14, 15, 16, 17, 18, 19, 20, 21, 22, 23, 24, 25, 26, 27, 28, 29, 30, 31);
        }
#define CHAIN_CALL(TXT) asm volatile(TXT : "+{s[48:63]}"(sv), "+{s[36:39]}"(sa), "+{s[40:47]}"(sb), "+{s35}"(sk) \
            : "{v[0:15]}"(c0), "{s[64:67]}"(rws), [s1] "n"(CH_S1), [a1] "n"(CH_A1), [b1] "n"(CH_B1), [s2] "n"(CH_S2), [a2] "n"(CH_A2), [b2] "n"(CH_B2), [rm] "n"(RG - 1), [rw] "n"(1 - RG), "{s[32:33]}"(0x3e0293eeull), \
              "{a[0:31]}"(ww[0]), "{a[32:63]}"(ww[1]), "{a[64:95]}"(ww[2]), "{a[96:127]}"(ww[3]), "{a[128:159]}"(ww[4]), "{a[160:191]}"(ww[5]), "{a[192:223]}"(ww[6]), "{a[224:255]}"(ww[7]), \
              "{v[92:123]}"(ww[8]), "{v[124:155]}"(ww[9]), "{v[156:187]}"(ww[10]), "{v[188:219]}"(ww[11]) \
            : "memory", "vcc", "scc", "m0", "v21", "v22", "v24", "v25", "v26", "v27", "v28", "v29", "v30", "v31", "v32", "v33", "v34", "v35", "v36", "v37", "v38", "v39", "v40", "v41", "v42", "v43", "v44", "v45", "v46", "v47", "v48", "v49", "v50", "v51", "v52", "v53", "v54", "v55", "v56", "v57", "v58", "v59", "v60", "v61", "v62", "v63", "v64", "v65", "v66", "v67", "v68", "v69", "v70", "v71", "v72", "v73", "v74", "v75", "v76", "v77", "v78", "v79", "v80", "v81", "v82", "v83", "v84", "v85", "v86", "v87", "v88", "v89", "v90", "v91", "v220", "v221", "v222", "v223", "v224", "v225", "v226", "v227", "v228", "v229", "v230", "v231", "v232", "v233", "v234", "v235", "v236", "v237", "v238", "v239", "v240", "v241", "v242", "v243", "v244", "v245", "v246", "v247", "v248", "v249", "v250", "v251", "v252", "v253", "s68", "s69", "s70", "s71", "s72", "s73", "s74", "s75", "s76", "s77", "s78", "s79")
        if constexpr (isV) CHAIN_CALL(CHAIN_V); else CHAIN_CALL(CHAIN_QK);
    }
    tid = wave * 64 + lane_id();
    asm volatile("s_waitcnt vmcnt(0)\n\ts_barrier" ::: "memory");
}

typedef __amdgpu_buffer_rsrc_t rsrc_t;
__device__ __forceinline__ v4u bld16(rsrc_t r, unsigned off) { return __builtin_bit_cast(v4u, __builtin_amdgcn_raw_buffer_load_b128(r, off, 0, 16)); }
__device__ __forceinline__ v2u bld8(rsrc_t r, unsigned off) { return __builtin_bit_cast(v2u, __builtin_amdgcn_raw_buffer_load_b64(r, off, 0, 16)); }
__device__ __forceinline__ void bst16(rsrc_t r, unsigned off, v4u v) { __builtin_amdgcn_raw_buffer_store_b128(__builtin_bit_cast(__attribute__((ext_vector_type(4))) unsigned, v), r, off, 0, 16); }
__device__ __forceinline__ void bst8(rsrc_t r, unsigned off, v2u v) { __builtin_amdgcn_raw_buffer_store_b64(__builtin_bit_cast(__attribute__((ext_vector_type(2))) unsigned, v), r, off, 0, 16); }

constexpr int L_FLAG = LDS_BYTES - 64;
#define otid() (a.pad0 * 64 + lane_id())
__device__ __forceinline__ v4u sld16(rsrc_t r, unsigned vo, unsigned so) { return __builtin_bit_cast(v4u, __builtin_amdgcn_raw_buffer_load_b128(r, vo, so, 16)); }
__device__ __forceinline__ v4u sentv() { unsigned x = SENT; asm volatile("" : "+v"(x)); return (v4u){x, x, x, x}; }
#define BW_SLEEP 16
template <int SLP = BW_SLEEP> __device__ __forceinline__ bool wave_wait(const unsigned* p, int stride, int n, unsigned target, unsigned* abortw, unsigned char* lds = nullptr) {
    const int lane = lane_id();
    for (unsigned spins = 0;;) {
        const unsigned v = lane < n ? __hip_atomic_load((gu32*)(p + lane * stride), RLX_AGENT) : target;
        if (!__any(v < target)) { if (lds && lane == 0) *(volatile LAS unsigned*)(lds + L_FLAG + 32) = v; return true; }
        __builtin_amdgcn_s_sleep(SLP);
        if (++spins > (SPIN_CAP << 1)) { __hip_atomic_store((gu32*)abortw, 1u, RLX_AGENT); return false; }
        if ((spins & 63u) == 0 && __builtin_amdgcn_readfirstlane(__hip_atomic_load((gu32*)abortw, RLX_AGENT)) == 1u) return false;
    }
}
__device__ __forceinline__ float gelu_tanh(float v) {
    const float e = v * (-2.302208198f - 0.1029432397f * (v * v));
    return v * __builtin_amdgcn_rcpf(1.f + __builtin_amdgcn_exp2f(e));
}
__device__ __forceinline__ v4u widen(v2u x, v2u y) {
    const auto s0 = __builtin_amdgcn_permlane16_swap(x.x, y.x, false, false);
    const auto s1 = __builtin_amdgcn_permlane16_swap(x.y, y.y, false, false);
    return (v4u){s0[0], s1[0], s0[1], s1[1]};
}
#define TD_P 190
#define TD_O 290
__device__ __forceinline__ v4u unsent4(v4u v) { return (v4u){unsent(v.x), unsent(v.y), unsent(v.z), unsent(v.w)}; }
__device__ __forceinline__ unsigned rt_now() { return (unsigned)__builtin_amdgcn_s_memrealtime(); }
__device__ __forceinline__ void rt_delay(unsigned ticks  , unsigned t0 = rt_now()) {
    while ((unsigned)__builtin_amdgcn_s_memrealtime() - t0 < ticks) __builtin_amdgcn_s_sleep(2);
}
__device__ __forceinline__ float wave_sum(float x) {
    x += __uint_as_float(__builtin_amdgcn_update_dpp(0, __float_as_uint(x), 0xB1, 0xF, 0xF, false));
    x += __uint_as_float(__builtin_amdgcn_update_dpp(0, __float_as_uint(x), 0x4E, 0xF, 0xF, false));
    x += __uint_as_float(__builtin_amdgcn_update_dpp(0, __float_as_uint(x), 0x124, 0xF, 0xF, false));
    x += __uint_as_float(__builtin_amdgcn_update_dpp(0, __float_as_uint(x), 0x128, 0xF, 0xF, false));
    return red32_add(red16_add(x));
}
__device__ __forceinline__ void ln_row(const v4u (&xr)[2], const f32x4 (&c1)[4], const f32x4 (&sh)[4], v4u (&o)[2]) {
    f32x2 v[8];
    f32x2 s2 = {0.f, 0.f};
#pragma unroll
    for (int j = 0; j < 8; ++j) { v[j] = (f32x2){lo16f(xr[j >> 2][j & 3]), hi16f(xr[j >> 2][j & 3])}; s2 += v[j]; }
    const float mean = wave_sum(s2[0] + s2[1]) * (1.f / DM);
    f32x2 q2 = {0.f, 0.f};
#pragma unroll
    for (int j = 0; j < 8; ++j) { const f32x2 d = v[j] - mean; q2 += d * d; }
    const float vs = wave_sum(q2[0] + q2[1]);
    const float mb = rbf(mean), vb = rbf(vs * (1.f / DM));
    const float rsd = 1.f / rbf(sqrtf(rbf(vb + 1e-5f)));
    auto r2 = [](f32x2 z) { const unsigned u = pack2(z[0], z[1]); return (f32x2){lo16f(u), hi16f(u)}; };
#pragma unroll
    for (int j = 0; j < 8; ++j) {
        const f32x2 cc = {c1[j >> 1][2 * (j & 1)], c1[j >> 1][2 * (j & 1) + 1]}, ss = {sh[j >> 1][2 * (j & 1)], sh[j >> 1][2 * (j & 1) + 1]};
        const f32x2 y = r2(r2(r2(v[j] - mb) * rsd) * cc) + ss;
        o[j >> 2][j & 3] = pack2(y[0], y[1]);
    }
}
template <int N> __device__ __forceinline__ void ld_mod(rsrc_t rs, unsigned* ctl, const int (&col)[N], f32x4* m) {
    for (unsigned spins = 0;;) {
        unsigned mx = 0;
#pragma unroll
        for (int i = 0; i < N; ++i) { const v4u v = bld16(rs, O_MOD + col[i] * 4); mx = max(mx, umax4(v)); m[i] = __builtin_bit_cast(f32x4, v); }
        if (!__any(mx == SENT)) break;
        __builtin_amdgcn_s_sleep(6);
        if (spin_fail(spins, ctl + C_ABORT)) asm volatile("s_endpgm");
    }
}
__device__ __forceinline__ void cp_delay(unsigned ticks, unsigned cw) {
    while ((((unsigned)__builtin_amdgcn_s_memrealtime() - (cw >> 8)) & 0xFFFFFFu) < ticks) __builtin_amdgcn_s_sleep(1);
}
__device__ __forceinline__ void set_m0(unsigned v) { asm volatile("s_mov_b32 m0, %0" :: "s"(v) : "m0"); }
__device__ __forceinline__ float sum8(float x) {
    x += __uint_as_float(__builtin_amdgcn_update_dpp(0, __float_as_uint(x), 0x128, 0xF, 0xF, false));
    return red32_add(red16_add(x));
}
__device__ __forceinline__ void mod8(const Args& a, int col, f32x4 (&m)[2]) {
    f32x4 p[8][2];
#pragma unroll
    for (int k = 0; k < 16; ++k) p[k >> 1][k & 1] = *(const f32x4*)(a.ws + O_MODP + ((k >> 1) * 2048 + col + 4 * (k & 1)) * 4);
#pragma unroll
    for (int h = 0; h < 2; ++h) {
        const f32x4 t = ((p[0][h] + p[1][h]) + (p[2][h] + p[3][h])) + ((p[4][h] + p[5][h]) + (p[6][h] + p[7][h]));
#pragma unroll
        for (int i = 0; i < 4; ++i) { float v = rbf(t[i]); if (col >= DM) v = rbf(1.f + v); m[h][i] = v; }
    }
}
__device__ __forceinline__ void mod_cols(const Args& a, rsrc_t rs, unsigned char* lds, int cg) {
    const int tid = otid();
    v4u wv[32];
#pragma unroll
    for (int i = 0; i < 32; ++i) wv[i] = __builtin_nontemporal_load((const v4u*)(a.w_mod + (size_t)((tid >> 3) + 32 * i) * 6144 + cg * 64 + 8 * (tid & 7)));
    f32x2 acc[4] = {{0.f, 0.f}, {0.f, 0.f}, {0.f, 0.f}, {0.f, 0.f}};
#pragma unroll
    for (int i = 0; i < 32; ++i) {
        const float sv = *(const LAS float*)(lds + 16384 + ((tid >> 3) + 32 * i) * 4);
#pragma unroll
        for (int j = 0; j < 4; ++j) acc[j] += sv * (f32x2){lo16f(wv[i][j]), hi16f(wv[i][j])};
    }
    unsigned char* red = lds + 20480;
    lds_barrier();
    *(LAS f32x4*)(red + (tid >> 3) * 272 + (tid & 7) * 32) = (f32x4){acc[0][0], acc[0][1], acc[1][0], acc[1][1]};
    *(LAS f32x4*)(red + (tid >> 3) * 272 + (tid & 7) * 32 + 16) = (f32x4){acc[2][0], acc[2][1], acc[3][0], acc[3][1]};
    lds_barrier();
    if (tid < 64) {
        f32x4 t = {0.f, 0.f, 0.f, 0.f};
#pragma unroll
        for (int i = 0; i < 8; ++i) t += *(const LAS f32x4*)(red + ((tid >> 4) * 8 + i) * 272 + (tid & 15) * 16);
#pragma unroll
        for (int i = 0; i < 4; ++i) { float v = rbf(red32_add(red16_add(t[i]))); if ((cg >> 4) == 4) v = rbf(1.f + v); t[i] = v; }
        if (tid < 16) bst16(rs, O_MOD + (cg * 64 + 4 * tid) * 4, unsent4(__builtin_bit_cast(v4u, t)));
    }
}
#define TCH 1
#define TOF 0
#define TK0 512
__device__ __forceinline__ void touch(const Args& a, unsigned char* lds, int xg, int wi) {
    const int lane = lane_id();
    constexpr int NU = (1024 - TK0) / 8 * 3;
    set_m0((unsigned)(size_t)(LAS unsigned char*)lds + LDS_BYTES - 1024);
    for (int v = wi; v < NU; v += 32) {
        const int u = TOF ? (v < 128 ? NU + v : v - 128) : v;
        const int m = u < NU ? u % 3 : 3;
        v4u r = mkrs(m == 0 ? a.Wv : m == 1 ? a.Wk : m == 2 ? a.Wq : a.Wo, DM * HD * 2);
        const unsigned vo = m < 3 ? (lane >> 3) * 8192 + (lane & 7) * 128 : (lane >> 1) * 2048 + (lane & 1) * 1024;
        const unsigned so = m < 3 ? (TK0 + 8 * (u / 3)) * 8192 + xg * 1024 : (512 * xg + 32 * ((u - NU) & 15)) * 2048 + (((u - NU) >> 4) & 1) * 512 + ((u - NU) >> 5) * 128;
        if (TCH == 1) asm volatile("s_nop 4\n\tbuffer_load_dword %0, %1, %2 offen lds" :: "v"(vo), "s"(r), "s"(so));
        else asm volatile("s_nop 4\n\tbuffer_load_dword %0, %1, %2 offen nt lds" :: "v"(vo), "s"(r), "s"(so));
    }
}
#define GO(kind, x) (C_G + ((kind) * 8 + (x)) * 32)
__device__ __forceinline__ void monitor(const Args& a, unsigned* ctl) {
    const int lane = lane_id();
    unsigned* const cr = (unsigned*)(a.ws + nearg(a.ws, __builtin_amdgcn_readfirstlane(*(const unsigned*)(a.ws + O_POL)), C_R * 4, 0));
    int last = 0, lv = 0, lo[2] = {-1, 0};
    for (unsigned spins = 0; last < NSEG || lo[0] < NSEG - 5 || lo[1] < NSEG - 4;) {
        const int v = (int)__hip_atomic_load((gu32*)PW_V(lane + (lane & 32)), RLX_AGENT) - 1, u = (int)__hip_atomic_load((gu32*)(PW_U(lane & 31) + (lane >> 5)), RLX_AGENT);
        int r = last;
        while (r < NSEG && !__any(v <= r)) ++r;
        int rv = lv;
        while (rv <= NSEG && !__any(lane < 32 && v < rv)) ++rv;
        if (r > last || rv > lv) { last = r; lv = rv; spins = 0; if (lane < 8) __hip_atomic_store((GAS u64*)(cr + 32 * lane), (u64)rv << 32 | (unsigned)r, RLX_AGENT); }
#pragma unroll
        for (int p = 0; p < 2; ++p)
            if (!__any((lane >> 5) == p && u <= max(lo[p], 0))) { lo[p] += 2; spins = 0; if (lane < 8) __hip_atomic_fetch_max((gu32*)(ctl + GO(1, lane) + p), (unsigned)lo[p], RLX_AGENT); }
        if (last < 6) __builtin_amdgcn_s_sleep(2); else __builtin_amdgcn_s_sleep(8);
        if (spin_fail(spins, ctl + C_ABORT)) return;
    }
}

__device__ __forceinline__ void ln1_rows(const Args& a, rsrc_t rs, unsigned char* lds, int idx  ) {
    const int tid = otid(), lane = tid & 63, wave = a.pad0;
    unsigned* const ctl = (unsigned*)(a.ws + O_CTL);
    const unsigned pol = __builtin_amdgcn_readfirstlane(*(const unsigned*)(a.ws + O_POL));
    const int r0 = 4 * idx + wave, nrow = 8;
    v4u xr[2];
#pragma unroll
    for (int c = 0; c < 2; ++c) xr[c] = *(const v4u*)(a.x + (size_t)r0 * DM + 512 * c + 8 * lane);
    {
        f32x4 m[2];
        mod8(a, 8 * tid, m);
        *(LAS f32x4*)(lds + 32 * tid) = m[0]; *(LAS f32x4*)(lds + 32 * tid + 16) = m[1];
        const v2u cr = *(const v2u*)(a.c + 4 * tid);
        f32x4 sv = {lo16f(cr.x), hi16f(cr.x), lo16f(cr.y), hi16f(cr.y)};
#pragma unroll
        for (int i = 0; i < 4; ++i) sv[i] = rbf(sv[i] / (1.f + __expf(-sv[i])));
        *(LAS f32x4*)(lds + 16384 + tid * 16) = sv;
    }
    lds_barrier();
    f32x4 c1[4], sh[4];
#pragma unroll
    for (int i = 0; i < 4; ++i) { const int col = 512 * (i >> 1) + 8 * lane + 4 * (i & 1); sh[i] = *(const LAS f32x4*)(lds + col * 4); c1[i] = *(const LAS f32x4*)(lds + (DM + col) * 4); }
    constexpr int MON = 51;
    const int nn = r0 == 4 * MON + 1 ? 2 * nrow - 1 : nrow;
    auto rowof = [&](int k) { return k < nrow ? r0 + 256 * k : r0 - 1 + 256 * (k - nrow + 1); };
    for (int k = 0; k < nn; ++k) {
        const int row = rowof(k);
        v4u o[2], xn[2];
#pragma unroll
        for (int c = 0; c < 2; ++c) xn[c] = *(const v4u*)(a.x + (size_t)min(rowof(k + 1), S_TOK - 1) * DM + 512 * c + 8 * lane);
        ln_row(xr, c1, sh, o);
#pragma unroll
        for (int c = 0; c < 2; ++c) { bst16(rs, nearg(a.ws, pol, O_D + (unsigned)(row >> 5) * D_SLOT + D_H1 + ((row >> 2) & 7) * 16384, 1) + (row & 3) * 2048 + 1024 * c + 16 * lane, o[c]); xr[c] = xn[c]; }
        if (k == 0) {
            wait_vm0();
            if (lane == 0) __hip_atomic_fetch_add((gu32*)(ctl + C_H + 32 * (r0 >> 5)), 1u, RLX_AGENT);
            RSTAMP(75, 2);
            if (TCH) touch(a, lds, idx & 7, (idx >> 3) * 4 + wave);
            if (r0 == 4 * MON) { monitor(a, ctl); asm volatile("s_endpgm"); }
        }
    }
    wait_vm0();
    if (lane >= 1 && lane < nn) __hip_atomic_fetch_add((gu32*)(ctl + C_H + 32 * (rowof(lane) >> 5)), 1u, RLX_AGENT);
    if (idx != MON) mod_cols(a, rs, lds, idx + 32);
    if (idx == MON + 8) mod_cols(a, rs, lds, MON + 32);
    lds_barrier();
}
__device__ __forceinline__ void sst16(v4u v, rsrc_t r, unsigned vo, unsigned so, int) { __builtin_amdgcn_raw_buffer_store_b128(v, r, vo, so, 16); asm volatile("s_nop 1" :: "v"(v) : "memory"); }
#define MFMA_A(acc, wv, xv) asm volatile("v_mfma_f32_16x16x32_bf16 %0, %1, %2, %0" : "+v"(acc) : "a"(wv), "v"(xv))
#define MFMA_V(acc, wv, xv) asm volatile("v_mfma_f32_16x16x32_bf16 %0, %1, %2, %0" : "+v"(acc) : "v"(wv), "v"(xv))
#define MFMA_A0(acc, wv, xv) asm volatile("v_mfma_f32_16x16x32_bf16 %0, %1, %2, 0" : "=&v"(acc) : "a"(wv), "v"(xv))
#define MFMA_V0(acc, wv, xv) asm volatile("v_mfma_f32_16x16x32_bf16 %0, %1, %2, 0" : "=&v"(acc) : "v"(wv), "v"(xv))
__device__ __forceinline__ float rb1(float v) { return __uint_as_float(pack2(0.f, v)); }
#define TD_L 24
#define TD_W 1000
#define XF1 64
#define XF2 51
#define XO 64
#define TLF 2
#define TD_S 40
__device__ __forceinline__ unsigned arrive(const char* ws, int cnt, int seg, int wave = 3) {
    unsigned old = 1u;
    asm volatile("s_cmp_eq_u32 %3, 3\n\ts_cselect_b64 exec, 1, 0\n\tglobal_atomic_add %0, %1, %0, %2 sc0\n\ts_mov_b64 exec, -1" : "+v"(old) : "v"((C_N + 64 * cnt + seg) * 4), "s"(ws), "s"(wave) : "memory", "scc");
    return old;
}
template <int N> __device__ __forceinline__ unsigned rdw(unsigned v) { unsigned r; asm volatile("s_waitcnt vmcnt(%2)\n\tv_readfirstlane_b32 %0, %1" : "=s"(r) : "v"(v), "n"(N) : "memory"); return r; }
template <int KIND> __device__ __forceinline__ void tell(unsigned* ctl, int seg, unsigned old, unsigned total, int n = 8, int x0 = 0) {
    const int lane = lane_id();
    if (__builtin_amdgcn_readfirstlane(old) == total - 1 && lane < n) __hip_atomic_fetch_max((gu32*)(ctl + GO(KIND, x0 + lane) + (seg & 1)), (unsigned)seg + 1, RLX_AGENT);
}
template <int KIND> __device__ __forceinline__ void tell_now(const Args& a, unsigned* ctl, int cnt, int seg, unsigned total, int n = 8, int x0 = 0) {
    if (a.pad0 == 3) { unsigned old = arrive(a.ws, cnt, seg); asm volatile("s_waitcnt vmcnt(0)" : "+v"(old)); tell<KIND>(ctl, seg, old, total, n, x0); }
}
__device__ __forceinline__ v2u pack4(f32x4 v) { return (v2u){pack2(v[0], v[1]), pack2(v[2], v[3])}; }

template <int OFF> __device__ __forceinline__ void dma1(v4u r, unsigned vo, unsigned so) { asm volatile("buffer_load_dwordx4 %0, %1, %2 offen offset:%3 sc1 lds" :: "v"(vo), "s"(r), "s"(so), "n"(OFF)); }
template <int O0, int ST> __device__ __forceinline__ void ld4(v4u (&d)[4], v4u r, unsigned vo, unsigned so) {
    asm volatile("buffer_load_dwordx4 %0, %4, %5, %6 offen offset:%7 sc1\n\tbuffer_load_dwordx4 %1, %4, %5, %6 offen offset:%8 sc1\n\t"
                 "buffer_load_dwordx4 %2, %4, %5, %6 offen offset:%9 sc1\n\tbuffer_load_dwordx4 %3, %4, %5, %6 offen offset:%10 sc1"
                 : "=&v"(d[0]), "=&v"(d[1]), "=&v"(d[2]), "=&v"(d[3]) : "v"(vo), "s"(r), "s"(so), "n"(O0), "n"(O0 + ST), "n"(O0 + 2 * ST), "n"(O0 + 3 * ST));
}
#define ROFF 0
#define XB 0
#define GO_(G) (G)
#define UPF 0
#define UPO 1
#define AFD(v) ((v) ? 2 : 1)
#define LV0 5
#define LVS 3
#define KTL 29
#define LQ 4
#define LV 12
constexpr int SM = 0, SV = 0;
#define PROJ_Q \
    "ds_read_b128 v[48:51], v2 offset:0\nds_read_b128 v[52:55], v2 offset:8192\nds_read_b128 v[56:59], v3 offset:0\nds_read_b128 v[60:63], v3 offset:8192\ns_mov_b32 s72, 0\n" \
    "s_mov_b32 s73, 0\nv_mov_b32 v21, 0\ns_add_u32 s97, s50, 1\ns_min_u32 s97, s97, 63\ns_sub_u32 s98, s50, s23\ns_add_u32 s99, s98, 1\ns_branch .Lprqslow_%=\n.Lprqgo_%=:\n" \
    "s_lshr_b32 s68, s55, 1\ns_xor_b32 s68, s68, s98\ns_bitcmp1_b32 s68, 0\ns_cbranch_scc0 .Lprqb2_%=\n.Lprqb1_%=:\ns_waitcnt lgkmcnt(4)\n" \
    "v_mfma_f32_16x16x32_bf16 v[24:27], a[0:3], v[48:51], 0\nds_read_b128 v[64:67], v2 offset:1024\ns_sub_u32 s38, s50, s24\n" \
    "v_mfma_f32_16x16x32_bf16 v[36:39], a[0:3], v[52:55], 0\nds_read_b128 v[68:71], v2 offset:9216\ns_max_i32 s38, s38, 0\n" \
    "v_mfma_f32_16x16x32_bf16 v[28:31], a[4:7], v[48:51], 0\ns_add_u32 m0, s52, 49152\ns_add_u32 s69, s54, 1536\ns_and_b32 s38, s38, s16\n" \
    "v_mfma_f32_16x16x32_bf16 v[40:43], a[4:7], v[52:55], 0\nbuffer_load_dwordx4 v1, s[64:67], s69 offen offset:0 sc1 lds\ns_lshl_b32 s38, s38, 19\n" \
    "v_mfma_f32_16x16x32_bf16 v[32:35], v[124:127], v[48:51], 0\ns_add_u32 s38, s38, s53\ns_add_u32 s74, s48, s38\ns_lshr_b32 s74, s74, 8\n" \
    "v_mfma_f32_16x16x32_bf16 v[44:47], v[124:127], v[52:55], 0\ns_waitcnt lgkmcnt(4)\ns_xnor_b32 s74, s74, s49\ns_and_b32 s74, s74, 0x2000\n" \
    "v_mfma_f32_16x16x32_bf16 v[24:27], a[8:11], v[56:59], v[24:27]\nds_read_b128 v[72:75], v3 offset:1024\ns_add_u32 s74, s74, s38\n" \
    "v_mfma_f32_16x16x32_bf16 v[36:39], a[8:11], v[60:63], v[36:39]\nds_read_b128 v[76:79], v3 offset:9216\ns_add_u32 s74, s74, s60\n" \
    "v_mfma_f32_16x16x32_bf16 v[28:31], a[12:15], v[56:59], v[28:31]\ns_add_u32 s68, s50, 1\ns_min_u32 s68, s68, 63\ns_lshl_b32 s68, s68, 20\n" \
    "v_mfma_f32_16x16x32_bf16 v[40:43], a[12:15], v[60:63], v[40:43]\ns_add_u32 s68, s68, s18\ns_add_u32 s70, s48, s68\ns_lshr_b32 s70, s70, 8\n" \
    "v_mfma_f32_16x16x32_bf16 v[32:35], v[128:131], v[56:59], v[32:35]\ns_xnor_b32 s70, s70, s49\ns_and_b32 s70, s70, 0x2000\ns_add_u32 s70, s70, s68\n" \
    "v_mfma_f32_16x16x32_bf16 v[44:47], v[128:131], v[60:63], v[44:47]\ns_waitcnt lgkmcnt(2)\ns_add_u32 s69, s54, 640\n" \
    "v_mfma_f32_16x16x32_bf16 v[24:27], a[16:19], v[64:67], v[24:27]\nds_read_b128 v[48:51], v2 offset:2048\nv_mfma_f32_16x16x32_bf16 v[36:39], a[16:19], v[68:71], v[36:39]\n" \
    "ds_read_b128 v[52:55], v2 offset:10240\nv_mfma_f32_16x16x32_bf16 v[28:31], a[20:23], v[64:67], v[28:31]\nbuffer_load_dwordx4 v1, s[64:67], s69 offen offset:1024 sc1 lds\n" \
    "s_add_u32 s69, s54, -256\nv_mfma_f32_16x16x32_bf16 v[40:43], a[20:23], v[68:71], v[40:43]\nbuffer_load_dwordx4 v1, s[64:67], s69 offen offset:2048 sc1 lds\n" \
    "v_mfma_f32_16x16x32_bf16 v[32:35], v[132:135], v[64:67], v[32:35]\nv_mfma_f32_16x16x32_bf16 v[44:47], v[132:135], v[68:71], v[44:47]\ns_waitcnt lgkmcnt(2)\n" \
    "v_mfma_f32_16x16x32_bf16 v[24:27], a[24:27], v[72:75], v[24:27]\nds_read_b128 v[56:59], v3 offset:2048\nv_mfma_f32_16x16x32_bf16 v[36:39], a[24:27], v[76:79], v[36:39]\n" \
    "ds_read_b128 v[60:63], v3 offset:10240\nv_mfma_f32_16x16x32_bf16 v[28:31], a[28:31], v[72:75], v[28:31]\ns_add_u32 s69, s54, -1152\n" \
    "buffer_load_dwordx4 v1, s[64:67], s69 offen offset:3072 sc1 lds\nv_mfma_f32_16x16x32_bf16 v[40:43], a[28:31], v[76:79], v[40:43]\ns_waitcnt vmcnt(7)\n" \
    "v_readfirstlane_b32 s42, v21\nv_mfma_f32_16x16x32_bf16 v[32:35], v[136:139], v[72:75], v[32:35]\nv_mfma_f32_16x16x32_bf16 v[44:47], v[136:139], v[76:79], v[44:47]\n" \
    "s_waitcnt lgkmcnt(2)\nv_mfma_f32_16x16x32_bf16 v[24:27], a[32:35], v[48:51], v[24:27]\nds_read_b128 v[64:67], v2 offset:3072\n" \
    "v_mfma_f32_16x16x32_bf16 v[36:39], a[32:35], v[52:55], v[36:39]\nds_read_b128 v[68:71], v2 offset:11264\nv_mfma_f32_16x16x32_bf16 v[28:31], a[36:39], v[48:51], v[28:31]\n" \
    "s_and_b32 s37, s50, s16\ns_lshl_b32 s37, s37, 19\ns_add_u32 s37, s37, s53\nv_mfma_f32_16x16x32_bf16 v[40:43], a[36:39], v[52:55], v[40:43]\ns_add_u32 s71, s48, s37\n" \
    "s_lshr_b32 s71, s71, 8\ns_xnor_b32 s71, s71, s49\nv_mfma_f32_16x16x32_bf16 v[32:35], v[140:143], v[48:51], v[32:35]\ns_and_b32 s71, s71, 0x2000\ns_add_u32 s71, s71, s37\n" \
    "s_mov_b32 m0, s56\nv_mfma_f32_16x16x32_bf16 v[44:47], v[140:143], v[52:55], v[44:47]\ns_waitcnt lgkmcnt(2)\nbuffer_load_dwordx4 v0, s[64:67], s74 offen offset:0 sc1 lds\n" \
    "v_mfma_f32_16x16x32_bf16 v[24:27], a[40:43], v[56:59], v[24:27]\nds_read_b128 v[72:75], v3 offset:3072\nv_mfma_f32_16x16x32_bf16 v[36:39], a[40:43], v[60:63], v[36:39]\n" \
    "ds_read_b128 v[76:79], v3 offset:11264\nv_mfma_f32_16x16x32_bf16 v[28:31], a[44:47], v[56:59], v[28:31]\nbuffer_load_dwordx4 v0, s[64:67], s74 offen offset:1024 sc1 lds\n" \
    "v_mfma_f32_16x16x32_bf16 v[40:43], a[44:47], v[60:63], v[40:43]\ns_cmp_eq_u32 s42, 63\ns_cbranch_scc1 .Lprqtl_%=\n.Lprqtlb_%=:\n" \
    "v_mfma_f32_16x16x32_bf16 v[32:35], v[144:147], v[56:59], v[32:35]\nv_mfma_f32_16x16x32_bf16 v[44:47], v[144:147], v[60:63], v[44:47]\ns_waitcnt lgkmcnt(0)\ns_barrier\n" \
    "v_mfma_f32_16x16x32_bf16 v[24:27], a[48:51], v[64:67], v[24:27]\nds_read_b128 v[48:51], v2 offset:16384\nv_mfma_f32_16x16x32_bf16 v[36:39], a[48:51], v[68:71], v[36:39]\n" \
    "ds_read_b128 v[52:55], v2 offset:24576\nv_mfma_f32_16x16x32_bf16 v[28:31], a[52:55], v[64:67], v[28:31]\ns_add_u32 s39, s50, 1\ns_add_u32 s97, s39, 1\n" \
    "s_min_u32 s97, s97, 63\nv_mfma_f32_16x16x32_bf16 v[40:43], a[52:55], v[68:71], v[40:43]\ns_sub_u32 s98, s39, s23\ns_add_u32 s99, s98, 1\n" \
    "v_mfma_f32_16x16x32_bf16 v[32:35], v[148:151], v[64:67], v[32:35]\ns_cmp_gt_i32 s51, s97\ns_cselect_b32 s96, s98, 0x7fffffff\ns_sub_u32 s39, s39, 4\n" \
    "v_mfma_f32_16x16x32_bf16 v[44:47], v[148:151], v[68:71], v[44:47]\ns_cmp_lt_u32 s39, 60\ns_cselect_b32 s96, s96, 0x7fffffff\n" \
    "v_mfma_f32_16x16x32_bf16 v[24:27], a[56:59], v[72:75], v[24:27]\nds_read_b128 v[56:59], v3 offset:16384\nv_mfma_f32_16x16x32_bf16 v[36:39], a[56:59], v[76:79], v[36:39]\n" \
    "ds_read_b128 v[60:63], v3 offset:24576\nv_mfma_f32_16x16x32_bf16 v[28:31], a[60:63], v[72:75], v[28:31]\nv_mfma_f32_16x16x32_bf16 v[40:43], a[60:63], v[76:79], v[40:43]\n" \
    "v_mfma_f32_16x16x32_bf16 v[32:35], v[152:155], v[72:75], v[32:35]\nv_mfma_f32_16x16x32_bf16 v[44:47], v[152:155], v[76:79], v[44:47]\ns_waitcnt lgkmcnt(2)\n" \
    "v_mfma_f32_16x16x32_bf16 v[24:27], a[64:67], v[48:51], v[24:27]\nds_read_b128 v[64:67], v2 offset:17408\nv_mfma_f32_16x16x32_bf16 v[36:39], a[64:67], v[52:55], v[36:39]\n" \
    "ds_read_b128 v[68:71], v2 offset:25600\nv_mfma_f32_16x16x32_bf16 v[28:31], a[68:71], v[48:51], v[28:31]\ns_add_u32 m0, s52, 0\ns_add_u32 s69, s70, 0\n" \
    "v_mfma_f32_16x16x32_bf16 v[40:43], a[68:71], v[52:55], v[40:43]\nbuffer_load_dwordx4 v1, s[64:67], s69 offen offset:0 sc1 lds\n" \
    "v_mfma_f32_16x16x32_bf16 v[32:35], v[156:159], v[48:51], v[32:35]\nv_mfma_f32_16x16x32_bf16 v[44:47], v[156:159], v[52:55], v[44:47]\ns_waitcnt lgkmcnt(2)\n" \
    "v_mfma_f32_16x16x32_bf16 v[24:27], a[72:75], v[56:59], v[24:27]\nds_read_b128 v[72:75], v3 offset:17408\nv_mfma_f32_16x16x32_bf16 v[36:39], a[72:75], v[60:63], v[36:39]\n" \
    "ds_read_b128 v[76:79], v3 offset:25600\nv_mfma_f32_16x16x32_bf16 v[28:31], a[76:79], v[56:59], v[28:31]\ns_add_u32 s69, s70, -896\n" \
    "buffer_load_dwordx4 v1, s[64:67], s69 offen offset:1024 sc1 lds\nv_mfma_f32_16x16x32_bf16 v[40:43], a[76:79], v[60:63], v[40:43]\n" \
    "v_mfma_f32_16x16x32_bf16 v[32:35], v[160:163], v[56:59], v[32:35]\nv_mfma_f32_16x16x32_bf16 v[44:47], v[160:163], v[60:63], v[44:47]\ns_waitcnt lgkmcnt(2)\n" \
    "v_mfma_f32_16x16x32_bf16 v[24:27], a[80:83], v[64:67], v[24:27]\nds_read_b128 v[48:51], v2 offset:18432\nv_mfma_f32_16x16x32_bf16 v[36:39], a[80:83], v[68:71], v[36:39]\n" \
    "ds_read_b128 v[52:55], v2 offset:26624\nv_mfma_f32_16x16x32_bf16 v[28:31], a[84:87], v[64:67], v[28:31]\ns_add_u32 s69, s70, -1792\n" \
    "buffer_load_dwordx4 v1, s[64:67], s69 offen offset:2048 sc1 lds\nv_mfma_f32_16x16x32_bf16 v[40:43], a[84:87], v[68:71], v[40:43]\ns_add_u32 s68, s59, s25\n" \
    "s_and_b32 s68, s68, 0xffff\nv_add_u32 v122, s68, v13\nv_mfma_f32_16x16x32_bf16 v[32:35], v[164:167], v[64:67], v[32:35]\nds_read_b128 v[80:83], v122 offset:0\n" \
    "v_mfma_f32_16x16x32_bf16 v[44:47], v[164:167], v[68:71], v[44:47]\ns_waitcnt lgkmcnt(3)\nds_read_b128 v[84:87], v122 offset:1024\n" \
    "v_mfma_f32_16x16x32_bf16 v[24:27], a[88:91], v[72:75], v[24:27]\nds_read_b128 v[56:59], v3 offset:18432\nv_mfma_f32_16x16x32_bf16 v[36:39], a[88:91], v[76:79], v[36:39]\n" \
    "ds_read_b128 v[60:63], v3 offset:26624\nv_mfma_f32_16x16x32_bf16 v[28:31], a[92:95], v[72:75], v[28:31]\ns_add_u32 s69, s70, -2688\n" \
    "buffer_load_dwordx4 v1, s[64:67], s69 offen offset:3072 sc1 lds\nv_mfma_f32_16x16x32_bf16 v[40:43], a[92:95], v[76:79], v[40:43]\nds_read_b128 v[88:91], v122 offset:2048\n" \
    "v_mfma_f32_16x16x32_bf16 v[32:35], v[168:171], v[72:75], v[32:35]\nds_read_b128 v[92:95], v122 offset:3072\n" \
    "v_mfma_f32_16x16x32_bf16 v[44:47], v[168:171], v[76:79], v[44:47]\ns_waitcnt lgkmcnt(6)\nv_mfma_f32_16x16x32_bf16 v[24:27], a[96:99], v[48:51], v[24:27]\n" \
    "ds_read_b128 v[64:67], v2 offset:19456\nv_mfma_f32_16x16x32_bf16 v[36:39], a[96:99], v[52:55], v[36:39]\nds_read_b128 v[68:71], v2 offset:27648\n" \
    "v_mfma_f32_16x16x32_bf16 v[28:31], a[100:103], v[48:51], v[28:31]\nbuffer_load_dword v20, v6, s[64:67], 0 offen sc1\n" \
    "v_mfma_f32_16x16x32_bf16 v[40:43], a[100:103], v[52:55], v[40:43]\nv_mfma_f32_16x16x32_bf16 v[32:35], v[172:175], v[48:51], v[32:35]\n" \
    "v_mfma_f32_16x16x32_bf16 v[44:47], v[172:175], v[52:55], v[44:47]\ns_waitcnt lgkmcnt(4)\nv_mfma_f32_16x16x32_bf16 v[24:27], a[104:107], v[56:59], v[24:27]\n" \
    "ds_read_b128 v[72:75], v3 offset:19456\nv_mfma_f32_16x16x32_bf16 v[36:39], a[104:107], v[60:63], v[36:39]\nds_read_b128 v[76:79], v3 offset:27648\n" \
    "v_mfma_f32_16x16x32_bf16 v[28:31], a[108:111], v[56:59], v[28:31]\ns_memrealtime s[72:73]\nv_mfma_f32_16x16x32_bf16 v[40:43], a[108:111], v[60:63], v[40:43]\n" \
    "v_mfma_f32_16x16x32_bf16 v[32:35], v[176:179], v[56:59], v[32:35]\nv_mfma_f32_16x16x32_bf16 v[44:47], v[176:179], v[60:63], v[44:47]\ns_waitcnt vmcnt(5) lgkmcnt(0)\n" \
    "s_barrier\nv_mfma_f32_16x16x32_bf16 v[24:27], a[112:115], v[64:67], v[24:27]\nds_read_b128 v[48:51], v2 offset:32768\nv_mov_b32 v123, s50\n" \
    "v_mfma_f32_16x16x32_bf16 v[36:39], a[112:115], v[68:71], v[36:39]\nds_read_b128 v[52:55], v2 offset:40960\n" \
    "v_mfma_f32_16x16x32_bf16 v[28:31], a[116:119], v[64:67], v[28:31]\nbuffer_store_dword v123, v5, s[64:67], 0 offen sc1\n" \
    "v_mfma_f32_16x16x32_bf16 v[40:43], a[116:119], v[68:71], v[40:43]\nds_read_b128 v[96:99], v8 offset:0\nv_mfma_f32_16x16x32_bf16 v[32:35], v[180:183], v[64:67], v[32:35]\n" \
    "ds_read_b128 v[100:103], v8 offset:2048\nv_mfma_f32_16x16x32_bf16 v[44:47], v[180:183], v[68:71], v[44:47]\nds_read_b128 v[104:107], v8 offset:4096\n" \
    "v_mfma_f32_16x16x32_bf16 v[24:27], a[120:123], v[72:75], v[24:27]\nds_read_b128 v[56:59], v3 offset:32768\n" \
    "v_mfma_f32_16x16x32_bf16 v[36:39], a[120:123], v[76:79], v[36:39]\nds_read_b128 v[60:63], v3 offset:40960\n" \
    "v_mfma_f32_16x16x32_bf16 v[28:31], a[124:127], v[72:75], v[28:31]\nds_read_b128 v[108:111], v8 offset:6144\n" \
    "v_mfma_f32_16x16x32_bf16 v[40:43], a[124:127], v[76:79], v[40:43]\nv_mfma_f32_16x16x32_bf16 v[32:35], v[184:187], v[72:75], v[32:35]\n" \
    "v_mfma_f32_16x16x32_bf16 v[44:47], v[184:187], v[76:79], v[44:47]\ns_waitcnt lgkmcnt(6)\nv_mfma_f32_16x16x32_bf16 v[24:27], a[128:131], v[48:51], v[24:27]\n" \
    "ds_read_b128 v[64:67], v2 offset:33792\nv_mfma_f32_16x16x32_bf16 v[36:39], a[128:131], v[52:55], v[36:39]\nds_read_b128 v[68:71], v2 offset:41984\n" \
    "v_mfma_f32_16x16x32_bf16 v[28:31], a[132:135], v[48:51], v[28:31]\ns_waitcnt lgkmcnt(2)\ns_add_u32 m0, s52, 16384\ns_add_u32 s69, s70, 512\n" \
    "v_mfma_f32_16x16x32_bf16 v[112:115], v[96:99], v[80:83], 0\nbuffer_load_dwordx4 v1, s[64:67], s69 offen offset:0 sc1 lds\n" \
    "v_mfma_f32_16x16x32_bf16 v[40:43], a[132:135], v[52:55], v[40:43]\nds_read_b128 v[96:99], v8 offset:32\n" \
    "v_mfma_f32_16x16x32_bf16 v[112:115], v[100:103], v[84:87], v[112:115]\nds_read_b128 v[100:103], v8 offset:2080\n" \
    "v_mfma_f32_16x16x32_bf16 v[32:35], v[188:191], v[48:51], v[32:35]\nv_mfma_f32_16x16x32_bf16 v[112:115], v[104:107], v[88:91], v[112:115]\n" \
    "ds_read_b128 v[104:107], v8 offset:4128\nv_mfma_f32_16x16x32_bf16 v[44:47], v[188:191], v[52:55], v[44:47]\n" \
    "v_mfma_f32_16x16x32_bf16 v[112:115], v[108:111], v[92:95], v[112:115]\nds_read_b128 v[108:111], v8 offset:6176\n" \
    "v_mfma_f32_16x16x32_bf16 v[24:27], a[136:139], v[56:59], v[24:27]\nds_read_b128 v[72:75], v3 offset:33792\n" \
    "v_mfma_f32_16x16x32_bf16 v[36:39], a[136:139], v[60:63], v[36:39]\nds_read_b128 v[76:79], v3 offset:41984\n" \
    "v_mfma_f32_16x16x32_bf16 v[28:31], a[140:143], v[56:59], v[28:31]\ns_add_u32 s69, s70, -384\nbuffer_load_dwordx4 v1, s[64:67], s69 offen offset:1024 sc1 lds\n" \
    "v_mfma_f32_16x16x32_bf16 v[40:43], a[140:143], v[60:63], v[40:43]\nv_cndmask_b32_e64 v112, v22, v112, s[80:81]\nv_cndmask_b32_e64 v113, v22, v113, s[82:83]\n" \
    "v_mfma_f32_16x16x32_bf16 v[32:35], v[192:195], v[56:59], v[32:35]\nv_cndmask_b32_e64 v114, v22, v114, s[84:85]\nv_cndmask_b32_e64 v115, v22, v115, s[86:87]\n" \
    "v_mfma_f32_16x16x32_bf16 v[44:47], v[192:195], v[60:63], v[44:47]\ns_waitcnt lgkmcnt(6)\nv_max3_f32 v120, v112, v113, v114\nv_max3_f32 v120, v120, v115, s33\n" \
    "v_mfma_f32_16x16x32_bf16 v[24:27], a[144:147], v[64:67], v[24:27]\nds_read_b128 v[48:51], v2 offset:34816\n" \
    "v_mfma_f32_16x16x32_bf16 v[36:39], a[144:147], v[68:71], v[36:39]\nds_read_b128 v[52:55], v2 offset:43008\n" \
    "v_mfma_f32_16x16x32_bf16 v[28:31], a[148:151], v[64:67], v[28:31]\ns_waitcnt lgkmcnt(4)\ns_add_u32 s69, s70, -1280\n" \
    "v_mfma_f32_16x16x32_bf16 v[116:119], v[96:99], v[80:83], 0\nbuffer_load_dwordx4 v1, s[64:67], s69 offen offset:2048 sc1 lds\n" \
    "v_mfma_f32_16x16x32_bf16 v[40:43], a[148:151], v[68:71], v[40:43]\nv_mfma_f32_16x16x32_bf16 v[116:119], v[100:103], v[84:87], v[116:119]\n" \
    "v_mfma_f32_16x16x32_bf16 v[32:35], v[196:199], v[64:67], v[32:35]\nv_mfma_f32_16x16x32_bf16 v[116:119], v[104:107], v[88:91], v[116:119]\n" \
    "v_mfma_f32_16x16x32_bf16 v[44:47], v[196:199], v[68:71], v[44:47]\nv_mfma_f32_16x16x32_bf16 v[116:119], v[108:111], v[92:95], v[116:119]\ns_waitcnt lgkmcnt(2)\n" \
    "v_mfma_f32_16x16x32_bf16 v[24:27], a[152:155], v[72:75], v[24:27]\nds_read_b128 v[56:59], v3 offset:34816\n" \
    "v_mfma_f32_16x16x32_bf16 v[36:39], a[152:155], v[76:79], v[36:39]\nds_read_b128 v[60:63], v3 offset:43008\n" \
    "v_mfma_f32_16x16x32_bf16 v[28:31], a[156:159], v[72:75], v[28:31]\ns_add_u32 s69, s70, -2176\nbuffer_load_dwordx4 v1, s[64:67], s69 offen offset:3072 sc1 lds\n" \
    "v_mfma_f32_16x16x32_bf16 v[40:43], a[156:159], v[76:79], v[40:43]\nv_cndmask_b32_e64 v116, v22, v116, s[88:89]\nv_cndmask_b32_e64 v117, v22, v117, s[90:91]\n" \
    "v_mfma_f32_16x16x32_bf16 v[32:35], v[200:203], v[72:75], v[32:35]\nv_cndmask_b32_e64 v118, v22, v118, s[92:93]\nv_cndmask_b32_e64 v119, v22, v119, s[94:95]\n" \
    "v_mfma_f32_16x16x32_bf16 v[44:47], v[200:203], v[76:79], v[44:47]\ns_waitcnt lgkmcnt(2)\nv_max3_f32 v120, v120, v116, v117\nv_max3_f32 v120, v120, v118, v119\n" \
    "v_mfma_f32_16x16x32_bf16 v[24:27], a[160:163], v[48:51], v[24:27]\nds_read_b128 v[64:67], v2 offset:35840\nv_mul_f32_e64 v122, -s32, v120\n" \
    "v_mfma_f32_16x16x32_bf16 v[36:39], a[160:163], v[52:55], v[36:39]\nds_read_b128 v[68:71], v2 offset:44032\nv_fma_f32 v112, v112, s32, v122\n" \
    "v_mfma_f32_16x16x32_bf16 v[28:31], a[164:167], v[48:51], v[28:31]\ns_waitcnt vmcnt(5)\nv_writelane_b32 v20, s72, 2\nv_fma_f32 v113, v113, s32, v122\n" \
    "v_mfma_f32_16x16x32_bf16 v[40:43], a[164:167], v[52:55], v[40:43]\nds_write_b32 v7, v20\nv_mfma_f32_16x16x32_bf16 v[32:35], v[204:207], v[48:51], v[32:35]\n" \
    "v_fma_f32 v114, v114, s32, v122\nv_fma_f32 v115, v115, s32, v122\nv_mfma_f32_16x16x32_bf16 v[44:47], v[204:207], v[52:55], v[44:47]\ns_waitcnt lgkmcnt(3)\n" \
    "v_fma_f32 v116, v116, s32, v122\nv_fma_f32 v117, v117, s32, v122\nv_mfma_f32_16x16x32_bf16 v[24:27], a[168:171], v[56:59], v[24:27]\n" \
    "ds_read_b128 v[72:75], v3 offset:35840\nv_fma_f32 v118, v118, s32, v122\nv_mfma_f32_16x16x32_bf16 v[36:39], a[168:171], v[60:63], v[36:39]\n" \
    "ds_read_b128 v[76:79], v3 offset:44032\nv_fma_f32 v119, v119, s32, v122\nv_mfma_f32_16x16x32_bf16 v[28:31], a[172:175], v[56:59], v[28:31]\nv_exp_f32 v112, v112\n" \
    "v_mfma_f32_16x16x32_bf16 v[40:43], a[172:175], v[60:63], v[40:43]\nv_exp_f32 v113, v113\nv_mfma_f32_16x16x32_bf16 v[32:35], v[208:211], v[56:59], v[32:35]\n" \
    "v_exp_f32 v114, v114\nv_mfma_f32_16x16x32_bf16 v[44:47], v[208:211], v[60:63], v[44:47]\ns_waitcnt lgkmcnt(0)\ns_barrier\n" \
    "v_mfma_f32_16x16x32_bf16 v[24:27], a[176:179], v[64:67], v[24:27]\nds_read_b128 v[48:51], v2 offset:49152\n" \
    "v_mfma_f32_16x16x32_bf16 v[36:39], a[176:179], v[68:71], v[36:39]\nds_read_b128 v[52:55], v2 offset:57344\n" \
    "v_mfma_f32_16x16x32_bf16 v[28:31], a[180:183], v[64:67], v[28:31]\nv_exp_f32 v115, v115\nv_mfma_f32_16x16x32_bf16 v[40:43], a[180:183], v[68:71], v[40:43]\n" \
    "v_exp_f32 v116, v116\nv_mfma_f32_16x16x32_bf16 v[32:35], v[212:215], v[64:67], v[32:35]\nv_exp_f32 v117, v117\n" \
    "v_mfma_f32_16x16x32_bf16 v[44:47], v[212:215], v[68:71], v[44:47]\nv_exp_f32 v118, v118\nv_mfma_f32_16x16x32_bf16 v[24:27], a[184:187], v[72:75], v[24:27]\n" \
    "ds_read_b128 v[56:59], v3 offset:49152\nv_mfma_f32_16x16x32_bf16 v[36:39], a[184:187], v[76:79], v[36:39]\nds_read_b128 v[60:63], v3 offset:57344\n" \
    "v_mfma_f32_16x16x32_bf16 v[28:31], a[188:191], v[72:75], v[28:31]\nv_exp_f32 v119, v119\nv_mfma_f32_16x16x32_bf16 v[40:43], a[188:191], v[76:79], v[40:43]\n" \
    "v_add_f32 v121, v112, v113\nv_add_f32 v121, v121, v114\nv_mfma_f32_16x16x32_bf16 v[32:35], v[216:219], v[72:75], v[32:35]\nv_add_f32 v121, v121, v115\n" \
    "v_add_f32 v121, v121, v116\nv_mfma_f32_16x16x32_bf16 v[44:47], v[216:219], v[76:79], v[44:47]\ns_waitcnt lgkmcnt(2)\nv_add_f32 v121, v121, v117\n" \
    "v_add_f32 v121, v121, v118\nv_mfma_f32_16x16x32_bf16 v[24:27], a[192:195], v[48:51], v[24:27]\nds_read_b128 v[64:67], v2 offset:50176\nv_add_f32 v121, v121, v119\n" \
    "v_mfma_f32_16x16x32_bf16 v[36:39], a[192:195], v[52:55], v[36:39]\nds_read_b128 v[68:71], v2 offset:58368\n" \
    "v_mfma_f32_16x16x32_bf16 v[28:31], a[196:199], v[48:51], v[28:31]\ns_add_u32 m0, s52, 32768\ns_add_u32 s69, s70, 1024\n" \
    "v_mfma_f32_16x16x32_bf16 v[40:43], a[196:199], v[52:55], v[40:43]\nbuffer_load_dwordx4 v1, s[64:67], s69 offen offset:0 sc1 lds\n" \
    "v_mfma_f32_16x16x32_bf16 v[32:35], v[220:223], v[48:51], v[32:35]\nds_read_b96 v[108:110], v15\nv_mfma_f32_16x16x32_bf16 v[44:47], v[220:223], v[52:55], v[44:47]\n" \
    "s_waitcnt lgkmcnt(3)\nv_mfma_f32_16x16x32_bf16 v[24:27], a[200:203], v[56:59], v[24:27]\nds_read_b128 v[72:75], v3 offset:50176\n" \
    "v_mfma_f32_16x16x32_bf16 v[36:39], a[200:203], v[60:63], v[36:39]\nds_read_b128 v[76:79], v3 offset:58368\n" \
    "v_mfma_f32_16x16x32_bf16 v[28:31], a[204:207], v[56:59], v[28:31]\ns_add_u32 s69, s70, 128\nbuffer_load_dwordx4 v1, s[64:67], s69 offen offset:1024 sc1 lds\n" \
    "v_mfma_f32_16x16x32_bf16 v[40:43], a[204:207], v[60:63], v[40:43]\nv_mfma_f32_16x16x32_bf16 v[32:35], v[224:227], v[56:59], v[32:35]\ns_waitcnt lgkmcnt(2)\n" \
    "v_readfirstlane_b32 s72, v108\nv_readfirstlane_b32 s73, v109\nv_mfma_f32_16x16x32_bf16 v[44:47], v[224:227], v[60:63], v[44:47]\nv_readfirstlane_b32 s40, v110\n" \
    "v_mfma_f32_16x16x32_bf16 v[24:27], a[208:211], v[64:67], v[24:27]\nds_read_b128 v[48:51], v2 offset:51200\n" \
    "v_mfma_f32_16x16x32_bf16 v[36:39], a[208:211], v[68:71], v[36:39]\nds_read_b128 v[52:55], v2 offset:59392\ns_lshr_b32 s41, s72, 8\n" \
    "v_mfma_f32_16x16x32_bf16 v[28:31], a[212:215], v[64:67], v[28:31]\ns_add_u32 s69, s70, -768\nbuffer_load_dwordx4 v1, s[64:67], s69 offen offset:2048 sc1 lds\n" \
    "v_mfma_f32_16x16x32_bf16 v[40:43], a[212:215], v[68:71], v[40:43]\ns_sub_u32 s41, s40, s41\ns_lshl_b32 s41, s41, 8\ns_and_b32 s72, s72, 127\n" \
    "v_mfma_f32_16x16x32_bf16 v[32:35], v[228:231], v[64:67], v[32:35]\ns_cmp_lt_i32 s41, s30\ns_subb_u32 s72, s72, 0\ns_max_i32 s72, s72, 0\n" \
    "v_mfma_f32_16x16x32_bf16 v[44:47], v[228:231], v[68:71], v[44:47]\ns_waitcnt lgkmcnt(2)\ns_cmp_gt_i32 s72, s96\ns_cselect_b32 s41, s73, 0x80000000\n" \
    "v_mfma_f32_16x16x32_bf16 v[24:27], a[216:219], v[72:75], v[24:27]\nds_read_b128 v[56:59], v3 offset:51200\n" \
    "v_mfma_f32_16x16x32_bf16 v[36:39], a[216:219], v[76:79], v[36:39]\nds_read_b128 v[60:63], v3 offset:59392\n" \
    "v_mfma_f32_16x16x32_bf16 v[28:31], a[220:223], v[72:75], v[28:31]\ns_add_u32 s69, s70, -1664\nbuffer_load_dwordx4 v1, s[64:67], s69 offen offset:3072 sc1 lds\n" \
    "v_mfma_f32_16x16x32_bf16 v[40:43], a[220:223], v[76:79], v[40:43]\nbuffer_load_dword v20, v6, s[64:67], 0 offen sc1\n" \
    "v_mfma_f32_16x16x32_bf16 v[32:35], v[232:235], v[72:75], v[32:35]\nv_mfma_f32_16x16x32_bf16 v[44:47], v[232:235], v[76:79], v[44:47]\ns_waitcnt lgkmcnt(2)\n" \
    "v_mfma_f32_16x16x32_bf16 v[24:27], a[224:227], v[48:51], v[24:27]\nds_read_b128 v[64:67], v2 offset:52224\n" \
    "v_mfma_f32_16x16x32_bf16 v[36:39], a[224:227], v[52:55], v[36:39]\nds_read_b128 v[68:71], v2 offset:60416\n" \
    "v_mfma_f32_16x16x32_bf16 v[28:31], a[228:231], v[48:51], v[28:31]\nds_read_b128 v[72:75], v3 offset:52224\n" \
    "v_mfma_f32_16x16x32_bf16 v[40:43], a[228:231], v[52:55], v[40:43]\nds_read_b128 v[76:79], v3 offset:60416\n" \
    "v_mfma_f32_16x16x32_bf16 v[32:35], v[236:239], v[48:51], v[32:35]\nv_mfma_f32_16x16x32_bf16 v[44:47], v[236:239], v[52:55], v[44:47]\ns_waitcnt lgkmcnt(4)\n" \
    "v_mfma_f32_16x16x32_bf16 v[32:35], v[240:243], v[56:59], v[32:35]\nv_add_u32 v122, s59, v11\nv_add_u32 v123, s59, v12\n" \
    "v_mfma_f32_16x16x32_bf16 v[44:47], v[240:243], v[60:63], v[44:47]\ns_waitcnt lgkmcnt(2)\ns_add_u32 s59, s59, 0x2000\ns_and_b32 s59, s59, 0xffff\n" \
    "v_mfma_f32_16x16x32_bf16 v[32:35], v[244:247], v[64:67], v[32:35]\ns_waitcnt lgkmcnt(0)\ns_barrier\nv_mfma_f32_16x16x32_bf16 v[44:47], v[244:247], v[68:71], v[44:47]\n" \
    "ds_read_b128 v[48:51], v2 offset:0\nv_mfma_f32_16x16x32_bf16 v[32:35], v[248:251], v[72:75], v[32:35]\nds_read_b128 v[52:55], v2 offset:8192\n" \
    "v_mfma_f32_16x16x32_bf16 v[44:47], v[248:251], v[76:79], v[44:47]\ns_mov_b32 s54, s70\ns_add_u32 s50, s50, 1\n" \
    "v_mfma_f32_16x16x32_bf16 v[24:27], a[232:235], v[56:59], v[24:27]\ns_nop 1\nv_mfma_f32_16x16x32_bf16 v[24:27], a[240:243], v[64:67], v[24:27]\ns_nop 0\n" \
    "v_cvt_pk_bf16_f32 v80, v32, v33\nv_cvt_pk_bf16_f32 v81, v34, v35\nv_mfma_f32_16x16x32_bf16 v[24:27], a[248:251], v[72:75], v[24:27]\nv_cvt_pk_bf16_f32 v82, v44, v45\n" \
    "v_cvt_pk_bf16_f32 v83, v46, v47\nv_mfma_f32_16x16x32_bf16 v[28:31], a[236:239], v[56:59], v[28:31]\nv_permlane16_swap_b32 v80, v82\n" \
    "v_mfma_f32_16x16x32_bf16 v[28:31], a[244:247], v[64:67], v[28:31]\nds_read_b128 v[56:59], v3 offset:0\nv_mfma_f32_16x16x32_bf16 v[28:31], a[252:255], v[72:75], v[28:31]\n" \
    "v_permlane16_swap_b32 v81, v83\nv_mfma_f32_16x16x32_bf16 v[36:39], a[232:235], v[60:63], v[36:39]\nbuffer_store_dwordx4 v[80:83], v4, s[64:67], s71 offen sc1\n" \
    "v_mfma_f32_16x16x32_bf16 v[36:39], a[240:243], v[68:71], v[36:39]\nv_cvt_pk_bf16_f32 v84, v24, v25\nv_cvt_pk_bf16_f32 v85, v26, v27\n" \
    "v_mfma_f32_16x16x32_bf16 v[36:39], a[248:251], v[76:79], v[36:39]\ns_nop 0\nv_cvt_pk_bf16_f32 v86, v28, v29\nv_cvt_pk_bf16_f32 v87, v30, v31\n" \
    "v_mfma_f32_16x16x32_bf16 v[40:43], a[236:239], v[60:63], v[40:43]\nv_permlane16_swap_b32 v84, v86\nv_mfma_f32_16x16x32_bf16 v[40:43], a[244:247], v[68:71], v[40:43]\n" \
    "ds_read_b128 v[60:63], v3 offset:8192\nv_mfma_f32_16x16x32_bf16 v[40:43], a[252:255], v[76:79], v[40:43]\nv_permlane16_swap_b32 v85, v87\nds_write_b128 v122, v[84:87]\n" \
    "s_cmp_gt_i32 s41, s99\nv_cvt_pk_bf16_f32 v88, v36, v37\nv_cvt_pk_bf16_f32 v89, v38, v39\ns_nop 2\nv_cvt_pk_bf16_f32 v90, v40, v41\nv_cvt_pk_bf16_f32 v91, v42, v43\n" \
    "s_nop 0\nv_permlane16_swap_b32 v88, v90\nv_permlane16_swap_b32 v89, v91\nds_write_b128 v123, v[88:91]\ns_cbranch_scc0 .Lprqmid_%=\n.Lprqb2_%=:\ns_waitcnt lgkmcnt(4)\n" \
    "v_mfma_f32_16x16x32_bf16 v[24:27], a[0:3], v[48:51], 0\nds_read_b128 v[64:67], v2 offset:1024\ns_sub_u32 s38, s50, s24\n" \
    "v_mfma_f32_16x16x32_bf16 v[36:39], a[0:3], v[52:55], 0\nds_read_b128 v[68:71], v2 offset:9216\ns_max_i32 s38, s38, 0\n" \
    "v_mfma_f32_16x16x32_bf16 v[28:31], a[4:7], v[48:51], 0\ns_add_u32 m0, s52, 49152\ns_add_u32 s69, s54, 1536\ns_and_b32 s38, s38, s16\n" \
    "v_mfma_f32_16x16x32_bf16 v[40:43], a[4:7], v[52:55], 0\nbuffer_load_dwordx4 v1, s[64:67], s69 offen offset:0 sc1 lds\ns_lshl_b32 s38, s38, 19\n" \
    "v_mfma_f32_16x16x32_bf16 v[32:35], v[124:127], v[48:51], 0\ns_add_u32 s38, s38, s53\ns_add_u32 s74, s48, s38\ns_lshr_b32 s74, s74, 8\n" \
    "v_mfma_f32_16x16x32_bf16 v[44:47], v[124:127], v[52:55], 0\ns_waitcnt lgkmcnt(4)\ns_xnor_b32 s74, s74, s49\ns_and_b32 s74, s74, 0x2000\n" \
    "v_mfma_f32_16x16x32_bf16 v[24:27], a[8:11], v[56:59], v[24:27]\nds_read_b128 v[72:75], v3 offset:1024\ns_add_u32 s74, s74, s38\n" \
    "v_mfma_f32_16x16x32_bf16 v[36:39], a[8:11], v[60:63], v[36:39]\nds_read_b128 v[76:79], v3 offset:9216\ns_add_u32 s74, s74, s60\n" \
    "v_mfma_f32_16x16x32_bf16 v[28:31], a[12:15], v[56:59], v[28:31]\ns_add_u32 s68, s50, 1\ns_min_u32 s68, s68, 63\ns_lshl_b32 s68, s68, 20\n" \
    "v_mfma_f32_16x16x32_bf16 v[40:43], a[12:15], v[60:63], v[40:43]\ns_add_u32 s68, s68, s18\ns_add_u32 s70, s48, s68\ns_lshr_b32 s70, s70, 8\n" \
    "v_mfma_f32_16x16x32_bf16 v[32:35], v[128:131], v[56:59], v[32:35]\ns_xnor_b32 s70, s70, s49\ns_and_b32 s70, s70, 0x2000\ns_add_u32 s70, s70, s68\n" \
    "v_mfma_f32_16x16x32_bf16 v[44:47], v[128:131], v[60:63], v[44:47]\ns_waitcnt lgkmcnt(2)\ns_add_u32 s69, s54, 640\n" \
    "v_mfma_f32_16x16x32_bf16 v[24:27], a[16:19], v[64:67], v[24:27]\nds_read_b128 v[48:51], v2 offset:2048\nv_mfma_f32_16x16x32_bf16 v[36:39], a[16:19], v[68:71], v[36:39]\n" \
    "ds_read_b128 v[52:55], v2 offset:10240\nv_mfma_f32_16x16x32_bf16 v[28:31], a[20:23], v[64:67], v[28:31]\nbuffer_load_dwordx4 v1, s[64:67], s69 offen offset:1024 sc1 lds\n" \
    "s_add_u32 s69, s54, -256\nv_mfma_f32_16x16x32_bf16 v[40:43], a[20:23], v[68:71], v[40:43]\nbuffer_load_dwordx4 v1, s[64:67], s69 offen offset:2048 sc1 lds\n" \
    "s_sub_u32 s77, s50, s23\nv_mfma_f32_16x16x32_bf16 v[32:35], v[132:135], v[64:67], v[32:35]\ns_max_i32 s76, s77, 0\ns_and_b32 s76, s76, s16\ns_lshl_b32 s76, s76, 19\n" \
    "v_mfma_f32_16x16x32_bf16 v[44:47], v[132:135], v[68:71], v[44:47]\ns_waitcnt lgkmcnt(2)\ns_add_u32 s76, s76, s20\ns_add_u32 s75, s48, s76\n" \
    "v_mfma_f32_16x16x32_bf16 v[24:27], a[24:27], v[72:75], v[24:27]\nds_read_b128 v[56:59], v3 offset:2048\ns_lshr_b32 s75, s75, 8\n" \
    "v_mfma_f32_16x16x32_bf16 v[36:39], a[24:27], v[76:79], v[36:39]\nds_read_b128 v[60:63], v3 offset:10240\ns_xnor_b32 s75, s75, s49\n" \
    "v_mfma_f32_16x16x32_bf16 v[28:31], a[28:31], v[72:75], v[28:31]\ns_add_u32 s69, s54, -1152\nbuffer_load_dwordx4 v1, s[64:67], s69 offen offset:3072 sc1 lds\n" \
    "v_mfma_f32_16x16x32_bf16 v[40:43], a[28:31], v[76:79], v[40:43]\ns_and_b32 s75, s75, 0x2000\ns_add_u32 s75, s75, s76\ns_add_u32 s75, s75, s57\n" \
    "v_mfma_f32_16x16x32_bf16 v[32:35], v[136:139], v[72:75], v[32:35]\nv_mfma_f32_16x16x32_bf16 v[44:47], v[136:139], v[76:79], v[44:47]\ns_waitcnt lgkmcnt(2)\n" \
    "v_mfma_f32_16x16x32_bf16 v[24:27], a[32:35], v[48:51], v[24:27]\nds_read_b128 v[64:67], v2 offset:3072\nv_mfma_f32_16x16x32_bf16 v[36:39], a[32:35], v[52:55], v[36:39]\n" \
    "ds_read_b128 v[68:71], v2 offset:11264\nv_mfma_f32_16x16x32_bf16 v[28:31], a[36:39], v[48:51], v[28:31]\ns_and_b32 s37, s50, s16\ns_lshl_b32 s37, s37, 19\n" \
    "s_add_u32 s37, s37, s53\nv_mfma_f32_16x16x32_bf16 v[40:43], a[36:39], v[52:55], v[40:43]\ns_add_u32 s71, s48, s37\ns_lshr_b32 s71, s71, 8\ns_xnor_b32 s71, s71, s49\n" \
    "v_mfma_f32_16x16x32_bf16 v[32:35], v[140:143], v[48:51], v[32:35]\ns_and_b32 s71, s71, 0x2000\ns_add_u32 s71, s71, s37\ns_mov_b32 m0, s56\n" \
    "v_mfma_f32_16x16x32_bf16 v[44:47], v[140:143], v[52:55], v[44:47]\ns_waitcnt lgkmcnt(2)\nbuffer_load_dwordx4 v0, s[64:67], s74 offen offset:0 sc1 lds\n" \
    "v_mfma_f32_16x16x32_bf16 v[24:27], a[40:43], v[56:59], v[24:27]\nds_read_b128 v[72:75], v3 offset:3072\nv_mfma_f32_16x16x32_bf16 v[36:39], a[40:43], v[60:63], v[36:39]\n" \
    "ds_read_b128 v[76:79], v3 offset:11264\nv_mfma_f32_16x16x32_bf16 v[28:31], a[44:47], v[56:59], v[28:31]\nbuffer_load_dwordx4 v0, s[64:67], s74 offen offset:1024 sc1 lds\n" \
    "v_mfma_f32_16x16x32_bf16 v[40:43], a[44:47], v[60:63], v[40:43]\nbuffer_load_dwordx4 v[96:99], v9, s[64:67], s75 offen offset:0 sc1\n" \
    "v_mfma_f32_16x16x32_bf16 v[32:35], v[144:147], v[56:59], v[32:35]\nbuffer_load_dwordx4 v[100:103], v9, s[64:67], s75 offen offset:512 sc1\n" \
    "v_mfma_f32_16x16x32_bf16 v[44:47], v[144:147], v[60:63], v[44:47]\ns_waitcnt vmcnt(14) lgkmcnt(0)\ns_barrier\n" \
    "v_mfma_f32_16x16x32_bf16 v[24:27], a[48:51], v[64:67], v[24:27]\nds_read_b128 v[48:51], v2 offset:16384\nv_mfma_f32_16x16x32_bf16 v[36:39], a[48:51], v[68:71], v[36:39]\n" \
    "ds_read_b128 v[52:55], v2 offset:24576\nv_mfma_f32_16x16x32_bf16 v[28:31], a[52:55], v[64:67], v[28:31]\n" \
    "buffer_load_dwordx4 v[104:107], v9, s[64:67], s75 offen offset:1024 sc1\ns_add_u32 s39, s50, 1\nv_mfma_f32_16x16x32_bf16 v[40:43], a[52:55], v[68:71], v[40:43]\n" \
    "buffer_load_dwordx4 v[108:111], v9, s[64:67], s75 offen offset:1536 sc1\ns_add_u32 s97, s39, 1\nv_mfma_f32_16x16x32_bf16 v[32:35], v[148:151], v[64:67], v[32:35]\n" \
    "s_lshl_b32 s78, s77, 20\ns_add_u32 s78, s78, s21\ns_add_u32 s76, s48, s78\nv_mfma_f32_16x16x32_bf16 v[44:47], v[148:151], v[68:71], v[44:47]\ns_lshr_b32 s76, s76, 8\n" \
    "s_xor_b32 s76, s76, s49\ns_and_b32 s76, s76, 0x2000\nv_mfma_f32_16x16x32_bf16 v[24:27], a[56:59], v[72:75], v[24:27]\nds_read_b128 v[56:59], v3 offset:16384\n" \
    "s_add_u32 s76, s76, s78\nv_mfma_f32_16x16x32_bf16 v[36:39], a[56:59], v[76:79], v[36:39]\nds_read_b128 v[60:63], v3 offset:24576\ns_min_u32 s97, s97, 63\n" \
    "v_mfma_f32_16x16x32_bf16 v[28:31], a[60:63], v[72:75], v[28:31]\ns_cmp_lt_i32 s77, 0\ns_cselect_b32 s76, 0x100000, s76\ns_add_u32 s78, s76, s58\n" \
    "v_mfma_f32_16x16x32_bf16 v[40:43], a[60:63], v[76:79], v[40:43]\ns_lshl_b32 s79, s77, 2\ns_add_u32 s79, s79, s22\ns_sub_u32 s98, s39, s23\n" \
    "v_mfma_f32_16x16x32_bf16 v[32:35], v[152:155], v[72:75], v[32:35]\ns_add_u32 s99, s98, 1\ns_cmp_gt_i32 s51, s97\ns_cselect_b32 s96, s98, 0x7fffffff\n" \
    "v_mfma_f32_16x16x32_bf16 v[44:47], v[152:155], v[76:79], v[44:47]\ns_waitcnt lgkmcnt(2)\ns_sub_u32 s39, s39, 4\n" \
    "v_mfma_f32_16x16x32_bf16 v[24:27], a[64:67], v[48:51], v[24:27]\nds_read_b128 v[64:67], v2 offset:17408\nv_mfma_f32_16x16x32_bf16 v[36:39], a[64:67], v[52:55], v[36:39]\n" \
    "ds_read_b128 v[68:71], v2 offset:25600\nv_mfma_f32_16x16x32_bf16 v[28:31], a[68:71], v[48:51], v[28:31]\ns_add_u32 m0, s52, 0\ns_add_u32 s69, s70, 0\n" \
    "v_mfma_f32_16x16x32_bf16 v[40:43], a[68:71], v[52:55], v[40:43]\nbuffer_load_dwordx4 v1, s[64:67], s69 offen offset:0 sc1 lds\n" \
    "v_mfma_f32_16x16x32_bf16 v[32:35], v[156:159], v[48:51], v[32:35]\ns_cmp_lt_u32 s39, 60\ns_cselect_b32 s96, s96, 0x7fffffff\n" \
    "v_mfma_f32_16x16x32_bf16 v[44:47], v[156:159], v[52:55], v[44:47]\ns_waitcnt lgkmcnt(2)\nv_mfma_f32_16x16x32_bf16 v[24:27], a[72:75], v[56:59], v[24:27]\n" \
    "ds_read_b128 v[72:75], v3 offset:17408\nv_mfma_f32_16x16x32_bf16 v[36:39], a[72:75], v[60:63], v[36:39]\nds_read_b128 v[76:79], v3 offset:25600\n" \
    "v_mfma_f32_16x16x32_bf16 v[28:31], a[76:79], v[56:59], v[28:31]\ns_add_u32 s69, s70, -896\nbuffer_load_dwordx4 v1, s[64:67], s69 offen offset:1024 sc1 lds\n" \
    "v_mfma_f32_16x16x32_bf16 v[40:43], a[76:79], v[60:63], v[40:43]\nv_mfma_f32_16x16x32_bf16 v[32:35], v[160:163], v[56:59], v[32:35]\n" \
    "v_mfma_f32_16x16x32_bf16 v[44:47], v[160:163], v[60:63], v[44:47]\ns_waitcnt lgkmcnt(2)\nv_mfma_f32_16x16x32_bf16 v[24:27], a[80:83], v[64:67], v[24:27]\n" \
    "ds_read_b128 v[48:51], v2 offset:18432\nv_mfma_f32_16x16x32_bf16 v[36:39], a[80:83], v[68:71], v[36:39]\nds_read_b128 v[52:55], v2 offset:26624\n" \
    "v_mfma_f32_16x16x32_bf16 v[28:31], a[84:87], v[64:67], v[28:31]\ns_add_u32 s69, s70, -1792\nbuffer_load_dwordx4 v1, s[64:67], s69 offen offset:2048 sc1 lds\n" \
    "v_mfma_f32_16x16x32_bf16 v[40:43], a[84:87], v[68:71], v[40:43]\ns_add_u32 s68, s59, s26\ns_and_b32 s68, s68, 0xffff\nv_add_u32 v122, s68, v13\n" \
    "v_mfma_f32_16x16x32_bf16 v[32:35], v[164:167], v[64:67], v[32:35]\nds_read_b128 v[80:83], v122 offset:0\nv_mfma_f32_16x16x32_bf16 v[44:47], v[164:167], v[68:71], v[44:47]\n" \
    "s_waitcnt lgkmcnt(3)\nds_read_b128 v[84:87], v122 offset:1024\nv_mfma_f32_16x16x32_bf16 v[24:27], a[88:91], v[72:75], v[24:27]\nds_read_b128 v[56:59], v3 offset:18432\n" \
    "v_mfma_f32_16x16x32_bf16 v[36:39], a[88:91], v[76:79], v[36:39]\nds_read_b128 v[60:63], v3 offset:26624\nv_mfma_f32_16x16x32_bf16 v[28:31], a[92:95], v[72:75], v[28:31]\n" \
    "s_add_u32 s69, s70, -2688\nbuffer_load_dwordx4 v1, s[64:67], s69 offen offset:3072 sc1 lds\nv_mfma_f32_16x16x32_bf16 v[40:43], a[92:95], v[76:79], v[40:43]\n" \
    "ds_read_b128 v[88:91], v122 offset:2048\nv_mfma_f32_16x16x32_bf16 v[32:35], v[168:171], v[72:75], v[32:35]\nds_read_b128 v[92:95], v122 offset:3072\n" \
    "v_mfma_f32_16x16x32_bf16 v[44:47], v[168:171], v[76:79], v[44:47]\ns_waitcnt lgkmcnt(6)\nv_mfma_f32_16x16x32_bf16 v[24:27], a[96:99], v[48:51], v[24:27]\n" \
    "ds_read_b128 v[64:67], v2 offset:19456\nv_mfma_f32_16x16x32_bf16 v[36:39], a[96:99], v[52:55], v[36:39]\nds_read_b128 v[68:71], v2 offset:27648\n" \
    "v_mfma_f32_16x16x32_bf16 v[28:31], a[100:103], v[48:51], v[28:31]\nbuffer_load_dword v20, v6, s[64:67], 0 offen sc1\n" \
    "v_mfma_f32_16x16x32_bf16 v[40:43], a[100:103], v[52:55], v[40:43]\nv_mfma_f32_16x16x32_bf16 v[32:35], v[172:175], v[48:51], v[32:35]\ns_waitcnt vmcnt(5)\n" \
    "v_max3_u32 v123, v96, v97, v98\nv_max3_u32 v123, v123, v99, v100\nv_mfma_f32_16x16x32_bf16 v[44:47], v[172:175], v[52:55], v[44:47]\ns_waitcnt lgkmcnt(4)\n" \
    "v_max3_u32 v123, v123, v101, v102\nv_max3_u32 v123, v123, v103, v104\nv_mfma_f32_16x16x32_bf16 v[24:27], a[104:107], v[56:59], v[24:27]\n" \
    "ds_read_b128 v[72:75], v3 offset:19456\nv_max3_u32 v123, v123, v105, v106\nv_mfma_f32_16x16x32_bf16 v[36:39], a[104:107], v[60:63], v[36:39]\n" \
    "ds_read_b128 v[76:79], v3 offset:27648\nv_max3_u32 v123, v123, v107, v108\nv_mfma_f32_16x16x32_bf16 v[28:31], a[108:111], v[56:59], v[28:31]\ns_memrealtime s[72:73]\n" \
    "v_max3_u32 v123, v123, v109, v110\nv_max_u32 v123, v123, v111\nv_mfma_f32_16x16x32_bf16 v[40:43], a[108:111], v[60:63], v[40:43]\nv_cmp_eq_u32 vcc, -1, v123\n" \
    "v_mfma_f32_16x16x32_bf16 v[32:35], v[176:179], v[56:59], v[32:35]\ns_cbranch_vccnz .Lprqkm_%=\n.Lprqkmb_%=:\n" \
    "v_mfma_f32_16x16x32_bf16 v[44:47], v[176:179], v[60:63], v[44:47]\ns_waitcnt lgkmcnt(0)\ns_barrier\nv_mfma_f32_16x16x32_bf16 v[24:27], a[112:115], v[64:67], v[24:27]\n" \
    "ds_read_b128 v[48:51], v2 offset:32768\nv_mov_b32 v123, s50\nv_mfma_f32_16x16x32_bf16 v[36:39], a[112:115], v[68:71], v[36:39]\nds_read_b128 v[52:55], v2 offset:40960\n" \
    "v_mfma_f32_16x16x32_bf16 v[28:31], a[116:119], v[64:67], v[28:31]\nv_mfma_f32_16x16x32_bf16 v[16:19], v[96:99], v[80:83], 0\n" \
    "buffer_store_dword v123, v5, s[64:67], 0 offen sc1\nv_mfma_f32_16x16x32_bf16 v[40:43], a[116:119], v[68:71], v[40:43]\n" \
    "v_mfma_f32_16x16x32_bf16 v[16:19], v[100:103], v[84:87], v[16:19]\nv_mfma_f32_16x16x32_bf16 v[32:35], v[180:183], v[64:67], v[32:35]\n" \
    "v_mfma_f32_16x16x32_bf16 v[16:19], v[104:107], v[88:91], v[16:19]\nv_mfma_f32_16x16x32_bf16 v[44:47], v[180:183], v[68:71], v[44:47]\n" \
    "v_mfma_f32_16x16x32_bf16 v[16:19], v[108:111], v[92:95], v[16:19]\nv_mfma_f32_16x16x32_bf16 v[24:27], a[120:123], v[72:75], v[24:27]\n" \
    "ds_read_b128 v[56:59], v3 offset:32768\nv_mfma_f32_16x16x32_bf16 v[36:39], a[120:123], v[76:79], v[36:39]\nds_read_b128 v[60:63], v3 offset:40960\n" \
    "v_mfma_f32_16x16x32_bf16 v[28:31], a[124:127], v[72:75], v[28:31]\nv_mfma_f32_16x16x32_bf16 v[40:43], a[124:127], v[76:79], v[40:43]\n" \
    "v_mfma_f32_16x16x32_bf16 v[32:35], v[184:187], v[72:75], v[32:35]\ns_nop 0\nv_max3_f32 v122, v120, v16, v17\nv_max3_f32 v122, v122, v18, v19\n" \
    "v_mfma_f32_16x16x32_bf16 v[44:47], v[184:187], v[76:79], v[44:47]\ns_waitcnt lgkmcnt(2)\nv_mov_b32 v123, v122\n" \
    "v_mfma_f32_16x16x32_bf16 v[24:27], a[128:131], v[48:51], v[24:27]\nds_read_b128 v[64:67], v2 offset:33792\n" \
    "v_mfma_f32_16x16x32_bf16 v[36:39], a[128:131], v[52:55], v[36:39]\nds_read_b128 v[68:71], v2 offset:41984\n" \
    "v_mfma_f32_16x16x32_bf16 v[28:31], a[132:135], v[48:51], v[28:31]\ns_add_u32 m0, s52, 16384\ns_add_u32 s69, s70, 512\n" \
    "v_mfma_f32_16x16x32_bf16 v[40:43], a[132:135], v[52:55], v[40:43]\nbuffer_load_dwordx4 v1, s[64:67], s69 offen offset:0 sc1 lds\n" \
    "v_mfma_f32_16x16x32_bf16 v[32:35], v[188:191], v[48:51], v[32:35]\nv_permlane16_swap_b32 v122, v123\nv_mfma_f32_16x16x32_bf16 v[44:47], v[188:191], v[52:55], v[44:47]\n" \
    "s_waitcnt lgkmcnt(2)\nv_max_f32 v122, v122, v123\nv_mov_b32 v123, v122\nv_mfma_f32_16x16x32_bf16 v[24:27], a[136:139], v[56:59], v[24:27]\n" \
    "ds_read_b128 v[72:75], v3 offset:33792\nv_mfma_f32_16x16x32_bf16 v[36:39], a[136:139], v[60:63], v[36:39]\nds_read_b128 v[76:79], v3 offset:41984\n" \
    "v_mfma_f32_16x16x32_bf16 v[28:31], a[140:143], v[56:59], v[28:31]\ns_add_u32 s69, s70, -384\nbuffer_load_dwordx4 v1, s[64:67], s69 offen offset:1024 sc1 lds\n" \
    "v_mfma_f32_16x16x32_bf16 v[40:43], a[140:143], v[60:63], v[40:43]\nv_permlane32_swap_b32 v122, v123\nv_mfma_f32_16x16x32_bf16 v[32:35], v[192:195], v[56:59], v[32:35]\n" \
    "v_max_f32 v122, v122, v123\nv_mul_f32_e64 v122, -s32, v122\nv_mfma_f32_16x16x32_bf16 v[44:47], v[192:195], v[60:63], v[44:47]\ns_waitcnt lgkmcnt(2)\n" \
    "v_fma_f32 v120, v120, s32, v122\nv_fma_f32 v16, v16, s32, v122\nv_mfma_f32_16x16x32_bf16 v[24:27], a[144:147], v[64:67], v[24:27]\nds_read_b128 v[48:51], v2 offset:34816\n" \
    "v_fma_f32 v17, v17, s32, v122\nv_mfma_f32_16x16x32_bf16 v[36:39], a[144:147], v[68:71], v[36:39]\nds_read_b128 v[52:55], v2 offset:43008\nv_fma_f32 v18, v18, s32, v122\n" \
    "v_mfma_f32_16x16x32_bf16 v[28:31], a[148:151], v[64:67], v[28:31]\ns_add_u32 s69, s70, -1280\nbuffer_load_dwordx4 v1, s[64:67], s69 offen offset:2048 sc1 lds\n" \
    "v_mfma_f32_16x16x32_bf16 v[40:43], a[148:151], v[68:71], v[40:43]\nv_fma_f32 v19, v19, s32, v122\nv_mfma_f32_16x16x32_bf16 v[32:35], v[196:199], v[64:67], v[32:35]\n" \
    "v_exp_f32 v120, v120\nv_mfma_f32_16x16x32_bf16 v[44:47], v[196:199], v[68:71], v[44:47]\ns_waitcnt lgkmcnt(2)\nv_exp_f32 v16, v16\n" \
    "v_mfma_f32_16x16x32_bf16 v[24:27], a[152:155], v[72:75], v[24:27]\nds_read_b128 v[56:59], v3 offset:34816\n" \
    "v_mfma_f32_16x16x32_bf16 v[36:39], a[152:155], v[76:79], v[36:39]\nds_read_b128 v[60:63], v3 offset:43008\n" \
    "v_mfma_f32_16x16x32_bf16 v[28:31], a[156:159], v[72:75], v[28:31]\ns_add_u32 s69, s70, -2176\nbuffer_load_dwordx4 v1, s[64:67], s69 offen offset:3072 sc1 lds\n" \
    "v_mfma_f32_16x16x32_bf16 v[40:43], a[156:159], v[76:79], v[40:43]\nv_exp_f32 v17, v17\nv_mfma_f32_16x16x32_bf16 v[32:35], v[200:203], v[72:75], v[32:35]\n" \
    "v_exp_f32 v18, v18\nv_mfma_f32_16x16x32_bf16 v[44:47], v[200:203], v[76:79], v[44:47]\ns_waitcnt lgkmcnt(2)\nv_exp_f32 v19, v19\n" \
    "v_mfma_f32_16x16x32_bf16 v[24:27], a[160:163], v[48:51], v[24:27]\nds_read_b128 v[64:67], v2 offset:35840\nv_add_f32 v122, v16, v17\n" \
    "v_mfma_f32_16x16x32_bf16 v[36:39], a[160:163], v[52:55], v[36:39]\nds_read_b128 v[68:71], v2 offset:44032\nv_add_f32 v123, v18, v19\n" \
    "v_mfma_f32_16x16x32_bf16 v[28:31], a[164:167], v[48:51], v[28:31]\ns_waitcnt vmcnt(5)\nv_writelane_b32 v20, s72, 2\nv_mul_f32 v121, v121, v120\n" \
    "v_mfma_f32_16x16x32_bf16 v[40:43], a[164:167], v[52:55], v[40:43]\nds_write_b32 v7, v20\nv_mfma_f32_16x16x32_bf16 v[32:35], v[204:207], v[48:51], v[32:35]\n" \
    "v_add_f32 v122, v122, v123\nv_fma_f32 v121, 0.5, v122, v121\nv_mfma_f32_16x16x32_bf16 v[44:47], v[204:207], v[52:55], v[44:47]\ns_waitcnt lgkmcnt(3)\nv_mov_b32 v123, v121\n" \
    "v_mfma_f32_16x16x32_bf16 v[24:27], a[168:171], v[56:59], v[24:27]\nds_read_b128 v[72:75], v3 offset:35840\n" \
    "v_mfma_f32_16x16x32_bf16 v[36:39], a[168:171], v[60:63], v[36:39]\nds_read_b128 v[76:79], v3 offset:44032\n" \
    "v_mfma_f32_16x16x32_bf16 v[28:31], a[172:175], v[56:59], v[28:31]\nv_permlane16_swap_b32 v121, v123\nv_mfma_f32_16x16x32_bf16 v[40:43], a[172:175], v[60:63], v[40:43]\n" \
    "v_add_f32 v121, v121, v123\nv_mov_b32 v123, v121\nv_mfma_f32_16x16x32_bf16 v[32:35], v[208:211], v[56:59], v[32:35]\n" \
    "v_mfma_f32_16x16x32_bf16 v[44:47], v[208:211], v[60:63], v[44:47]\ns_waitcnt lgkmcnt(0)\ns_barrier\nv_mfma_f32_16x16x32_bf16 v[24:27], a[176:179], v[64:67], v[24:27]\n" \
    "ds_read_b128 v[48:51], v2 offset:49152\nv_mfma_f32_16x16x32_bf16 v[36:39], a[176:179], v[68:71], v[36:39]\nds_read_b128 v[52:55], v2 offset:57344\n" \
    "v_mfma_f32_16x16x32_bf16 v[28:31], a[180:183], v[64:67], v[28:31]\nv_permlane32_swap_b32 v121, v123\nv_mfma_f32_16x16x32_bf16 v[40:43], a[180:183], v[68:71], v[40:43]\n" \
    "ds_read_b96 v[108:110], v15\nv_add_f32 v121, v121, v123\nv_mfma_f32_16x16x32_bf16 v[32:35], v[212:215], v[64:67], v[32:35]\nv_rcp_f32 v121, v121\n" \
    "v_mfma_f32_16x16x32_bf16 v[44:47], v[212:215], v[68:71], v[44:47]\nv_mul_f32 v120, v120, v121\nv_mul_f32 v16, v16, v121\n" \
    "v_mfma_f32_16x16x32_bf16 v[24:27], a[184:187], v[72:75], v[24:27]\nds_read_b128 v[56:59], v3 offset:49152\nv_mul_f32 v17, v17, v121\n" \
    "v_mfma_f32_16x16x32_bf16 v[36:39], a[184:187], v[76:79], v[36:39]\nds_read_b128 v[60:63], v3 offset:57344\nv_mul_f32 v18, v18, v121\n" \
    "v_mfma_f32_16x16x32_bf16 v[28:31], a[188:191], v[72:75], v[28:31]\nv_mul_f32 v19, v19, v121\nv_mul_f32 v112, v112, v120\n" \
    "v_mfma_f32_16x16x32_bf16 v[40:43], a[188:191], v[76:79], v[40:43]\nv_mul_f32 v113, v113, v120\nv_mul_f32 v114, v114, v120\n" \
    "v_mfma_f32_16x16x32_bf16 v[32:35], v[216:219], v[72:75], v[32:35]\nv_mul_f32 v115, v115, v120\nv_mul_f32 v116, v116, v120\n" \
    "v_mfma_f32_16x16x32_bf16 v[44:47], v[216:219], v[76:79], v[44:47]\ns_waitcnt lgkmcnt(3)\nv_mul_f32 v117, v117, v120\nv_mul_f32 v118, v118, v120\n" \
    "v_mfma_f32_16x16x32_bf16 v[24:27], a[192:195], v[48:51], v[24:27]\nds_read_b128 v[64:67], v2 offset:50176\nv_mul_f32 v119, v119, v120\n" \
    "v_mfma_f32_16x16x32_bf16 v[36:39], a[192:195], v[52:55], v[36:39]\nds_read_b128 v[68:71], v2 offset:58368\nv_cvt_pk_bf16_f32 v16, v16, v17\n" \
    "v_mfma_f32_16x16x32_bf16 v[28:31], a[196:199], v[48:51], v[28:31]\ns_add_u32 m0, s52, 32768\ns_add_u32 s69, s70, 1024\nv_cvt_pk_bf16_f32 v17, v18, v19\n" \
    "v_mfma_f32_16x16x32_bf16 v[40:43], a[196:199], v[52:55], v[40:43]\nbuffer_load_dwordx4 v1, s[64:67], s69 offen offset:0 sc1 lds\nv_cvt_pk_bf16_f32 v112, v112, v113\n" \
    "v_mfma_f32_16x16x32_bf16 v[32:35], v[220:223], v[48:51], v[32:35]\nv_cvt_pk_bf16_f32 v113, v114, v115\nv_cvt_pk_bf16_f32 v114, v116, v117\n" \
    "v_mfma_f32_16x16x32_bf16 v[44:47], v[220:223], v[52:55], v[44:47]\ns_waitcnt lgkmcnt(2)\nv_cvt_pk_bf16_f32 v115, v118, v119\n" \
    "v_mfma_f32_16x16x32_bf16 v[24:27], a[200:203], v[56:59], v[24:27]\nds_read_b128 v[72:75], v3 offset:50176\n" \
    "v_mfma_f32_16x16x32_bf16 v[36:39], a[200:203], v[60:63], v[36:39]\nds_read_b128 v[76:79], v3 offset:58368\n" \
    "v_mfma_f32_16x16x32_bf16 v[28:31], a[204:207], v[56:59], v[28:31]\ns_add_u32 s69, s70, 128\nbuffer_load_dwordx4 v1, s[64:67], s69 offen offset:1024 sc1 lds\n" \
    "v_mfma_f32_16x16x32_bf16 v[40:43], a[204:207], v[60:63], v[40:43]\nbuffer_store_dwordx4 v[112:115], v0, s[64:67], s78 offen sc1\n" \
    "v_mfma_f32_16x16x32_bf16 v[32:35], v[224:227], v[56:59], v[32:35]\nbuffer_store_dwordx2 v[16:17], v10, s[64:67], s76 offen sc1\n" \
    "v_mfma_f32_16x16x32_bf16 v[44:47], v[224:227], v[60:63], v[44:47]\ns_waitcnt lgkmcnt(2)\nv_mov_b32 v21, 1\n" \
    "v_mfma_f32_16x16x32_bf16 v[24:27], a[208:211], v[64:67], v[24:27]\nds_read_b128 v[48:51], v2 offset:51200\n" \
    "v_mfma_f32_16x16x32_bf16 v[36:39], a[208:211], v[68:71], v[36:39]\nds_read_b128 v[52:55], v2 offset:59392\n" \
    "v_mfma_f32_16x16x32_bf16 v[28:31], a[212:215], v[64:67], v[28:31]\ns_add_u32 s69, s70, -768\nbuffer_load_dwordx4 v1, s[64:67], s69 offen offset:2048 sc1 lds\n" \
    "v_mfma_f32_16x16x32_bf16 v[40:43], a[212:215], v[68:71], v[40:43]\ns_mov_b64 exec, 1\nbuffer_atomic_add v21, off, s[64:67], s79 sc0\ns_mov_b64 exec, -1\n" \
    "v_mfma_f32_16x16x32_bf16 v[32:35], v[228:231], v[64:67], v[32:35]\nv_readfirstlane_b32 s72, v108\nv_readfirstlane_b32 s73, v109\n" \
    "v_mfma_f32_16x16x32_bf16 v[44:47], v[228:231], v[68:71], v[44:47]\ns_waitcnt lgkmcnt(2)\nv_readfirstlane_b32 s40, v110\n" \
    "v_mfma_f32_16x16x32_bf16 v[24:27], a[216:219], v[72:75], v[24:27]\nds_read_b128 v[56:59], v3 offset:51200\n" \
    "v_mfma_f32_16x16x32_bf16 v[36:39], a[216:219], v[76:79], v[36:39]\nds_read_b128 v[60:63], v3 offset:59392\ns_lshr_b32 s41, s72, 8\n" \
    "v_mfma_f32_16x16x32_bf16 v[28:31], a[220:223], v[72:75], v[28:31]\ns_add_u32 s69, s70, -1664\nbuffer_load_dwordx4 v1, s[64:67], s69 offen offset:3072 sc1 lds\n" \
    "v_mfma_f32_16x16x32_bf16 v[40:43], a[220:223], v[76:79], v[40:43]\nbuffer_load_dword v20, v6, s[64:67], 0 offen sc1\ns_sub_u32 s41, s40, s41\n" \
    "v_mfma_f32_16x16x32_bf16 v[32:35], v[232:235], v[72:75], v[32:35]\ns_lshl_b32 s41, s41, 8\ns_and_b32 s72, s72, 127\n" \
    "v_mfma_f32_16x16x32_bf16 v[44:47], v[232:235], v[76:79], v[44:47]\ns_waitcnt lgkmcnt(2)\ns_cmp_lt_i32 s41, s30\ns_subb_u32 s72, s72, 0\n" \
    "v_mfma_f32_16x16x32_bf16 v[24:27], a[224:227], v[48:51], v[24:27]\nds_read_b128 v[64:67], v2 offset:52224\ns_max_i32 s72, s72, 0\n" \
    "v_mfma_f32_16x16x32_bf16 v[36:39], a[224:227], v[52:55], v[36:39]\nds_read_b128 v[68:71], v2 offset:60416\n" \
    "v_mfma_f32_16x16x32_bf16 v[28:31], a[228:231], v[48:51], v[28:31]\nds_read_b128 v[72:75], v3 offset:52224\n" \
    "v_mfma_f32_16x16x32_bf16 v[40:43], a[228:231], v[52:55], v[40:43]\nds_read_b128 v[76:79], v3 offset:60416\n" \
    "v_mfma_f32_16x16x32_bf16 v[32:35], v[236:239], v[48:51], v[32:35]\ns_cmp_gt_i32 s72, s96\ns_cselect_b32 s41, s73, 0x80000000\n" \
    "v_mfma_f32_16x16x32_bf16 v[44:47], v[236:239], v[52:55], v[44:47]\ns_waitcnt lgkmcnt(4)\nv_mfma_f32_16x16x32_bf16 v[32:35], v[240:243], v[56:59], v[32:35]\n" \
    "v_add_u32 v122, s59, v11\nv_add_u32 v123, s59, v12\nv_mfma_f32_16x16x32_bf16 v[44:47], v[240:243], v[60:63], v[44:47]\ns_waitcnt lgkmcnt(2)\ns_add_u32 s59, s59, 0x2000\n" \
    "s_and_b32 s59, s59, 0xffff\nv_mfma_f32_16x16x32_bf16 v[32:35], v[244:247], v[64:67], v[32:35]\ns_waitcnt lgkmcnt(0)\ns_barrier\n" \
    "v_mfma_f32_16x16x32_bf16 v[44:47], v[244:247], v[68:71], v[44:47]\nds_read_b128 v[48:51], v2 offset:0\nv_mfma_f32_16x16x32_bf16 v[32:35], v[248:251], v[72:75], v[32:35]\n" \
    "ds_read_b128 v[52:55], v2 offset:8192\nv_mfma_f32_16x16x32_bf16 v[44:47], v[248:251], v[76:79], v[44:47]\ns_mov_b32 s54, s70\ns_add_u32 s50, s50, 1\n" \
    "v_mfma_f32_16x16x32_bf16 v[24:27], a[232:235], v[56:59], v[24:27]\ns_nop 1\nv_mfma_f32_16x16x32_bf16 v[24:27], a[240:243], v[64:67], v[24:27]\ns_nop 0\n" \
    "v_cvt_pk_bf16_f32 v80, v32, v33\nv_cvt_pk_bf16_f32 v81, v34, v35\nv_mfma_f32_16x16x32_bf16 v[24:27], a[248:251], v[72:75], v[24:27]\nv_cvt_pk_bf16_f32 v82, v44, v45\n" \
    "v_cvt_pk_bf16_f32 v83, v46, v47\nv_mfma_f32_16x16x32_bf16 v[28:31], a[236:239], v[56:59], v[28:31]\nv_permlane16_swap_b32 v80, v82\n" \
    "v_mfma_f32_16x16x32_bf16 v[28:31], a[244:247], v[64:67], v[28:31]\nds_read_b128 v[56:59], v3 offset:0\nv_mfma_f32_16x16x32_bf16 v[28:31], a[252:255], v[72:75], v[28:31]\n" \
    "v_permlane16_swap_b32 v81, v83\nv_mfma_f32_16x16x32_bf16 v[36:39], a[232:235], v[60:63], v[36:39]\nbuffer_store_dwordx4 v[80:83], v4, s[64:67], s71 offen sc1\n" \
    "v_mfma_f32_16x16x32_bf16 v[36:39], a[240:243], v[68:71], v[36:39]\nv_cvt_pk_bf16_f32 v84, v24, v25\nv_cvt_pk_bf16_f32 v85, v26, v27\n" \
    "v_mfma_f32_16x16x32_bf16 v[36:39], a[248:251], v[76:79], v[36:39]\ns_nop 0\nv_cvt_pk_bf16_f32 v86, v28, v29\nv_cvt_pk_bf16_f32 v87, v30, v31\n" \
    "v_mfma_f32_16x16x32_bf16 v[40:43], a[236:239], v[60:63], v[40:43]\nv_permlane16_swap_b32 v84, v86\nv_mfma_f32_16x16x32_bf16 v[40:43], a[244:247], v[68:71], v[40:43]\n" \
    "ds_read_b128 v[60:63], v3 offset:8192\nv_mfma_f32_16x16x32_bf16 v[40:43], a[252:255], v[76:79], v[40:43]\nv_permlane16_swap_b32 v85, v87\nds_write_b128 v122, v[84:87]\n" \
    "s_cmp_gt_i32 s41, s99\nv_cvt_pk_bf16_f32 v88, v36, v37\nv_cvt_pk_bf16_f32 v89, v38, v39\ns_nop 2\nv_cvt_pk_bf16_f32 v90, v40, v41\nv_cvt_pk_bf16_f32 v91, v42, v43\n" \
    "s_nop 0\nv_permlane16_swap_b32 v88, v90\nv_permlane16_swap_b32 v89, v91\nds_write_b128 v123, v[88:91]\ns_cbranch_scc1 .Lprqb1_%=\n.Lprqmid_%=:\n" \
    "s_cmp_eq_u32 s96, 0x7fffffff\ns_cbranch_scc1 .Lprqslow_%=\ns_waitcnt vmcnt(1)\nds_write_b32 v7, v20\nv_readfirstlane_b32 s42, v21\ns_cmp_eq_u32 s42, 63\n" \
    "s_cbranch_scc1 .Lprqtn_%=\n.Lprqtnb_%=:\ns_waitcnt lgkmcnt(0)\ns_barrier\nds_read_b64 v[96:97], v15\ns_waitcnt lgkmcnt(0)\nv_readfirstlane_b32 s78, v96\n" \
    "v_readfirstlane_b32 s73, v97\ns_and_b32 s72, s78, 127\ns_cmp_gt_i32 s51, s97\ns_cbranch_scc0 .Lprqslow_%=\ns_cmp_gt_i32 s72, s98\ns_cbranch_scc0 .Lprqslow_%=\n" \
    "s_cmp_gt_i32 s73, s99\ns_cbranch_scc0 .Lprqslow_%=\n.Lprqclk_%=:\ns_memrealtime s[40:41]\ns_mov_b32 s72, s78\ns_waitcnt lgkmcnt(0)\ns_lshr_b32 s41, s72, 8\n" \
    "s_sub_u32 s41, s40, s41\ns_lshl_b32 s41, s41, 8\ns_and_b32 s72, s72, 127\ns_cmp_lt_i32 s41, s31\ns_subb_u32 s72, s72, 0\ns_max_i32 s72, s72, 0\ns_cmp_gt_i32 s72, s98\n" \
    "s_cbranch_scc0 .Lprqclk_%=\ns_branch .Lprqgo_%=\n.Lprqslow_%=:\ns_waitcnt vmcnt(0) lgkmcnt(0)\nv_readfirstlane_b32 s42, v21\ns_cmp_eq_u32 s42, 63\n" \
    "s_cbranch_scc1 .Lprqtm_%=\n.Lprqtmb_%=:\ns_cmp_eq_u32 s50, 64\ns_cbranch_scc1 .Lprqdone_%=\ns_barrier\nv_mov_b32 v96, s50\n" \
    "buffer_store_dword v96, v5, s[64:67], 0 offen sc1\ns_cmp_gt_i32 s51, s97\ns_cbranch_scc0 .Lprqpoll_%=\ns_cmp_gt_i32 s72, s98\ns_cbranch_scc0 .Lprqpoll_%=\n" \
    "s_cmp_gt_i32 s73, s99\ns_cbranch_scc0 .Lprqpoll_%=\ns_branch .Lprqgo_%=\n.Lprqpoll_%=:\ns_mov_b32 s68, 0\ns_cmp_lg_u32 s55, 0\ns_cbranch_scc1 .Lprqjoin_%=\n.Lprqpl2_%=:\n" \
    "s_cmp_gt_i32 s51, s97\ns_cbranch_scc1 .Lprqpl3_%=\nv_lshlrev_b32 v96, 3, v0\nbuffer_load_dword v96, v96, s[64:67], s36 offen sc1\ns_waitcnt vmcnt(0)\n" \
    "v_cmp_gt_u32 vcc, 32, v96\ns_nop 4\ns_ff1_i32_b64 s51, vcc\ns_min_u32 s51, s51, 64\n.Lprqpl3_%=:\nbuffer_load_dword v96, v6, s[64:67], 0 offen sc1\ns_waitcnt vmcnt(0)\n" \
    "v_readlane_b32 s73, v96, 1\n.Lprqpl4_%=:\nv_readlane_b32 s72, v96, 0\ns_memrealtime s[70:71]\ns_and_b32 s78, s72, 127\ns_waitcnt lgkmcnt(0)\ns_mov_b32 s40, s70\n" \
    "s_lshr_b32 s41, s72, 8\ns_sub_u32 s41, s40, s41\ns_lshl_b32 s41, s41, 8\ns_and_b32 s72, s72, 127\ns_cmp_lt_i32 s41, s31\ns_subb_u32 s72, s72, 0\ns_max_i32 s72, s72, 0\n" \
    "s_cmp_gt_i32 s51, s97\ns_cbranch_scc0 .Lprqok_%=\ns_cmp_gt_i32 s72, s98\ns_cbranch_scc0 .Lprqok_%=\ns_cmp_gt_i32 s73, s99\ns_cbranch_scc0 .Lprqok_%=\nv_mov_b32 v96, s51\n" \
    "ds_write_b32 v7, v96\n.Lprqjoin_%=:\ns_waitcnt lgkmcnt(0)\ns_barrier\nds_read_b32 v96, v15\ns_waitcnt lgkmcnt(0)\nv_readfirstlane_b32 s51, v96\ns_branch .Lprqgo_%=\n" \
    ".Lprqok_%=:\ns_sleep 1\ns_cmp_gt_i32 s78, s98\ns_cselect_b32 s69, s73, s99\ns_cmp_gt_i32 s51, s97\ns_cselect_b32 s69, s69, s99\ns_cmp_gt_i32 s69, s99\n" \
    "s_cbranch_scc1 .Lprqpl4_%=\ns_sleep 5\ns_add_u32 s68, s68, 1\ns_and_b32 s43, s68, 63\ns_cbranch_scc1 .Lprqpl2_%=\ns_cmp_gt_u32 s68, 0x200000\ns_cbranch_scc1 .Lprqabort_%=\n" \
    "buffer_load_dword v97, off, s[64:67], 0 sc1\ns_waitcnt vmcnt(0)\nv_readfirstlane_b32 s43, v97\ns_cmp_eq_u32 s43, 1\ns_cbranch_scc0 .Lprqpl2_%=\ns_endpgm\n.Lprqtl_%=:\n" \
    "v_mov_b32 v21, 0\ns_cmp_lt_i32 s77, 0\ns_cbranch_scc1 .Lprqtlb_%=\ns_add_u32 s42, s77, 1\nv_mov_b32 v123, s42\ns_and_b32 s42, s77, 1\ns_lshl_b32 s42, s42, 2\n" \
    "buffer_atomic_umax v123, v14, s[64:67], s42 offen sc1\ns_branch .Lprqtlb_%=\n.Lprqkm_%=:\ns_cmp_lt_i32 s77, 0\ns_cbranch_scc1 .Lprqkmb_%=\ns_mov_b32 s42, 0\n.Lprqkm2_%=:\n" \
    "buffer_load_dwordx4 v[96:99], v9, s[64:67], s75 offen offset:0 sc1\nbuffer_load_dwordx4 v[100:103], v9, s[64:67], s75 offen offset:512 sc1\n" \
    "buffer_load_dwordx4 v[104:107], v9, s[64:67], s75 offen offset:1024 sc1\nbuffer_load_dwordx4 v[108:111], v9, s[64:67], s75 offen offset:1536 sc1\ns_waitcnt vmcnt(0)\n" \
    "v_max3_u32 v123, v96, v97, v98\nv_max3_u32 v123, v123, v99, v100\nv_max3_u32 v123, v123, v101, v102\nv_max3_u32 v123, v123, v103, v104\nv_max3_u32 v123, v123, v105, v106\n" \
    "v_max3_u32 v123, v123, v107, v108\nv_max3_u32 v123, v123, v109, v110\nv_max_u32 v123, v123, v111\nv_cmp_eq_u32 vcc, -1, v123\ns_cbranch_vccz .Lprqkmb_%=\n" \
    "s_add_u32 s42, s42, 1\ns_and_b32 s43, s42, 63\ns_cbranch_scc1 .Lprqkm2_%=\ns_cmp_gt_u32 s42, 0x200000\ns_cbranch_scc1 .Lprqabort_%=\n" \
    "buffer_load_dword v123, off, s[64:67], 0 sc1\ns_waitcnt vmcnt(0)\nv_readfirstlane_b32 s43, v123\ns_cmp_eq_u32 s43, 1\ns_cbranch_scc0 .Lprqkm2_%=\ns_endpgm\n.Lprqtn_%=:\n" \
    "v_mov_b32 v21, 0\ns_cmp_lt_i32 s77, 0\ns_cbranch_scc1 .Lprqtnb_%=\ns_add_u32 s42, s77, 1\nv_mov_b32 v123, s42\ns_and_b32 s42, s77, 1\ns_lshl_b32 s42, s42, 2\n" \
    "buffer_atomic_umax v123, v14, s[64:67], s42 offen sc1\ns_branch .Lprqtnb_%=\n.Lprqtm_%=:\nv_mov_b32 v21, 0\ns_cmp_lt_i32 s77, 0\ns_cbranch_scc1 .Lprqtmb_%=\n" \
    "s_add_u32 s42, s77, 1\nv_mov_b32 v123, s42\ns_and_b32 s42, s77, 1\ns_lshl_b32 s42, s42, 2\nbuffer_atomic_umax v123, v14, s[64:67], s42 offen sc1\ns_branch .Lprqtmb_%=\n" \
    ".Lprqabort_%=:\nv_mov_b32 v96, 1\nbuffer_store_dword v96, off, s[64:67], 0 sc1\ns_endpgm\n.Lprqdone_%=:\n"
#define PROJ_V \
    "ds_read_b128 v[48:51], v2 offset:0\nds_read_b128 v[52:55], v2 offset:8192\nds_read_b128 v[56:59], v3 offset:0\nds_read_b128 v[60:63], v3 offset:8192\ns_mov_b32 s72, 0\n" \
    "s_mov_b32 s73, 0\nv_mov_b32 v21, 0\ns_add_u32 s97, s50, 1\ns_min_u32 s97, s97, 63\ns_lshr_b32 s98, s50, s27\ns_add_u32 s98, s98, s28\ns_min_u32 s98, s98, s29\n" \
    "s_sub_u32 s98, s50, s98\ns_sub_u32 s99, s50, s17\ns_branch .Lprvslow_%=\n.Lprvgo_%=:\ns_waitcnt lgkmcnt(2)\nv_mfma_f32_16x16x32_bf16 v[24:27], a[0:3], v[48:51], 0\n" \
    "ds_read_b128 v[64:67], v2 offset:1024\nv_mfma_f32_16x16x32_bf16 v[36:39], a[0:3], v[52:55], 0\nds_read_b128 v[68:71], v2 offset:9216\n" \
    "v_mfma_f32_16x16x32_bf16 v[28:31], a[4:7], v[48:51], 0\ns_add_u32 m0, s52, 49152\ns_add_u32 s69, s54, 1536\ns_and_b32 s76, s50, s16\n" \
    "v_mfma_f32_16x16x32_bf16 v[40:43], a[4:7], v[52:55], 0\nbuffer_load_dwordx4 v1, s[64:67], s69 offen offset:0 sc1 lds\ns_lshl_b32 s76, s76, 20\n" \
    "v_mfma_f32_16x16x32_bf16 v[32:35], v[124:127], v[48:51], 0\ns_add_u32 s76, s76, s56\ns_add_u32 s74, s48, s76\ns_lshr_b32 s74, s74, 8\n" \
    "v_mfma_f32_16x16x32_bf16 v[44:47], v[124:127], v[52:55], 0\ns_waitcnt lgkmcnt(2)\ns_xor_b32 s74, s74, s49\ns_and_b32 s74, s74, 0x2000\n" \
    "v_mfma_f32_16x16x32_bf16 v[24:27], a[8:11], v[56:59], v[24:27]\nds_read_b128 v[72:75], v3 offset:1024\ns_add_u32 s74, s74, s76\n" \
    "v_mfma_f32_16x16x32_bf16 v[36:39], a[8:11], v[60:63], v[36:39]\nds_read_b128 v[76:79], v3 offset:9216\nv_mfma_f32_16x16x32_bf16 v[28:31], a[12:15], v[56:59], v[28:31]\n" \
    "s_add_u32 s68, s50, 1\ns_min_u32 s68, s68, 63\ns_lshl_b32 s68, s68, 20\nv_mfma_f32_16x16x32_bf16 v[40:43], a[12:15], v[60:63], v[40:43]\ns_add_u32 s68, s68, s18\n" \
    "s_add_u32 s70, s48, s68\ns_lshr_b32 s70, s70, 8\nv_mfma_f32_16x16x32_bf16 v[32:35], v[128:131], v[56:59], v[32:35]\ns_xnor_b32 s70, s70, s49\ns_and_b32 s70, s70, 0x2000\n" \
    "s_add_u32 s70, s70, s68\nv_mfma_f32_16x16x32_bf16 v[44:47], v[128:131], v[60:63], v[44:47]\ns_waitcnt lgkmcnt(2)\ns_add_u32 s69, s54, 640\n" \
    "v_mfma_f32_16x16x32_bf16 v[24:27], a[16:19], v[64:67], v[24:27]\nds_read_b128 v[48:51], v2 offset:2048\nv_mfma_f32_16x16x32_bf16 v[36:39], a[16:19], v[68:71], v[36:39]\n" \
    "ds_read_b128 v[52:55], v2 offset:10240\nv_mfma_f32_16x16x32_bf16 v[28:31], a[20:23], v[64:67], v[28:31]\nbuffer_load_dwordx4 v1, s[64:67], s69 offen offset:1024 sc1 lds\n" \
    "s_add_u32 s69, s54, -256\nv_mfma_f32_16x16x32_bf16 v[40:43], a[20:23], v[68:71], v[40:43]\nbuffer_load_dwordx4 v1, s[64:67], s69 offen offset:2048 sc1 lds\n" \
    "v_mfma_f32_16x16x32_bf16 v[32:35], v[132:135], v[64:67], v[32:35]\nbuffer_store_dwordx4 v[16:19], v8, s[64:67], s74 offen sc1\n" \
    "v_mfma_f32_16x16x32_bf16 v[44:47], v[132:135], v[68:71], v[44:47]\ns_waitcnt lgkmcnt(2)\nv_mfma_f32_16x16x32_bf16 v[24:27], a[24:27], v[72:75], v[24:27]\n" \
    "ds_read_b128 v[56:59], v3 offset:2048\nv_mfma_f32_16x16x32_bf16 v[36:39], a[24:27], v[76:79], v[36:39]\nds_read_b128 v[60:63], v3 offset:10240\n" \
    "v_mfma_f32_16x16x32_bf16 v[28:31], a[28:31], v[72:75], v[28:31]\ns_add_u32 s69, s54, -1152\nbuffer_load_dwordx4 v1, s[64:67], s69 offen offset:3072 sc1 lds\n" \
    "v_mfma_f32_16x16x32_bf16 v[40:43], a[28:31], v[76:79], v[40:43]\nv_mfma_f32_16x16x32_bf16 v[32:35], v[136:139], v[72:75], v[32:35]\n" \
    "v_mfma_f32_16x16x32_bf16 v[44:47], v[136:139], v[76:79], v[44:47]\ns_waitcnt lgkmcnt(2)\nv_mfma_f32_16x16x32_bf16 v[24:27], a[32:35], v[48:51], v[24:27]\n" \
    "ds_read_b128 v[64:67], v2 offset:3072\nv_mfma_f32_16x16x32_bf16 v[36:39], a[32:35], v[52:55], v[36:39]\nds_read_b128 v[68:71], v2 offset:11264\n" \
    "v_mfma_f32_16x16x32_bf16 v[28:31], a[36:39], v[48:51], v[28:31]\ns_and_b32 s37, s50, s16\ns_lshl_b32 s37, s37, 19\ns_add_u32 s37, s37, s53\n" \
    "v_mfma_f32_16x16x32_bf16 v[40:43], a[36:39], v[52:55], v[40:43]\ns_add_u32 s71, s48, s37\ns_lshr_b32 s71, s71, 8\ns_xnor_b32 s71, s71, s49\n" \
    "v_mfma_f32_16x16x32_bf16 v[32:35], v[140:143], v[48:51], v[32:35]\ns_and_b32 s71, s71, 0x2000\ns_add_u32 s71, s71, s37\n" \
    "v_mfma_f32_16x16x32_bf16 v[44:47], v[140:143], v[52:55], v[44:47]\ns_waitcnt lgkmcnt(2)\nv_mfma_f32_16x16x32_bf16 v[24:27], a[40:43], v[56:59], v[24:27]\n" \
    "ds_read_b128 v[72:75], v3 offset:3072\nv_mfma_f32_16x16x32_bf16 v[36:39], a[40:43], v[60:63], v[36:39]\nds_read_b128 v[76:79], v3 offset:11264\ns_add_u32 s75, s74, 4096\n" \
    "v_mfma_f32_16x16x32_bf16 v[28:31], a[44:47], v[56:59], v[28:31]\nbuffer_store_dwordx4 v[16:19], v8, s[64:67], s75 offen sc1\n" \
    "v_mfma_f32_16x16x32_bf16 v[40:43], a[44:47], v[60:63], v[40:43]\nv_mfma_f32_16x16x32_bf16 v[32:35], v[144:147], v[56:59], v[32:35]\n" \
    "v_mfma_f32_16x16x32_bf16 v[44:47], v[144:147], v[60:63], v[44:47]\ns_waitcnt vmcnt(17) lgkmcnt(0)\ns_barrier\n" \
    "v_mfma_f32_16x16x32_bf16 v[24:27], a[48:51], v[64:67], v[24:27]\nds_read_b128 v[48:51], v2 offset:16384\nv_mfma_f32_16x16x32_bf16 v[36:39], a[48:51], v[68:71], v[36:39]\n" \
    "ds_read_b128 v[52:55], v2 offset:24576\nv_mfma_f32_16x16x32_bf16 v[28:31], a[52:55], v[64:67], v[28:31]\ns_add_u32 s39, s50, 1\ns_add_u32 s97, s39, 1\n" \
    "s_min_u32 s97, s97, 63\nv_mfma_f32_16x16x32_bf16 v[40:43], a[52:55], v[68:71], v[40:43]\ns_lshr_b32 s98, s39, s27\ns_add_u32 s98, s98, s28\ns_min_u32 s98, s98, s29\n" \
    "v_mfma_f32_16x16x32_bf16 v[32:35], v[148:151], v[64:67], v[32:35]\ns_sub_u32 s98, s39, s98\ns_sub_u32 s99, s39, s17\n" \
    "v_mfma_f32_16x16x32_bf16 v[44:47], v[148:151], v[68:71], v[44:47]\ns_cmp_gt_i32 s51, s97\ns_cselect_b32 s96, s98, 0x7fffffff\ns_sub_u32 s39, s39, 4\n" \
    "v_mfma_f32_16x16x32_bf16 v[24:27], a[56:59], v[72:75], v[24:27]\nds_read_b128 v[56:59], v3 offset:16384\nv_mfma_f32_16x16x32_bf16 v[36:39], a[56:59], v[76:79], v[36:39]\n" \
    "ds_read_b128 v[60:63], v3 offset:24576\nv_mfma_f32_16x16x32_bf16 v[28:31], a[60:63], v[72:75], v[28:31]\ns_cmp_lt_u32 s39, 60\ns_cselect_b32 s96, s96, 0x7fffffff\n" \
    "v_mfma_f32_16x16x32_bf16 v[40:43], a[60:63], v[76:79], v[40:43]\nv_mfma_f32_16x16x32_bf16 v[32:35], v[152:155], v[72:75], v[32:35]\ns_add_u32 s75, s74, 16384\n" \
    "v_mfma_f32_16x16x32_bf16 v[44:47], v[152:155], v[76:79], v[44:47]\ns_waitcnt lgkmcnt(2)\nv_mfma_f32_16x16x32_bf16 v[24:27], a[64:67], v[48:51], v[24:27]\n" \
    "ds_read_b128 v[64:67], v2 offset:17408\nv_mfma_f32_16x16x32_bf16 v[36:39], a[64:67], v[52:55], v[36:39]\nds_read_b128 v[68:71], v2 offset:25600\n" \
    "v_mfma_f32_16x16x32_bf16 v[28:31], a[68:71], v[48:51], v[28:31]\ns_add_u32 m0, s52, 0\ns_add_u32 s69, s70, 0\n" \
    "v_mfma_f32_16x16x32_bf16 v[40:43], a[68:71], v[52:55], v[40:43]\nbuffer_load_dwordx4 v1, s[64:67], s69 offen offset:0 sc1 lds\n" \
    "v_mfma_f32_16x16x32_bf16 v[32:35], v[156:159], v[48:51], v[32:35]\nbuffer_store_dwordx4 v[16:19], v8, s[64:67], s75 offen sc1\n" \
    "v_mfma_f32_16x16x32_bf16 v[44:47], v[156:159], v[52:55], v[44:47]\ns_waitcnt lgkmcnt(2)\nv_mfma_f32_16x16x32_bf16 v[24:27], a[72:75], v[56:59], v[24:27]\n" \
    "ds_read_b128 v[72:75], v3 offset:17408\nv_mfma_f32_16x16x32_bf16 v[36:39], a[72:75], v[60:63], v[36:39]\nds_read_b128 v[76:79], v3 offset:25600\n" \
    "v_mfma_f32_16x16x32_bf16 v[28:31], a[76:79], v[56:59], v[28:31]\ns_add_u32 s69, s70, -896\nbuffer_load_dwordx4 v1, s[64:67], s69 offen offset:1024 sc1 lds\n" \
    "v_mfma_f32_16x16x32_bf16 v[40:43], a[76:79], v[60:63], v[40:43]\nv_mfma_f32_16x16x32_bf16 v[32:35], v[160:163], v[56:59], v[32:35]\n" \
    "v_mfma_f32_16x16x32_bf16 v[44:47], v[160:163], v[60:63], v[44:47]\ns_waitcnt lgkmcnt(2)\nv_mfma_f32_16x16x32_bf16 v[24:27], a[80:83], v[64:67], v[24:27]\n" \
    "ds_read_b128 v[48:51], v2 offset:18432\nv_mfma_f32_16x16x32_bf16 v[36:39], a[80:83], v[68:71], v[36:39]\nds_read_b128 v[52:55], v2 offset:26624\ns_add_u32 s75, s74, 20480\n" \
    "v_mfma_f32_16x16x32_bf16 v[28:31], a[84:87], v[64:67], v[28:31]\ns_add_u32 s69, s70, -1792\nbuffer_load_dwordx4 v1, s[64:67], s69 offen offset:2048 sc1 lds\n" \
    "v_mfma_f32_16x16x32_bf16 v[40:43], a[84:87], v[68:71], v[40:43]\nv_xor_b32 v6, v15, v6\nv_mfma_f32_16x16x32_bf16 v[32:35], v[164:167], v[64:67], v[32:35]\n" \
    "buffer_store_dwordx4 v[16:19], v8, s[64:67], s75 offen sc1\nv_mfma_f32_16x16x32_bf16 v[44:47], v[164:167], v[68:71], v[44:47]\ns_waitcnt lgkmcnt(2)\n" \
    "s_sub_u32 s74, s54, s19\ns_xor_b32 s74, s74, s57\nv_mfma_f32_16x16x32_bf16 v[24:27], a[88:91], v[72:75], v[24:27]\nds_read_b128 v[56:59], v3 offset:18432\n" \
    "v_mfma_f32_16x16x32_bf16 v[36:39], a[88:91], v[76:79], v[36:39]\nds_read_b128 v[60:63], v3 offset:26624\nv_mfma_f32_16x16x32_bf16 v[28:31], a[92:95], v[72:75], v[28:31]\n" \
    "s_add_u32 s69, s70, -2688\nbuffer_load_dwordx4 v1, s[64:67], s69 offen offset:3072 sc1 lds\nv_mfma_f32_16x16x32_bf16 v[40:43], a[92:95], v[76:79], v[40:43]\n" \
    "v_mfma_f32_16x16x32_bf16 v[32:35], v[168:171], v[72:75], v[32:35]\nv_mfma_f32_16x16x32_bf16 v[44:47], v[168:171], v[76:79], v[44:47]\ns_waitcnt lgkmcnt(2)\n" \
    "v_mfma_f32_16x16x32_bf16 v[24:27], a[96:99], v[48:51], v[24:27]\nds_read_b128 v[64:67], v2 offset:19456\nv_mfma_f32_16x16x32_bf16 v[36:39], a[96:99], v[52:55], v[36:39]\n" \
    "ds_read_b128 v[68:71], v2 offset:27648\nv_mfma_f32_16x16x32_bf16 v[28:31], a[100:103], v[48:51], v[28:31]\nbuffer_load_dword v20, v6, s[64:67], 0 offen sc1\n" \
    "v_mfma_f32_16x16x32_bf16 v[40:43], a[100:103], v[52:55], v[40:43]\nv_mfma_f32_16x16x32_bf16 v[32:35], v[172:175], v[48:51], v[32:35]\n" \
    "buffer_store_dwordx4 v[16:19], v8, s[64:67], s74 offen sc1\nv_mfma_f32_16x16x32_bf16 v[44:47], v[172:175], v[52:55], v[44:47]\ns_waitcnt lgkmcnt(2)\n" \
    "v_mfma_f32_16x16x32_bf16 v[24:27], a[104:107], v[56:59], v[24:27]\nds_read_b128 v[72:75], v3 offset:19456\n" \
    "v_mfma_f32_16x16x32_bf16 v[36:39], a[104:107], v[60:63], v[36:39]\nds_read_b128 v[76:79], v3 offset:27648\n" \
    "v_mfma_f32_16x16x32_bf16 v[28:31], a[108:111], v[56:59], v[28:31]\nv_mfma_f32_16x16x32_bf16 v[40:43], a[108:111], v[60:63], v[40:43]\n" \
    "v_mfma_f32_16x16x32_bf16 v[32:35], v[176:179], v[56:59], v[32:35]\nv_mfma_f32_16x16x32_bf16 v[44:47], v[176:179], v[60:63], v[44:47]\ns_waitcnt vmcnt(14) lgkmcnt(0)\n" \
    "s_barrier\nv_mfma_f32_16x16x32_bf16 v[24:27], a[112:115], v[64:67], v[24:27]\nds_read_b128 v[48:51], v2 offset:32768\nv_mov_b32 v123, s50\n" \
    "v_mfma_f32_16x16x32_bf16 v[36:39], a[112:115], v[68:71], v[36:39]\nds_read_b128 v[52:55], v2 offset:40960\n" \
    "v_mfma_f32_16x16x32_bf16 v[28:31], a[116:119], v[64:67], v[28:31]\nbuffer_store_dword v123, v5, s[64:67], 0 offen sc1\n" \
    "v_mfma_f32_16x16x32_bf16 v[40:43], a[116:119], v[68:71], v[40:43]\nv_mfma_f32_16x16x32_bf16 v[32:35], v[180:183], v[64:67], v[32:35]\n" \
    "v_mfma_f32_16x16x32_bf16 v[44:47], v[180:183], v[68:71], v[44:47]\nv_mfma_f32_16x16x32_bf16 v[24:27], a[120:123], v[72:75], v[24:27]\n" \
    "ds_read_b128 v[56:59], v3 offset:32768\nv_mfma_f32_16x16x32_bf16 v[36:39], a[120:123], v[76:79], v[36:39]\nds_read_b128 v[60:63], v3 offset:40960\n" \
    "s_add_u32 s75, s74, 4096\nv_mfma_f32_16x16x32_bf16 v[28:31], a[124:127], v[72:75], v[28:31]\nbuffer_store_dwordx4 v[16:19], v8, s[64:67], s75 offen sc1\n" \
    "v_mfma_f32_16x16x32_bf16 v[40:43], a[124:127], v[76:79], v[40:43]\nv_mfma_f32_16x16x32_bf16 v[32:35], v[184:187], v[72:75], v[32:35]\n" \
    "v_mfma_f32_16x16x32_bf16 v[44:47], v[184:187], v[76:79], v[44:47]\ns_waitcnt lgkmcnt(2)\nv_mfma_f32_16x16x32_bf16 v[24:27], a[128:131], v[48:51], v[24:27]\n" \
    "ds_read_b128 v[64:67], v2 offset:33792\nv_mfma_f32_16x16x32_bf16 v[36:39], a[128:131], v[52:55], v[36:39]\nds_read_b128 v[68:71], v2 offset:41984\n" \
    "v_mfma_f32_16x16x32_bf16 v[28:31], a[132:135], v[48:51], v[28:31]\ns_add_u32 m0, s52, 16384\ns_add_u32 s69, s70, 512\n" \
    "v_mfma_f32_16x16x32_bf16 v[40:43], a[132:135], v[52:55], v[40:43]\nbuffer_load_dwordx4 v1, s[64:67], s69 offen offset:0 sc1 lds\n" \
    "v_mfma_f32_16x16x32_bf16 v[32:35], v[188:191], v[48:51], v[32:35]\nv_mfma_f32_16x16x32_bf16 v[44:47], v[188:191], v[52:55], v[44:47]\ns_waitcnt lgkmcnt(2)\n" \
    "v_mfma_f32_16x16x32_bf16 v[24:27], a[136:139], v[56:59], v[24:27]\nds_read_b128 v[72:75], v3 offset:33792\n" \
    "v_mfma_f32_16x16x32_bf16 v[36:39], a[136:139], v[60:63], v[36:39]\nds_read_b128 v[76:79], v3 offset:41984\n" \
    "v_mfma_f32_16x16x32_bf16 v[28:31], a[140:143], v[56:59], v[28:31]\ns_add_u32 s69, s70, -384\nbuffer_load_dwordx4 v1, s[64:67], s69 offen offset:1024 sc1 lds\n" \
    "v_mfma_f32_16x16x32_bf16 v[40:43], a[140:143], v[60:63], v[40:43]\nv_mfma_f32_16x16x32_bf16 v[32:35], v[192:195], v[56:59], v[32:35]\ns_add_u32 s75, s74, 16384\n" \
    "v_mfma_f32_16x16x32_bf16 v[44:47], v[192:195], v[60:63], v[44:47]\ns_waitcnt lgkmcnt(2)\nv_mfma_f32_16x16x32_bf16 v[24:27], a[144:147], v[64:67], v[24:27]\n" \
    "ds_read_b128 v[48:51], v2 offset:34816\nv_mfma_f32_16x16x32_bf16 v[36:39], a[144:147], v[68:71], v[36:39]\nds_read_b128 v[52:55], v2 offset:43008\n" \
    "v_mfma_f32_16x16x32_bf16 v[28:31], a[148:151], v[64:67], v[28:31]\ns_add_u32 s69, s70, -1280\nbuffer_load_dwordx4 v1, s[64:67], s69 offen offset:2048 sc1 lds\n" \
    "v_mfma_f32_16x16x32_bf16 v[40:43], a[148:151], v[68:71], v[40:43]\nbuffer_store_dwordx4 v[16:19], v8, s[64:67], s75 offen sc1\n" \
    "v_mfma_f32_16x16x32_bf16 v[32:35], v[196:199], v[64:67], v[32:35]\nv_mfma_f32_16x16x32_bf16 v[44:47], v[196:199], v[68:71], v[44:47]\ns_waitcnt lgkmcnt(2)\n" \
    "v_mfma_f32_16x16x32_bf16 v[24:27], a[152:155], v[72:75], v[24:27]\nds_read_b128 v[56:59], v3 offset:34816\n" \
    "v_mfma_f32_16x16x32_bf16 v[36:39], a[152:155], v[76:79], v[36:39]\nds_read_b128 v[60:63], v3 offset:43008\n" \
    "v_mfma_f32_16x16x32_bf16 v[28:31], a[156:159], v[72:75], v[28:31]\ns_add_u32 s69, s70, -2176\nbuffer_load_dwordx4 v1, s[64:67], s69 offen offset:3072 sc1 lds\n" \
    "v_mfma_f32_16x16x32_bf16 v[40:43], a[156:159], v[76:79], v[40:43]\nv_mfma_f32_16x16x32_bf16 v[32:35], v[200:203], v[72:75], v[32:35]\n" \
    "v_mfma_f32_16x16x32_bf16 v[44:47], v[200:203], v[76:79], v[44:47]\ns_waitcnt lgkmcnt(2)\nv_mfma_f32_16x16x32_bf16 v[24:27], a[160:163], v[48:51], v[24:27]\n" \
    "ds_read_b128 v[64:67], v2 offset:35840\nv_mfma_f32_16x16x32_bf16 v[36:39], a[160:163], v[52:55], v[36:39]\nds_read_b128 v[68:71], v2 offset:44032\n" \
    "s_add_u32 s75, s74, 20480\nv_mfma_f32_16x16x32_bf16 v[28:31], a[164:167], v[48:51], v[28:31]\ns_waitcnt vmcnt(8)\nds_write_b32 v7, v20\n" \
    "v_mfma_f32_16x16x32_bf16 v[40:43], a[164:167], v[52:55], v[40:43]\nbuffer_store_dwordx4 v[16:19], v8, s[64:67], s75 offen sc1\n" \
    "v_mfma_f32_16x16x32_bf16 v[32:35], v[204:207], v[48:51], v[32:35]\ns_and_b32 s76, s50, s16\ns_lshl_b32 s76, s76, 19\ns_add_u32 s76, s76, s20\n" \
    "v_mfma_f32_16x16x32_bf16 v[44:47], v[204:207], v[52:55], v[44:47]\ns_waitcnt lgkmcnt(3)\ns_add_u32 s74, s48, s76\ns_lshr_b32 s74, s74, 8\n" \
    "v_mfma_f32_16x16x32_bf16 v[24:27], a[168:171], v[56:59], v[24:27]\nds_read_b128 v[72:75], v3 offset:35840\ns_xor_b32 s74, s74, s49\n" \
    "v_mfma_f32_16x16x32_bf16 v[36:39], a[168:171], v[60:63], v[36:39]\nds_read_b128 v[76:79], v3 offset:44032\ns_and_b32 s74, s74, 0x2000\n" \
    "v_mfma_f32_16x16x32_bf16 v[28:31], a[172:175], v[56:59], v[28:31]\ns_add_u32 s74, s74, s76\ns_xor_b32 s74, s74, s58\n" \
    "v_mfma_f32_16x16x32_bf16 v[40:43], a[172:175], v[60:63], v[40:43]\nv_mfma_f32_16x16x32_bf16 v[32:35], v[208:211], v[56:59], v[32:35]\n" \
    "v_mfma_f32_16x16x32_bf16 v[44:47], v[208:211], v[60:63], v[44:47]\ns_waitcnt lgkmcnt(0)\ns_barrier\nv_mfma_f32_16x16x32_bf16 v[24:27], a[176:179], v[64:67], v[24:27]\n" \
    "ds_read_b128 v[48:51], v2 offset:49152\nv_mfma_f32_16x16x32_bf16 v[36:39], a[176:179], v[68:71], v[36:39]\nds_read_b128 v[52:55], v2 offset:57344\n" \
    "v_mfma_f32_16x16x32_bf16 v[28:31], a[180:183], v[64:67], v[28:31]\nds_read_b96 v[108:110], v14\nv_mfma_f32_16x16x32_bf16 v[40:43], a[180:183], v[68:71], v[40:43]\n" \
    "v_mfma_f32_16x16x32_bf16 v[32:35], v[212:215], v[64:67], v[32:35]\nbuffer_store_dwordx4 v[16:19], v8, s[64:67], s74 offen sc1\n" \
    "v_mfma_f32_16x16x32_bf16 v[44:47], v[212:215], v[68:71], v[44:47]\nv_mfma_f32_16x16x32_bf16 v[24:27], a[184:187], v[72:75], v[24:27]\n" \
    "ds_read_b128 v[56:59], v3 offset:49152\nv_mfma_f32_16x16x32_bf16 v[36:39], a[184:187], v[76:79], v[36:39]\nds_read_b128 v[60:63], v3 offset:57344\n" \
    "v_mfma_f32_16x16x32_bf16 v[28:31], a[188:191], v[72:75], v[28:31]\ns_waitcnt lgkmcnt(2)\nv_readfirstlane_b32 s72, v108\nv_readfirstlane_b32 s73, v109\n" \
    "v_mfma_f32_16x16x32_bf16 v[40:43], a[188:191], v[76:79], v[40:43]\nv_mfma_f32_16x16x32_bf16 v[32:35], v[216:219], v[72:75], v[32:35]\ns_and_b32 s72, s72, 127\n" \
    "s_cmp_gt_i32 s72, s96\ns_cselect_b32 s41, s73, 0x80000000\nv_mfma_f32_16x16x32_bf16 v[44:47], v[216:219], v[76:79], v[44:47]\n" \
    "v_mfma_f32_16x16x32_bf16 v[24:27], a[192:195], v[48:51], v[24:27]\nds_read_b128 v[64:67], v2 offset:50176\n" \
    "v_mfma_f32_16x16x32_bf16 v[36:39], a[192:195], v[52:55], v[36:39]\nds_read_b128 v[68:71], v2 offset:58368\n" \
    "v_mfma_f32_16x16x32_bf16 v[28:31], a[196:199], v[48:51], v[28:31]\ns_add_u32 m0, s52, 32768\ns_add_u32 s69, s70, 1024\n" \
    "v_mfma_f32_16x16x32_bf16 v[40:43], a[196:199], v[52:55], v[40:43]\nbuffer_load_dwordx4 v1, s[64:67], s69 offen offset:0 sc1 lds\n" \
    "v_mfma_f32_16x16x32_bf16 v[32:35], v[220:223], v[48:51], v[32:35]\nv_mfma_f32_16x16x32_bf16 v[44:47], v[220:223], v[52:55], v[44:47]\ns_waitcnt lgkmcnt(2)\n" \
    "v_mfma_f32_16x16x32_bf16 v[24:27], a[200:203], v[56:59], v[24:27]\nds_read_b128 v[72:75], v3 offset:50176\n" \
    "v_mfma_f32_16x16x32_bf16 v[36:39], a[200:203], v[60:63], v[36:39]\nds_read_b128 v[76:79], v3 offset:58368\ns_add_u32 s75, s74, 4096\n" \
    "v_mfma_f32_16x16x32_bf16 v[28:31], a[204:207], v[56:59], v[28:31]\ns_add_u32 s69, s70, 128\nbuffer_load_dwordx4 v1, s[64:67], s69 offen offset:1024 sc1 lds\n" \
    "v_mfma_f32_16x16x32_bf16 v[40:43], a[204:207], v[60:63], v[40:43]\nbuffer_store_dwordx4 v[16:19], v8, s[64:67], s75 offen sc1\n" \
    "v_mfma_f32_16x16x32_bf16 v[32:35], v[224:227], v[56:59], v[32:35]\ns_add_u32 s76, s50, 1\ns_and_b32 s76, s76, s16\ns_lshl_b32 s76, s76, 18\n" \
    "v_mfma_f32_16x16x32_bf16 v[44:47], v[224:227], v[60:63], v[44:47]\ns_waitcnt lgkmcnt(2)\ns_add_u32 s76, s76, s59\ns_add_u32 s77, s48, s76\n" \
    "v_mfma_f32_16x16x32_bf16 v[24:27], a[208:211], v[64:67], v[24:27]\nds_read_b128 v[48:51], v2 offset:51200\ns_lshr_b32 s77, s77, 8\n" \
    "v_mfma_f32_16x16x32_bf16 v[36:39], a[208:211], v[68:71], v[36:39]\nds_read_b128 v[52:55], v2 offset:59392\ns_xor_b32 s77, s77, s49\n" \
    "v_mfma_f32_16x16x32_bf16 v[28:31], a[212:215], v[64:67], v[28:31]\ns_add_u32 s69, s70, -768\nbuffer_load_dwordx4 v1, s[64:67], s69 offen offset:2048 sc1 lds\n" \
    "v_mfma_f32_16x16x32_bf16 v[40:43], a[212:215], v[68:71], v[40:43]\ns_and_b32 s77, s77, 0x2000\ns_add_u32 s77, s77, s76\ns_add_u32 s77, s77, s60\n" \
    "v_mfma_f32_16x16x32_bf16 v[32:35], v[228:231], v[64:67], v[32:35]\ns_cmp_lt_u32 s50, 63\ns_cselect_b32 s74, s77, s74\n" \
    "v_mfma_f32_16x16x32_bf16 v[44:47], v[228:231], v[68:71], v[44:47]\ns_waitcnt lgkmcnt(2)\nv_mfma_f32_16x16x32_bf16 v[24:27], a[216:219], v[72:75], v[24:27]\n" \
    "ds_read_b128 v[56:59], v3 offset:51200\nv_mfma_f32_16x16x32_bf16 v[36:39], a[216:219], v[76:79], v[36:39]\nds_read_b128 v[60:63], v3 offset:59392\n" \
    "v_mfma_f32_16x16x32_bf16 v[28:31], a[220:223], v[72:75], v[28:31]\ns_add_u32 s69, s70, -1664\nbuffer_load_dwordx4 v1, s[64:67], s69 offen offset:3072 sc1 lds\n" \
    "v_mfma_f32_16x16x32_bf16 v[40:43], a[220:223], v[76:79], v[40:43]\nv_mfma_f32_16x16x32_bf16 v[32:35], v[232:235], v[72:75], v[32:35]\n" \
    "buffer_store_dwordx4 v[16:19], v8, s[64:67], s74 offen sc1\nv_mfma_f32_16x16x32_bf16 v[44:47], v[232:235], v[76:79], v[44:47]\ns_waitcnt lgkmcnt(2)\n" \
    "v_mfma_f32_16x16x32_bf16 v[24:27], a[224:227], v[48:51], v[24:27]\nds_read_b128 v[64:67], v2 offset:52224\n" \
    "v_mfma_f32_16x16x32_bf16 v[36:39], a[224:227], v[52:55], v[36:39]\nds_read_b128 v[68:71], v2 offset:60416\n" \
    "v_mfma_f32_16x16x32_bf16 v[28:31], a[228:231], v[48:51], v[28:31]\nds_read_b128 v[72:75], v3 offset:52224\n" \
    "v_mfma_f32_16x16x32_bf16 v[40:43], a[228:231], v[52:55], v[40:43]\nds_read_b128 v[76:79], v3 offset:60416\n" \
    "v_mfma_f32_16x16x32_bf16 v[32:35], v[236:239], v[48:51], v[32:35]\nv_mfma_f32_16x16x32_bf16 v[44:47], v[236:239], v[52:55], v[44:47]\ns_waitcnt lgkmcnt(4)\n" \
    "v_mfma_f32_16x16x32_bf16 v[32:35], v[240:243], v[56:59], v[32:35]\ns_xor_b32 s75, s71, 0x2000\ns_add_u32 s76, s75, 0x1000\n" \
    "v_mfma_f32_16x16x32_bf16 v[44:47], v[240:243], v[60:63], v[44:47]\ns_waitcnt lgkmcnt(2)\nv_mfma_f32_16x16x32_bf16 v[32:35], v[244:247], v[64:67], v[32:35]\n" \
    "s_waitcnt lgkmcnt(0)\ns_barrier\nv_mfma_f32_16x16x32_bf16 v[44:47], v[244:247], v[68:71], v[44:47]\nds_read_b128 v[48:51], v2 offset:0\n" \
    "v_mfma_f32_16x16x32_bf16 v[32:35], v[248:251], v[72:75], v[32:35]\nds_read_b128 v[52:55], v2 offset:8192\n" \
    "v_mfma_f32_16x16x32_bf16 v[44:47], v[248:251], v[76:79], v[44:47]\ns_mov_b32 s54, s70\ns_add_u32 s50, s50, 1\n" \
    "v_mfma_f32_16x16x32_bf16 v[24:27], a[232:235], v[56:59], v[24:27]\ns_nop 1\nv_mfma_f32_16x16x32_bf16 v[24:27], a[240:243], v[64:67], v[24:27]\ns_nop 0\n" \
    "v_cvt_pk_bf16_f32 v80, v32, v33\nv_cvt_pk_bf16_f32 v81, v34, v35\nv_mfma_f32_16x16x32_bf16 v[24:27], a[248:251], v[72:75], v[24:27]\nv_cvt_pk_bf16_f32 v82, v44, v45\n" \
    "v_cvt_pk_bf16_f32 v83, v46, v47\nv_mfma_f32_16x16x32_bf16 v[28:31], a[236:239], v[56:59], v[28:31]\nv_permlane16_swap_b32 v80, v82\n" \
    "v_mfma_f32_16x16x32_bf16 v[28:31], a[244:247], v[64:67], v[28:31]\nds_read_b128 v[56:59], v3 offset:0\nv_mfma_f32_16x16x32_bf16 v[28:31], a[252:255], v[72:75], v[28:31]\n" \
    "v_permlane16_swap_b32 v81, v83\nv_mfma_f32_16x16x32_bf16 v[36:39], a[232:235], v[60:63], v[36:39]\nbuffer_store_dwordx4 v[80:83], v4, s[64:67], s71 offen sc1\n" \
    "v_mfma_f32_16x16x32_bf16 v[36:39], a[240:243], v[68:71], v[36:39]\nv_cvt_pk_bf16_f32 v84, v24, v25\nv_cvt_pk_bf16_f32 v85, v26, v27\n" \
    "v_mfma_f32_16x16x32_bf16 v[36:39], a[248:251], v[76:79], v[36:39]\ns_nop 0\nv_cvt_pk_bf16_f32 v86, v28, v29\nv_cvt_pk_bf16_f32 v87, v30, v31\n" \
    "v_mfma_f32_16x16x32_bf16 v[40:43], a[236:239], v[60:63], v[40:43]\nbuffer_store_dwordx4 v[84:87], v8, s[64:67], s75 offen sc1\n" \
    "v_mfma_f32_16x16x32_bf16 v[40:43], a[244:247], v[68:71], v[40:43]\nds_read_b128 v[60:63], v3 offset:8192\n" \
    "v_mfma_f32_16x16x32_bf16 v[40:43], a[252:255], v[76:79], v[40:43]\ns_cmp_gt_i32 s41, s99\nv_cvt_pk_bf16_f32 v88, v36, v37\nv_cvt_pk_bf16_f32 v89, v38, v39\ns_nop 4\n" \
    "v_cvt_pk_bf16_f32 v90, v40, v41\nv_cvt_pk_bf16_f32 v91, v42, v43\nbuffer_store_dwordx4 v[88:91], v8, s[64:67], s76 offen sc1\ns_cbranch_scc1 .Lprvgo_%=\n.Lprvslow_%=:\n" \
    "s_waitcnt vmcnt(0) lgkmcnt(0)\ns_cmp_eq_u32 s50, 64\ns_cbranch_scc1 .Lprvdone_%=\ns_barrier\nv_mov_b32 v96, s50\nbuffer_store_dword v96, v5, s[64:67], 0 offen sc1\n" \
    "s_cmp_gt_i32 s51, s97\ns_cbranch_scc0 .Lprvpoll_%=\ns_cmp_gt_i32 s72, s98\ns_cbranch_scc0 .Lprvpoll_%=\ns_cmp_gt_i32 s73, s99\ns_cbranch_scc0 .Lprvpoll_%=\n" \
    "s_branch .Lprvgo_%=\n.Lprvpoll_%=:\ns_mov_b32 s68, 0\ns_cmp_lg_u32 s55, 0\ns_cbranch_scc1 .Lprvjoin_%=\n.Lprvpl2_%=:\ns_cmp_gt_i32 s51, s97\ns_cbranch_scc1 .Lprvpl3_%=\n" \
    "v_lshlrev_b32 v96, 3, v0\nbuffer_load_dword v96, v96, s[64:67], s36 offen sc1\ns_waitcnt vmcnt(0)\nv_cmp_gt_u32 vcc, 32, v96\ns_nop 4\ns_ff1_i32_b64 s51, vcc\n" \
    "s_min_u32 s51, s51, 64\n.Lprvpl3_%=:\nbuffer_load_dword v96, v6, s[64:67], 0 offen sc1\ns_waitcnt vmcnt(0)\nv_readlane_b32 s73, v96, 1\n.Lprvpl4_%=:\n" \
    "v_readlane_b32 s72, v96, 0\ns_and_b32 s72, s72, 127\ns_cmp_gt_i32 s51, s97\ns_cbranch_scc0 .Lprvok_%=\ns_cmp_gt_i32 s72, s98\ns_cbranch_scc0 .Lprvok_%=\n" \
    "s_cmp_gt_i32 s73, s99\ns_cbranch_scc0 .Lprvok_%=\nv_mov_b32 v96, s51\nds_write_b32 v7, v96\n.Lprvjoin_%=:\ns_waitcnt lgkmcnt(0)\ns_barrier\nds_read_b32 v96, v14\n" \
    "s_waitcnt lgkmcnt(0)\nv_readfirstlane_b32 s51, v96\ns_branch .Lprvgo_%=\n.Lprvok_%=:\ns_sleep 5\ns_add_u32 s68, s68, 1\ns_and_b32 s43, s68, 63\ns_cbranch_scc1 .Lprvpl2_%=\n" \
    "s_cmp_gt_u32 s68, 0x200000\ns_cbranch_scc1 .Lprvabort_%=\nbuffer_load_dword v97, off, s[64:67], 0 sc1\ns_waitcnt vmcnt(0)\nv_readfirstlane_b32 s43, v97\n" \
    "s_cmp_eq_u32 s43, 1\ns_cbranch_scc0 .Lprvpl2_%=\ns_endpgm\n.Lprvabort_%=:\nv_mov_b32 v96, 1\nbuffer_store_dword v96, off, s[64:67], 0 sc1\ns_endpgm\n.Lprvdone_%=:\n"
#define ROLE_R \
    "v_mov_b32 v17, 0\ns_add_u32 s70, s50, 2\ns_add_u32 s71, s50, -2\ns_mov_b32 s68, 0\ns_branch .Lr11_%=\n.Lr10_%=:\nbuffer_load_dword v17, v8, s[64:67], 0 offen sc1\n" \
    "s_waitcnt vmcnt(0)\n.Lr11_%=:\nv_cmp_lt_i32 vcc, s70, v17\ns_nop 3\ns_and_b32 s69, vcc_lo, 0xff\ns_cmp_eq_u32 s69, 0xff\ns_cbranch_scc0 .Lr12_%=\ns_mov_b32 s68, 0\n" \
    "s_add_u32 s69, s50, 2\ns_and_b32 s69, s69, s41\ns_lshl_b32 s69, s69, 19\ns_add_u32 s69, s69, s35\ns_add_u32 s84, s48, s69\ns_lshr_b32 s84, s84, 8\ns_xor_b32 s84, s84, s49\n" \
    "s_and_b32 s84, s84, 0x2000\ns_add_u32 s84, s84, s69\n.Lr13_%=:\ns_add_u32 s85, s84, 0\nbuffer_load_dwordx4 v[72:75], v0, s[64:67], s85 offen offset:0 sc1\n" \
    "buffer_load_dwordx4 v[76:79], v0, s[64:67], s85 offen offset:1024 sc1\nbuffer_load_dwordx4 v[80:83], v0, s[64:67], s85 offen offset:2048 sc1\n" \
    "buffer_load_dwordx4 v[84:87], v0, s[64:67], s85 offen offset:3072 sc1\nbuffer_load_dwordx4 v[88:91], v1, s[64:67], s85 offen offset:0 sc1\n" \
    "buffer_load_dwordx4 v[92:95], v1, s[64:67], s85 offen offset:1024 sc1\nbuffer_load_dwordx4 v[96:99], v1, s[64:67], s85 offen offset:2048 sc1\n" \
    "buffer_load_dwordx4 v[100:103], v1, s[64:67], s85 offen offset:3072 sc1\ns_add_u32 s85, s84, 16384\nbuffer_load_dwordx4 v[104:107], v0, s[64:67], s85 offen offset:0 sc1\n" \
    "buffer_load_dwordx4 v[108:111], v0, s[64:67], s85 offen offset:1024 sc1\nbuffer_load_dwordx4 v[112:115], v0, s[64:67], s85 offen offset:2048 sc1\n.Lr14_%=:\n" \
    "v_mov_b32 v22, 0\ns_waitcnt vmcnt(10)\nv_mfma_f32_16x16x32_bf16 v[24:27], v[72:75], a[0:3], 0\nbuffer_load_dwordx4 v[116:119], v0, s[64:67], s85 offen offset:3072 sc1\n" \
    "v_mfma_f32_16x16x32_bf16 v[28:31], v[72:75], a[16:19], 0\ns_waitcnt vmcnt(10)\nv_mfma_f32_16x16x32_bf16 v[24:27], v[76:79], a[4:7], v[24:27]\n" \
    "buffer_load_dwordx4 v[72:75], v1, s[64:67], s85 offen offset:0 sc1\nv_mfma_f32_16x16x32_bf16 v[28:31], v[76:79], a[20:23], v[28:31]\ns_waitcnt vmcnt(10)\n" \
    "v_mfma_f32_16x16x32_bf16 v[24:27], v[80:83], a[8:11], v[24:27]\nbuffer_load_dwordx4 v[76:79], v1, s[64:67], s85 offen offset:1024 sc1\n" \
    "v_mfma_f32_16x16x32_bf16 v[28:31], v[80:83], a[24:27], v[28:31]\ns_waitcnt vmcnt(10)\nv_mfma_f32_16x16x32_bf16 v[24:27], v[84:87], a[12:15], v[24:27]\n" \
    "buffer_load_dwordx4 v[80:83], v1, s[64:67], s85 offen offset:2048 sc1\nv_mfma_f32_16x16x32_bf16 v[28:31], v[84:87], a[28:31], v[28:31]\ns_waitcnt vmcnt(10)\n" \
    "v_mfma_f32_16x16x32_bf16 v[32:35], v[88:91], a[0:3], 0\nbuffer_load_dwordx4 v[84:87], v1, s[64:67], s85 offen offset:3072 sc1\n" \
    "v_mfma_f32_16x16x32_bf16 v[36:39], v[88:91], a[16:19], 0\ns_waitcnt vmcnt(10)\nv_mfma_f32_16x16x32_bf16 v[32:35], v[92:95], a[4:7], v[32:35]\ns_add_u32 s85, s84, 32768\n" \
    "buffer_load_dwordx4 v[88:91], v0, s[64:67], s85 offen offset:0 sc1\nv_mfma_f32_16x16x32_bf16 v[36:39], v[92:95], a[20:23], v[36:39]\nv_cvt_pk_bf16_f32 v120, v24, v25\n" \
    "v_cvt_pk_bf16_f32 v122, v26, v27\ns_waitcnt vmcnt(10)\nv_mfma_f32_16x16x32_bf16 v[32:35], v[96:99], a[8:11], v[32:35]\n" \
    "buffer_load_dwordx4 v[92:95], v0, s[64:67], s85 offen offset:1024 sc1\nv_mfma_f32_16x16x32_bf16 v[36:39], v[96:99], a[24:27], v[36:39]\nv_cvt_pk_bf16_f32 v152, v28, v29\n" \
    "v_cvt_pk_bf16_f32 v154, v30, v31\ns_waitcnt vmcnt(10)\nv_mfma_f32_16x16x32_bf16 v[32:35], v[100:103], a[12:15], v[32:35]\n" \
    "buffer_load_dwordx4 v[96:99], v0, s[64:67], s85 offen offset:2048 sc1\nv_mfma_f32_16x16x32_bf16 v[36:39], v[100:103], a[28:31], v[36:39]\ns_waitcnt vmcnt(10)\n" \
    "v_mfma_f32_16x16x32_bf16 v[24:27], v[104:107], a[32:35], 0\nbuffer_load_dwordx4 v[100:103], v0, s[64:67], s85 offen offset:3072 sc1\n" \
    "v_mfma_f32_16x16x32_bf16 v[28:31], v[104:107], a[48:51], 0\ns_waitcnt vmcnt(10)\nv_mfma_f32_16x16x32_bf16 v[24:27], v[108:111], a[36:39], v[24:27]\n" \
    "buffer_load_dwordx4 v[104:107], v1, s[64:67], s85 offen offset:0 sc1\nv_mfma_f32_16x16x32_bf16 v[28:31], v[108:111], a[52:55], v[28:31]\nv_cvt_pk_bf16_f32 v121, v32, v33\n" \
    "v_cvt_pk_bf16_f32 v123, v34, v35\ns_waitcnt vmcnt(10)\nv_mfma_f32_16x16x32_bf16 v[24:27], v[112:115], a[40:43], v[24:27]\n" \
    "buffer_load_dwordx4 v[108:111], v1, s[64:67], s85 offen offset:1024 sc1\nv_mfma_f32_16x16x32_bf16 v[28:31], v[112:115], a[56:59], v[28:31]\n" \
    "v_cvt_pk_bf16_f32 v153, v36, v37\nv_cvt_pk_bf16_f32 v155, v38, v39\ns_waitcnt vmcnt(10)\nv_mfma_f32_16x16x32_bf16 v[24:27], v[116:119], a[44:47], v[24:27]\n" \
    "buffer_load_dwordx4 v[112:115], v1, s[64:67], s85 offen offset:2048 sc1\nv_mfma_f32_16x16x32_bf16 v[28:31], v[116:119], a[60:63], v[28:31]\ns_waitcnt vmcnt(10)\n" \
    "v_mfma_f32_16x16x32_bf16 v[32:35], v[72:75], a[32:35], 0\nbuffer_load_dwordx4 v[116:119], v1, s[64:67], s85 offen offset:3072 sc1\n" \
    "v_mfma_f32_16x16x32_bf16 v[36:39], v[72:75], a[48:51], 0\ns_waitcnt vmcnt(10)\nv_mfma_f32_16x16x32_bf16 v[32:35], v[76:79], a[36:39], v[32:35]\ns_add_u32 s85, s84, 49152\n" \
    "buffer_load_dwordx4 v[72:75], v0, s[64:67], s85 offen offset:0 sc1\nv_mfma_f32_16x16x32_bf16 v[36:39], v[76:79], a[52:55], v[36:39]\nv_cvt_pk_bf16_f32 v124, v24, v25\n" \
    "v_cvt_pk_bf16_f32 v126, v26, v27\ns_waitcnt vmcnt(10)\nv_mfma_f32_16x16x32_bf16 v[32:35], v[80:83], a[40:43], v[32:35]\n" \
    "buffer_load_dwordx4 v[76:79], v0, s[64:67], s85 offen offset:1024 sc1\nv_mfma_f32_16x16x32_bf16 v[36:39], v[80:83], a[56:59], v[36:39]\nv_cvt_pk_bf16_f32 v156, v28, v29\n" \
    "v_cvt_pk_bf16_f32 v158, v30, v31\ns_waitcnt vmcnt(10)\nv_mfma_f32_16x16x32_bf16 v[32:35], v[84:87], a[44:47], v[32:35]\n" \
    "buffer_load_dwordx4 v[80:83], v0, s[64:67], s85 offen offset:2048 sc1\nv_mfma_f32_16x16x32_bf16 v[36:39], v[84:87], a[60:63], v[36:39]\ns_waitcnt vmcnt(10)\n" \
    "v_mfma_f32_16x16x32_bf16 v[24:27], v[88:91], a[64:67], 0\nbuffer_load_dwordx4 v[84:87], v0, s[64:67], s85 offen offset:3072 sc1\n" \
    "v_mfma_f32_16x16x32_bf16 v[28:31], v[88:91], a[80:83], 0\ns_waitcnt vmcnt(10)\nv_mfma_f32_16x16x32_bf16 v[24:27], v[92:95], a[68:71], v[24:27]\n" \
    "buffer_load_dwordx4 v[88:91], v1, s[64:67], s85 offen offset:0 sc1\nv_mfma_f32_16x16x32_bf16 v[28:31], v[92:95], a[84:87], v[28:31]\nv_cvt_pk_bf16_f32 v125, v32, v33\n" \
    "v_cvt_pk_bf16_f32 v127, v34, v35\ns_waitcnt vmcnt(10)\nv_mfma_f32_16x16x32_bf16 v[24:27], v[96:99], a[72:75], v[24:27]\n" \
    "buffer_load_dwordx4 v[92:95], v1, s[64:67], s85 offen offset:1024 sc1\nv_mfma_f32_16x16x32_bf16 v[28:31], v[96:99], a[88:91], v[28:31]\nv_cvt_pk_bf16_f32 v157, v36, v37\n" \
    "v_cvt_pk_bf16_f32 v159, v38, v39\ns_waitcnt vmcnt(10)\nv_mfma_f32_16x16x32_bf16 v[24:27], v[100:103], a[76:79], v[24:27]\n" \
    "buffer_load_dwordx4 v[96:99], v1, s[64:67], s85 offen offset:2048 sc1\nv_mfma_f32_16x16x32_bf16 v[28:31], v[100:103], a[92:95], v[28:31]\ns_waitcnt vmcnt(10)\n" \
    "v_mfma_f32_16x16x32_bf16 v[32:35], v[104:107], a[64:67], 0\nbuffer_load_dwordx4 v[100:103], v1, s[64:67], s85 offen offset:3072 sc1\n" \
    "v_mfma_f32_16x16x32_bf16 v[36:39], v[104:107], a[80:83], 0\ns_waitcnt vmcnt(10)\nv_mfma_f32_16x16x32_bf16 v[32:35], v[108:111], a[68:71], v[32:35]\n" \
    "s_add_u32 s85, s84, 65536\nbuffer_load_dwordx4 v[104:107], v0, s[64:67], s85 offen offset:0 sc1\nv_mfma_f32_16x16x32_bf16 v[36:39], v[108:111], a[84:87], v[36:39]\n" \
    "v_cvt_pk_bf16_f32 v128, v24, v25\nv_cvt_pk_bf16_f32 v130, v26, v27\ns_waitcnt vmcnt(10)\nv_mfma_f32_16x16x32_bf16 v[32:35], v[112:115], a[72:75], v[32:35]\n" \
    "buffer_load_dwordx4 v[108:111], v0, s[64:67], s85 offen offset:1024 sc1\nv_mfma_f32_16x16x32_bf16 v[36:39], v[112:115], a[88:91], v[36:39]\n" \
    "v_cvt_pk_bf16_f32 v160, v28, v29\nv_cvt_pk_bf16_f32 v162, v30, v31\ns_waitcnt vmcnt(10)\nv_mfma_f32_16x16x32_bf16 v[32:35], v[116:119], a[76:79], v[32:35]\n" \
    "buffer_load_dwordx4 v[112:115], v0, s[64:67], s85 offen offset:2048 sc1\nv_mfma_f32_16x16x32_bf16 v[36:39], v[116:119], a[92:95], v[36:39]\ns_waitcnt vmcnt(10)\n" \
    "v_mfma_f32_16x16x32_bf16 v[24:27], v[72:75], a[96:99], 0\nbuffer_load_dwordx4 v[116:119], v0, s[64:67], s85 offen offset:3072 sc1\n" \
    "v_mfma_f32_16x16x32_bf16 v[28:31], v[72:75], a[112:115], 0\ns_waitcnt vmcnt(10)\nv_mfma_f32_16x16x32_bf16 v[24:27], v[76:79], a[100:103], v[24:27]\n" \
    "buffer_load_dwordx4 v[72:75], v1, s[64:67], s85 offen offset:0 sc1\nv_mfma_f32_16x16x32_bf16 v[28:31], v[76:79], a[116:119], v[28:31]\nv_cvt_pk_bf16_f32 v129, v32, v33\n" \
    "v_cvt_pk_bf16_f32 v131, v34, v35\ns_waitcnt vmcnt(10)\nv_mfma_f32_16x16x32_bf16 v[24:27], v[80:83], a[104:107], v[24:27]\n" \
    "buffer_load_dwordx4 v[76:79], v1, s[64:67], s85 offen offset:1024 sc1\nv_mfma_f32_16x16x32_bf16 v[28:31], v[80:83], a[120:123], v[28:31]\nv_cvt_pk_bf16_f32 v161, v36, v37\n" \
    "v_cvt_pk_bf16_f32 v163, v38, v39\ns_waitcnt vmcnt(10)\nv_mfma_f32_16x16x32_bf16 v[24:27], v[84:87], a[108:111], v[24:27]\n" \
    "buffer_load_dwordx4 v[80:83], v1, s[64:67], s85 offen offset:2048 sc1\nv_mfma_f32_16x16x32_bf16 v[28:31], v[84:87], a[124:127], v[28:31]\ns_waitcnt vmcnt(10)\n" \
    "v_mfma_f32_16x16x32_bf16 v[32:35], v[88:91], a[96:99], 0\nbuffer_load_dwordx4 v[84:87], v1, s[64:67], s85 offen offset:3072 sc1\n" \
    "v_mfma_f32_16x16x32_bf16 v[36:39], v[88:91], a[112:115], 0\ns_waitcnt vmcnt(10)\nv_mfma_f32_16x16x32_bf16 v[32:35], v[92:95], a[100:103], v[32:35]\n" \
    "s_add_u32 s85, s84, 81920\nbuffer_load_dwordx4 v[88:91], v0, s[64:67], s85 offen offset:0 sc1\nv_mfma_f32_16x16x32_bf16 v[36:39], v[92:95], a[116:119], v[36:39]\n" \
    "v_cvt_pk_bf16_f32 v132, v24, v25\nv_cvt_pk_bf16_f32 v134, v26, v27\ns_waitcnt vmcnt(10)\nv_mfma_f32_16x16x32_bf16 v[32:35], v[96:99], a[104:107], v[32:35]\n" \
    "buffer_load_dwordx4 v[92:95], v0, s[64:67], s85 offen offset:1024 sc1\nv_mfma_f32_16x16x32_bf16 v[36:39], v[96:99], a[120:123], v[36:39]\nv_cvt_pk_bf16_f32 v164, v28, v29\n" \
    "v_cvt_pk_bf16_f32 v166, v30, v31\ns_waitcnt vmcnt(10)\nv_mfma_f32_16x16x32_bf16 v[32:35], v[100:103], a[108:111], v[32:35]\n" \
    "buffer_load_dwordx4 v[96:99], v0, s[64:67], s85 offen offset:2048 sc1\nv_mfma_f32_16x16x32_bf16 v[36:39], v[100:103], a[124:127], v[36:39]\ns_waitcnt vmcnt(10)\n" \
    "v_mfma_f32_16x16x32_bf16 v[24:27], v[104:107], a[128:131], 0\nbuffer_load_dwordx4 v[100:103], v0, s[64:67], s85 offen offset:3072 sc1\n" \
    "v_mfma_f32_16x16x32_bf16 v[28:31], v[104:107], a[144:147], 0\ns_waitcnt vmcnt(10)\nv_mfma_f32_16x16x32_bf16 v[24:27], v[108:111], a[132:135], v[24:27]\n" \
    "buffer_load_dwordx4 v[104:107], v1, s[64:67], s85 offen offset:0 sc1\nv_mfma_f32_16x16x32_bf16 v[28:31], v[108:111], a[148:151], v[28:31]\n" \
    "v_cvt_pk_bf16_f32 v133, v32, v33\nv_cvt_pk_bf16_f32 v135, v34, v35\ns_waitcnt vmcnt(10)\nv_mfma_f32_16x16x32_bf16 v[24:27], v[112:115], a[136:139], v[24:27]\n" \
    "buffer_load_dwordx4 v[108:111], v1, s[64:67], s85 offen offset:1024 sc1\nv_mfma_f32_16x16x32_bf16 v[28:31], v[112:115], a[152:155], v[28:31]\n" \
    "v_cvt_pk_bf16_f32 v165, v36, v37\nv_cvt_pk_bf16_f32 v167, v38, v39\ns_waitcnt vmcnt(10)\nv_mfma_f32_16x16x32_bf16 v[24:27], v[116:119], a[140:143], v[24:27]\n" \
    "buffer_load_dwordx4 v[112:115], v1, s[64:67], s85 offen offset:2048 sc1\nv_mfma_f32_16x16x32_bf16 v[28:31], v[116:119], a[156:159], v[28:31]\ns_waitcnt vmcnt(10)\n" \
    "v_mfma_f32_16x16x32_bf16 v[32:35], v[72:75], a[128:131], 0\nbuffer_load_dwordx4 v[116:119], v1, s[64:67], s85 offen offset:3072 sc1\n" \
    "v_mfma_f32_16x16x32_bf16 v[36:39], v[72:75], a[144:147], 0\ns_waitcnt vmcnt(10)\nv_mfma_f32_16x16x32_bf16 v[32:35], v[76:79], a[132:135], v[32:35]\n" \
    "s_add_u32 s85, s84, 98304\nbuffer_load_dwordx4 v[72:75], v0, s[64:67], s85 offen offset:0 sc1\nv_mfma_f32_16x16x32_bf16 v[36:39], v[76:79], a[148:151], v[36:39]\n" \
    "v_cvt_pk_bf16_f32 v136, v24, v25\nv_cvt_pk_bf16_f32 v138, v26, v27\ns_waitcnt vmcnt(10)\nv_mfma_f32_16x16x32_bf16 v[32:35], v[80:83], a[136:139], v[32:35]\n" \
    "buffer_load_dwordx4 v[76:79], v0, s[64:67], s85 offen offset:1024 sc1\nv_mfma_f32_16x16x32_bf16 v[36:39], v[80:83], a[152:155], v[36:39]\nv_cvt_pk_bf16_f32 v168, v28, v29\n" \
    "v_cvt_pk_bf16_f32 v170, v30, v31\ns_waitcnt vmcnt(10)\nv_mfma_f32_16x16x32_bf16 v[32:35], v[84:87], a[140:143], v[32:35]\n" \
    "buffer_load_dwordx4 v[80:83], v0, s[64:67], s85 offen offset:2048 sc1\nv_mfma_f32_16x16x32_bf16 v[36:39], v[84:87], a[156:159], v[36:39]\ns_waitcnt vmcnt(10)\n" \
    "v_mfma_f32_16x16x32_bf16 v[24:27], v[88:91], a[160:163], 0\nbuffer_load_dwordx4 v[84:87], v0, s[64:67], s85 offen offset:3072 sc1\n" \
    "v_mfma_f32_16x16x32_bf16 v[28:31], v[88:91], a[176:179], 0\ns_waitcnt vmcnt(10)\nv_mfma_f32_16x16x32_bf16 v[24:27], v[92:95], a[164:167], v[24:27]\n" \
    "buffer_load_dwordx4 v[88:91], v1, s[64:67], s85 offen offset:0 sc1\nv_mfma_f32_16x16x32_bf16 v[28:31], v[92:95], a[180:183], v[28:31]\nv_cvt_pk_bf16_f32 v137, v32, v33\n" \
    "v_cvt_pk_bf16_f32 v139, v34, v35\ns_waitcnt vmcnt(10)\nv_mfma_f32_16x16x32_bf16 v[24:27], v[96:99], a[168:171], v[24:27]\n" \
    "buffer_load_dwordx4 v[92:95], v1, s[64:67], s85 offen offset:1024 sc1\nv_mfma_f32_16x16x32_bf16 v[28:31], v[96:99], a[184:187], v[28:31]\nv_cvt_pk_bf16_f32 v169, v36, v37\n" \
    "v_cvt_pk_bf16_f32 v171, v38, v39\ns_waitcnt vmcnt(10)\nv_mfma_f32_16x16x32_bf16 v[24:27], v[100:103], a[172:175], v[24:27]\n" \
    "buffer_load_dwordx4 v[96:99], v1, s[64:67], s85 offen offset:2048 sc1\nv_mfma_f32_16x16x32_bf16 v[28:31], v[100:103], a[188:191], v[28:31]\ns_waitcnt vmcnt(10)\n" \
    "v_mfma_f32_16x16x32_bf16 v[32:35], v[104:107], a[160:163], 0\nbuffer_load_dwordx4 v[100:103], v1, s[64:67], s85 offen offset:3072 sc1\n" \
    "v_mfma_f32_16x16x32_bf16 v[36:39], v[104:107], a[176:179], 0\ns_waitcnt vmcnt(10)\nv_mfma_f32_16x16x32_bf16 v[32:35], v[108:111], a[164:167], v[32:35]\n" \
    "s_add_u32 s85, s84, 114688\nbuffer_load_dwordx4 v[104:107], v0, s[64:67], s85 offen offset:0 sc1\nv_mfma_f32_16x16x32_bf16 v[36:39], v[108:111], a[180:183], v[36:39]\n" \
    "v_cvt_pk_bf16_f32 v140, v24, v25\nv_cvt_pk_bf16_f32 v142, v26, v27\ns_waitcnt vmcnt(10)\nv_mfma_f32_16x16x32_bf16 v[32:35], v[112:115], a[168:171], v[32:35]\n" \
    "buffer_load_dwordx4 v[108:111], v0, s[64:67], s85 offen offset:1024 sc1\nv_mfma_f32_16x16x32_bf16 v[36:39], v[112:115], a[184:187], v[36:39]\n" \
    "v_cvt_pk_bf16_f32 v172, v28, v29\nv_cvt_pk_bf16_f32 v174, v30, v31\ns_waitcnt vmcnt(10)\nv_mfma_f32_16x16x32_bf16 v[32:35], v[116:119], a[172:175], v[32:35]\n" \
    "buffer_load_dwordx4 v[112:115], v0, s[64:67], s85 offen offset:2048 sc1\nv_mfma_f32_16x16x32_bf16 v[36:39], v[116:119], a[188:191], v[36:39]\ns_waitcnt vmcnt(10)\n" \
    "v_mfma_f32_16x16x32_bf16 v[24:27], v[72:75], a[192:195], 0\nbuffer_load_dwordx4 v[116:119], v0, s[64:67], s85 offen offset:3072 sc1\n" \
    "v_mfma_f32_16x16x32_bf16 v[28:31], v[72:75], a[208:211], 0\ns_waitcnt vmcnt(10)\nv_mfma_f32_16x16x32_bf16 v[24:27], v[76:79], a[196:199], v[24:27]\n" \
    "buffer_load_dwordx4 v[72:75], v1, s[64:67], s85 offen offset:0 sc1\nv_mfma_f32_16x16x32_bf16 v[28:31], v[76:79], a[212:215], v[28:31]\nv_cvt_pk_bf16_f32 v141, v32, v33\n" \
    "v_cvt_pk_bf16_f32 v143, v34, v35\ns_waitcnt vmcnt(10)\nv_mfma_f32_16x16x32_bf16 v[24:27], v[80:83], a[200:203], v[24:27]\n" \
    "buffer_load_dwordx4 v[76:79], v1, s[64:67], s85 offen offset:1024 sc1\nv_mfma_f32_16x16x32_bf16 v[28:31], v[80:83], a[216:219], v[28:31]\nv_cvt_pk_bf16_f32 v173, v36, v37\n" \
    "v_cvt_pk_bf16_f32 v175, v38, v39\ns_waitcnt vmcnt(10)\nv_mfma_f32_16x16x32_bf16 v[24:27], v[84:87], a[204:207], v[24:27]\n" \
    "buffer_load_dwordx4 v[80:83], v1, s[64:67], s85 offen offset:2048 sc1\nv_mfma_f32_16x16x32_bf16 v[28:31], v[84:87], a[220:223], v[28:31]\ns_waitcnt vmcnt(10)\n" \
    "v_mfma_f32_16x16x32_bf16 v[32:35], v[88:91], a[192:195], 0\nbuffer_load_dwordx4 v[84:87], v1, s[64:67], s85 offen offset:3072 sc1\n" \
    "v_mfma_f32_16x16x32_bf16 v[36:39], v[88:91], a[208:211], 0\ns_waitcnt vmcnt(10)\nv_mfma_f32_16x16x32_bf16 v[32:35], v[92:95], a[196:199], v[32:35]\n" \
    "v_mfma_f32_16x16x32_bf16 v[36:39], v[92:95], a[212:215], v[36:39]\nv_cvt_pk_bf16_f32 v144, v24, v25\nv_cvt_pk_bf16_f32 v146, v26, v27\ns_waitcnt vmcnt(9)\n" \
    "v_mfma_f32_16x16x32_bf16 v[32:35], v[96:99], a[200:203], v[32:35]\nv_mfma_f32_16x16x32_bf16 v[36:39], v[96:99], a[216:219], v[36:39]\nv_cvt_pk_bf16_f32 v176, v28, v29\n" \
    "v_cvt_pk_bf16_f32 v178, v30, v31\ns_waitcnt vmcnt(8)\nv_mfma_f32_16x16x32_bf16 v[32:35], v[100:103], a[204:207], v[32:35]\n" \
    "v_mfma_f32_16x16x32_bf16 v[36:39], v[100:103], a[220:223], v[36:39]\ns_waitcnt vmcnt(7)\nv_mfma_f32_16x16x32_bf16 v[24:27], v[104:107], a[224:227], 0\n" \
    "v_mfma_f32_16x16x32_bf16 v[28:31], v[104:107], a[240:243], 0\ns_waitcnt vmcnt(6)\nv_mfma_f32_16x16x32_bf16 v[24:27], v[108:111], a[228:231], v[24:27]\n" \
    "v_mfma_f32_16x16x32_bf16 v[28:31], v[108:111], a[244:247], v[28:31]\ns_nop 0\nv_cvt_pk_bf16_f32 v145, v32, v33\nv_cvt_pk_bf16_f32 v147, v34, v35\ns_waitcnt vmcnt(5)\n" \
    "v_mfma_f32_16x16x32_bf16 v[24:27], v[112:115], a[232:235], v[24:27]\nv_mfma_f32_16x16x32_bf16 v[28:31], v[112:115], a[248:251], v[28:31]\nv_cvt_pk_bf16_f32 v177, v36, v37\n" \
    "v_cvt_pk_bf16_f32 v179, v38, v39\ns_waitcnt vmcnt(4)\nv_mfma_f32_16x16x32_bf16 v[24:27], v[116:119], a[236:239], v[24:27]\n" \
    "v_mfma_f32_16x16x32_bf16 v[28:31], v[116:119], a[252:255], v[28:31]\ns_waitcnt vmcnt(3)\nv_mfma_f32_16x16x32_bf16 v[32:35], v[72:75], a[224:227], 0\n" \
    "v_mfma_f32_16x16x32_bf16 v[36:39], v[72:75], a[240:243], 0\ns_waitcnt vmcnt(2)\nv_mfma_f32_16x16x32_bf16 v[32:35], v[76:79], a[228:231], v[32:35]\n" \
    "v_mfma_f32_16x16x32_bf16 v[36:39], v[76:79], a[244:247], v[36:39]\ns_nop 0\nv_cvt_pk_bf16_f32 v148, v24, v25\nv_cvt_pk_bf16_f32 v150, v26, v27\ns_waitcnt vmcnt(1)\n" \
    "v_mfma_f32_16x16x32_bf16 v[32:35], v[80:83], a[232:235], v[32:35]\nv_mfma_f32_16x16x32_bf16 v[36:39], v[80:83], a[248:251], v[36:39]\nv_cvt_pk_bf16_f32 v180, v28, v29\n" \
    "v_cvt_pk_bf16_f32 v182, v30, v31\ns_waitcnt vmcnt(0)\nv_mfma_f32_16x16x32_bf16 v[32:35], v[84:87], a[236:239], v[32:35]\n" \
    "v_mfma_f32_16x16x32_bf16 v[36:39], v[84:87], a[252:255], v[36:39]\ns_nop 6\nv_cvt_pk_bf16_f32 v149, v32, v33\nv_cvt_pk_bf16_f32 v151, v34, v35\n" \
    "v_cvt_pk_bf16_f32 v181, v36, v37\nv_cvt_pk_bf16_f32 v183, v38, v39\ns_add_u32 s50, s50, 2\ns_memrealtime s[72:73]\ns_waitcnt lgkmcnt(0)\ns_mov_b32 s51, s72\n" \
    "s_add_u32 s69, s50, 0\ns_and_b32 s69, s69, s41\ns_lshl_b32 s69, s69, 20\ns_add_u32 s69, s69, s32\ns_add_u32 s74, s48, s69\ns_lshr_b32 s74, s74, 8\ns_xor_b32 s74, s74, s49\n" \
    "s_and_b32 s74, s74, 0x2000\ns_add_u32 s74, s74, s69\ns_add_u32 s69, s50, 0\ns_and_b32 s69, s69, s41\ns_lshl_b32 s69, s69, 19\ns_add_u32 s69, s69, s33\n" \
    "s_add_u32 s87, s48, s69\ns_lshr_b32 s87, s87, 8\ns_xor_b32 s87, s87, s49\ns_and_b32 s87, s87, 0x2000\ns_add_u32 s87, s87, s69\ns_add_u32 s69, s50, 1\n" \
    "s_and_b32 s69, s69, s41\ns_lshl_b32 s69, s69, 18\ns_add_u32 s69, s69, s34\ns_add_u32 s88, s48, s69\ns_lshr_b32 s88, s88, 8\ns_xor_b32 s88, s88, s49\n" \
    "s_and_b32 s88, s88, 0x2000\ns_add_u32 s88, s88, s69\ns_xor_b32 s89, s88, 0x1000\ns_mov_b32 s68, 0\ns_add_u32 s78, s87, s38\n.Lr20_%=:\ns_sub_u32 s70, s51, 52\n.Lr29_%=:\n" \
    "s_sleep 1\ns_memrealtime s[72:73]\ns_waitcnt lgkmcnt(0)\ns_sub_u32 s72, s72, s70\ns_cmp_lt_i32 s72, 0\ns_cbranch_scc1 .Lr29_%=\n" \
    "buffer_load_dword v20, off, s[64:67], s78 sc1\ns_waitcnt vmcnt(0)\nv_readfirstlane_b32 s70, v20\ns_memrealtime s[72:73]\ns_cmp_eq_u32 s70, -1\ns_cbranch_scc1 .Lr21_%=\n" \
    "s_lshr_b32 s71, s70, 8\ns_waitcnt lgkmcnt(0)\ns_sub_u32 s71, s72, s71\ns_and_b32 s71, s71, 0xffffff\ns_sub_u32 s51, s72, s71\ns_add_u32 s51, s51, 176\n" \
    "s_and_b32 s70, s70, 128\ns_lshl_b32 s52, s70, 5\ns_xor_b32 s87, s87, s52\ns_mov_b32 s68, 0\ns_sub_u32 s70, s51, s54\n.Lr30_%=:\ns_memrealtime s[72:73]\n" \
    "s_waitcnt lgkmcnt(0)\ns_sub_u32 s72, s72, s70\ns_cmp_lt_i32 s72, 0\ns_cbranch_scc1 .Lr30_%=\nbuffer_load_dwordx4 v[40:43], v0, s[64:67], s74 offen offset:0 sc1\n" \
    "buffer_load_dwordx4 v[44:47], v0, s[64:67], s74 offen offset:1024 sc1\ns_add_u32 s69, s74, 0x4000\nbuffer_load_dwordx4 v[48:51], v0, s[64:67], s69 offen offset:0 sc1\n" \
    "buffer_load_dwordx4 v[52:55], v0, s[64:67], s69 offen offset:1024 sc1\n.Lr31_%=:\ns_memrealtime s[72:73]\ns_waitcnt lgkmcnt(0)\ns_sub_u32 s72, s72, s51\n" \
    "s_cmp_lt_i32 s72, -3\ns_cbranch_scc1 .Lr31_%=\ns_cmp_ge_i32 s72, 0\ns_cbranch_scc1 .Lr32_%=\ns_nop 2\ns_cmp_ge_i32 s72, -1\ns_cbranch_scc1 .Lr32_%=\ns_nop 2\n" \
    "s_cmp_ge_i32 s72, -2\ns_cbranch_scc1 .Lr32_%=\ns_nop 2\n.Lr32_%=:\nbuffer_load_dwordx4 v[100:103], v0, s[64:67], s87 offen offset:0 sc1\n" \
    "buffer_load_dwordx4 v[104:107], v0, s[64:67], s87 offen offset:1024 sc1\nbuffer_load_dwordx4 v[108:111], v0, s[64:67], s87 offen offset:2048 sc1\n" \
    "buffer_load_dwordx4 v[112:115], v0, s[64:67], s87 offen offset:3072 sc1\nbuffer_load_dwordx2 v[116:117], v5, s[64:67], s87 offen offset:0 sc1\n" \
    "buffer_load_dwordx2 v[118:119], v5, s[64:67], s87 offen offset:128 sc1\n.Lr24_%=:\nv_mov_b64 v[24:25], 0\nv_mov_b64 v[26:27], 0\nv_mov_b64 v[28:29], 0\n" \
    "v_mov_b64 v[30:31], 0\ns_waitcnt vmcnt(6)\nv_max3_u32 v22, v40, v41, v42\nv_max3_u32 v22, v22, v43, v44\nv_max3_u32 v22, v22, v45, v46\nv_max3_u32 v22, v22, v47, v48\n" \
    "v_max3_u32 v22, v22, v49, v50\nv_max3_u32 v22, v22, v51, v52\nv_max3_u32 v22, v22, v53, v54\nv_max_u32 v22, v22, v55\ns_waitcnt vmcnt(5)\n" \
    "v_smfmac_f32_16x16x64_bf16 v[24:27], v[100:103], v[120:127], v4\nv_max3_u32 v22, v22, v100, v101\nv_smfmac_f32_16x16x64_bf16 v[28:31], v[100:103], v[152:159], v4\n" \
    "v_max3_u32 v22, v22, v102, v103\ns_waitcnt vmcnt(4)\nv_smfmac_f32_16x16x64_bf16 v[24:27], v[104:107], v[128:135], v4\nv_max3_u32 v22, v22, v104, v105\n" \
    "v_smfmac_f32_16x16x64_bf16 v[28:31], v[104:107], v[160:167], v4\nv_max3_u32 v22, v22, v106, v107\ns_waitcnt vmcnt(3)\n" \
    "v_smfmac_f32_16x16x64_bf16 v[24:27], v[108:111], v[136:143], v4\nv_max3_u32 v22, v22, v108, v109\nv_smfmac_f32_16x16x64_bf16 v[28:31], v[108:111], v[168:175], v4\n" \
    "v_max3_u32 v22, v22, v110, v111\ns_waitcnt vmcnt(2)\nv_smfmac_f32_16x16x64_bf16 v[24:27], v[112:115], v[144:151], v4\nv_max3_u32 v22, v22, v112, v113\n" \
    "v_smfmac_f32_16x16x64_bf16 v[28:31], v[112:115], v[176:183], v4\nv_max3_u32 v22, v22, v114, v115\ns_waitcnt vmcnt(0)\n" \
    "v_smfmac_f32_16x16x64_bf16 v[24:27], v[116:119], v[40:47], v4\nv_max3_u32 v22, v22, v116, v117\nv_smfmac_f32_16x16x64_bf16 v[28:31], v[116:119], v[48:55], v4\n" \
    "v_max3_u32 v22, v22, v118, v119\nv_cmp_eq_u32 vcc, -1, v22\ns_nop 5\nv_permlane32_swap_b32 v24, v28\nv_permlane32_swap_b32 v25, v29\nv_permlane32_swap_b32 v26, v30\n" \
    "v_permlane32_swap_b32 v27, v31\nv_pk_add_f32 v[24:25], v[24:25], v[28:29]\nv_pk_add_f32 v[26:27], v[26:27], v[30:31]\nds_write2_b32 v6, v24, v25 offset0:0 offset1:36\n" \
    "ds_write2_b32 v6, v26, v27 offset0:72 offset1:108\ns_cbranch_vccnz .Lr25_%=\ns_waitcnt lgkmcnt(0)\ns_barrier\nds_read_b128 v[24:27], v7 offset:0\n" \
    "ds_read_b128 v[28:31], v7 offset:1152\nds_read_b128 v[32:35], v7 offset:2304\nds_read_b128 v[36:39], v7 offset:3456\ns_waitcnt lgkmcnt(2)\n" \
    "v_pk_add_f32 v[24:25], v[24:25], v[28:29]\nv_pk_add_f32 v[26:27], v[26:27], v[30:31]\ns_waitcnt lgkmcnt(0)\nv_pk_add_f32 v[32:33], v[32:33], v[36:37]\n" \
    "v_pk_add_f32 v[34:35], v[34:35], v[38:39]\nv_pk_add_f32 v[24:25], v[24:25], v[32:33]\nv_pk_add_f32 v[26:27], v[26:27], v[34:35]\nv_cvt_pk_bf16_f32 v248, v24, v25\n" \
    "v_cvt_pk_bf16_f32 v249, v26, v27\ns_nop 0\nv_permlane32_swap_b32 v248, v250\nv_permlane32_swap_b32 v249, v251\ns_mov_b32 exec_hi, 0\n" \
    "buffer_store_dwordx4 v[248:251], v0, s[64:67], s88 offen sc1\nbuffer_store_dwordx4 v[248:251], v0, s[64:67], s89 offen sc1\ns_mov_b32 exec_hi, -1\ns_add_u32 s51, s51, s55\n" \
    "s_add_u32 s51, s51, s55\ns_add_u32 s70, s50, 2\ns_add_u32 s71, s50, -2\ns_mov_b32 s68, 0\ns_branch .Lr37_%=\n.Lr36_%=:\nbuffer_load_dword v17, v8, s[64:67], 0 offen sc1\n" \
    "s_waitcnt vmcnt(0)\n.Lr37_%=:\nv_cmp_lt_i32 vcc, s70, v17\ns_nop 3\ns_and_b32 s69, vcc_lo, 0xff\ns_cmp_eq_u32 s69, 0xff\ns_cbranch_scc0 .Lr38_%=\ns_mov_b32 s68, 0\n" \
    "s_add_u32 s69, s50, 2\ns_and_b32 s69, s69, s41\ns_lshl_b32 s69, s69, 19\ns_add_u32 s69, s69, s35\ns_add_u32 s84, s48, s69\ns_lshr_b32 s84, s84, 8\ns_xor_b32 s84, s84, s49\n" \
    "s_and_b32 s84, s84, 0x2000\ns_add_u32 s84, s84, s69\n.Lr39_%=:\ns_add_u32 s85, s84, 0\nbuffer_load_dwordx4 v[72:75], v0, s[64:67], s85 offen offset:0 sc1\n" \
    "buffer_load_dwordx4 v[76:79], v0, s[64:67], s85 offen offset:1024 sc1\nbuffer_load_dwordx4 v[80:83], v0, s[64:67], s85 offen offset:2048 sc1\n" \
    "buffer_load_dwordx4 v[84:87], v0, s[64:67], s85 offen offset:3072 sc1\nbuffer_load_dwordx4 v[88:91], v1, s[64:67], s85 offen offset:0 sc1\n" \
    "buffer_load_dwordx4 v[92:95], v1, s[64:67], s85 offen offset:1024 sc1\nbuffer_load_dwordx4 v[96:99], v1, s[64:67], s85 offen offset:2048 sc1\n" \
    "buffer_load_dwordx4 v[100:103], v1, s[64:67], s85 offen offset:3072 sc1\ns_add_u32 s85, s84, 16384\nbuffer_load_dwordx4 v[104:107], v0, s[64:67], s85 offen offset:0 sc1\n" \
    "buffer_load_dwordx4 v[108:111], v0, s[64:67], s85 offen offset:1024 sc1\nbuffer_load_dwordx4 v[112:115], v0, s[64:67], s85 offen offset:2048 sc1\n.Lr40_%=:\n" \
    "v_mov_b32 v22, 0\ns_waitcnt vmcnt(10)\nv_mfma_f32_16x16x32_bf16 v[24:27], v[72:75], a[0:3], 0\nbuffer_load_dwordx4 v[116:119], v0, s[64:67], s85 offen offset:3072 sc1\n" \
    "v_mfma_f32_16x16x32_bf16 v[28:31], v[72:75], a[16:19], 0\ns_waitcnt vmcnt(10)\nv_mfma_f32_16x16x32_bf16 v[24:27], v[76:79], a[4:7], v[24:27]\n" \
    "buffer_load_dwordx4 v[72:75], v1, s[64:67], s85 offen offset:0 sc1\nv_mfma_f32_16x16x32_bf16 v[28:31], v[76:79], a[20:23], v[28:31]\ns_waitcnt vmcnt(10)\n" \
    "v_mfma_f32_16x16x32_bf16 v[24:27], v[80:83], a[8:11], v[24:27]\nbuffer_load_dwordx4 v[76:79], v1, s[64:67], s85 offen offset:1024 sc1\n" \
    "v_mfma_f32_16x16x32_bf16 v[28:31], v[80:83], a[24:27], v[28:31]\ns_waitcnt vmcnt(10)\nv_mfma_f32_16x16x32_bf16 v[24:27], v[84:87], a[12:15], v[24:27]\n" \
    "buffer_load_dwordx4 v[80:83], v1, s[64:67], s85 offen offset:2048 sc1\nv_mfma_f32_16x16x32_bf16 v[28:31], v[84:87], a[28:31], v[28:31]\ns_waitcnt vmcnt(10)\n" \
    "v_mfma_f32_16x16x32_bf16 v[32:35], v[88:91], a[0:3], 0\nbuffer_load_dwordx4 v[84:87], v1, s[64:67], s85 offen offset:3072 sc1\n" \
    "v_mfma_f32_16x16x32_bf16 v[36:39], v[88:91], a[16:19], 0\ns_waitcnt vmcnt(10)\nv_mfma_f32_16x16x32_bf16 v[32:35], v[92:95], a[4:7], v[32:35]\ns_add_u32 s85, s84, 32768\n" \
    "buffer_load_dwordx4 v[88:91], v0, s[64:67], s85 offen offset:0 sc1\nv_mfma_f32_16x16x32_bf16 v[36:39], v[92:95], a[20:23], v[36:39]\nv_cvt_pk_bf16_f32 v184, v24, v25\n" \
    "v_cvt_pk_bf16_f32 v186, v26, v27\ns_waitcnt vmcnt(10)\nv_mfma_f32_16x16x32_bf16 v[32:35], v[96:99], a[8:11], v[32:35]\n" \
    "buffer_load_dwordx4 v[92:95], v0, s[64:67], s85 offen offset:1024 sc1\nv_mfma_f32_16x16x32_bf16 v[36:39], v[96:99], a[24:27], v[36:39]\nv_cvt_pk_bf16_f32 v216, v28, v29\n" \
    "v_cvt_pk_bf16_f32 v218, v30, v31\ns_waitcnt vmcnt(10)\nv_mfma_f32_16x16x32_bf16 v[32:35], v[100:103], a[12:15], v[32:35]\n" \
    "buffer_load_dwordx4 v[96:99], v0, s[64:67], s85 offen offset:2048 sc1\nv_mfma_f32_16x16x32_bf16 v[36:39], v[100:103], a[28:31], v[36:39]\ns_waitcnt vmcnt(10)\n" \
    "v_mfma_f32_16x16x32_bf16 v[24:27], v[104:107], a[32:35], 0\nbuffer_load_dwordx4 v[100:103], v0, s[64:67], s85 offen offset:3072 sc1\n" \
    "v_mfma_f32_16x16x32_bf16 v[28:31], v[104:107], a[48:51], 0\ns_waitcnt vmcnt(10)\nv_mfma_f32_16x16x32_bf16 v[24:27], v[108:111], a[36:39], v[24:27]\n" \
    "buffer_load_dwordx4 v[104:107], v1, s[64:67], s85 offen offset:0 sc1\nv_mfma_f32_16x16x32_bf16 v[28:31], v[108:111], a[52:55], v[28:31]\nv_cvt_pk_bf16_f32 v185, v32, v33\n" \
    "v_cvt_pk_bf16_f32 v187, v34, v35\ns_waitcnt vmcnt(10)\nv_mfma_f32_16x16x32_bf16 v[24:27], v[112:115], a[40:43], v[24:27]\n" \
    "buffer_load_dwordx4 v[108:111], v1, s[64:67], s85 offen offset:1024 sc1\nv_mfma_f32_16x16x32_bf16 v[28:31], v[112:115], a[56:59], v[28:31]\n" \
    "v_cvt_pk_bf16_f32 v217, v36, v37\nv_cvt_pk_bf16_f32 v219, v38, v39\ns_waitcnt vmcnt(10)\nv_mfma_f32_16x16x32_bf16 v[24:27], v[116:119], a[44:47], v[24:27]\n" \
    "buffer_load_dwordx4 v[112:115], v1, s[64:67], s85 offen offset:2048 sc1\nv_mfma_f32_16x16x32_bf16 v[28:31], v[116:119], a[60:63], v[28:31]\ns_waitcnt vmcnt(10)\n" \
    "v_mfma_f32_16x16x32_bf16 v[32:35], v[72:75], a[32:35], 0\nbuffer_load_dwordx4 v[116:119], v1, s[64:67], s85 offen offset:3072 sc1\n" \
    "v_mfma_f32_16x16x32_bf16 v[36:39], v[72:75], a[48:51], 0\ns_waitcnt vmcnt(10)\nv_mfma_f32_16x16x32_bf16 v[32:35], v[76:79], a[36:39], v[32:35]\ns_add_u32 s85, s84, 49152\n" \
    "buffer_load_dwordx4 v[72:75], v0, s[64:67], s85 offen offset:0 sc1\nv_mfma_f32_16x16x32_bf16 v[36:39], v[76:79], a[52:55], v[36:39]\nv_cvt_pk_bf16_f32 v188, v24, v25\n" \
    "v_cvt_pk_bf16_f32 v190, v26, v27\ns_waitcnt vmcnt(10)\nv_mfma_f32_16x16x32_bf16 v[32:35], v[80:83], a[40:43], v[32:35]\n" \
    "buffer_load_dwordx4 v[76:79], v0, s[64:67], s85 offen offset:1024 sc1\nv_mfma_f32_16x16x32_bf16 v[36:39], v[80:83], a[56:59], v[36:39]\nv_cvt_pk_bf16_f32 v220, v28, v29\n" \
    "v_cvt_pk_bf16_f32 v222, v30, v31\ns_waitcnt vmcnt(10)\nv_mfma_f32_16x16x32_bf16 v[32:35], v[84:87], a[44:47], v[32:35]\n" \
    "buffer_load_dwordx4 v[80:83], v0, s[64:67], s85 offen offset:2048 sc1\nv_mfma_f32_16x16x32_bf16 v[36:39], v[84:87], a[60:63], v[36:39]\ns_waitcnt vmcnt(10)\n" \
    "v_mfma_f32_16x16x32_bf16 v[24:27], v[88:91], a[64:67], 0\nbuffer_load_dwordx4 v[84:87], v0, s[64:67], s85 offen offset:3072 sc1\n" \
    "v_mfma_f32_16x16x32_bf16 v[28:31], v[88:91], a[80:83], 0\ns_waitcnt vmcnt(10)\nv_mfma_f32_16x16x32_bf16 v[24:27], v[92:95], a[68:71], v[24:27]\n" \
    "buffer_load_dwordx4 v[88:91], v1, s[64:67], s85 offen offset:0 sc1\nv_mfma_f32_16x16x32_bf16 v[28:31], v[92:95], a[84:87], v[28:31]\nv_cvt_pk_bf16_f32 v189, v32, v33\n" \
    "v_cvt_pk_bf16_f32 v191, v34, v35\ns_waitcnt vmcnt(10)\nv_mfma_f32_16x16x32_bf16 v[24:27], v[96:99], a[72:75], v[24:27]\n" \
    "buffer_load_dwordx4 v[92:95], v1, s[64:67], s85 offen offset:1024 sc1\nv_mfma_f32_16x16x32_bf16 v[28:31], v[96:99], a[88:91], v[28:31]\nv_cvt_pk_bf16_f32 v221, v36, v37\n" \
    "v_cvt_pk_bf16_f32 v223, v38, v39\ns_waitcnt vmcnt(10)\nv_mfma_f32_16x16x32_bf16 v[24:27], v[100:103], a[76:79], v[24:27]\n" \
    "buffer_load_dwordx4 v[96:99], v1, s[64:67], s85 offen offset:2048 sc1\nv_mfma_f32_16x16x32_bf16 v[28:31], v[100:103], a[92:95], v[28:31]\ns_waitcnt vmcnt(10)\n" \
    "v_mfma_f32_16x16x32_bf16 v[32:35], v[104:107], a[64:67], 0\nbuffer_load_dwordx4 v[100:103], v1, s[64:67], s85 offen offset:3072 sc1\n" \
    "v_mfma_f32_16x16x32_bf16 v[36:39], v[104:107], a[80:83], 0\ns_waitcnt vmcnt(10)\nv_mfma_f32_16x16x32_bf16 v[32:35], v[108:111], a[68:71], v[32:35]\n" \
    "s_add_u32 s85, s84, 65536\nbuffer_load_dwordx4 v[104:107], v0, s[64:67], s85 offen offset:0 sc1\nv_mfma_f32_16x16x32_bf16 v[36:39], v[108:111], a[84:87], v[36:39]\n" \
    "v_cvt_pk_bf16_f32 v192, v24, v25\nv_cvt_pk_bf16_f32 v194, v26, v27\ns_waitcnt vmcnt(10)\nv_mfma_f32_16x16x32_bf16 v[32:35], v[112:115], a[72:75], v[32:35]\n" \
    "buffer_load_dwordx4 v[108:111], v0, s[64:67], s85 offen offset:1024 sc1\nv_mfma_f32_16x16x32_bf16 v[36:39], v[112:115], a[88:91], v[36:39]\n" \
    "v_cvt_pk_bf16_f32 v224, v28, v29\nv_cvt_pk_bf16_f32 v226, v30, v31\ns_waitcnt vmcnt(10)\nv_mfma_f32_16x16x32_bf16 v[32:35], v[116:119], a[76:79], v[32:35]\n" \
    "buffer_load_dwordx4 v[112:115], v0, s[64:67], s85 offen offset:2048 sc1\nv_mfma_f32_16x16x32_bf16 v[36:39], v[116:119], a[92:95], v[36:39]\ns_waitcnt vmcnt(10)\n" \
    "v_mfma_f32_16x16x32_bf16 v[24:27], v[72:75], a[96:99], 0\nbuffer_load_dwordx4 v[116:119], v0, s[64:67], s85 offen offset:3072 sc1\n" \
    "v_mfma_f32_16x16x32_bf16 v[28:31], v[72:75], a[112:115], 0\ns_waitcnt vmcnt(10)\nv_mfma_f32_16x16x32_bf16 v[24:27], v[76:79], a[100:103], v[24:27]\n" \
    "buffer_load_dwordx4 v[72:75], v1, s[64:67], s85 offen offset:0 sc1\nv_mfma_f32_16x16x32_bf16 v[28:31], v[76:79], a[116:119], v[28:31]\nv_cvt_pk_bf16_f32 v193, v32, v33\n" \
    "v_cvt_pk_bf16_f32 v195, v34, v35\ns_waitcnt vmcnt(10)\nv_mfma_f32_16x16x32_bf16 v[24:27], v[80:83], a[104:107], v[24:27]\n" \
    "buffer_load_dwordx4 v[76:79], v1, s[64:67], s85 offen offset:1024 sc1\nv_mfma_f32_16x16x32_bf16 v[28:31], v[80:83], a[120:123], v[28:31]\nv_cvt_pk_bf16_f32 v225, v36, v37\n" \
    "v_cvt_pk_bf16_f32 v227, v38, v39\ns_waitcnt vmcnt(10)\nv_mfma_f32_16x16x32_bf16 v[24:27], v[84:87], a[108:111], v[24:27]\n" \
    "buffer_load_dwordx4 v[80:83], v1, s[64:67], s85 offen offset:2048 sc1\nv_mfma_f32_16x16x32_bf16 v[28:31], v[84:87], a[124:127], v[28:31]\ns_waitcnt vmcnt(10)\n" \
    "v_mfma_f32_16x16x32_bf16 v[32:35], v[88:91], a[96:99], 0\nbuffer_load_dwordx4 v[84:87], v1, s[64:67], s85 offen offset:3072 sc1\n" \
    "v_mfma_f32_16x16x32_bf16 v[36:39], v[88:91], a[112:115], 0\ns_waitcnt vmcnt(10)\nv_mfma_f32_16x16x32_bf16 v[32:35], v[92:95], a[100:103], v[32:35]\n" \
    "s_add_u32 s85, s84, 81920\nbuffer_load_dwordx4 v[88:91], v0, s[64:67], s85 offen offset:0 sc1\nv_mfma_f32_16x16x32_bf16 v[36:39], v[92:95], a[116:119], v[36:39]\n" \
    "v_cvt_pk_bf16_f32 v196, v24, v25\nv_cvt_pk_bf16_f32 v198, v26, v27\ns_waitcnt vmcnt(10)\nv_mfma_f32_16x16x32_bf16 v[32:35], v[96:99], a[104:107], v[32:35]\n" \
    "buffer_load_dwordx4 v[92:95], v0, s[64:67], s85 offen offset:1024 sc1\nv_mfma_f32_16x16x32_bf16 v[36:39], v[96:99], a[120:123], v[36:39]\nv_cvt_pk_bf16_f32 v228, v28, v29\n" \
    "v_cvt_pk_bf16_f32 v230, v30, v31\ns_waitcnt vmcnt(10)\nv_mfma_f32_16x16x32_bf16 v[32:35], v[100:103], a[108:111], v[32:35]\n" \
    "buffer_load_dwordx4 v[96:99], v0, s[64:67], s85 offen offset:2048 sc1\nv_mfma_f32_16x16x32_bf16 v[36:39], v[100:103], a[124:127], v[36:39]\ns_waitcnt vmcnt(10)\n" \
    "v_mfma_f32_16x16x32_bf16 v[24:27], v[104:107], a[128:131], 0\nbuffer_load_dwordx4 v[100:103], v0, s[64:67], s85 offen offset:3072 sc1\n" \
    "v_mfma_f32_16x16x32_bf16 v[28:31], v[104:107], a[144:147], 0\ns_waitcnt vmcnt(10)\nv_mfma_f32_16x16x32_bf16 v[24:27], v[108:111], a[132:135], v[24:27]\n" \
    "buffer_load_dwordx4 v[104:107], v1, s[64:67], s85 offen offset:0 sc1\nv_mfma_f32_16x16x32_bf16 v[28:31], v[108:111], a[148:151], v[28:31]\n" \
    "v_cvt_pk_bf16_f32 v197, v32, v33\nv_cvt_pk_bf16_f32 v199, v34, v35\ns_waitcnt vmcnt(10)\nv_mfma_f32_16x16x32_bf16 v[24:27], v[112:115], a[136:139], v[24:27]\n" \
    "buffer_load_dwordx4 v[108:111], v1, s[64:67], s85 offen offset:1024 sc1\nv_mfma_f32_16x16x32_bf16 v[28:31], v[112:115], a[152:155], v[28:31]\n" \
    "v_cvt_pk_bf16_f32 v229, v36, v37\nv_cvt_pk_bf16_f32 v231, v38, v39\ns_waitcnt vmcnt(10)\nv_mfma_f32_16x16x32_bf16 v[24:27], v[116:119], a[140:143], v[24:27]\n" \
    "buffer_load_dwordx4 v[112:115], v1, s[64:67], s85 offen offset:2048 sc1\nv_mfma_f32_16x16x32_bf16 v[28:31], v[116:119], a[156:159], v[28:31]\ns_waitcnt vmcnt(10)\n" \
    "v_mfma_f32_16x16x32_bf16 v[32:35], v[72:75], a[128:131], 0\nbuffer_load_dwordx4 v[116:119], v1, s[64:67], s85 offen offset:3072 sc1\n" \
    "v_mfma_f32_16x16x32_bf16 v[36:39], v[72:75], a[144:147], 0\ns_waitcnt vmcnt(10)\nv_mfma_f32_16x16x32_bf16 v[32:35], v[76:79], a[132:135], v[32:35]\n" \
    "s_add_u32 s85, s84, 98304\nbuffer_load_dwordx4 v[72:75], v0, s[64:67], s85 offen offset:0 sc1\nv_mfma_f32_16x16x32_bf16 v[36:39], v[76:79], a[148:151], v[36:39]\n" \
    "v_cvt_pk_bf16_f32 v200, v24, v25\nv_cvt_pk_bf16_f32 v202, v26, v27\ns_waitcnt vmcnt(10)\nv_mfma_f32_16x16x32_bf16 v[32:35], v[80:83], a[136:139], v[32:35]\n" \
    "buffer_load_dwordx4 v[76:79], v0, s[64:67], s85 offen offset:1024 sc1\nv_mfma_f32_16x16x32_bf16 v[36:39], v[80:83], a[152:155], v[36:39]\nv_cvt_pk_bf16_f32 v232, v28, v29\n" \
    "v_cvt_pk_bf16_f32 v234, v30, v31\ns_waitcnt vmcnt(10)\nv_mfma_f32_16x16x32_bf16 v[32:35], v[84:87], a[140:143], v[32:35]\n" \
    "buffer_load_dwordx4 v[80:83], v0, s[64:67], s85 offen offset:2048 sc1\nv_mfma_f32_16x16x32_bf16 v[36:39], v[84:87], a[156:159], v[36:39]\ns_waitcnt vmcnt(10)\n" \
    "v_mfma_f32_16x16x32_bf16 v[24:27], v[88:91], a[160:163], 0\nbuffer_load_dwordx4 v[84:87], v0, s[64:67], s85 offen offset:3072 sc1\n" \
    "v_mfma_f32_16x16x32_bf16 v[28:31], v[88:91], a[176:179], 0\ns_waitcnt vmcnt(10)\nv_mfma_f32_16x16x32_bf16 v[24:27], v[92:95], a[164:167], v[24:27]\n" \
    "buffer_load_dwordx4 v[88:91], v1, s[64:67], s85 offen offset:0 sc1\nv_mfma_f32_16x16x32_bf16 v[28:31], v[92:95], a[180:183], v[28:31]\nv_cvt_pk_bf16_f32 v201, v32, v33\n" \
    "v_cvt_pk_bf16_f32 v203, v34, v35\ns_waitcnt vmcnt(10)\nv_mfma_f32_16x16x32_bf16 v[24:27], v[96:99], a[168:171], v[24:27]\n" \
    "buffer_load_dwordx4 v[92:95], v1, s[64:67], s85 offen offset:1024 sc1\nv_mfma_f32_16x16x32_bf16 v[28:31], v[96:99], a[184:187], v[28:31]\nv_cvt_pk_bf16_f32 v233, v36, v37\n" \
    "v_cvt_pk_bf16_f32 v235, v38, v39\ns_waitcnt vmcnt(10)\nv_mfma_f32_16x16x32_bf16 v[24:27], v[100:103], a[172:175], v[24:27]\n" \
    "buffer_load_dwordx4 v[96:99], v1, s[64:67], s85 offen offset:2048 sc1\nv_mfma_f32_16x16x32_bf16 v[28:31], v[100:103], a[188:191], v[28:31]\ns_waitcnt vmcnt(10)\n" \
    "v_mfma_f32_16x16x32_bf16 v[32:35], v[104:107], a[160:163], 0\nbuffer_load_dwordx4 v[100:103], v1, s[64:67], s85 offen offset:3072 sc1\n" \
    "v_mfma_f32_16x16x32_bf16 v[36:39], v[104:107], a[176:179], 0\ns_waitcnt vmcnt(10)\nv_mfma_f32_16x16x32_bf16 v[32:35], v[108:111], a[164:167], v[32:35]\n" \
    "s_add_u32 s85, s84, 114688\nbuffer_load_dwordx4 v[104:107], v0, s[64:67], s85 offen offset:0 sc1\nv_mfma_f32_16x16x32_bf16 v[36:39], v[108:111], a[180:183], v[36:39]\n" \
    "v_cvt_pk_bf16_f32 v204, v24, v25\nv_cvt_pk_bf16_f32 v206, v26, v27\ns_waitcnt vmcnt(10)\nv_mfma_f32_16x16x32_bf16 v[32:35], v[112:115], a[168:171], v[32:35]\n" \
    "buffer_load_dwordx4 v[108:111], v0, s[64:67], s85 offen offset:1024 sc1\nv_mfma_f32_16x16x32_bf16 v[36:39], v[112:115], a[184:187], v[36:39]\n" \
    "v_cvt_pk_bf16_f32 v236, v28, v29\nv_cvt_pk_bf16_f32 v238, v30, v31\ns_waitcnt vmcnt(10)\nv_mfma_f32_16x16x32_bf16 v[32:35], v[116:119], a[172:175], v[32:35]\n" \
    "buffer_load_dwordx4 v[112:115], v0, s[64:67], s85 offen offset:2048 sc1\nv_mfma_f32_16x16x32_bf16 v[36:39], v[116:119], a[188:191], v[36:39]\ns_waitcnt vmcnt(10)\n" \
    "v_mfma_f32_16x16x32_bf16 v[24:27], v[72:75], a[192:195], 0\nbuffer_load_dwordx4 v[116:119], v0, s[64:67], s85 offen offset:3072 sc1\n" \
    "v_mfma_f32_16x16x32_bf16 v[28:31], v[72:75], a[208:211], 0\ns_waitcnt vmcnt(10)\nv_mfma_f32_16x16x32_bf16 v[24:27], v[76:79], a[196:199], v[24:27]\n" \
    "buffer_load_dwordx4 v[72:75], v1, s[64:67], s85 offen offset:0 sc1\nv_mfma_f32_16x16x32_bf16 v[28:31], v[76:79], a[212:215], v[28:31]\nv_cvt_pk_bf16_f32 v205, v32, v33\n" \
    "v_cvt_pk_bf16_f32 v207, v34, v35\ns_waitcnt vmcnt(10)\nv_mfma_f32_16x16x32_bf16 v[24:27], v[80:83], a[200:203], v[24:27]\n" \
    "buffer_load_dwordx4 v[76:79], v1, s[64:67], s85 offen offset:1024 sc1\nv_mfma_f32_16x16x32_bf16 v[28:31], v[80:83], a[216:219], v[28:31]\nv_cvt_pk_bf16_f32 v237, v36, v37\n" \
    "v_cvt_pk_bf16_f32 v239, v38, v39\ns_waitcnt vmcnt(10)\nv_mfma_f32_16x16x32_bf16 v[24:27], v[84:87], a[204:207], v[24:27]\n" \
    "buffer_load_dwordx4 v[80:83], v1, s[64:67], s85 offen offset:2048 sc1\nv_mfma_f32_16x16x32_bf16 v[28:31], v[84:87], a[220:223], v[28:31]\ns_waitcnt vmcnt(10)\n" \
    "v_mfma_f32_16x16x32_bf16 v[32:35], v[88:91], a[192:195], 0\nbuffer_load_dwordx4 v[84:87], v1, s[64:67], s85 offen offset:3072 sc1\n" \
    "v_mfma_f32_16x16x32_bf16 v[36:39], v[88:91], a[208:211], 0\ns_waitcnt vmcnt(10)\nv_mfma_f32_16x16x32_bf16 v[32:35], v[92:95], a[196:199], v[32:35]\n" \
    "v_mfma_f32_16x16x32_bf16 v[36:39], v[92:95], a[212:215], v[36:39]\nv_cvt_pk_bf16_f32 v208, v24, v25\nv_cvt_pk_bf16_f32 v210, v26, v27\ns_waitcnt vmcnt(9)\n" \
    "v_mfma_f32_16x16x32_bf16 v[32:35], v[96:99], a[200:203], v[32:35]\nv_mfma_f32_16x16x32_bf16 v[36:39], v[96:99], a[216:219], v[36:39]\nv_cvt_pk_bf16_f32 v240, v28, v29\n" \
    "v_cvt_pk_bf16_f32 v242, v30, v31\ns_waitcnt vmcnt(8)\nv_mfma_f32_16x16x32_bf16 v[32:35], v[100:103], a[204:207], v[32:35]\n" \
    "v_mfma_f32_16x16x32_bf16 v[36:39], v[100:103], a[220:223], v[36:39]\ns_waitcnt vmcnt(7)\nv_mfma_f32_16x16x32_bf16 v[24:27], v[104:107], a[224:227], 0\n" \
    "v_mfma_f32_16x16x32_bf16 v[28:31], v[104:107], a[240:243], 0\ns_waitcnt vmcnt(6)\nv_mfma_f32_16x16x32_bf16 v[24:27], v[108:111], a[228:231], v[24:27]\n" \
    "v_mfma_f32_16x16x32_bf16 v[28:31], v[108:111], a[244:247], v[28:31]\ns_nop 0\nv_cvt_pk_bf16_f32 v209, v32, v33\nv_cvt_pk_bf16_f32 v211, v34, v35\ns_waitcnt vmcnt(5)\n" \
    "v_mfma_f32_16x16x32_bf16 v[24:27], v[112:115], a[232:235], v[24:27]\nv_mfma_f32_16x16x32_bf16 v[28:31], v[112:115], a[248:251], v[28:31]\nv_cvt_pk_bf16_f32 v241, v36, v37\n" \
    "v_cvt_pk_bf16_f32 v243, v38, v39\ns_waitcnt vmcnt(4)\nv_mfma_f32_16x16x32_bf16 v[24:27], v[116:119], a[236:239], v[24:27]\n" \
    "v_mfma_f32_16x16x32_bf16 v[28:31], v[116:119], a[252:255], v[28:31]\ns_waitcnt vmcnt(3)\nv_mfma_f32_16x16x32_bf16 v[32:35], v[72:75], a[224:227], 0\n" \
    "v_mfma_f32_16x16x32_bf16 v[36:39], v[72:75], a[240:243], 0\ns_waitcnt vmcnt(2)\nv_mfma_f32_16x16x32_bf16 v[32:35], v[76:79], a[228:231], v[32:35]\n" \
    "v_mfma_f32_16x16x32_bf16 v[36:39], v[76:79], a[244:247], v[36:39]\ns_nop 0\nv_cvt_pk_bf16_f32 v212, v24, v25\nv_cvt_pk_bf16_f32 v214, v26, v27\ns_waitcnt vmcnt(1)\n" \
    "v_mfma_f32_16x16x32_bf16 v[32:35], v[80:83], a[232:235], v[32:35]\nv_mfma_f32_16x16x32_bf16 v[36:39], v[80:83], a[248:251], v[36:39]\nv_cvt_pk_bf16_f32 v244, v28, v29\n" \
    "v_cvt_pk_bf16_f32 v246, v30, v31\ns_waitcnt vmcnt(0)\nv_mfma_f32_16x16x32_bf16 v[32:35], v[84:87], a[236:239], v[32:35]\n" \
    "v_mfma_f32_16x16x32_bf16 v[36:39], v[84:87], a[252:255], v[36:39]\ns_nop 6\nv_cvt_pk_bf16_f32 v213, v32, v33\nv_cvt_pk_bf16_f32 v215, v34, v35\n" \
    "v_cvt_pk_bf16_f32 v245, v36, v37\nv_cvt_pk_bf16_f32 v247, v38, v39\ns_add_u32 s50, s50, 2\ns_add_u32 s69, s50, 0\ns_and_b32 s69, s69, s41\ns_lshl_b32 s69, s69, 20\n" \
    "s_add_u32 s69, s69, s32\ns_add_u32 s74, s48, s69\ns_lshr_b32 s74, s74, 8\ns_xor_b32 s74, s74, s49\ns_and_b32 s74, s74, 0x2000\ns_add_u32 s74, s74, s69\n" \
    "s_add_u32 s69, s50, 0\ns_and_b32 s69, s69, s41\ns_lshl_b32 s69, s69, 19\ns_add_u32 s69, s69, s33\ns_add_u32 s87, s48, s69\ns_lshr_b32 s87, s87, 8\ns_xor_b32 s87, s87, s49\n" \
    "s_and_b32 s87, s87, 0x2000\ns_add_u32 s87, s87, s69\ns_add_u32 s69, s50, 1\ns_and_b32 s69, s69, s41\ns_lshl_b32 s69, s69, 18\ns_add_u32 s69, s69, s34\n" \
    "s_add_u32 s88, s48, s69\ns_lshr_b32 s88, s88, 8\ns_xor_b32 s88, s88, s49\ns_and_b32 s88, s88, 0x2000\ns_add_u32 s88, s88, s69\ns_xor_b32 s89, s88, 0x1000\n" \
    "s_mov_b32 s68, 0\ns_add_u32 s78, s87, s38\n.Lr46_%=:\ns_sub_u32 s70, s51, 52\n.Lr55_%=:\ns_sleep 1\ns_memrealtime s[72:73]\ns_waitcnt lgkmcnt(0)\ns_sub_u32 s72, s72, s70\n" \
    "s_cmp_lt_i32 s72, 0\ns_cbranch_scc1 .Lr55_%=\nbuffer_load_dword v20, off, s[64:67], s78 sc1\ns_waitcnt vmcnt(0)\nv_readfirstlane_b32 s70, v20\ns_memrealtime s[72:73]\n" \
    "s_cmp_eq_u32 s70, -1\ns_cbranch_scc1 .Lr47_%=\ns_lshr_b32 s71, s70, 8\ns_waitcnt lgkmcnt(0)\ns_sub_u32 s71, s72, s71\ns_and_b32 s71, s71, 0xffffff\n" \
    "s_sub_u32 s51, s72, s71\ns_add_u32 s51, s51, 176\ns_and_b32 s70, s70, 128\ns_lshl_b32 s52, s70, 5\ns_xor_b32 s87, s87, s52\ns_mov_b32 s68, 0\ns_sub_u32 s70, s51, s54\n" \
    ".Lr56_%=:\ns_memrealtime s[72:73]\ns_waitcnt lgkmcnt(0)\ns_sub_u32 s72, s72, s70\ns_cmp_lt_i32 s72, 0\ns_cbranch_scc1 .Lr56_%=\n" \
    "buffer_load_dwordx4 v[56:59], v0, s[64:67], s74 offen offset:0 sc1\nbuffer_load_dwordx4 v[60:63], v0, s[64:67], s74 offen offset:1024 sc1\ns_add_u32 s69, s74, 0x4000\n" \
    "buffer_load_dwordx4 v[64:67], v0, s[64:67], s69 offen offset:0 sc1\nbuffer_load_dwordx4 v[68:71], v0, s[64:67], s69 offen offset:1024 sc1\n.Lr57_%=:\n" \
    "s_memrealtime s[72:73]\ns_waitcnt lgkmcnt(0)\ns_sub_u32 s72, s72, s51\ns_cmp_lt_i32 s72, -3\ns_cbranch_scc1 .Lr57_%=\ns_cmp_ge_i32 s72, 0\ns_cbranch_scc1 .Lr58_%=\n" \
    "s_nop 2\ns_cmp_ge_i32 s72, -1\ns_cbranch_scc1 .Lr58_%=\ns_nop 2\ns_cmp_ge_i32 s72, -2\ns_cbranch_scc1 .Lr58_%=\ns_nop 2\n.Lr58_%=:\n" \
    "buffer_load_dwordx4 v[100:103], v0, s[64:67], s87 offen offset:0 sc1\nbuffer_load_dwordx4 v[104:107], v0, s[64:67], s87 offen offset:1024 sc1\n" \
    "buffer_load_dwordx4 v[108:111], v0, s[64:67], s87 offen offset:2048 sc1\nbuffer_load_dwordx4 v[112:115], v0, s[64:67], s87 offen offset:3072 sc1\n" \
    "buffer_load_dwordx2 v[116:117], v5, s[64:67], s87 offen offset:0 sc1\nbuffer_load_dwordx2 v[118:119], v5, s[64:67], s87 offen offset:128 sc1\n.Lr50_%=:\n" \
    "v_mov_b64 v[24:25], 0\nv_mov_b64 v[26:27], 0\nv_mov_b64 v[28:29], 0\nv_mov_b64 v[30:31], 0\ns_waitcnt vmcnt(6)\nv_max3_u32 v22, v56, v57, v58\n" \
    "v_max3_u32 v22, v22, v59, v60\nv_max3_u32 v22, v22, v61, v62\nv_max3_u32 v22, v22, v63, v64\nv_max3_u32 v22, v22, v65, v66\nv_max3_u32 v22, v22, v67, v68\n" \
    "v_max3_u32 v22, v22, v69, v70\nv_max_u32 v22, v22, v71\ns_waitcnt vmcnt(5)\nv_smfmac_f32_16x16x64_bf16 v[24:27], v[100:103], v[184:191], v4\n" \
    "v_max3_u32 v22, v22, v100, v101\nv_smfmac_f32_16x16x64_bf16 v[28:31], v[100:103], v[216:223], v4\nv_max3_u32 v22, v22, v102, v103\ns_waitcnt vmcnt(4)\n" \
    "v_smfmac_f32_16x16x64_bf16 v[24:27], v[104:107], v[192:199], v4\nv_max3_u32 v22, v22, v104, v105\nv_smfmac_f32_16x16x64_bf16 v[28:31], v[104:107], v[224:231], v4\n" \
    "v_max3_u32 v22, v22, v106, v107\ns_waitcnt vmcnt(3)\nv_smfmac_f32_16x16x64_bf16 v[24:27], v[108:111], v[200:207], v4\nv_max3_u32 v22, v22, v108, v109\n" \
    "v_smfmac_f32_16x16x64_bf16 v[28:31], v[108:111], v[232:239], v4\nv_max3_u32 v22, v22, v110, v111\ns_waitcnt vmcnt(2)\n" \
    "v_smfmac_f32_16x16x64_bf16 v[24:27], v[112:115], v[208:215], v4\nv_max3_u32 v22, v22, v112, v113\nv_smfmac_f32_16x16x64_bf16 v[28:31], v[112:115], v[240:247], v4\n" \
    "v_max3_u32 v22, v22, v114, v115\ns_waitcnt vmcnt(0)\nv_smfmac_f32_16x16x64_bf16 v[24:27], v[116:119], v[56:63], v4\nv_max3_u32 v22, v22, v116, v117\n" \
    "v_smfmac_f32_16x16x64_bf16 v[28:31], v[116:119], v[64:71], v4\nv_max3_u32 v22, v22, v118, v119\nv_cmp_eq_u32 vcc, -1, v22\ns_nop 5\nv_permlane32_swap_b32 v24, v28\n" \
    "v_permlane32_swap_b32 v25, v29\nv_permlane32_swap_b32 v26, v30\nv_permlane32_swap_b32 v27, v31\nv_pk_add_f32 v[24:25], v[24:25], v[28:29]\n" \
    "v_pk_add_f32 v[26:27], v[26:27], v[30:31]\nds_write2_b32 v6, v24, v25 offset0:0 offset1:36\nds_write2_b32 v6, v26, v27 offset0:72 offset1:108\ns_cbranch_vccnz .Lr51_%=\n" \
    "s_waitcnt lgkmcnt(0)\ns_barrier\nds_read_b128 v[24:27], v7 offset:0\nds_read_b128 v[28:31], v7 offset:1152\nds_read_b128 v[32:35], v7 offset:2304\n" \
    "ds_read_b128 v[36:39], v7 offset:3456\ns_waitcnt lgkmcnt(2)\nv_pk_add_f32 v[24:25], v[24:25], v[28:29]\nv_pk_add_f32 v[26:27], v[26:27], v[30:31]\ns_waitcnt lgkmcnt(0)\n" \
    "v_pk_add_f32 v[32:33], v[32:33], v[36:37]\nv_pk_add_f32 v[34:35], v[34:35], v[38:39]\nv_pk_add_f32 v[24:25], v[24:25], v[32:33]\nv_pk_add_f32 v[26:27], v[26:27], v[34:35]\n" \
    "v_cvt_pk_bf16_f32 v248, v24, v25\nv_cvt_pk_bf16_f32 v249, v26, v27\ns_nop 0\nv_permlane32_swap_b32 v248, v250\nv_permlane32_swap_b32 v249, v251\ns_mov_b32 exec_hi, 0\n" \
    "buffer_store_dwordx4 v[248:251], v0, s[64:67], s88 offen sc1\nbuffer_store_dwordx4 v[248:251], v0, s[64:67], s89 offen sc1\ns_mov_b32 exec_hi, -1\ns_add_u32 s51, s51, s55\n" \
    "s_add_u32 s51, s51, s55\ns_mov_b32 m0, s59\ns_mov_b32 s69, s58\nbuffer_load_dwordx4 v2, s[60:63], s69 offen lds\ns_add_u32 m0, m0, 0x400\ns_add_u32 s69, s69, 32768\n" \
    "buffer_load_dwordx4 v2, s[60:63], s69 offen lds\ns_add_u32 m0, m0, 0x400\ns_add_u32 s69, s69, 98304\nbuffer_load_dwordx4 v2, s[60:63], s69 offen lds\n" \
    "s_add_u32 m0, m0, 0x400\ns_add_u32 s69, s69, 32768\nbuffer_load_dwordx4 v2, s[60:63], s69 offen lds\ns_add_u32 m0, m0, 0x400\ns_add_u32 s69, s69, 98304\n" \
    "buffer_load_dwordx4 v2, s[60:63], s69 offen lds\ns_add_u32 m0, m0, 0x400\ns_add_u32 s69, s69, 32768\nbuffer_load_dwordx4 v2, s[60:63], s69 offen lds\n" \
    "s_add_u32 m0, m0, 0x400\ns_add_u32 s69, s69, 98304\nbuffer_load_dwordx4 v2, s[60:63], s69 offen lds\ns_add_u32 m0, m0, 0x400\ns_add_u32 s69, s69, 32768\n" \
    "buffer_load_dwordx4 v2, s[60:63], s69 offen lds\ns_add_u32 m0, m0, 0x400\ns_add_u32 s69, s69, 98304\nbuffer_load_dwordx4 v2, s[60:63], s69 offen lds\n" \
    "s_add_u32 m0, m0, 0x400\ns_add_u32 s69, s69, 32768\nbuffer_load_dwordx4 v2, s[60:63], s69 offen lds\ns_add_u32 m0, m0, 0x400\ns_add_u32 s69, s69, 98304\n" \
    "buffer_load_dwordx4 v2, s[60:63], s69 offen lds\ns_add_u32 m0, m0, 0x400\ns_add_u32 s69, s69, 32768\nbuffer_load_dwordx4 v2, s[60:63], s69 offen lds\n" \
    "s_add_u32 m0, m0, 0x400\ns_add_u32 s69, s69, 98304\nbuffer_load_dwordx4 v2, s[60:63], s69 offen lds\ns_add_u32 m0, m0, 0x400\ns_add_u32 s69, s69, 32768\n" \
    "buffer_load_dwordx4 v2, s[60:63], s69 offen lds\ns_add_u32 m0, m0, 0x400\ns_add_u32 s69, s69, 98304\nbuffer_load_dwordx4 v2, s[60:63], s69 offen lds\n" \
    "s_add_u32 m0, m0, 0x400\ns_add_u32 s69, s69, 32768\nbuffer_load_dwordx4 v2, s[60:63], s69 offen lds\ns_add_u32 m0, m0, 0x400\ns_add_u32 s69, s69, 98304\ns_mov_b32 s68, 0\n" \
    "v_mov_b32 v18, v11\n.Lr3_%=:\nbuffer_load_dwordx4 v[72:75], v18, s[64:67], s42 offen sc1\ns_waitcnt vmcnt(0)\nv_max3_u32 v19, v72, v73, v74\nv_max_u32 v19, v19, v75\n" \
    "v_cmp_eq_u32 vcc, -1, v19\ns_cbranch_vccnz .Lr4_%=\nds_write_b128 v16, v[72:75]\ns_mov_b64 exec, 0xff\nv_mov_b32 v8, s39\ns_mov_b64 exec, -1\ns_add_u32 s69, s50, -2\n" \
    "s_lshl_b32 s69, s69, 20\ns_add_u32 s69, s69, s36\ns_add_u32 s75, s48, s69\ns_lshr_b32 s75, s75, 8\ns_xor_b32 s75, s75, s49\ns_and_b32 s75, s75, 0x2000\n" \
    "s_add_u32 s75, s75, s69\ns_add_u32 s76, s75, 0x4000\ns_add_u32 s77, s75, s37\ns_add_u32 s82, s50, -2\ns_lshl_b32 s80, s82, 10\ns_lshl_b32 s82, s82, 2\n" \
    "s_add_u32 s82, s82, s40\ns_add_u32 s69, s50, 2\ns_and_b32 s69, s69, s41\ns_lshl_b32 s69, s69, 19\ns_add_u32 s69, s69, s35\ns_add_u32 s84, s48, s69\ns_lshr_b32 s84, s84, 8\n" \
    "s_xor_b32 s84, s84, s49\ns_and_b32 s84, s84, 0x2000\ns_add_u32 s84, s84, s69\ns_mov_b32 s86, 0\ns_branch .Lr9_%=\n.Lr5_%=:\ns_mov_b32 s68, 0\ns_add_u32 s78, s87, s38\n" \
    ".Lr62_%=:\ns_sub_u32 s70, s51, 52\n.Lr71_%=:\ns_sleep 1\ns_memrealtime s[72:73]\ns_waitcnt lgkmcnt(0)\ns_sub_u32 s72, s72, s70\ns_cmp_lt_i32 s72, 0\n" \
    "s_cbranch_scc1 .Lr71_%=\nbuffer_load_dword v20, off, s[64:67], s78 sc1\ns_waitcnt vmcnt(0)\nv_readfirstlane_b32 s70, v20\ns_memrealtime s[72:73]\ns_cmp_eq_u32 s70, -1\n" \
    "s_cbranch_scc1 .Lr63_%=\ns_lshr_b32 s71, s70, 8\ns_waitcnt lgkmcnt(0)\ns_sub_u32 s71, s72, s71\ns_and_b32 s71, s71, 0xffffff\ns_sub_u32 s51, s72, s71\n" \
    "s_add_u32 s51, s51, 176\ns_and_b32 s70, s70, 128\ns_lshl_b32 s52, s70, 5\ns_xor_b32 s87, s87, s52\ns_mov_b32 s68, 0\ns_sub_u32 s70, s51, s54\n.Lr72_%=:\n" \
    "s_memrealtime s[72:73]\ns_waitcnt lgkmcnt(0)\ns_sub_u32 s72, s72, s70\ns_cmp_lt_i32 s72, 0\ns_cbranch_scc1 .Lr72_%=\n" \
    "buffer_load_dwordx4 v[56:59], v0, s[64:67], s74 offen offset:0 sc1\nbuffer_load_dwordx4 v[60:63], v0, s[64:67], s74 offen offset:1024 sc1\ns_add_u32 s69, s74, 0x4000\n" \
    "buffer_load_dwordx4 v[64:67], v0, s[64:67], s69 offen offset:0 sc1\nbuffer_load_dwordx4 v[68:71], v0, s[64:67], s69 offen offset:1024 sc1\n.Lr73_%=:\n" \
    "s_memrealtime s[72:73]\ns_waitcnt lgkmcnt(0)\ns_sub_u32 s72, s72, s51\ns_cmp_lt_i32 s72, -3\ns_cbranch_scc1 .Lr73_%=\ns_cmp_ge_i32 s72, 0\ns_cbranch_scc1 .Lr74_%=\n" \
    "s_nop 2\ns_cmp_ge_i32 s72, -1\ns_cbranch_scc1 .Lr74_%=\ns_nop 2\ns_cmp_ge_i32 s72, -2\ns_cbranch_scc1 .Lr74_%=\ns_nop 2\n.Lr74_%=:\n" \
    "buffer_load_dwordx4 v[100:103], v0, s[64:67], s87 offen offset:0 sc1\nbuffer_load_dwordx4 v[104:107], v0, s[64:67], s87 offen offset:1024 sc1\n" \
    "buffer_load_dwordx4 v[108:111], v0, s[64:67], s87 offen offset:2048 sc1\nbuffer_load_dwordx4 v[112:115], v0, s[64:67], s87 offen offset:3072 sc1\n" \
    "buffer_load_dwordx2 v[116:117], v5, s[64:67], s87 offen offset:0 sc1\nbuffer_load_dwordx2 v[118:119], v5, s[64:67], s87 offen offset:128 sc1\n.Lr66_%=:\n" \
    "v_mov_b64 v[24:25], 0\nv_mov_b64 v[26:27], 0\nv_mov_b64 v[28:29], 0\nv_mov_b64 v[30:31], 0\ns_waitcnt vmcnt(6)\nv_max3_u32 v22, v56, v57, v58\n" \
    "v_max3_u32 v22, v22, v59, v60\nv_max3_u32 v22, v22, v61, v62\nv_max3_u32 v22, v22, v63, v64\nv_max3_u32 v22, v22, v65, v66\nv_max3_u32 v22, v22, v67, v68\n" \
    "v_max3_u32 v22, v22, v69, v70\nv_max_u32 v22, v22, v71\ns_waitcnt vmcnt(5)\nv_smfmac_f32_16x16x64_bf16 v[24:27], v[100:103], v[184:191], v4\n" \
    "v_max3_u32 v22, v22, v100, v101\nv_smfmac_f32_16x16x64_bf16 v[28:31], v[100:103], v[216:223], v4\nv_max3_u32 v22, v22, v102, v103\n" \
    "buffer_load_dwordx4 v[100:103], v19, s[64:67], s75 offen offset:3072 sc1\ns_waitcnt vmcnt(5)\nv_smfmac_f32_16x16x64_bf16 v[24:27], v[104:107], v[192:199], v4\n" \
    "v_max3_u32 v22, v22, v104, v105\nv_smfmac_f32_16x16x64_bf16 v[28:31], v[104:107], v[224:231], v4\nv_max3_u32 v22, v22, v106, v107\n" \
    "buffer_load_dwordx4 v[104:107], v18, s[64:67], s76 offen offset:0 sc1\ns_waitcnt vmcnt(5)\nv_smfmac_f32_16x16x64_bf16 v[24:27], v[108:111], v[200:207], v4\n" \
    "v_max3_u32 v22, v22, v108, v109\nv_smfmac_f32_16x16x64_bf16 v[28:31], v[108:111], v[232:239], v4\nv_max3_u32 v22, v22, v110, v111\ns_waitcnt vmcnt(4)\n" \
    "v_smfmac_f32_16x16x64_bf16 v[24:27], v[112:115], v[208:215], v4\nv_max3_u32 v22, v22, v112, v113\nv_smfmac_f32_16x16x64_bf16 v[28:31], v[112:115], v[240:247], v4\n" \
    "v_max3_u32 v22, v22, v114, v115\ns_waitcnt vmcnt(2)\nv_smfmac_f32_16x16x64_bf16 v[24:27], v[116:119], v[56:63], v4\nv_max3_u32 v22, v22, v116, v117\n" \
    "v_smfmac_f32_16x16x64_bf16 v[28:31], v[116:119], v[64:71], v4\nv_max3_u32 v22, v22, v118, v119\nbuffer_load_dwordx4 v[108:111], v18, s[64:67], s76 offen offset:1024 sc1\n" \
    "v_cmp_eq_u32 vcc, -1, v22\ns_nop 4\nv_permlane32_swap_b32 v24, v28\nv_permlane32_swap_b32 v25, v29\nv_permlane32_swap_b32 v26, v30\nv_permlane32_swap_b32 v27, v31\n" \
    "v_pk_add_f32 v[24:25], v[24:25], v[28:29]\nv_pk_add_f32 v[26:27], v[26:27], v[30:31]\nds_write2_b32 v6, v24, v25 offset0:0 offset1:36\n" \
    "ds_write2_b32 v6, v26, v27 offset0:72 offset1:108\ns_cbranch_vccnz .Lr67_%=\nbuffer_load_dwordx4 v[112:115], v18, s[64:67], s76 offen offset:2048 sc1\n" \
    "s_waitcnt lgkmcnt(0)\ns_barrier\nds_read_b128 v[24:27], v7 offset:0\nds_read_b128 v[28:31], v7 offset:1152\nds_read_b128 v[32:35], v7 offset:2304\n" \
    "ds_read_b128 v[36:39], v7 offset:3456\ns_waitcnt lgkmcnt(2)\nv_pk_add_f32 v[24:25], v[24:25], v[28:29]\nv_pk_add_f32 v[26:27], v[26:27], v[30:31]\ns_waitcnt lgkmcnt(0)\n" \
    "v_pk_add_f32 v[32:33], v[32:33], v[36:37]\nv_pk_add_f32 v[34:35], v[34:35], v[38:39]\nv_pk_add_f32 v[24:25], v[24:25], v[32:33]\nv_pk_add_f32 v[26:27], v[26:27], v[34:35]\n" \
    "v_cvt_pk_bf16_f32 v248, v24, v25\nv_cvt_pk_bf16_f32 v249, v26, v27\ns_nop 0\nv_permlane32_swap_b32 v248, v250\nv_permlane32_swap_b32 v249, v251\ns_mov_b32 exec_hi, 0\n" \
    "buffer_store_dwordx4 v[248:251], v0, s[64:67], s88 offen sc1\nbuffer_store_dwordx4 v[248:251], v0, s[64:67], s89 offen sc1\ns_mov_b32 exec_hi, -1\ns_add_u32 s51, s51, s55\n" \
    "s_add_u32 s51, s51, s55\n.Lr9_%=:\ns_mov_b32 s68, 0\ns_cmp_eq_u32 s86, 1\ns_cbranch_scc1 .Lr79_%=\ns_add_u32 s70, s50, 2\ns_add_u32 s71, s50, -2\ns_mov_b32 s68, 0\n" \
    "s_branch .Lr86_%=\n.Lr85_%=:\nbuffer_load_dword v17, v8, s[64:67], 0 offen sc1\ns_waitcnt vmcnt(0)\n.Lr86_%=:\nv_cmp_lt_i32 vcc, s70, v17\ns_nop 3\n" \
    "s_and_b32 s69, vcc_lo, 0xff\ns_cmp_eq_u32 s69, 0xff\ns_cbranch_scc0 .Lr87_%=\nv_readlane_b32 s69, v17, 9\ns_cmp_gt_i32 s69, s71\ns_cbranch_scc0 .Lr87_%=\ns_mov_b32 s68, 0\n" \
    ".Lr78_%=:\nbuffer_load_dwordx4 v[72:75], v0, s[64:67], s75 offen offset:0 sc1\nbuffer_load_dwordx4 v[76:79], v0, s[64:67], s75 offen offset:1024 sc1\n" \
    "buffer_load_dwordx4 v[80:83], v0, s[64:67], s75 offen offset:2048 sc1\nbuffer_load_dwordx4 v[84:87], v0, s[64:67], s75 offen offset:3072 sc1\n" \
    "buffer_load_dwordx4 v[88:91], v1, s[64:67], s75 offen offset:0 sc1\nbuffer_load_dwordx4 v[92:95], v1, s[64:67], s75 offen offset:1024 sc1\n" \
    "buffer_load_dwordx4 v[96:99], v1, s[64:67], s75 offen offset:2048 sc1\nbuffer_load_dwordx4 v[100:103], v1, s[64:67], s75 offen offset:3072 sc1\n" \
    "buffer_load_dwordx4 v[104:107], v0, s[64:67], s76 offen offset:0 sc1\nbuffer_load_dwordx4 v[108:111], v0, s[64:67], s76 offen offset:1024 sc1\n" \
    "buffer_load_dwordx4 v[112:115], v0, s[64:67], s76 offen offset:2048 sc1\n.Lr79_%=:\nv_mov_b32 v22, 0\ns_waitcnt vmcnt(10)\n" \
    "v_mfma_f32_16x16x32_bf16 v[24:27], v[120:123], v[72:75], 0\nbuffer_load_dwordx4 v[116:119], v0, s[64:67], s76 offen offset:3072 sc1\n" \
    "v_mfma_f32_16x16x32_bf16 v[28:31], v[152:155], v[72:75], 0\nv_max3_u32 v22, v22, v72, v73\nv_max3_u32 v22, v22, v74, v75\ns_waitcnt vmcnt(10)\n" \
    "v_mfma_f32_16x16x32_bf16 v[32:35], v[120:123], v[76:79], 0\nbuffer_load_dwordx4 v[72:75], v1, s[64:67], s76 offen offset:0 sc1\n" \
    "v_mfma_f32_16x16x32_bf16 v[36:39], v[152:155], v[76:79], 0\nv_max3_u32 v22, v22, v76, v77\nv_max3_u32 v22, v22, v78, v79\ns_waitcnt vmcnt(10)\n" \
    "v_mfma_f32_16x16x32_bf16 v[24:27], v[124:127], v[80:83], v[24:27]\nbuffer_load_dwordx4 v[76:79], v1, s[64:67], s76 offen offset:1024 sc1\n" \
    "v_mfma_f32_16x16x32_bf16 v[28:31], v[156:159], v[80:83], v[28:31]\nv_max3_u32 v22, v22, v80, v81\nv_max3_u32 v22, v22, v82, v83\ns_waitcnt vmcnt(10)\n" \
    "v_mfma_f32_16x16x32_bf16 v[32:35], v[124:127], v[84:87], v[32:35]\nbuffer_load_dwordx4 v[80:83], v1, s[64:67], s76 offen offset:2048 sc1\n" \
    "v_mfma_f32_16x16x32_bf16 v[36:39], v[156:159], v[84:87], v[36:39]\nv_max3_u32 v22, v22, v84, v85\nv_max3_u32 v22, v22, v86, v87\ns_waitcnt vmcnt(10)\n" \
    "v_mfma_f32_16x16x32_bf16 v[24:27], v[128:131], v[88:91], v[24:27]\nbuffer_load_dwordx4 v[84:87], v1, s[64:67], s76 offen offset:3072 sc1\n" \
    "v_mfma_f32_16x16x32_bf16 v[28:31], v[160:163], v[88:91], v[28:31]\nv_max3_u32 v22, v22, v88, v89\nv_max3_u32 v22, v22, v90, v91\ns_waitcnt vmcnt(10)\n" \
    "v_mfma_f32_16x16x32_bf16 v[32:35], v[128:131], v[92:95], v[32:35]\nbuffer_load_dwordx4 v[88:91], v0, s[64:67], s77 offen offset:0 sc1\n" \
    "v_mfma_f32_16x16x32_bf16 v[36:39], v[160:163], v[92:95], v[36:39]\nv_max3_u32 v22, v22, v92, v93\nv_max3_u32 v22, v22, v94, v95\ns_waitcnt vmcnt(10)\n" \
    "v_mfma_f32_16x16x32_bf16 v[24:27], v[132:135], v[96:99], v[24:27]\nbuffer_load_dwordx4 v[92:95], v0, s[64:67], s77 offen offset:1024 sc1\n" \
    "v_mfma_f32_16x16x32_bf16 v[28:31], v[164:167], v[96:99], v[28:31]\nv_max3_u32 v22, v22, v96, v97\nv_max3_u32 v22, v22, v98, v99\ns_waitcnt vmcnt(10)\n" \
    "v_mfma_f32_16x16x32_bf16 v[32:35], v[132:135], v[100:103], v[32:35]\nbuffer_load_dwordx4 v[96:99], v0, s[64:67], s77 offen offset:2048 sc1\n" \
    "v_mfma_f32_16x16x32_bf16 v[36:39], v[164:167], v[100:103], v[36:39]\nv_max3_u32 v22, v22, v100, v101\nv_max3_u32 v22, v22, v102, v103\ns_waitcnt vmcnt(10)\n" \
    "v_mfma_f32_16x16x32_bf16 v[24:27], v[136:139], v[104:107], v[24:27]\nbuffer_load_dwordx4 v[100:103], v0, s[64:67], s77 offen offset:3072 sc1\n" \
    "v_mfma_f32_16x16x32_bf16 v[28:31], v[168:171], v[104:107], v[28:31]\nv_max3_u32 v22, v22, v104, v105\nv_max3_u32 v22, v22, v106, v107\ns_waitcnt vmcnt(10)\n" \
    "v_mfma_f32_16x16x32_bf16 v[32:35], v[136:139], v[108:111], v[32:35]\ns_add_u32 s85, s84, 0\nbuffer_load_dwordx4 v[104:107], v0, s[64:67], s85 offen offset:0 sc1\n" \
    "v_mfma_f32_16x16x32_bf16 v[36:39], v[168:171], v[108:111], v[36:39]\nv_max3_u32 v22, v22, v108, v109\nv_max3_u32 v22, v22, v110, v111\ns_waitcnt vmcnt(10)\n" \
    "v_mfma_f32_16x16x32_bf16 v[24:27], v[140:143], v[112:115], v[24:27]\nbuffer_load_dwordx4 v[108:111], v0, s[64:67], s85 offen offset:1024 sc1\n" \
    "v_mfma_f32_16x16x32_bf16 v[28:31], v[172:175], v[112:115], v[28:31]\nv_max3_u32 v22, v22, v112, v113\nv_max3_u32 v22, v22, v114, v115\ns_waitcnt vmcnt(10)\n" \
    "v_mfma_f32_16x16x32_bf16 v[32:35], v[140:143], v[116:119], v[32:35]\nbuffer_load_dwordx4 v[112:115], v0, s[64:67], s85 offen offset:2048 sc1\n" \
    "v_mfma_f32_16x16x32_bf16 v[36:39], v[172:175], v[116:119], v[36:39]\nv_max3_u32 v22, v22, v116, v117\nv_max3_u32 v22, v22, v118, v119\ns_waitcnt vmcnt(10)\n" \
    "v_mfma_f32_16x16x32_bf16 v[24:27], v[144:147], v[72:75], v[24:27]\nbuffer_load_dwordx4 v[116:119], v0, s[64:67], s85 offen offset:3072 sc1\n" \
    "v_mfma_f32_16x16x32_bf16 v[28:31], v[176:179], v[72:75], v[28:31]\nv_max3_u32 v22, v22, v72, v73\nv_max3_u32 v22, v22, v74, v75\ns_waitcnt vmcnt(10)\n" \
    "v_mfma_f32_16x16x32_bf16 v[32:35], v[144:147], v[76:79], v[32:35]\nbuffer_load_dwordx4 v[72:75], v1, s[64:67], s85 offen offset:0 sc1\n" \
    "v_mfma_f32_16x16x32_bf16 v[36:39], v[176:179], v[76:79], v[36:39]\nv_max3_u32 v22, v22, v76, v77\nv_max3_u32 v22, v22, v78, v79\ns_waitcnt vmcnt(10)\n" \
    "v_mfma_f32_16x16x32_bf16 v[24:27], v[148:151], v[80:83], v[24:27]\nbuffer_load_dwordx4 v[76:79], v1, s[64:67], s85 offen offset:1024 sc1\n" \
    "v_mfma_f32_16x16x32_bf16 v[28:31], v[180:183], v[80:83], v[28:31]\nv_max3_u32 v22, v22, v80, v81\nv_max3_u32 v22, v22, v82, v83\ns_waitcnt vmcnt(10)\n" \
    "v_mfma_f32_16x16x32_bf16 v[32:35], v[148:151], v[84:87], v[32:35]\nbuffer_load_dwordx4 v[80:83], v1, s[64:67], s85 offen offset:2048 sc1\n" \
    "v_mfma_f32_16x16x32_bf16 v[36:39], v[180:183], v[84:87], v[36:39]\nv_max3_u32 v22, v22, v84, v85\nv_max3_u32 v22, v22, v86, v87\ns_waitcnt vmcnt(10)\n" \
    "v_mfma_f32_16x16x32_bf16 v[24:27], v[40:43], v[88:91], v[24:27]\nbuffer_load_dwordx4 v[84:87], v1, s[64:67], s85 offen offset:3072 sc1\n" \
    "v_mfma_f32_16x16x32_bf16 v[28:31], v[48:51], v[88:91], v[28:31]\nv_max3_u32 v22, v22, v88, v89\nv_max3_u32 v22, v22, v90, v91\ns_waitcnt vmcnt(10)\n" \
    "v_mfma_f32_16x16x32_bf16 v[32:35], v[40:43], v[92:95], v[32:35]\ns_add_u32 s85, s84, 16384\nbuffer_load_dwordx4 v[88:91], v0, s[64:67], s85 offen offset:0 sc1\n" \
    "v_mfma_f32_16x16x32_bf16 v[36:39], v[48:51], v[92:95], v[36:39]\nv_max3_u32 v22, v22, v92, v93\nv_max3_u32 v22, v22, v94, v95\ns_waitcnt vmcnt(10)\n" \
    "v_mfma_f32_16x16x32_bf16 v[24:27], v[44:47], v[96:99], v[24:27]\nbuffer_load_dwordx4 v[92:95], v0, s[64:67], s85 offen offset:1024 sc1\n" \
    "v_mfma_f32_16x16x32_bf16 v[28:31], v[52:55], v[96:99], v[28:31]\nv_max3_u32 v22, v22, v96, v97\nv_max3_u32 v22, v22, v98, v99\ns_waitcnt vmcnt(10)\n" \
    "v_mfma_f32_16x16x32_bf16 v[32:35], v[44:47], v[100:103], v[32:35]\nbuffer_load_dwordx4 v[96:99], v0, s[64:67], s85 offen offset:2048 sc1\n" \
    "v_mfma_f32_16x16x32_bf16 v[36:39], v[52:55], v[100:103], v[36:39]\nv_max3_u32 v22, v22, v100, v101\nv_max3_u32 v22, v22, v102, v103\nv_cmp_eq_u32 vcc, -1, v22\n" \
    "s_cbranch_vccnz .Lr80_%=\nds_write_b128 v9, v[24:27] offset:0\nds_write_b128 v9, v[28:31] offset:64\ns_waitcnt vmcnt(10)\n" \
    "v_mfma_f32_16x16x32_bf16 v[24:27], v[104:107], a[0:3], 0\nbuffer_load_dwordx4 v[100:103], v0, s[64:67], s85 offen offset:3072 sc1\n" \
    "v_mfma_f32_16x16x32_bf16 v[28:31], v[104:107], a[16:19], 0\nds_write_b128 v9, v[32:35] offset:2304\nds_write_b128 v9, v[36:39] offset:2368\ns_waitcnt vmcnt(10)\n" \
    "v_mfma_f32_16x16x32_bf16 v[24:27], v[108:111], a[4:7], v[24:27]\nbuffer_load_dwordx4 v[104:107], v1, s[64:67], s85 offen offset:0 sc1\n" \
    "v_mfma_f32_16x16x32_bf16 v[28:31], v[108:111], a[20:23], v[28:31]\nv_add_u32 v252, s80, v3\nds_read_b64 v[252:253], v252\nds_read_b128 v[18:21], v16\ns_waitcnt vmcnt(10)\n" \
    "v_mfma_f32_16x16x32_bf16 v[24:27], v[112:115], a[8:11], v[24:27]\nbuffer_load_dwordx4 v[108:111], v1, s[64:67], s85 offen offset:1024 sc1\n" \
    "v_mfma_f32_16x16x32_bf16 v[28:31], v[112:115], a[24:27], v[28:31]\ns_waitcnt lgkmcnt(0)\ns_barrier\nds_read_b128 v[40:43], v10 offset:0\n" \
    "ds_read_b128 v[44:47], v10 offset:4608\ns_waitcnt vmcnt(10)\nv_mfma_f32_16x16x32_bf16 v[24:27], v[116:119], a[12:15], v[24:27]\n" \
    "buffer_load_dwordx4 v[112:115], v1, s[64:67], s85 offen offset:2048 sc1\nv_mfma_f32_16x16x32_bf16 v[28:31], v[116:119], a[28:31], v[28:31]\n" \
    "ds_read_b128 v[48:51], v10 offset:9216\nds_read_b128 v[52:55], v10 offset:13824\nv_lshlrev_b32 v248, 16, v252\nv_and_b32 v249, 0xffff0000, v252\n" \
    "v_lshlrev_b32 v250, 16, v253\ns_waitcnt vmcnt(10)\nv_mfma_f32_16x16x32_bf16 v[32:35], v[72:75], a[0:3], 0\n" \
    "buffer_load_dwordx4 v[116:119], v1, s[64:67], s85 offen offset:3072 sc1\nv_mfma_f32_16x16x32_bf16 v[36:39], v[72:75], a[16:19], 0\nv_and_b32 v251, 0xffff0000, v253\n" \
    "s_waitcnt vmcnt(10)\nv_mfma_f32_16x16x32_bf16 v[32:35], v[76:79], a[4:7], v[32:35]\ns_add_u32 s85, s84, 32768\n" \
    "buffer_load_dwordx4 v[72:75], v0, s[64:67], s85 offen offset:0 sc1\nv_mfma_f32_16x16x32_bf16 v[36:39], v[76:79], a[20:23], v[36:39]\nv_cvt_pk_bf16_f32 v120, v24, v25\n" \
    "v_cvt_pk_bf16_f32 v122, v26, v27\ns_waitcnt lgkmcnt(0)\nv_add_f32 v40, v40, v44\nv_add_f32 v41, v41, v45\nv_add_f32 v42, v42, v46\nv_add_f32 v43, v43, v47\n" \
    "s_waitcnt vmcnt(10)\nv_mfma_f32_16x16x32_bf16 v[32:35], v[80:83], a[8:11], v[32:35]\nbuffer_load_dwordx4 v[76:79], v0, s[64:67], s85 offen offset:1024 sc1\n" \
    "v_mfma_f32_16x16x32_bf16 v[36:39], v[80:83], a[24:27], v[36:39]\nv_cvt_pk_bf16_f32 v152, v28, v29\nv_cvt_pk_bf16_f32 v154, v30, v31\nv_add_f32 v48, v48, v52\n" \
    "v_add_f32 v49, v49, v53\nv_add_f32 v50, v50, v54\nv_add_f32 v51, v51, v55\nv_add_f32 v40, v40, v48\ns_waitcnt vmcnt(10)\n" \
    "v_mfma_f32_16x16x32_bf16 v[32:35], v[84:87], a[12:15], v[32:35]\nbuffer_load_dwordx4 v[80:83], v0, s[64:67], s85 offen offset:2048 sc1\n" \
    "v_mfma_f32_16x16x32_bf16 v[36:39], v[84:87], a[28:31], v[36:39]\nv_add_f32 v41, v41, v49\nv_add_f32 v42, v42, v50\nv_add_f32 v43, v43, v51\nv_fma_f32 v248, v40, v18, v248\n" \
    "v_fma_f32 v249, v41, v19, v249\nv_fma_f32 v250, v42, v20, v250\nv_fma_f32 v251, v43, v21, v251\ns_waitcnt vmcnt(10)\n" \
    "v_mfma_f32_16x16x32_bf16 v[24:27], v[88:91], a[32:35], 0\nbuffer_load_dwordx4 v[84:87], v0, s[64:67], s85 offen offset:3072 sc1\n" \
    "v_mfma_f32_16x16x32_bf16 v[28:31], v[88:91], a[48:51], 0\nv_cvt_pk_bf16_f32 v40, v248, v249\nv_cvt_pk_bf16_f32 v41, v250, v251\nv_lshlrev_b32 v248, 16, v40\n" \
    "v_and_b32 v249, 0xffff0000, v40\nv_lshlrev_b32 v250, 16, v41\nv_and_b32 v251, 0xffff0000, v41\nv_add_f32 v18, v248, v249\ns_waitcnt vmcnt(10)\n" \
    "v_mfma_f32_16x16x32_bf16 v[24:27], v[92:95], a[36:39], v[24:27]\nbuffer_load_dwordx4 v[88:91], v1, s[64:67], s85 offen offset:0 sc1\n" \
    "v_mfma_f32_16x16x32_bf16 v[28:31], v[92:95], a[52:55], v[28:31]\nv_cvt_pk_bf16_f32 v121, v32, v33\nv_cvt_pk_bf16_f32 v123, v34, v35\nv_add_f32 v19, v250, v251\n" \
    "v_min_u32 v40, -2, v40\nv_min_u32 v41, -2, v41\nv_add_f32 v18, v18, v19\nv_permlane32_swap_b32 v40, v42\ns_waitcnt vmcnt(10)\n" \
    "v_mfma_f32_16x16x32_bf16 v[24:27], v[96:99], a[40:43], v[24:27]\nbuffer_load_dwordx4 v[92:95], v1, s[64:67], s85 offen offset:1024 sc1\n" \
    "v_mfma_f32_16x16x32_bf16 v[28:31], v[96:99], a[56:59], v[28:31]\nv_cvt_pk_bf16_f32 v153, v36, v37\nv_cvt_pk_bf16_f32 v155, v38, v39\nv_permlane32_swap_b32 v41, v43\n" \
    "buffer_store_dwordx4 v[40:43], v12, s[64:67], s75 offen sc1\ns_waitcnt vmcnt(11)\nv_mfma_f32_16x16x32_bf16 v[24:27], v[100:103], a[44:47], v[24:27]\n" \
    "buffer_load_dwordx4 v[96:99], v1, s[64:67], s85 offen offset:2048 sc1\nv_mfma_f32_16x16x32_bf16 v[28:31], v[100:103], a[60:63], v[28:31]\n" \
    "v_add_f32_dpp v19, v18, v18 row_ror:8 row_mask:0xf bank_mask:0xf\nv_add_f32_dpp v20, v18, v18 row_ror:8 row_mask:0xf bank_mask:0xf\ns_nop 1\n" \
    "v_permlane16_swap_b32 v19, v20\nv_add_f32 v18, v19, v20\nv_add_f32 v19, v19, v20\ns_nop 1\nv_permlane32_swap_b32 v18, v19\ns_waitcnt vmcnt(11)\n" \
    "v_mfma_f32_16x16x32_bf16 v[32:35], v[104:107], a[32:35], 0\nbuffer_load_dwordx4 v[100:103], v1, s[64:67], s85 offen offset:3072 sc1\n" \
    "v_mfma_f32_16x16x32_bf16 v[36:39], v[104:107], a[48:51], 0\nv_add_f32 v18, v18, v19\nv_mul_f32 v18, 0x3d000000, v18\nv_sub_f32 v44, v248, v18\nv_sub_f32 v45, v249, v18\n" \
    "v_sub_f32 v46, v250, v18\nv_sub_f32 v47, v251, v18\nv_mul_f32 v19, v44, v44\ns_waitcnt vmcnt(11)\nv_mfma_f32_16x16x32_bf16 v[32:35], v[108:111], a[36:39], v[32:35]\n" \
    "s_add_u32 s85, s84, 49152\nbuffer_load_dwordx4 v[104:107], v0, s[64:67], s85 offen offset:0 sc1\nv_mfma_f32_16x16x32_bf16 v[36:39], v[108:111], a[52:55], v[36:39]\n" \
    "v_cvt_pk_bf16_f32 v124, v24, v25\nv_cvt_pk_bf16_f32 v126, v26, v27\nv_fmac_f32 v19, v45, v45\nv_fmac_f32 v19, v46, v46\nv_fmac_f32 v19, v47, v47\nv_min_u32 v18, -2, v18\n" \
    "s_nop 0\nv_add_f32_dpp v20, v19, v19 row_ror:8 row_mask:0xf bank_mask:0xf\ns_waitcnt vmcnt(11)\nv_mfma_f32_16x16x32_bf16 v[32:35], v[112:115], a[40:43], v[32:35]\n" \
    "buffer_load_dwordx4 v[108:111], v0, s[64:67], s85 offen offset:1024 sc1\nv_mfma_f32_16x16x32_bf16 v[36:39], v[112:115], a[56:59], v[36:39]\n" \
    "v_cvt_pk_bf16_f32 v156, v28, v29\nv_cvt_pk_bf16_f32 v158, v30, v31\nv_add_f32_dpp v21, v19, v19 row_ror:8 row_mask:0xf bank_mask:0xf\ns_nop 1\n" \
    "v_permlane16_swap_b32 v20, v21\nv_add_f32 v19, v20, v21\nv_add_f32 v20, v20, v21\ns_waitcnt vmcnt(11)\nv_mfma_f32_16x16x32_bf16 v[32:35], v[116:119], a[44:47], v[32:35]\n" \
    "buffer_load_dwordx4 v[112:115], v0, s[64:67], s85 offen offset:2048 sc1\nv_mfma_f32_16x16x32_bf16 v[36:39], v[116:119], a[60:63], v[36:39]\nv_permlane32_swap_b32 v19, v20\n" \
    "v_add_f32 v19, v19, v20\nv_min_u32 v19, -2, v19\nv_mov_b32 v22, 1\nbuffer_store_dwordx2 v[18:19], v13, s[64:67], s75 offen sc1\ns_waitcnt vmcnt(12)\n" \
    "v_mfma_f32_16x16x32_bf16 v[24:27], v[72:75], a[64:67], 0\nbuffer_load_dwordx4 v[116:119], v0, s[64:67], s85 offen offset:3072 sc1\n" \
    "v_mfma_f32_16x16x32_bf16 v[28:31], v[72:75], a[80:83], 0\ns_add_u32 s69, s50, -1\nv_mov_b32 v22, s69\ns_barrier\nbuffer_store_dword v22, v14, s[64:67], 0 offen sc1\n" \
    "s_waitcnt vmcnt(13)\nv_mfma_f32_16x16x32_bf16 v[24:27], v[76:79], a[68:71], v[24:27]\nbuffer_load_dwordx4 v[72:75], v1, s[64:67], s85 offen offset:0 sc1\n" \
    "v_mfma_f32_16x16x32_bf16 v[28:31], v[76:79], a[84:87], v[28:31]\nv_cvt_pk_bf16_f32 v125, v32, v33\nv_cvt_pk_bf16_f32 v127, v34, v35\ns_add_u32 s69, s50, 2\n" \
    "s_and_b32 s69, s69, s41\ns_lshl_b32 s69, s69, 20\ns_add_u32 s69, s69, s32\ns_add_u32 s74, s48, s69\ns_waitcnt vmcnt(13)\n" \
    "v_mfma_f32_16x16x32_bf16 v[24:27], v[80:83], a[72:75], v[24:27]\nbuffer_load_dwordx4 v[76:79], v1, s[64:67], s85 offen offset:1024 sc1\n" \
    "v_mfma_f32_16x16x32_bf16 v[28:31], v[80:83], a[88:91], v[28:31]\nv_cvt_pk_bf16_f32 v157, v36, v37\nv_cvt_pk_bf16_f32 v159, v38, v39\ns_lshr_b32 s74, s74, 8\n" \
    "s_xor_b32 s74, s74, s49\ns_and_b32 s74, s74, 0x2000\ns_add_u32 s74, s74, s69\ns_add_u32 s69, s50, 2\ns_waitcnt vmcnt(13)\n" \
    "v_mfma_f32_16x16x32_bf16 v[24:27], v[84:87], a[76:79], v[24:27]\nbuffer_load_dwordx4 v[80:83], v1, s[64:67], s85 offen offset:2048 sc1\n" \
    "v_mfma_f32_16x16x32_bf16 v[28:31], v[84:87], a[92:95], v[28:31]\ns_and_b32 s69, s69, s41\ns_lshl_b32 s69, s69, 19\ns_add_u32 s69, s69, s33\ns_add_u32 s87, s48, s69\n" \
    "s_lshr_b32 s87, s87, 8\ns_xor_b32 s87, s87, s49\ns_and_b32 s87, s87, 0x2000\ns_waitcnt vmcnt(13)\nv_mfma_f32_16x16x32_bf16 v[32:35], v[88:91], a[64:67], 0\n" \
    "buffer_load_dwordx4 v[84:87], v1, s[64:67], s85 offen offset:3072 sc1\nv_mfma_f32_16x16x32_bf16 v[36:39], v[88:91], a[80:83], 0\ns_add_u32 s87, s87, s69\n" \
    "s_add_u32 s69, s50, 3\ns_and_b32 s69, s69, s41\ns_lshl_b32 s69, s69, 18\ns_add_u32 s69, s69, s34\ns_add_u32 s88, s48, s69\ns_lshr_b32 s88, s88, 8\ns_waitcnt vmcnt(13)\n" \
    "v_mfma_f32_16x16x32_bf16 v[32:35], v[92:95], a[68:71], v[32:35]\ns_add_u32 s85, s84, 65536\nbuffer_load_dwordx4 v[88:91], v0, s[64:67], s85 offen offset:0 sc1\n" \
    "v_mfma_f32_16x16x32_bf16 v[36:39], v[92:95], a[84:87], v[36:39]\nv_cvt_pk_bf16_f32 v128, v24, v25\nv_cvt_pk_bf16_f32 v130, v26, v27\ns_xor_b32 s88, s88, s49\n" \
    "s_and_b32 s88, s88, 0x2000\ns_add_u32 s88, s88, s69\ns_xor_b32 s89, s88, 0x1000\ns_add_u32 s69, s50, 0\ns_waitcnt vmcnt(12)\n" \
    "v_mfma_f32_16x16x32_bf16 v[32:35], v[96:99], a[72:75], v[32:35]\nbuffer_load_dwordx4 v[92:95], v0, s[64:67], s85 offen offset:1024 sc1\n" \
    "v_mfma_f32_16x16x32_bf16 v[36:39], v[96:99], a[88:91], v[36:39]\nv_cvt_pk_bf16_f32 v160, v28, v29\nv_cvt_pk_bf16_f32 v162, v30, v31\ns_lshl_b32 s69, s69, 20\n" \
    "s_add_u32 s69, s69, s36\ns_add_u32 s75, s48, s69\ns_lshr_b32 s75, s75, 8\ns_xor_b32 s75, s75, s49\ns_waitcnt vmcnt(12)\n" \
    "v_mfma_f32_16x16x32_bf16 v[32:35], v[100:103], a[76:79], v[32:35]\nbuffer_load_dwordx4 v[96:99], v0, s[64:67], s85 offen offset:2048 sc1\n" \
    "v_mfma_f32_16x16x32_bf16 v[36:39], v[100:103], a[92:95], v[36:39]\ns_and_b32 s75, s75, 0x2000\ns_add_u32 s75, s75, s69\ns_add_u32 s76, s75, 0x4000\n" \
    "s_add_u32 s77, s75, s37\ns_add_u32 s82, s50, 0\ns_lshl_b32 s80, s82, 10\ns_lshl_b32 s82, s82, 2\ns_waitcnt vmcnt(12)\n" \
    "v_mfma_f32_16x16x32_bf16 v[24:27], v[104:107], a[96:99], 0\nbuffer_load_dwordx4 v[100:103], v0, s[64:67], s85 offen offset:3072 sc1\n" \
    "v_mfma_f32_16x16x32_bf16 v[28:31], v[104:107], a[112:115], 0\ns_add_u32 s82, s82, s40\ns_waitcnt vmcnt(12)\n" \
    "v_mfma_f32_16x16x32_bf16 v[24:27], v[108:111], a[100:103], v[24:27]\nbuffer_load_dwordx4 v[104:107], v1, s[64:67], s85 offen offset:0 sc1\n" \
    "v_mfma_f32_16x16x32_bf16 v[28:31], v[108:111], a[116:119], v[28:31]\nv_cvt_pk_bf16_f32 v129, v32, v33\nv_cvt_pk_bf16_f32 v131, v34, v35\ns_waitcnt vmcnt(12)\n" \
    "v_mfma_f32_16x16x32_bf16 v[24:27], v[112:115], a[104:107], v[24:27]\nbuffer_load_dwordx4 v[108:111], v1, s[64:67], s85 offen offset:1024 sc1\n" \
    "v_mfma_f32_16x16x32_bf16 v[28:31], v[112:115], a[120:123], v[28:31]\nv_cvt_pk_bf16_f32 v161, v36, v37\nv_cvt_pk_bf16_f32 v163, v38, v39\ns_waitcnt vmcnt(11)\n" \
    "v_mfma_f32_16x16x32_bf16 v[24:27], v[116:119], a[108:111], v[24:27]\nbuffer_load_dwordx4 v[112:115], v1, s[64:67], s85 offen offset:2048 sc1\n" \
    "v_mfma_f32_16x16x32_bf16 v[28:31], v[116:119], a[124:127], v[28:31]\ns_waitcnt vmcnt(10)\nv_mfma_f32_16x16x32_bf16 v[32:35], v[72:75], a[96:99], 0\n" \
    "buffer_load_dwordx4 v[116:119], v1, s[64:67], s85 offen offset:3072 sc1\nv_mfma_f32_16x16x32_bf16 v[36:39], v[72:75], a[112:115], 0\ns_waitcnt vmcnt(10)\n" \
    "v_mfma_f32_16x16x32_bf16 v[32:35], v[76:79], a[100:103], v[32:35]\ns_add_u32 s85, s84, 81920\nbuffer_load_dwordx4 v[72:75], v0, s[64:67], s85 offen offset:0 sc1\n" \
    "v_mfma_f32_16x16x32_bf16 v[36:39], v[76:79], a[116:119], v[36:39]\nv_cvt_pk_bf16_f32 v132, v24, v25\nv_cvt_pk_bf16_f32 v134, v26, v27\ns_waitcnt vmcnt(10)\n" \
    "v_mfma_f32_16x16x32_bf16 v[32:35], v[80:83], a[104:107], v[32:35]\nbuffer_load_dwordx4 v[76:79], v0, s[64:67], s85 offen offset:1024 sc1\n" \
    "v_mfma_f32_16x16x32_bf16 v[36:39], v[80:83], a[120:123], v[36:39]\nv_cvt_pk_bf16_f32 v164, v28, v29\nv_cvt_pk_bf16_f32 v166, v30, v31\ns_waitcnt vmcnt(10)\n" \
    "v_mfma_f32_16x16x32_bf16 v[32:35], v[84:87], a[108:111], v[32:35]\nbuffer_load_dwordx4 v[80:83], v0, s[64:67], s85 offen offset:2048 sc1\n" \
    "v_mfma_f32_16x16x32_bf16 v[36:39], v[84:87], a[124:127], v[36:39]\ns_waitcnt vmcnt(10)\nv_mfma_f32_16x16x32_bf16 v[24:27], v[88:91], a[128:131], 0\n" \
    "buffer_load_dwordx4 v[84:87], v0, s[64:67], s85 offen offset:3072 sc1\nv_mfma_f32_16x16x32_bf16 v[28:31], v[88:91], a[144:147], 0\ns_waitcnt vmcnt(10)\n" \
    "v_mfma_f32_16x16x32_bf16 v[24:27], v[92:95], a[132:135], v[24:27]\nbuffer_load_dwordx4 v[88:91], v1, s[64:67], s85 offen offset:0 sc1\n" \
    "v_mfma_f32_16x16x32_bf16 v[28:31], v[92:95], a[148:151], v[28:31]\nv_cvt_pk_bf16_f32 v133, v32, v33\nv_cvt_pk_bf16_f32 v135, v34, v35\ns_waitcnt vmcnt(10)\n" \
    "v_mfma_f32_16x16x32_bf16 v[24:27], v[96:99], a[136:139], v[24:27]\nbuffer_load_dwordx4 v[92:95], v1, s[64:67], s85 offen offset:1024 sc1\n" \
    "v_mfma_f32_16x16x32_bf16 v[28:31], v[96:99], a[152:155], v[28:31]\nv_cvt_pk_bf16_f32 v165, v36, v37\nv_cvt_pk_bf16_f32 v167, v38, v39\ns_waitcnt vmcnt(10)\n" \
    "v_mfma_f32_16x16x32_bf16 v[24:27], v[100:103], a[140:143], v[24:27]\nbuffer_load_dwordx4 v[96:99], v1, s[64:67], s85 offen offset:2048 sc1\n" \
    "v_mfma_f32_16x16x32_bf16 v[28:31], v[100:103], a[156:159], v[28:31]\ns_waitcnt vmcnt(10)\nv_mfma_f32_16x16x32_bf16 v[32:35], v[104:107], a[128:131], 0\n" \
    "buffer_load_dwordx4 v[100:103], v1, s[64:67], s85 offen offset:3072 sc1\nv_mfma_f32_16x16x32_bf16 v[36:39], v[104:107], a[144:147], 0\ns_waitcnt vmcnt(10)\n" \
    "v_mfma_f32_16x16x32_bf16 v[32:35], v[108:111], a[132:135], v[32:35]\ns_add_u32 s85, s84, 98304\nbuffer_load_dwordx4 v[104:107], v0, s[64:67], s85 offen offset:0 sc1\n" \
    "v_mfma_f32_16x16x32_bf16 v[36:39], v[108:111], a[148:151], v[36:39]\nv_cvt_pk_bf16_f32 v136, v24, v25\nv_cvt_pk_bf16_f32 v138, v26, v27\ns_waitcnt vmcnt(10)\n" \
    "v_mfma_f32_16x16x32_bf16 v[32:35], v[112:115], a[136:139], v[32:35]\nbuffer_load_dwordx4 v[108:111], v0, s[64:67], s85 offen offset:1024 sc1\n" \
    "v_mfma_f32_16x16x32_bf16 v[36:39], v[112:115], a[152:155], v[36:39]\nv_cvt_pk_bf16_f32 v168, v28, v29\nv_cvt_pk_bf16_f32 v170, v30, v31\ns_waitcnt vmcnt(10)\n" \
    "v_mfma_f32_16x16x32_bf16 v[32:35], v[116:119], a[140:143], v[32:35]\nbuffer_load_dwordx4 v[112:115], v0, s[64:67], s85 offen offset:2048 sc1\n" \
    "v_mfma_f32_16x16x32_bf16 v[36:39], v[116:119], a[156:159], v[36:39]\ns_waitcnt vmcnt(10)\nv_mfma_f32_16x16x32_bf16 v[24:27], v[72:75], a[160:163], 0\n" \
    "buffer_load_dwordx4 v[116:119], v0, s[64:67], s85 offen offset:3072 sc1\nv_mfma_f32_16x16x32_bf16 v[28:31], v[72:75], a[176:179], 0\n" \
    "buffer_load_dword v17, v8, s[64:67], 0 offen sc1\ns_waitcnt vmcnt(11)\nv_mfma_f32_16x16x32_bf16 v[24:27], v[76:79], a[164:167], v[24:27]\n" \
    "buffer_load_dwordx4 v[72:75], v1, s[64:67], s85 offen offset:0 sc1\nv_mfma_f32_16x16x32_bf16 v[28:31], v[76:79], a[180:183], v[28:31]\nv_cvt_pk_bf16_f32 v137, v32, v33\n" \
    "v_cvt_pk_bf16_f32 v139, v34, v35\ns_waitcnt vmcnt(11)\nv_mfma_f32_16x16x32_bf16 v[24:27], v[80:83], a[168:171], v[24:27]\n" \
    "buffer_load_dwordx4 v[76:79], v1, s[64:67], s85 offen offset:1024 sc1\nv_mfma_f32_16x16x32_bf16 v[28:31], v[80:83], a[184:187], v[28:31]\nv_cvt_pk_bf16_f32 v169, v36, v37\n" \
    "v_cvt_pk_bf16_f32 v171, v38, v39\ns_waitcnt vmcnt(11)\nv_mfma_f32_16x16x32_bf16 v[24:27], v[84:87], a[172:175], v[24:27]\n" \
    "buffer_load_dwordx4 v[80:83], v1, s[64:67], s85 offen offset:2048 sc1\nv_mfma_f32_16x16x32_bf16 v[28:31], v[84:87], a[188:191], v[28:31]\ns_waitcnt vmcnt(11)\n" \
    "v_mfma_f32_16x16x32_bf16 v[32:35], v[88:91], a[160:163], 0\nbuffer_load_dwordx4 v[84:87], v1, s[64:67], s85 offen offset:3072 sc1\n" \
    "v_mfma_f32_16x16x32_bf16 v[36:39], v[88:91], a[176:179], 0\ns_waitcnt vmcnt(11)\nv_mfma_f32_16x16x32_bf16 v[32:35], v[92:95], a[164:167], v[32:35]\n" \
    "s_add_u32 s85, s84, 114688\nbuffer_load_dwordx4 v[88:91], v0, s[64:67], s85 offen offset:0 sc1\nv_mfma_f32_16x16x32_bf16 v[36:39], v[92:95], a[180:183], v[36:39]\n" \
    "v_cvt_pk_bf16_f32 v140, v24, v25\nv_cvt_pk_bf16_f32 v142, v26, v27\ns_waitcnt vmcnt(11)\nv_mfma_f32_16x16x32_bf16 v[32:35], v[96:99], a[168:171], v[32:35]\n" \
    "buffer_load_dwordx4 v[92:95], v0, s[64:67], s85 offen offset:1024 sc1\nv_mfma_f32_16x16x32_bf16 v[36:39], v[96:99], a[184:187], v[36:39]\nv_cvt_pk_bf16_f32 v172, v28, v29\n" \
    "v_cvt_pk_bf16_f32 v174, v30, v31\ns_add_u32 s69, s50, 4\ns_and_b32 s69, s69, s41\ns_lshl_b32 s69, s69, 19\ns_add_u32 s69, s69, s35\ns_add_u32 s84, s48, s69\n" \
    "s_waitcnt vmcnt(11)\nv_mfma_f32_16x16x32_bf16 v[32:35], v[100:103], a[172:175], v[32:35]\nbuffer_load_dwordx4 v[96:99], v0, s[64:67], s85 offen offset:2048 sc1\n" \
    "v_mfma_f32_16x16x32_bf16 v[36:39], v[100:103], a[188:191], v[36:39]\ns_lshr_b32 s84, s84, 8\ns_xor_b32 s84, s84, s49\ns_and_b32 s84, s84, 0x2000\ns_add_u32 s84, s84, s69\n" \
    "s_waitcnt vmcnt(11)\nv_mfma_f32_16x16x32_bf16 v[24:27], v[104:107], a[192:195], 0\nbuffer_load_dwordx4 v[100:103], v0, s[64:67], s85 offen offset:3072 sc1\n" \
    "v_mfma_f32_16x16x32_bf16 v[28:31], v[104:107], a[208:211], 0\ns_waitcnt vmcnt(11)\nv_mfma_f32_16x16x32_bf16 v[24:27], v[108:111], a[196:199], v[24:27]\n" \
    "buffer_load_dwordx4 v[104:107], v1, s[64:67], s85 offen offset:0 sc1\nv_mfma_f32_16x16x32_bf16 v[28:31], v[108:111], a[212:215], v[28:31]\n" \
    "v_cvt_pk_bf16_f32 v141, v32, v33\nv_cvt_pk_bf16_f32 v143, v34, v35\ns_waitcnt vmcnt(11)\nv_mfma_f32_16x16x32_bf16 v[24:27], v[112:115], a[200:203], v[24:27]\n" \
    "buffer_load_dwordx4 v[108:111], v1, s[64:67], s85 offen offset:1024 sc1\nv_mfma_f32_16x16x32_bf16 v[28:31], v[112:115], a[216:219], v[28:31]\n" \
    "v_cvt_pk_bf16_f32 v173, v36, v37\nv_cvt_pk_bf16_f32 v175, v38, v39\ns_waitcnt vmcnt(11)\nv_mfma_f32_16x16x32_bf16 v[24:27], v[116:119], a[204:207], v[24:27]\n" \
    "buffer_load_dwordx4 v[112:115], v1, s[64:67], s85 offen offset:2048 sc1\nv_mfma_f32_16x16x32_bf16 v[28:31], v[116:119], a[220:223], v[28:31]\ns_waitcnt vmcnt(10)\n" \
    "v_mfma_f32_16x16x32_bf16 v[32:35], v[72:75], a[192:195], 0\nbuffer_load_dwordx4 v[116:119], v1, s[64:67], s85 offen offset:3072 sc1\n" \
    "v_mfma_f32_16x16x32_bf16 v[36:39], v[72:75], a[208:211], 0\ns_mov_b32 s86, 0\nv_mov_b32 v18, 0x7f000000\ns_add_u32 s70, s50, 4\ns_add_u32 s71, s50, 0\n" \
    "v_cmp_lt_i32 vcc, s70, v17\ns_nop 3\ns_and_b32 s69, vcc_lo, 0xff\ns_cmp_eq_u32 s69, 0xff\ns_cbranch_scc0 .Lr88_%=\nv_readlane_b32 s69, v17, 10\ns_cmp_ge_i32 s69, s71\n" \
    "s_cbranch_scc0 .Lr88_%=\ns_mov_b32 s86, 1\nv_mov_b32 v18, v0\n.Lr88_%=:\nv_add_u32 v19, 0x1000, v18\ns_waitcnt vmcnt(10)\n" \
    "v_mfma_f32_16x16x32_bf16 v[32:35], v[76:79], a[196:199], v[32:35]\nv_mfma_f32_16x16x32_bf16 v[36:39], v[76:79], a[212:215], v[36:39]\nv_cvt_pk_bf16_f32 v144, v24, v25\n" \
    "v_cvt_pk_bf16_f32 v146, v26, v27\nbuffer_load_dwordx4 v[72:75], v18, s[64:67], s75 offen offset:0 sc1\ns_waitcnt vmcnt(10)\n" \
    "v_mfma_f32_16x16x32_bf16 v[32:35], v[80:83], a[200:203], v[32:35]\nv_mfma_f32_16x16x32_bf16 v[36:39], v[80:83], a[216:219], v[36:39]\nv_cvt_pk_bf16_f32 v176, v28, v29\n" \
    "v_cvt_pk_bf16_f32 v178, v30, v31\nbuffer_load_dwordx4 v[76:79], v18, s[64:67], s75 offen offset:1024 sc1\ns_waitcnt vmcnt(10)\n" \
    "v_mfma_f32_16x16x32_bf16 v[32:35], v[84:87], a[204:207], v[32:35]\nv_mfma_f32_16x16x32_bf16 v[36:39], v[84:87], a[220:223], v[36:39]\n" \
    "buffer_load_dwordx4 v[80:83], v18, s[64:67], s75 offen offset:2048 sc1\ns_waitcnt vmcnt(10)\nv_mfma_f32_16x16x32_bf16 v[24:27], v[88:91], a[224:227], 0\n" \
    "v_mfma_f32_16x16x32_bf16 v[28:31], v[88:91], a[240:243], 0\nbuffer_load_dwordx4 v[84:87], v18, s[64:67], s75 offen offset:3072 sc1\ns_waitcnt vmcnt(10)\n" \
    "v_mfma_f32_16x16x32_bf16 v[24:27], v[92:95], a[228:231], v[24:27]\nv_mfma_f32_16x16x32_bf16 v[28:31], v[92:95], a[244:247], v[28:31]\nv_cvt_pk_bf16_f32 v145, v32, v33\n" \
    "v_cvt_pk_bf16_f32 v147, v34, v35\nbuffer_load_dwordx4 v[88:91], v19, s[64:67], s75 offen offset:0 sc1\ns_waitcnt vmcnt(10)\n" \
    "v_mfma_f32_16x16x32_bf16 v[24:27], v[96:99], a[232:235], v[24:27]\nv_mfma_f32_16x16x32_bf16 v[28:31], v[96:99], a[248:251], v[28:31]\nv_cvt_pk_bf16_f32 v177, v36, v37\n" \
    "v_cvt_pk_bf16_f32 v179, v38, v39\nbuffer_load_dwordx4 v[92:95], v19, s[64:67], s75 offen offset:1024 sc1\ns_waitcnt vmcnt(10)\n" \
    "v_mfma_f32_16x16x32_bf16 v[24:27], v[100:103], a[236:239], v[24:27]\nv_mfma_f32_16x16x32_bf16 v[28:31], v[100:103], a[252:255], v[28:31]\n" \
    "buffer_load_dwordx4 v[96:99], v19, s[64:67], s75 offen offset:2048 sc1\ns_waitcnt vmcnt(10)\nv_mfma_f32_16x16x32_bf16 v[32:35], v[104:107], a[224:227], 0\n" \
    "v_mfma_f32_16x16x32_bf16 v[36:39], v[104:107], a[240:243], 0\ns_waitcnt vmcnt(9)\nv_mfma_f32_16x16x32_bf16 v[32:35], v[108:111], a[228:231], v[32:35]\n" \
    "v_mfma_f32_16x16x32_bf16 v[36:39], v[108:111], a[244:247], v[36:39]\nv_cvt_pk_bf16_f32 v148, v24, v25\nv_cvt_pk_bf16_f32 v150, v26, v27\ns_waitcnt vmcnt(8)\n" \
    "v_mfma_f32_16x16x32_bf16 v[32:35], v[112:115], a[232:235], v[32:35]\nv_mfma_f32_16x16x32_bf16 v[36:39], v[112:115], a[248:251], v[36:39]\nv_cvt_pk_bf16_f32 v180, v28, v29\n" \
    "v_cvt_pk_bf16_f32 v182, v30, v31\ns_waitcnt vmcnt(7)\nv_mfma_f32_16x16x32_bf16 v[32:35], v[116:119], a[236:239], v[32:35]\n" \
    "v_mfma_f32_16x16x32_bf16 v[36:39], v[116:119], a[252:255], v[36:39]\ns_nop 6\nv_cvt_pk_bf16_f32 v149, v32, v33\nv_cvt_pk_bf16_f32 v151, v34, v35\n" \
    "v_cvt_pk_bf16_f32 v181, v36, v37\nv_cvt_pk_bf16_f32 v183, v38, v39\ns_add_u32 s50, s50, 2\ns_mov_b32 s68, 0\ns_add_u32 s78, s87, s38\n.Lr91_%=:\ns_sub_u32 s70, s51, 52\n" \
    ".Lr100_%=:\ns_sleep 1\ns_memrealtime s[72:73]\ns_waitcnt lgkmcnt(0)\ns_sub_u32 s72, s72, s70\ns_cmp_lt_i32 s72, 0\ns_cbranch_scc1 .Lr100_%=\n" \
    "buffer_load_dword v20, off, s[64:67], s78 sc1\ns_waitcnt vmcnt(0)\nv_readfirstlane_b32 s70, v20\ns_memrealtime s[72:73]\ns_cmp_eq_u32 s70, -1\ns_cbranch_scc1 .Lr92_%=\n" \
    "s_lshr_b32 s71, s70, 8\ns_waitcnt lgkmcnt(0)\ns_sub_u32 s71, s72, s71\ns_and_b32 s71, s71, 0xffffff\ns_sub_u32 s51, s72, s71\ns_add_u32 s51, s51, 176\n" \
    "s_and_b32 s70, s70, 128\ns_lshl_b32 s52, s70, 5\ns_xor_b32 s87, s87, s52\ns_mov_b32 s68, 0\ns_sub_u32 s70, s51, s54\n.Lr101_%=:\ns_memrealtime s[72:73]\n" \
    "s_waitcnt lgkmcnt(0)\ns_sub_u32 s72, s72, s70\ns_cmp_lt_i32 s72, 0\ns_cbranch_scc1 .Lr101_%=\nbuffer_load_dwordx4 v[40:43], v0, s[64:67], s74 offen offset:0 sc1\n" \
    "buffer_load_dwordx4 v[44:47], v0, s[64:67], s74 offen offset:1024 sc1\ns_add_u32 s69, s74, 0x4000\nbuffer_load_dwordx4 v[48:51], v0, s[64:67], s69 offen offset:0 sc1\n" \
    "buffer_load_dwordx4 v[52:55], v0, s[64:67], s69 offen offset:1024 sc1\n.Lr102_%=:\ns_memrealtime s[72:73]\ns_waitcnt lgkmcnt(0)\ns_sub_u32 s72, s72, s51\n" \
    "s_cmp_lt_i32 s72, -3\ns_cbranch_scc1 .Lr102_%=\ns_cmp_ge_i32 s72, 0\ns_cbranch_scc1 .Lr103_%=\ns_nop 2\ns_cmp_ge_i32 s72, -1\ns_cbranch_scc1 .Lr103_%=\ns_nop 2\n" \
    "s_cmp_ge_i32 s72, -2\ns_cbranch_scc1 .Lr103_%=\ns_nop 2\n.Lr103_%=:\nbuffer_load_dwordx4 v[100:103], v0, s[64:67], s87 offen offset:0 sc1\n" \
    "buffer_load_dwordx4 v[104:107], v0, s[64:67], s87 offen offset:1024 sc1\nbuffer_load_dwordx4 v[108:111], v0, s[64:67], s87 offen offset:2048 sc1\n" \
    "buffer_load_dwordx4 v[112:115], v0, s[64:67], s87 offen offset:3072 sc1\nbuffer_load_dwordx2 v[116:117], v5, s[64:67], s87 offen offset:0 sc1\n" \
    "buffer_load_dwordx2 v[118:119], v5, s[64:67], s87 offen offset:128 sc1\n.Lr95_%=:\nv_mov_b64 v[24:25], 0\nv_mov_b64 v[26:27], 0\nv_mov_b64 v[28:29], 0\n" \
    "v_mov_b64 v[30:31], 0\ns_waitcnt vmcnt(6)\nv_max3_u32 v22, v40, v41, v42\nv_max3_u32 v22, v22, v43, v44\nv_max3_u32 v22, v22, v45, v46\nv_max3_u32 v22, v22, v47, v48\n" \
    "v_max3_u32 v22, v22, v49, v50\nv_max3_u32 v22, v22, v51, v52\nv_max3_u32 v22, v22, v53, v54\nv_max_u32 v22, v22, v55\ns_waitcnt vmcnt(5)\n" \
    "v_smfmac_f32_16x16x64_bf16 v[24:27], v[100:103], v[120:127], v4\nv_max3_u32 v22, v22, v100, v101\nv_smfmac_f32_16x16x64_bf16 v[28:31], v[100:103], v[152:159], v4\n" \
    "v_max3_u32 v22, v22, v102, v103\nbuffer_load_dwordx4 v[100:103], v19, s[64:67], s75 offen offset:3072 sc1\ns_waitcnt vmcnt(5)\n" \
    "v_smfmac_f32_16x16x64_bf16 v[24:27], v[104:107], v[128:135], v4\nv_max3_u32 v22, v22, v104, v105\nv_smfmac_f32_16x16x64_bf16 v[28:31], v[104:107], v[160:167], v4\n" \
    "v_max3_u32 v22, v22, v106, v107\nbuffer_load_dwordx4 v[104:107], v18, s[64:67], s76 offen offset:0 sc1\ns_waitcnt vmcnt(5)\n" \
    "v_smfmac_f32_16x16x64_bf16 v[24:27], v[108:111], v[136:143], v4\nv_max3_u32 v22, v22, v108, v109\nv_smfmac_f32_16x16x64_bf16 v[28:31], v[108:111], v[168:175], v4\n" \
    "v_max3_u32 v22, v22, v110, v111\ns_waitcnt vmcnt(4)\nv_smfmac_f32_16x16x64_bf16 v[24:27], v[112:115], v[144:151], v4\nv_max3_u32 v22, v22, v112, v113\n" \
    "v_smfmac_f32_16x16x64_bf16 v[28:31], v[112:115], v[176:183], v4\nv_max3_u32 v22, v22, v114, v115\ns_waitcnt vmcnt(2)\n" \
    "v_smfmac_f32_16x16x64_bf16 v[24:27], v[116:119], v[40:47], v4\nv_max3_u32 v22, v22, v116, v117\nv_smfmac_f32_16x16x64_bf16 v[28:31], v[116:119], v[48:55], v4\n" \
    "v_max3_u32 v22, v22, v118, v119\nbuffer_load_dwordx4 v[108:111], v18, s[64:67], s76 offen offset:1024 sc1\nv_cmp_eq_u32 vcc, -1, v22\ns_nop 4\n" \
    "v_permlane32_swap_b32 v24, v28\nv_permlane32_swap_b32 v25, v29\nv_permlane32_swap_b32 v26, v30\nv_permlane32_swap_b32 v27, v31\nv_pk_add_f32 v[24:25], v[24:25], v[28:29]\n" \
    "v_pk_add_f32 v[26:27], v[26:27], v[30:31]\nds_write2_b32 v6, v24, v25 offset0:0 offset1:36\nds_write2_b32 v6, v26, v27 offset0:72 offset1:108\ns_cbranch_vccnz .Lr96_%=\n" \
    "buffer_load_dwordx4 v[112:115], v18, s[64:67], s76 offen offset:2048 sc1\ns_waitcnt lgkmcnt(0)\ns_barrier\nds_read_b128 v[24:27], v7 offset:0\n" \
    "ds_read_b128 v[28:31], v7 offset:1152\nds_read_b128 v[32:35], v7 offset:2304\nds_read_b128 v[36:39], v7 offset:3456\ns_waitcnt lgkmcnt(2)\n" \
    "v_pk_add_f32 v[24:25], v[24:25], v[28:29]\nv_pk_add_f32 v[26:27], v[26:27], v[30:31]\ns_waitcnt lgkmcnt(0)\nv_pk_add_f32 v[32:33], v[32:33], v[36:37]\n" \
    "v_pk_add_f32 v[34:35], v[34:35], v[38:39]\nv_pk_add_f32 v[24:25], v[24:25], v[32:33]\nv_pk_add_f32 v[26:27], v[26:27], v[34:35]\nv_cvt_pk_bf16_f32 v248, v24, v25\n" \
    "v_cvt_pk_bf16_f32 v249, v26, v27\ns_nop 0\nv_permlane32_swap_b32 v248, v250\nv_permlane32_swap_b32 v249, v251\ns_mov_b32 exec_hi, 0\n" \
    "buffer_store_dwordx4 v[248:251], v0, s[64:67], s88 offen sc1\nbuffer_store_dwordx4 v[248:251], v0, s[64:67], s89 offen sc1\ns_mov_b32 exec_hi, -1\ns_add_u32 s51, s51, s55\n" \
    "s_add_u32 s51, s51, s55\ns_mov_b32 s68, 0\ns_cmp_eq_u32 s86, 1\ns_cbranch_scc1 .Lr108_%=\ns_add_u32 s70, s50, 2\ns_add_u32 s71, s50, -2\ns_mov_b32 s68, 0\n" \
    "s_branch .Lr115_%=\n.Lr114_%=:\nbuffer_load_dword v17, v8, s[64:67], 0 offen sc1\ns_waitcnt vmcnt(0)\n.Lr115_%=:\nv_cmp_lt_i32 vcc, s70, v17\ns_nop 3\n" \
    "s_and_b32 s69, vcc_lo, 0xff\ns_cmp_eq_u32 s69, 0xff\ns_cbranch_scc0 .Lr116_%=\nv_readlane_b32 s69, v17, 9\ns_cmp_gt_i32 s69, s71\ns_cbranch_scc0 .Lr116_%=\n" \
    "s_mov_b32 s68, 0\n.Lr107_%=:\nbuffer_load_dwordx4 v[72:75], v0, s[64:67], s75 offen offset:0 sc1\nbuffer_load_dwordx4 v[76:79], v0, s[64:67], s75 offen offset:1024 sc1\n" \
    "buffer_load_dwordx4 v[80:83], v0, s[64:67], s75 offen offset:2048 sc1\nbuffer_load_dwordx4 v[84:87], v0, s[64:67], s75 offen offset:3072 sc1\n" \
    "buffer_load_dwordx4 v[88:91], v1, s[64:67], s75 offen offset:0 sc1\nbuffer_load_dwordx4 v[92:95], v1, s[64:67], s75 offen offset:1024 sc1\n" \
    "buffer_load_dwordx4 v[96:99], v1, s[64:67], s75 offen offset:2048 sc1\nbuffer_load_dwordx4 v[100:103], v1, s[64:67], s75 offen offset:3072 sc1\n" \
    "buffer_load_dwordx4 v[104:107], v0, s[64:67], s76 offen offset:0 sc1\nbuffer_load_dwordx4 v[108:111], v0, s[64:67], s76 offen offset:1024 sc1\n" \
    "buffer_load_dwordx4 v[112:115], v0, s[64:67], s76 offen offset:2048 sc1\n.Lr108_%=:\nv_mov_b32 v22, 0\ns_waitcnt vmcnt(10)\n" \
    "v_mfma_f32_16x16x32_bf16 v[24:27], v[184:187], v[72:75], 0\nbuffer_load_dwordx4 v[116:119], v0, s[64:67], s76 offen offset:3072 sc1\n" \
    "v_mfma_f32_16x16x32_bf16 v[28:31], v[216:219], v[72:75], 0\nv_max3_u32 v22, v22, v72, v73\nv_max3_u32 v22, v22, v74, v75\ns_waitcnt vmcnt(10)\n" \
    "v_mfma_f32_16x16x32_bf16 v[32:35], v[184:187], v[76:79], 0\nbuffer_load_dwordx4 v[72:75], v1, s[64:67], s76 offen offset:0 sc1\n" \
    "v_mfma_f32_16x16x32_bf16 v[36:39], v[216:219], v[76:79], 0\nv_max3_u32 v22, v22, v76, v77\nv_max3_u32 v22, v22, v78, v79\ns_waitcnt vmcnt(10)\n" \
    "v_mfma_f32_16x16x32_bf16 v[24:27], v[188:191], v[80:83], v[24:27]\nbuffer_load_dwordx4 v[76:79], v1, s[64:67], s76 offen offset:1024 sc1\n" \
    "v_mfma_f32_16x16x32_bf16 v[28:31], v[220:223], v[80:83], v[28:31]\nv_max3_u32 v22, v22, v80, v81\nv_max3_u32 v22, v22, v82, v83\ns_waitcnt vmcnt(10)\n" \
    "v_mfma_f32_16x16x32_bf16 v[32:35], v[188:191], v[84:87], v[32:35]\nbuffer_load_dwordx4 v[80:83], v1, s[64:67], s76 offen offset:2048 sc1\n" \
    "v_mfma_f32_16x16x32_bf16 v[36:39], v[220:223], v[84:87], v[36:39]\nv_max3_u32 v22, v22, v84, v85\nv_max3_u32 v22, v22, v86, v87\ns_waitcnt vmcnt(10)\n" \
    "v_mfma_f32_16x16x32_bf16 v[24:27], v[192:195], v[88:91], v[24:27]\nbuffer_load_dwordx4 v[84:87], v1, s[64:67], s76 offen offset:3072 sc1\n" \
    "v_mfma_f32_16x16x32_bf16 v[28:31], v[224:227], v[88:91], v[28:31]\nv_max3_u32 v22, v22, v88, v89\nv_max3_u32 v22, v22, v90, v91\ns_waitcnt vmcnt(10)\n" \
    "v_mfma_f32_16x16x32_bf16 v[32:35], v[192:195], v[92:95], v[32:35]\nbuffer_load_dwordx4 v[88:91], v0, s[64:67], s77 offen offset:0 sc1\n" \
    "v_mfma_f32_16x16x32_bf16 v[36:39], v[224:227], v[92:95], v[36:39]\nv_max3_u32 v22, v22, v92, v93\nv_max3_u32 v22, v22, v94, v95\ns_waitcnt vmcnt(10)\n" \
    "v_mfma_f32_16x16x32_bf16 v[24:27], v[196:199], v[96:99], v[24:27]\nbuffer_load_dwordx4 v[92:95], v0, s[64:67], s77 offen offset:1024 sc1\n" \
    "v_mfma_f32_16x16x32_bf16 v[28:31], v[228:231], v[96:99], v[28:31]\nv_max3_u32 v22, v22, v96, v97\nv_max3_u32 v22, v22, v98, v99\ns_waitcnt vmcnt(10)\n" \
    "v_mfma_f32_16x16x32_bf16 v[32:35], v[196:199], v[100:103], v[32:35]\nbuffer_load_dwordx4 v[96:99], v0, s[64:67], s77 offen offset:2048 sc1\n" \
    "v_mfma_f32_16x16x32_bf16 v[36:39], v[228:231], v[100:103], v[36:39]\nv_max3_u32 v22, v22, v100, v101\nv_max3_u32 v22, v22, v102, v103\ns_waitcnt vmcnt(10)\n" \
    "v_mfma_f32_16x16x32_bf16 v[24:27], v[200:203], v[104:107], v[24:27]\nbuffer_load_dwordx4 v[100:103], v0, s[64:67], s77 offen offset:3072 sc1\n" \
    "v_mfma_f32_16x16x32_bf16 v[28:31], v[232:235], v[104:107], v[28:31]\nv_max3_u32 v22, v22, v104, v105\nv_max3_u32 v22, v22, v106, v107\ns_waitcnt vmcnt(10)\n" \
    "v_mfma_f32_16x16x32_bf16 v[32:35], v[200:203], v[108:111], v[32:35]\ns_add_u32 s85, s84, 0\nbuffer_load_dwordx4 v[104:107], v0, s[64:67], s85 offen offset:0 sc1\n" \
    "v_mfma_f32_16x16x32_bf16 v[36:39], v[232:235], v[108:111], v[36:39]\nv_max3_u32 v22, v22, v108, v109\nv_max3_u32 v22, v22, v110, v111\ns_waitcnt vmcnt(10)\n" \
    "v_mfma_f32_16x16x32_bf16 v[24:27], v[204:207], v[112:115], v[24:27]\nbuffer_load_dwordx4 v[108:111], v0, s[64:67], s85 offen offset:1024 sc1\n" \
    "v_mfma_f32_16x16x32_bf16 v[28:31], v[236:239], v[112:115], v[28:31]\nv_max3_u32 v22, v22, v112, v113\nv_max3_u32 v22, v22, v114, v115\ns_waitcnt vmcnt(10)\n" \
    "v_mfma_f32_16x16x32_bf16 v[32:35], v[204:207], v[116:119], v[32:35]\nbuffer_load_dwordx4 v[112:115], v0, s[64:67], s85 offen offset:2048 sc1\n" \
    "v_mfma_f32_16x16x32_bf16 v[36:39], v[236:239], v[116:119], v[36:39]\nv_max3_u32 v22, v22, v116, v117\nv_max3_u32 v22, v22, v118, v119\ns_waitcnt vmcnt(10)\n" \
    "v_mfma_f32_16x16x32_bf16 v[24:27], v[208:211], v[72:75], v[24:27]\nbuffer_load_dwordx4 v[116:119], v0, s[64:67], s85 offen offset:3072 sc1\n" \
    "v_mfma_f32_16x16x32_bf16 v[28:31], v[240:243], v[72:75], v[28:31]\nv_max3_u32 v22, v22, v72, v73\nv_max3_u32 v22, v22, v74, v75\ns_waitcnt vmcnt(10)\n" \
    "v_mfma_f32_16x16x32_bf16 v[32:35], v[208:211], v[76:79], v[32:35]\nbuffer_load_dwordx4 v[72:75], v1, s[64:67], s85 offen offset:0 sc1\n" \
    "v_mfma_f32_16x16x32_bf16 v[36:39], v[240:243], v[76:79], v[36:39]\nv_max3_u32 v22, v22, v76, v77\nv_max3_u32 v22, v22, v78, v79\ns_waitcnt vmcnt(10)\n" \
    "v_mfma_f32_16x16x32_bf16 v[24:27], v[212:215], v[80:83], v[24:27]\nbuffer_load_dwordx4 v[76:79], v1, s[64:67], s85 offen offset:1024 sc1\n" \
    "v_mfma_f32_16x16x32_bf16 v[28:31], v[244:247], v[80:83], v[28:31]\nv_max3_u32 v22, v22, v80, v81\nv_max3_u32 v22, v22, v82, v83\ns_waitcnt vmcnt(10)\n" \
    "v_mfma_f32_16x16x32_bf16 v[32:35], v[212:215], v[84:87], v[32:35]\nbuffer_load_dwordx4 v[80:83], v1, s[64:67], s85 offen offset:2048 sc1\n" \
    "v_mfma_f32_16x16x32_bf16 v[36:39], v[244:247], v[84:87], v[36:39]\nv_max3_u32 v22, v22, v84, v85\nv_max3_u32 v22, v22, v86, v87\ns_waitcnt vmcnt(10)\n" \
    "v_mfma_f32_16x16x32_bf16 v[24:27], v[56:59], v[88:91], v[24:27]\nbuffer_load_dwordx4 v[84:87], v1, s[64:67], s85 offen offset:3072 sc1\n" \
    "v_mfma_f32_16x16x32_bf16 v[28:31], v[64:67], v[88:91], v[28:31]\nv_max3_u32 v22, v22, v88, v89\nv_max3_u32 v22, v22, v90, v91\ns_waitcnt vmcnt(10)\n" \
    "v_mfma_f32_16x16x32_bf16 v[32:35], v[56:59], v[92:95], v[32:35]\ns_add_u32 s85, s84, 16384\nbuffer_load_dwordx4 v[88:91], v0, s[64:67], s85 offen offset:0 sc1\n" \
    "v_mfma_f32_16x16x32_bf16 v[36:39], v[64:67], v[92:95], v[36:39]\nv_max3_u32 v22, v22, v92, v93\nv_max3_u32 v22, v22, v94, v95\ns_waitcnt vmcnt(10)\n" \
    "v_mfma_f32_16x16x32_bf16 v[24:27], v[60:63], v[96:99], v[24:27]\nbuffer_load_dwordx4 v[92:95], v0, s[64:67], s85 offen offset:1024 sc1\n" \
    "v_mfma_f32_16x16x32_bf16 v[28:31], v[68:71], v[96:99], v[28:31]\nv_max3_u32 v22, v22, v96, v97\nv_max3_u32 v22, v22, v98, v99\ns_waitcnt vmcnt(10)\n" \
    "v_mfma_f32_16x16x32_bf16 v[32:35], v[60:63], v[100:103], v[32:35]\nbuffer_load_dwordx4 v[96:99], v0, s[64:67], s85 offen offset:2048 sc1\n" \
    "v_mfma_f32_16x16x32_bf16 v[36:39], v[68:71], v[100:103], v[36:39]\nv_max3_u32 v22, v22, v100, v101\nv_max3_u32 v22, v22, v102, v103\nv_cmp_eq_u32 vcc, -1, v22\n" \
    "s_cbranch_vccnz .Lr109_%=\nds_write_b128 v9, v[24:27] offset:0\nds_write_b128 v9, v[28:31] offset:64\ns_waitcnt vmcnt(10)\n" \
    "v_mfma_f32_16x16x32_bf16 v[24:27], v[104:107], a[0:3], 0\nbuffer_load_dwordx4 v[100:103], v0, s[64:67], s85 offen offset:3072 sc1\n" \
    "v_mfma_f32_16x16x32_bf16 v[28:31], v[104:107], a[16:19], 0\nds_write_b128 v9, v[32:35] offset:2304\nds_write_b128 v9, v[36:39] offset:2368\ns_waitcnt vmcnt(10)\n" \
    "v_mfma_f32_16x16x32_bf16 v[24:27], v[108:111], a[4:7], v[24:27]\nbuffer_load_dwordx4 v[104:107], v1, s[64:67], s85 offen offset:0 sc1\n" \
    "v_mfma_f32_16x16x32_bf16 v[28:31], v[108:111], a[20:23], v[28:31]\nv_add_u32 v252, s80, v3\nds_read_b64 v[252:253], v252\nds_read_b128 v[18:21], v16\ns_waitcnt vmcnt(10)\n" \
    "v_mfma_f32_16x16x32_bf16 v[24:27], v[112:115], a[8:11], v[24:27]\nbuffer_load_dwordx4 v[108:111], v1, s[64:67], s85 offen offset:1024 sc1\n" \
    "v_mfma_f32_16x16x32_bf16 v[28:31], v[112:115], a[24:27], v[28:31]\ns_waitcnt lgkmcnt(0)\ns_barrier\nds_read_b128 v[56:59], v10 offset:0\n" \
    "ds_read_b128 v[60:63], v10 offset:4608\ns_waitcnt vmcnt(10)\nv_mfma_f32_16x16x32_bf16 v[24:27], v[116:119], a[12:15], v[24:27]\n" \
    "buffer_load_dwordx4 v[112:115], v1, s[64:67], s85 offen offset:2048 sc1\nv_mfma_f32_16x16x32_bf16 v[28:31], v[116:119], a[28:31], v[28:31]\n" \
    "ds_read_b128 v[64:67], v10 offset:9216\nds_read_b128 v[68:71], v10 offset:13824\nv_lshlrev_b32 v248, 16, v252\nv_and_b32 v249, 0xffff0000, v252\n" \
    "v_lshlrev_b32 v250, 16, v253\ns_waitcnt vmcnt(10)\nv_mfma_f32_16x16x32_bf16 v[32:35], v[72:75], a[0:3], 0\n" \
    "buffer_load_dwordx4 v[116:119], v1, s[64:67], s85 offen offset:3072 sc1\nv_mfma_f32_16x16x32_bf16 v[36:39], v[72:75], a[16:19], 0\nv_and_b32 v251, 0xffff0000, v253\n" \
    "s_waitcnt vmcnt(10)\nv_mfma_f32_16x16x32_bf16 v[32:35], v[76:79], a[4:7], v[32:35]\ns_add_u32 s85, s84, 32768\n" \
    "buffer_load_dwordx4 v[72:75], v0, s[64:67], s85 offen offset:0 sc1\nv_mfma_f32_16x16x32_bf16 v[36:39], v[76:79], a[20:23], v[36:39]\nv_cvt_pk_bf16_f32 v184, v24, v25\n" \
    "v_cvt_pk_bf16_f32 v186, v26, v27\ns_waitcnt lgkmcnt(0)\nv_add_f32 v56, v56, v60\nv_add_f32 v57, v57, v61\nv_add_f32 v58, v58, v62\nv_add_f32 v59, v59, v63\n" \
    "s_waitcnt vmcnt(10)\nv_mfma_f32_16x16x32_bf16 v[32:35], v[80:83], a[8:11], v[32:35]\nbuffer_load_dwordx4 v[76:79], v0, s[64:67], s85 offen offset:1024 sc1\n" \
    "v_mfma_f32_16x16x32_bf16 v[36:39], v[80:83], a[24:27], v[36:39]\nv_cvt_pk_bf16_f32 v216, v28, v29\nv_cvt_pk_bf16_f32 v218, v30, v31\nv_add_f32 v64, v64, v68\n" \
    "v_add_f32 v65, v65, v69\nv_add_f32 v66, v66, v70\nv_add_f32 v67, v67, v71\nv_add_f32 v56, v56, v64\ns_waitcnt vmcnt(10)\n" \
    "v_mfma_f32_16x16x32_bf16 v[32:35], v[84:87], a[12:15], v[32:35]\nbuffer_load_dwordx4 v[80:83], v0, s[64:67], s85 offen offset:2048 sc1\n" \
    "v_mfma_f32_16x16x32_bf16 v[36:39], v[84:87], a[28:31], v[36:39]\nv_add_f32 v57, v57, v65\nv_add_f32 v58, v58, v66\nv_add_f32 v59, v59, v67\nv_fma_f32 v248, v56, v18, v248\n" \
    "v_fma_f32 v249, v57, v19, v249\nv_fma_f32 v250, v58, v20, v250\nv_fma_f32 v251, v59, v21, v251\ns_waitcnt vmcnt(10)\n" \
    "v_mfma_f32_16x16x32_bf16 v[24:27], v[88:91], a[32:35], 0\nbuffer_load_dwordx4 v[84:87], v0, s[64:67], s85 offen offset:3072 sc1\n" \
    "v_mfma_f32_16x16x32_bf16 v[28:31], v[88:91], a[48:51], 0\nv_cvt_pk_bf16_f32 v56, v248, v249\nv_cvt_pk_bf16_f32 v57, v250, v251\nv_lshlrev_b32 v248, 16, v56\n" \
    "v_and_b32 v249, 0xffff0000, v56\nv_lshlrev_b32 v250, 16, v57\nv_and_b32 v251, 0xffff0000, v57\nv_add_f32 v18, v248, v249\ns_waitcnt vmcnt(10)\n" \
    "v_mfma_f32_16x16x32_bf16 v[24:27], v[92:95], a[36:39], v[24:27]\nbuffer_load_dwordx4 v[88:91], v1, s[64:67], s85 offen offset:0 sc1\n" \
    "v_mfma_f32_16x16x32_bf16 v[28:31], v[92:95], a[52:55], v[28:31]\nv_cvt_pk_bf16_f32 v185, v32, v33\nv_cvt_pk_bf16_f32 v187, v34, v35\nv_add_f32 v19, v250, v251\n" \
    "v_min_u32 v56, -2, v56\nv_min_u32 v57, -2, v57\nv_add_f32 v18, v18, v19\nv_permlane32_swap_b32 v56, v58\ns_waitcnt vmcnt(10)\n" \
    "v_mfma_f32_16x16x32_bf16 v[24:27], v[96:99], a[40:43], v[24:27]\nbuffer_load_dwordx4 v[92:95], v1, s[64:67], s85 offen offset:1024 sc1\n" \
    "v_mfma_f32_16x16x32_bf16 v[28:31], v[96:99], a[56:59], v[28:31]\nv_cvt_pk_bf16_f32 v217, v36, v37\nv_cvt_pk_bf16_f32 v219, v38, v39\nv_permlane32_swap_b32 v57, v59\n" \
    "buffer_store_dwordx4 v[56:59], v12, s[64:67], s75 offen sc1\ns_waitcnt vmcnt(11)\nv_mfma_f32_16x16x32_bf16 v[24:27], v[100:103], a[44:47], v[24:27]\n" \
    "buffer_load_dwordx4 v[96:99], v1, s[64:67], s85 offen offset:2048 sc1\nv_mfma_f32_16x16x32_bf16 v[28:31], v[100:103], a[60:63], v[28:31]\n" \
    "v_add_f32_dpp v19, v18, v18 row_ror:8 row_mask:0xf bank_mask:0xf\nv_add_f32_dpp v20, v18, v18 row_ror:8 row_mask:0xf bank_mask:0xf\ns_nop 1\n" \
    "v_permlane16_swap_b32 v19, v20\nv_add_f32 v18, v19, v20\nv_add_f32 v19, v19, v20\ns_nop 1\nv_permlane32_swap_b32 v18, v19\ns_waitcnt vmcnt(11)\n" \
    "v_mfma_f32_16x16x32_bf16 v[32:35], v[104:107], a[32:35], 0\nbuffer_load_dwordx4 v[100:103], v1, s[64:67], s85 offen offset:3072 sc1\n" \
    "v_mfma_f32_16x16x32_bf16 v[36:39], v[104:107], a[48:51], 0\nv_add_f32 v18, v18, v19\nv_mul_f32 v18, 0x3d000000, v18\nv_sub_f32 v60, v248, v18\nv_sub_f32 v61, v249, v18\n" \
    "v_sub_f32 v62, v250, v18\nv_sub_f32 v63, v251, v18\nv_mul_f32 v19, v60, v60\ns_waitcnt vmcnt(11)\nv_mfma_f32_16x16x32_bf16 v[32:35], v[108:111], a[36:39], v[32:35]\n" \
    "s_add_u32 s85, s84, 49152\nbuffer_load_dwordx4 v[104:107], v0, s[64:67], s85 offen offset:0 sc1\nv_mfma_f32_16x16x32_bf16 v[36:39], v[108:111], a[52:55], v[36:39]\n" \
    "v_cvt_pk_bf16_f32 v188, v24, v25\nv_cvt_pk_bf16_f32 v190, v26, v27\nv_fmac_f32 v19, v61, v61\nv_fmac_f32 v19, v62, v62\nv_fmac_f32 v19, v63, v63\nv_min_u32 v18, -2, v18\n" \
    "s_nop 0\nv_add_f32_dpp v20, v19, v19 row_ror:8 row_mask:0xf bank_mask:0xf\ns_waitcnt vmcnt(11)\nv_mfma_f32_16x16x32_bf16 v[32:35], v[112:115], a[40:43], v[32:35]\n" \
    "buffer_load_dwordx4 v[108:111], v0, s[64:67], s85 offen offset:1024 sc1\nv_mfma_f32_16x16x32_bf16 v[36:39], v[112:115], a[56:59], v[36:39]\n" \
    "v_cvt_pk_bf16_f32 v220, v28, v29\nv_cvt_pk_bf16_f32 v222, v30, v31\nv_add_f32_dpp v21, v19, v19 row_ror:8 row_mask:0xf bank_mask:0xf\ns_nop 1\n" \
    "v_permlane16_swap_b32 v20, v21\nv_add_f32 v19, v20, v21\nv_add_f32 v20, v20, v21\ns_waitcnt vmcnt(11)\nv_mfma_f32_16x16x32_bf16 v[32:35], v[116:119], a[44:47], v[32:35]\n" \
    "buffer_load_dwordx4 v[112:115], v0, s[64:67], s85 offen offset:2048 sc1\nv_mfma_f32_16x16x32_bf16 v[36:39], v[116:119], a[60:63], v[36:39]\nv_permlane32_swap_b32 v19, v20\n" \
    "v_add_f32 v19, v19, v20\nv_min_u32 v19, -2, v19\nv_mov_b32 v22, 1\nbuffer_store_dwordx2 v[18:19], v13, s[64:67], s75 offen sc1\ns_waitcnt vmcnt(12)\n" \
    "v_mfma_f32_16x16x32_bf16 v[24:27], v[72:75], a[64:67], 0\nbuffer_load_dwordx4 v[116:119], v0, s[64:67], s85 offen offset:3072 sc1\n" \
    "v_mfma_f32_16x16x32_bf16 v[28:31], v[72:75], a[80:83], 0\ns_add_u32 s69, s50, -1\nv_mov_b32 v22, s69\ns_barrier\nbuffer_store_dword v22, v14, s[64:67], 0 offen sc1\n" \
    "s_waitcnt vmcnt(13)\nv_mfma_f32_16x16x32_bf16 v[24:27], v[76:79], a[68:71], v[24:27]\nbuffer_load_dwordx4 v[72:75], v1, s[64:67], s85 offen offset:0 sc1\n" \
    "v_mfma_f32_16x16x32_bf16 v[28:31], v[76:79], a[84:87], v[28:31]\nv_cvt_pk_bf16_f32 v189, v32, v33\nv_cvt_pk_bf16_f32 v191, v34, v35\ns_add_u32 s69, s50, 2\n" \
    "s_and_b32 s69, s69, s41\ns_lshl_b32 s69, s69, 20\ns_add_u32 s69, s69, s32\ns_add_u32 s74, s48, s69\ns_waitcnt vmcnt(13)\n" \
    "v_mfma_f32_16x16x32_bf16 v[24:27], v[80:83], a[72:75], v[24:27]\nbuffer_load_dwordx4 v[76:79], v1, s[64:67], s85 offen offset:1024 sc1\n" \
    "v_mfma_f32_16x16x32_bf16 v[28:31], v[80:83], a[88:91], v[28:31]\nv_cvt_pk_bf16_f32 v221, v36, v37\nv_cvt_pk_bf16_f32 v223, v38, v39\ns_lshr_b32 s74, s74, 8\n" \
    "s_xor_b32 s74, s74, s49\ns_and_b32 s74, s74, 0x2000\ns_add_u32 s74, s74, s69\ns_add_u32 s69, s50, 2\ns_waitcnt vmcnt(13)\n" \
    "v_mfma_f32_16x16x32_bf16 v[24:27], v[84:87], a[76:79], v[24:27]\nbuffer_load_dwordx4 v[80:83], v1, s[64:67], s85 offen offset:2048 sc1\n" \
    "v_mfma_f32_16x16x32_bf16 v[28:31], v[84:87], a[92:95], v[28:31]\ns_and_b32 s69, s69, s41\ns_lshl_b32 s69, s69, 19\ns_add_u32 s69, s69, s33\ns_add_u32 s87, s48, s69\n" \
    "s_lshr_b32 s87, s87, 8\ns_xor_b32 s87, s87, s49\ns_and_b32 s87, s87, 0x2000\ns_waitcnt vmcnt(13)\nv_mfma_f32_16x16x32_bf16 v[32:35], v[88:91], a[64:67], 0\n" \
    "buffer_load_dwordx4 v[84:87], v1, s[64:67], s85 offen offset:3072 sc1\nv_mfma_f32_16x16x32_bf16 v[36:39], v[88:91], a[80:83], 0\ns_add_u32 s87, s87, s69\n" \
    "s_add_u32 s69, s50, 3\ns_and_b32 s69, s69, s41\ns_lshl_b32 s69, s69, 18\ns_add_u32 s69, s69, s34\ns_add_u32 s88, s48, s69\ns_lshr_b32 s88, s88, 8\ns_waitcnt vmcnt(13)\n" \
    "v_mfma_f32_16x16x32_bf16 v[32:35], v[92:95], a[68:71], v[32:35]\ns_add_u32 s85, s84, 65536\nbuffer_load_dwordx4 v[88:91], v0, s[64:67], s85 offen offset:0 sc1\n" \
    "v_mfma_f32_16x16x32_bf16 v[36:39], v[92:95], a[84:87], v[36:39]\nv_cvt_pk_bf16_f32 v192, v24, v25\nv_cvt_pk_bf16_f32 v194, v26, v27\ns_xor_b32 s88, s88, s49\n" \
    "s_and_b32 s88, s88, 0x2000\ns_add_u32 s88, s88, s69\ns_xor_b32 s89, s88, 0x1000\ns_add_u32 s69, s50, 0\ns_waitcnt vmcnt(12)\n" \
    "v_mfma_f32_16x16x32_bf16 v[32:35], v[96:99], a[72:75], v[32:35]\nbuffer_load_dwordx4 v[92:95], v0, s[64:67], s85 offen offset:1024 sc1\n" \
    "v_mfma_f32_16x16x32_bf16 v[36:39], v[96:99], a[88:91], v[36:39]\nv_cvt_pk_bf16_f32 v224, v28, v29\nv_cvt_pk_bf16_f32 v226, v30, v31\ns_lshl_b32 s69, s69, 20\n" \
    "s_add_u32 s69, s69, s36\ns_add_u32 s75, s48, s69\ns_lshr_b32 s75, s75, 8\ns_xor_b32 s75, s75, s49\ns_waitcnt vmcnt(12)\n" \
    "v_mfma_f32_16x16x32_bf16 v[32:35], v[100:103], a[76:79], v[32:35]\nbuffer_load_dwordx4 v[96:99], v0, s[64:67], s85 offen offset:2048 sc1\n" \
    "v_mfma_f32_16x16x32_bf16 v[36:39], v[100:103], a[92:95], v[36:39]\ns_and_b32 s75, s75, 0x2000\ns_add_u32 s75, s75, s69\ns_add_u32 s76, s75, 0x4000\n" \
    "s_add_u32 s77, s75, s37\ns_add_u32 s82, s50, 0\ns_lshl_b32 s80, s82, 10\ns_lshl_b32 s82, s82, 2\ns_waitcnt vmcnt(12)\n" \
    "v_mfma_f32_16x16x32_bf16 v[24:27], v[104:107], a[96:99], 0\nbuffer_load_dwordx4 v[100:103], v0, s[64:67], s85 offen offset:3072 sc1\n" \
    "v_mfma_f32_16x16x32_bf16 v[28:31], v[104:107], a[112:115], 0\ns_add_u32 s82, s82, s40\ns_waitcnt vmcnt(12)\n" \
    "v_mfma_f32_16x16x32_bf16 v[24:27], v[108:111], a[100:103], v[24:27]\nbuffer_load_dwordx4 v[104:107], v1, s[64:67], s85 offen offset:0 sc1\n" \
    "v_mfma_f32_16x16x32_bf16 v[28:31], v[108:111], a[116:119], v[28:31]\nv_cvt_pk_bf16_f32 v193, v32, v33\nv_cvt_pk_bf16_f32 v195, v34, v35\ns_waitcnt vmcnt(12)\n" \
    "v_mfma_f32_16x16x32_bf16 v[24:27], v[112:115], a[104:107], v[24:27]\nbuffer_load_dwordx4 v[108:111], v1, s[64:67], s85 offen offset:1024 sc1\n" \
    "v_mfma_f32_16x16x32_bf16 v[28:31], v[112:115], a[120:123], v[28:31]\nv_cvt_pk_bf16_f32 v225, v36, v37\nv_cvt_pk_bf16_f32 v227, v38, v39\ns_waitcnt vmcnt(11)\n" \
    "v_mfma_f32_16x16x32_bf16 v[24:27], v[116:119], a[108:111], v[24:27]\nbuffer_load_dwordx4 v[112:115], v1, s[64:67], s85 offen offset:2048 sc1\n" \
    "v_mfma_f32_16x16x32_bf16 v[28:31], v[116:119], a[124:127], v[28:31]\ns_waitcnt vmcnt(10)\nv_mfma_f32_16x16x32_bf16 v[32:35], v[72:75], a[96:99], 0\n" \
    "buffer_load_dwordx4 v[116:119], v1, s[64:67], s85 offen offset:3072 sc1\nv_mfma_f32_16x16x32_bf16 v[36:39], v[72:75], a[112:115], 0\ns_waitcnt vmcnt(10)\n" \
    "v_mfma_f32_16x16x32_bf16 v[32:35], v[76:79], a[100:103], v[32:35]\ns_add_u32 s85, s84, 81920\nbuffer_load_dwordx4 v[72:75], v0, s[64:67], s85 offen offset:0 sc1\n" \
    "v_mfma_f32_16x16x32_bf16 v[36:39], v[76:79], a[116:119], v[36:39]\nv_cvt_pk_bf16_f32 v196, v24, v25\nv_cvt_pk_bf16_f32 v198, v26, v27\ns_waitcnt vmcnt(10)\n" \
    "v_mfma_f32_16x16x32_bf16 v[32:35], v[80:83], a[104:107], v[32:35]\nbuffer_load_dwordx4 v[76:79], v0, s[64:67], s85 offen offset:1024 sc1\n" \
    "v_mfma_f32_16x16x32_bf16 v[36:39], v[80:83], a[120:123], v[36:39]\nv_cvt_pk_bf16_f32 v228, v28, v29\nv_cvt_pk_bf16_f32 v230, v30, v31\ns_waitcnt vmcnt(10)\n" \
    "v_mfma_f32_16x16x32_bf16 v[32:35], v[84:87], a[108:111], v[32:35]\nbuffer_load_dwordx4 v[80:83], v0, s[64:67], s85 offen offset:2048 sc1\n" \
    "v_mfma_f32_16x16x32_bf16 v[36:39], v[84:87], a[124:127], v[36:39]\ns_waitcnt vmcnt(10)\nv_mfma_f32_16x16x32_bf16 v[24:27], v[88:91], a[128:131], 0\n" \
    "buffer_load_dwordx4 v[84:87], v0, s[64:67], s85 offen offset:3072 sc1\nv_mfma_f32_16x16x32_bf16 v[28:31], v[88:91], a[144:147], 0\ns_waitcnt vmcnt(10)\n" \
    "v_mfma_f32_16x16x32_bf16 v[24:27], v[92:95], a[132:135], v[24:27]\nbuffer_load_dwordx4 v[88:91], v1, s[64:67], s85 offen offset:0 sc1\n" \
    "v_mfma_f32_16x16x32_bf16 v[28:31], v[92:95], a[148:151], v[28:31]\nv_cvt_pk_bf16_f32 v197, v32, v33\nv_cvt_pk_bf16_f32 v199, v34, v35\ns_waitcnt vmcnt(10)\n" \
    "v_mfma_f32_16x16x32_bf16 v[24:27], v[96:99], a[136:139], v[24:27]\nbuffer_load_dwordx4 v[92:95], v1, s[64:67], s85 offen offset:1024 sc1\n" \
    "v_mfma_f32_16x16x32_bf16 v[28:31], v[96:99], a[152:155], v[28:31]\nv_cvt_pk_bf16_f32 v229, v36, v37\nv_cvt_pk_bf16_f32 v231, v38, v39\ns_waitcnt vmcnt(10)\n" \
    "v_mfma_f32_16x16x32_bf16 v[24:27], v[100:103], a[140:143], v[24:27]\nbuffer_load_dwordx4 v[96:99], v1, s[64:67], s85 offen offset:2048 sc1\n" \
    "v_mfma_f32_16x16x32_bf16 v[28:31], v[100:103], a[156:159], v[28:31]\ns_waitcnt vmcnt(10)\nv_mfma_f32_16x16x32_bf16 v[32:35], v[104:107], a[128:131], 0\n" \
    "buffer_load_dwordx4 v[100:103], v1, s[64:67], s85 offen offset:3072 sc1\nv_mfma_f32_16x16x32_bf16 v[36:39], v[104:107], a[144:147], 0\ns_waitcnt vmcnt(10)\n" \
    "v_mfma_f32_16x16x32_bf16 v[32:35], v[108:111], a[132:135], v[32:35]\ns_add_u32 s85, s84, 98304\nbuffer_load_dwordx4 v[104:107], v0, s[64:67], s85 offen offset:0 sc1\n" \
    "v_mfma_f32_16x16x32_bf16 v[36:39], v[108:111], a[148:151], v[36:39]\nv_cvt_pk_bf16_f32 v200, v24, v25\nv_cvt_pk_bf16_f32 v202, v26, v27\ns_waitcnt vmcnt(10)\n" \
    "v_mfma_f32_16x16x32_bf16 v[32:35], v[112:115], a[136:139], v[32:35]\nbuffer_load_dwordx4 v[108:111], v0, s[64:67], s85 offen offset:1024 sc1\n" \
    "v_mfma_f32_16x16x32_bf16 v[36:39], v[112:115], a[152:155], v[36:39]\nv_cvt_pk_bf16_f32 v232, v28, v29\nv_cvt_pk_bf16_f32 v234, v30, v31\ns_waitcnt vmcnt(10)\n" \
    "v_mfma_f32_16x16x32_bf16 v[32:35], v[116:119], a[140:143], v[32:35]\nbuffer_load_dwordx4 v[112:115], v0, s[64:67], s85 offen offset:2048 sc1\n" \
    "v_mfma_f32_16x16x32_bf16 v[36:39], v[116:119], a[156:159], v[36:39]\ns_waitcnt vmcnt(10)\nv_mfma_f32_16x16x32_bf16 v[24:27], v[72:75], a[160:163], 0\n" \
    "buffer_load_dwordx4 v[116:119], v0, s[64:67], s85 offen offset:3072 sc1\nv_mfma_f32_16x16x32_bf16 v[28:31], v[72:75], a[176:179], 0\n" \
    "buffer_load_dword v17, v8, s[64:67], 0 offen sc1\ns_waitcnt vmcnt(11)\nv_mfma_f32_16x16x32_bf16 v[24:27], v[76:79], a[164:167], v[24:27]\n" \
    "buffer_load_dwordx4 v[72:75], v1, s[64:67], s85 offen offset:0 sc1\nv_mfma_f32_16x16x32_bf16 v[28:31], v[76:79], a[180:183], v[28:31]\nv_cvt_pk_bf16_f32 v201, v32, v33\n" \
    "v_cvt_pk_bf16_f32 v203, v34, v35\ns_waitcnt vmcnt(11)\nv_mfma_f32_16x16x32_bf16 v[24:27], v[80:83], a[168:171], v[24:27]\n" \
    "buffer_load_dwordx4 v[76:79], v1, s[64:67], s85 offen offset:1024 sc1\nv_mfma_f32_16x16x32_bf16 v[28:31], v[80:83], a[184:187], v[28:31]\nv_cvt_pk_bf16_f32 v233, v36, v37\n" \
    "v_cvt_pk_bf16_f32 v235, v38, v39\ns_waitcnt vmcnt(11)\nv_mfma_f32_16x16x32_bf16 v[24:27], v[84:87], a[172:175], v[24:27]\n" \
    "buffer_load_dwordx4 v[80:83], v1, s[64:67], s85 offen offset:2048 sc1\nv_mfma_f32_16x16x32_bf16 v[28:31], v[84:87], a[188:191], v[28:31]\ns_waitcnt vmcnt(11)\n" \
    "v_mfma_f32_16x16x32_bf16 v[32:35], v[88:91], a[160:163], 0\nbuffer_load_dwordx4 v[84:87], v1, s[64:67], s85 offen offset:3072 sc1\n" \
    "v_mfma_f32_16x16x32_bf16 v[36:39], v[88:91], a[176:179], 0\ns_waitcnt vmcnt(11)\nv_mfma_f32_16x16x32_bf16 v[32:35], v[92:95], a[164:167], v[32:35]\n" \
    "s_add_u32 s85, s84, 114688\nbuffer_load_dwordx4 v[88:91], v0, s[64:67], s85 offen offset:0 sc1\nv_mfma_f32_16x16x32_bf16 v[36:39], v[92:95], a[180:183], v[36:39]\n" \
    "v_cvt_pk_bf16_f32 v204, v24, v25\nv_cvt_pk_bf16_f32 v206, v26, v27\ns_waitcnt vmcnt(11)\nv_mfma_f32_16x16x32_bf16 v[32:35], v[96:99], a[168:171], v[32:35]\n" \
    "buffer_load_dwordx4 v[92:95], v0, s[64:67], s85 offen offset:1024 sc1\nv_mfma_f32_16x16x32_bf16 v[36:39], v[96:99], a[184:187], v[36:39]\nv_cvt_pk_bf16_f32 v236, v28, v29\n" \
    "v_cvt_pk_bf16_f32 v238, v30, v31\ns_add_u32 s69, s50, 4\ns_and_b32 s69, s69, s41\ns_lshl_b32 s69, s69, 19\ns_add_u32 s69, s69, s35\ns_add_u32 s84, s48, s69\n" \
    "s_waitcnt vmcnt(11)\nv_mfma_f32_16x16x32_bf16 v[32:35], v[100:103], a[172:175], v[32:35]\nbuffer_load_dwordx4 v[96:99], v0, s[64:67], s85 offen offset:2048 sc1\n" \
    "v_mfma_f32_16x16x32_bf16 v[36:39], v[100:103], a[188:191], v[36:39]\ns_lshr_b32 s84, s84, 8\ns_xor_b32 s84, s84, s49\ns_and_b32 s84, s84, 0x2000\ns_add_u32 s84, s84, s69\n" \
    "s_waitcnt vmcnt(11)\nv_mfma_f32_16x16x32_bf16 v[24:27], v[104:107], a[192:195], 0\nbuffer_load_dwordx4 v[100:103], v0, s[64:67], s85 offen offset:3072 sc1\n" \
    "v_mfma_f32_16x16x32_bf16 v[28:31], v[104:107], a[208:211], 0\ns_waitcnt vmcnt(11)\nv_mfma_f32_16x16x32_bf16 v[24:27], v[108:111], a[196:199], v[24:27]\n" \
    "buffer_load_dwordx4 v[104:107], v1, s[64:67], s85 offen offset:0 sc1\nv_mfma_f32_16x16x32_bf16 v[28:31], v[108:111], a[212:215], v[28:31]\n" \
    "v_cvt_pk_bf16_f32 v205, v32, v33\nv_cvt_pk_bf16_f32 v207, v34, v35\ns_waitcnt vmcnt(11)\nv_mfma_f32_16x16x32_bf16 v[24:27], v[112:115], a[200:203], v[24:27]\n" \
    "buffer_load_dwordx4 v[108:111], v1, s[64:67], s85 offen offset:1024 sc1\nv_mfma_f32_16x16x32_bf16 v[28:31], v[112:115], a[216:219], v[28:31]\n" \
    "v_cvt_pk_bf16_f32 v237, v36, v37\nv_cvt_pk_bf16_f32 v239, v38, v39\ns_waitcnt vmcnt(11)\nv_mfma_f32_16x16x32_bf16 v[24:27], v[116:119], a[204:207], v[24:27]\n" \
    "buffer_load_dwordx4 v[112:115], v1, s[64:67], s85 offen offset:2048 sc1\nv_mfma_f32_16x16x32_bf16 v[28:31], v[116:119], a[220:223], v[28:31]\ns_waitcnt vmcnt(10)\n" \
    "v_mfma_f32_16x16x32_bf16 v[32:35], v[72:75], a[192:195], 0\nbuffer_load_dwordx4 v[116:119], v1, s[64:67], s85 offen offset:3072 sc1\n" \
    "v_mfma_f32_16x16x32_bf16 v[36:39], v[72:75], a[208:211], 0\ns_mov_b32 s86, 0\nv_mov_b32 v18, 0x7f000000\ns_add_u32 s70, s50, 4\ns_add_u32 s71, s50, 0\n" \
    "v_cmp_lt_i32 vcc, s70, v17\ns_nop 3\ns_and_b32 s69, vcc_lo, 0xff\ns_cmp_eq_u32 s69, 0xff\ns_cbranch_scc0 .Lr117_%=\nv_readlane_b32 s69, v17, 10\ns_cmp_ge_i32 s69, s71\n" \
    "s_cbranch_scc0 .Lr117_%=\ns_mov_b32 s86, 1\nv_mov_b32 v18, v0\n.Lr117_%=:\nv_add_u32 v19, 0x1000, v18\ns_waitcnt vmcnt(10)\n" \
    "v_mfma_f32_16x16x32_bf16 v[32:35], v[76:79], a[196:199], v[32:35]\nv_mfma_f32_16x16x32_bf16 v[36:39], v[76:79], a[212:215], v[36:39]\nv_cvt_pk_bf16_f32 v208, v24, v25\n" \
    "v_cvt_pk_bf16_f32 v210, v26, v27\nbuffer_load_dwordx4 v[72:75], v18, s[64:67], s75 offen offset:0 sc1\ns_waitcnt vmcnt(10)\n" \
    "v_mfma_f32_16x16x32_bf16 v[32:35], v[80:83], a[200:203], v[32:35]\nv_mfma_f32_16x16x32_bf16 v[36:39], v[80:83], a[216:219], v[36:39]\nv_cvt_pk_bf16_f32 v240, v28, v29\n" \
    "v_cvt_pk_bf16_f32 v242, v30, v31\nbuffer_load_dwordx4 v[76:79], v18, s[64:67], s75 offen offset:1024 sc1\ns_waitcnt vmcnt(10)\n" \
    "v_mfma_f32_16x16x32_bf16 v[32:35], v[84:87], a[204:207], v[32:35]\nv_mfma_f32_16x16x32_bf16 v[36:39], v[84:87], a[220:223], v[36:39]\n" \
    "buffer_load_dwordx4 v[80:83], v18, s[64:67], s75 offen offset:2048 sc1\ns_waitcnt vmcnt(10)\nv_mfma_f32_16x16x32_bf16 v[24:27], v[88:91], a[224:227], 0\n" \
    "v_mfma_f32_16x16x32_bf16 v[28:31], v[88:91], a[240:243], 0\nbuffer_load_dwordx4 v[84:87], v18, s[64:67], s75 offen offset:3072 sc1\ns_waitcnt vmcnt(10)\n" \
    "v_mfma_f32_16x16x32_bf16 v[24:27], v[92:95], a[228:231], v[24:27]\nv_mfma_f32_16x16x32_bf16 v[28:31], v[92:95], a[244:247], v[28:31]\nv_cvt_pk_bf16_f32 v209, v32, v33\n" \
    "v_cvt_pk_bf16_f32 v211, v34, v35\nbuffer_load_dwordx4 v[88:91], v19, s[64:67], s75 offen offset:0 sc1\ns_waitcnt vmcnt(10)\n" \
    "v_mfma_f32_16x16x32_bf16 v[24:27], v[96:99], a[232:235], v[24:27]\nv_mfma_f32_16x16x32_bf16 v[28:31], v[96:99], a[248:251], v[28:31]\nv_cvt_pk_bf16_f32 v241, v36, v37\n" \
    "v_cvt_pk_bf16_f32 v243, v38, v39\nbuffer_load_dwordx4 v[92:95], v19, s[64:67], s75 offen offset:1024 sc1\ns_waitcnt vmcnt(10)\n" \
    "v_mfma_f32_16x16x32_bf16 v[24:27], v[100:103], a[236:239], v[24:27]\nv_mfma_f32_16x16x32_bf16 v[28:31], v[100:103], a[252:255], v[28:31]\n" \
    "buffer_load_dwordx4 v[96:99], v19, s[64:67], s75 offen offset:2048 sc1\ns_waitcnt vmcnt(10)\nv_mfma_f32_16x16x32_bf16 v[32:35], v[104:107], a[224:227], 0\n" \
    "v_mfma_f32_16x16x32_bf16 v[36:39], v[104:107], a[240:243], 0\ns_waitcnt vmcnt(9)\nv_mfma_f32_16x16x32_bf16 v[32:35], v[108:111], a[228:231], v[32:35]\n" \
    "v_mfma_f32_16x16x32_bf16 v[36:39], v[108:111], a[244:247], v[36:39]\nv_cvt_pk_bf16_f32 v212, v24, v25\nv_cvt_pk_bf16_f32 v214, v26, v27\ns_waitcnt vmcnt(8)\n" \
    "v_mfma_f32_16x16x32_bf16 v[32:35], v[112:115], a[232:235], v[32:35]\nv_mfma_f32_16x16x32_bf16 v[36:39], v[112:115], a[248:251], v[36:39]\nv_cvt_pk_bf16_f32 v244, v28, v29\n" \
    "v_cvt_pk_bf16_f32 v246, v30, v31\ns_waitcnt vmcnt(7)\nv_mfma_f32_16x16x32_bf16 v[32:35], v[116:119], a[236:239], v[32:35]\n" \
    "v_mfma_f32_16x16x32_bf16 v[36:39], v[116:119], a[252:255], v[36:39]\ns_nop 6\nv_cvt_pk_bf16_f32 v213, v32, v33\nv_cvt_pk_bf16_f32 v215, v34, v35\n" \
    "v_cvt_pk_bf16_f32 v245, v36, v37\nv_cvt_pk_bf16_f32 v247, v38, v39\ns_add_u32 s50, s50, 2\ns_cmp_lt_u32 s50, 62\ns_cbranch_scc1 .Lr5_%=\ns_cmp_eq_u32 s50, 63\n" \
    "s_cbranch_scc1 .Lr6_%=\ns_add_u32 s69, s50, 0\ns_and_b32 s69, s69, s41\ns_lshl_b32 s69, s69, 20\ns_add_u32 s69, s69, s32\ns_add_u32 s74, s48, s69\ns_lshr_b32 s74, s74, 8\n" \
    "s_xor_b32 s74, s74, s49\ns_and_b32 s74, s74, 0x2000\ns_add_u32 s74, s74, s69\ns_add_u32 s69, s50, 0\ns_and_b32 s69, s69, s41\ns_lshl_b32 s69, s69, 19\n" \
    "s_add_u32 s69, s69, s33\ns_add_u32 s87, s48, s69\ns_lshr_b32 s87, s87, 8\ns_xor_b32 s87, s87, s49\ns_and_b32 s87, s87, 0x2000\ns_add_u32 s87, s87, s69\n" \
    "s_add_u32 s69, s50, 1\ns_and_b32 s69, s69, s41\ns_lshl_b32 s69, s69, 18\ns_add_u32 s69, s69, s34\ns_add_u32 s88, s48, s69\ns_lshr_b32 s88, s88, 8\ns_xor_b32 s88, s88, s49\n" \
    "s_and_b32 s88, s88, 0x2000\ns_add_u32 s88, s88, s69\ns_xor_b32 s89, s88, 0x1000\ns_mov_b32 s68, 0\ns_add_u32 s78, s87, s38\n.Lr120_%=:\ns_sub_u32 s70, s51, 52\n.Lr129_%=:\n" \
    "s_sleep 1\ns_memrealtime s[72:73]\ns_waitcnt lgkmcnt(0)\ns_sub_u32 s72, s72, s70\ns_cmp_lt_i32 s72, 0\ns_cbranch_scc1 .Lr129_%=\n" \
    "buffer_load_dword v20, off, s[64:67], s78 sc1\ns_waitcnt vmcnt(0)\nv_readfirstlane_b32 s70, v20\ns_memrealtime s[72:73]\ns_cmp_eq_u32 s70, -1\ns_cbranch_scc1 .Lr121_%=\n" \
    "s_lshr_b32 s71, s70, 8\ns_waitcnt lgkmcnt(0)\ns_sub_u32 s71, s72, s71\ns_and_b32 s71, s71, 0xffffff\ns_sub_u32 s51, s72, s71\ns_add_u32 s51, s51, 176\n" \
    "s_and_b32 s70, s70, 128\ns_lshl_b32 s52, s70, 5\ns_xor_b32 s87, s87, s52\ns_mov_b32 s68, 0\ns_sub_u32 s70, s51, s54\n.Lr130_%=:\ns_memrealtime s[72:73]\n" \
    "s_waitcnt lgkmcnt(0)\ns_sub_u32 s72, s72, s70\ns_cmp_lt_i32 s72, 0\ns_cbranch_scc1 .Lr130_%=\nbuffer_load_dwordx4 v[56:59], v0, s[64:67], s74 offen offset:0 sc1\n" \
    "buffer_load_dwordx4 v[60:63], v0, s[64:67], s74 offen offset:1024 sc1\ns_add_u32 s69, s74, 0x4000\nbuffer_load_dwordx4 v[64:67], v0, s[64:67], s69 offen offset:0 sc1\n" \
    "buffer_load_dwordx4 v[68:71], v0, s[64:67], s69 offen offset:1024 sc1\n.Lr131_%=:\ns_memrealtime s[72:73]\ns_waitcnt lgkmcnt(0)\ns_sub_u32 s72, s72, s51\n" \
    "s_cmp_lt_i32 s72, -3\ns_cbranch_scc1 .Lr131_%=\ns_cmp_ge_i32 s72, 0\ns_cbranch_scc1 .Lr132_%=\ns_nop 2\ns_cmp_ge_i32 s72, -1\ns_cbranch_scc1 .Lr132_%=\ns_nop 2\n" \
    "s_cmp_ge_i32 s72, -2\ns_cbranch_scc1 .Lr132_%=\ns_nop 2\n.Lr132_%=:\nbuffer_load_dwordx4 v[100:103], v0, s[64:67], s87 offen offset:0 sc1\n" \
    "buffer_load_dwordx4 v[104:107], v0, s[64:67], s87 offen offset:1024 sc1\nbuffer_load_dwordx4 v[108:111], v0, s[64:67], s87 offen offset:2048 sc1\n" \
    "buffer_load_dwordx4 v[112:115], v0, s[64:67], s87 offen offset:3072 sc1\nbuffer_load_dwordx2 v[116:117], v5, s[64:67], s87 offen offset:0 sc1\n" \
    "buffer_load_dwordx2 v[118:119], v5, s[64:67], s87 offen offset:128 sc1\n.Lr124_%=:\nv_mov_b64 v[24:25], 0\nv_mov_b64 v[26:27], 0\nv_mov_b64 v[28:29], 0\n" \
    "v_mov_b64 v[30:31], 0\ns_waitcnt vmcnt(6)\nv_max3_u32 v22, v56, v57, v58\nv_max3_u32 v22, v22, v59, v60\nv_max3_u32 v22, v22, v61, v62\nv_max3_u32 v22, v22, v63, v64\n" \
    "v_max3_u32 v22, v22, v65, v66\nv_max3_u32 v22, v22, v67, v68\nv_max3_u32 v22, v22, v69, v70\nv_max_u32 v22, v22, v71\ns_waitcnt vmcnt(5)\n" \
    "v_smfmac_f32_16x16x64_bf16 v[24:27], v[100:103], v[184:191], v4\nv_max3_u32 v22, v22, v100, v101\nv_smfmac_f32_16x16x64_bf16 v[28:31], v[100:103], v[216:223], v4\n" \
    "v_max3_u32 v22, v22, v102, v103\ns_waitcnt vmcnt(4)\nv_smfmac_f32_16x16x64_bf16 v[24:27], v[104:107], v[192:199], v4\nv_max3_u32 v22, v22, v104, v105\n" \
    "v_smfmac_f32_16x16x64_bf16 v[28:31], v[104:107], v[224:231], v4\nv_max3_u32 v22, v22, v106, v107\ns_waitcnt vmcnt(3)\n" \
    "v_smfmac_f32_16x16x64_bf16 v[24:27], v[108:111], v[200:207], v4\nv_max3_u32 v22, v22, v108, v109\nv_smfmac_f32_16x16x64_bf16 v[28:31], v[108:111], v[232:239], v4\n" \
    "v_max3_u32 v22, v22, v110, v111\ns_waitcnt vmcnt(2)\nv_smfmac_f32_16x16x64_bf16 v[24:27], v[112:115], v[208:215], v4\nv_max3_u32 v22, v22, v112, v113\n" \
    "v_smfmac_f32_16x16x64_bf16 v[28:31], v[112:115], v[240:247], v4\nv_max3_u32 v22, v22, v114, v115\ns_waitcnt vmcnt(0)\n" \
    "v_smfmac_f32_16x16x64_bf16 v[24:27], v[116:119], v[56:63], v4\nv_max3_u32 v22, v22, v116, v117\nv_smfmac_f32_16x16x64_bf16 v[28:31], v[116:119], v[64:71], v4\n" \
    "v_max3_u32 v22, v22, v118, v119\nv_cmp_eq_u32 vcc, -1, v22\ns_nop 5\nv_permlane32_swap_b32 v24, v28\nv_permlane32_swap_b32 v25, v29\nv_permlane32_swap_b32 v26, v30\n" \
    "v_permlane32_swap_b32 v27, v31\nv_pk_add_f32 v[24:25], v[24:25], v[28:29]\nv_pk_add_f32 v[26:27], v[26:27], v[30:31]\nds_write2_b32 v6, v24, v25 offset0:0 offset1:36\n" \
    "ds_write2_b32 v6, v26, v27 offset0:72 offset1:108\ns_cbranch_vccnz .Lr125_%=\ns_waitcnt lgkmcnt(0)\ns_barrier\nds_read_b128 v[24:27], v7 offset:0\n" \
    "ds_read_b128 v[28:31], v7 offset:1152\nds_read_b128 v[32:35], v7 offset:2304\nds_read_b128 v[36:39], v7 offset:3456\ns_waitcnt lgkmcnt(2)\n" \
    "v_pk_add_f32 v[24:25], v[24:25], v[28:29]\nv_pk_add_f32 v[26:27], v[26:27], v[30:31]\ns_waitcnt lgkmcnt(0)\nv_pk_add_f32 v[32:33], v[32:33], v[36:37]\n" \
    "v_pk_add_f32 v[34:35], v[34:35], v[38:39]\nv_pk_add_f32 v[24:25], v[24:25], v[32:33]\nv_pk_add_f32 v[26:27], v[26:27], v[34:35]\nv_cvt_pk_bf16_f32 v248, v24, v25\n" \
    "v_cvt_pk_bf16_f32 v249, v26, v27\ns_nop 0\nv_permlane32_swap_b32 v248, v250\nv_permlane32_swap_b32 v249, v251\ns_mov_b32 exec_hi, 0\n" \
    "buffer_store_dwordx4 v[248:251], v0, s[64:67], s88 offen sc1\nbuffer_store_dwordx4 v[248:251], v0, s[64:67], s89 offen sc1\ns_mov_b32 exec_hi, -1\ns_add_u32 s51, s51, s55\n" \
    "s_add_u32 s51, s51, s55\n.Lr6_%=:\ns_waitcnt vmcnt(0)\ns_add_u32 s70, s50, 2\ns_add_u32 s71, s50, -2\ns_mov_b32 s68, 0\ns_branch .Lr137_%=\n.Lr136_%=:\n" \
    "buffer_load_dword v17, v8, s[64:67], 0 offen sc1\ns_waitcnt vmcnt(0)\n.Lr137_%=:\nv_readlane_b32 s69, v17, 9\ns_cmp_gt_i32 s69, s71\ns_cbranch_scc0 .Lr138_%=\n" \
    "s_mov_b32 s68, 0\ns_add_u32 s69, s50, -2\ns_lshl_b32 s69, s69, 20\ns_add_u32 s69, s69, s36\ns_add_u32 s75, s48, s69\ns_lshr_b32 s75, s75, 8\ns_xor_b32 s75, s75, s49\n" \
    "s_and_b32 s75, s75, 0x2000\ns_add_u32 s75, s75, s69\ns_add_u32 s76, s75, 0x4000\ns_add_u32 s77, s75, s37\ns_add_u32 s82, s50, -2\ns_lshl_b32 s80, s82, 10\n" \
    "s_lshl_b32 s82, s82, 2\ns_add_u32 s82, s82, s40\n.Lr139_%=:\nbuffer_load_dwordx4 v[72:75], v0, s[64:67], s75 offen offset:0 sc1\n" \
    "buffer_load_dwordx4 v[76:79], v0, s[64:67], s75 offen offset:1024 sc1\nbuffer_load_dwordx4 v[80:83], v0, s[64:67], s75 offen offset:2048 sc1\n" \
    "buffer_load_dwordx4 v[84:87], v0, s[64:67], s75 offen offset:3072 sc1\nbuffer_load_dwordx4 v[88:91], v1, s[64:67], s75 offen offset:0 sc1\n" \
    "buffer_load_dwordx4 v[92:95], v1, s[64:67], s75 offen offset:1024 sc1\nbuffer_load_dwordx4 v[96:99], v1, s[64:67], s75 offen offset:2048 sc1\n" \
    "buffer_load_dwordx4 v[100:103], v1, s[64:67], s75 offen offset:3072 sc1\nbuffer_load_dwordx4 v[104:107], v0, s[64:67], s76 offen offset:0 sc1\n" \
    "buffer_load_dwordx4 v[108:111], v0, s[64:67], s76 offen offset:1024 sc1\nbuffer_load_dwordx4 v[112:115], v0, s[64:67], s76 offen offset:2048 sc1\n.Lr140_%=:\n" \
    "v_mov_b32 v22, 0\ns_waitcnt vmcnt(10)\nv_mfma_f32_16x16x32_bf16 v[24:27], v[120:123], v[72:75], 0\nbuffer_load_dwordx4 v[116:119], v0, s[64:67], s76 offen offset:3072 sc1\n" \
    "v_mfma_f32_16x16x32_bf16 v[28:31], v[152:155], v[72:75], 0\nv_max3_u32 v22, v22, v72, v73\nv_max3_u32 v22, v22, v74, v75\ns_waitcnt vmcnt(10)\n" \
    "v_mfma_f32_16x16x32_bf16 v[32:35], v[120:123], v[76:79], 0\nbuffer_load_dwordx4 v[72:75], v1, s[64:67], s76 offen offset:0 sc1\n" \
    "v_mfma_f32_16x16x32_bf16 v[36:39], v[152:155], v[76:79], 0\nv_max3_u32 v22, v22, v76, v77\nv_max3_u32 v22, v22, v78, v79\ns_waitcnt vmcnt(10)\n" \
    "v_mfma_f32_16x16x32_bf16 v[24:27], v[124:127], v[80:83], v[24:27]\nbuffer_load_dwordx4 v[76:79], v1, s[64:67], s76 offen offset:1024 sc1\n" \
    "v_mfma_f32_16x16x32_bf16 v[28:31], v[156:159], v[80:83], v[28:31]\nv_max3_u32 v22, v22, v80, v81\nv_max3_u32 v22, v22, v82, v83\ns_waitcnt vmcnt(10)\n" \
    "v_mfma_f32_16x16x32_bf16 v[32:35], v[124:127], v[84:87], v[32:35]\nbuffer_load_dwordx4 v[80:83], v1, s[64:67], s76 offen offset:2048 sc1\n" \
    "v_mfma_f32_16x16x32_bf16 v[36:39], v[156:159], v[84:87], v[36:39]\nv_max3_u32 v22, v22, v84, v85\nv_max3_u32 v22, v22, v86, v87\ns_waitcnt vmcnt(10)\n" \
    "v_mfma_f32_16x16x32_bf16 v[24:27], v[128:131], v[88:91], v[24:27]\nbuffer_load_dwordx4 v[84:87], v1, s[64:67], s76 offen offset:3072 sc1\n" \
    "v_mfma_f32_16x16x32_bf16 v[28:31], v[160:163], v[88:91], v[28:31]\nv_max3_u32 v22, v22, v88, v89\nv_max3_u32 v22, v22, v90, v91\ns_waitcnt vmcnt(10)\n" \
    "v_mfma_f32_16x16x32_bf16 v[32:35], v[128:131], v[92:95], v[32:35]\nbuffer_load_dwordx4 v[88:91], v0, s[64:67], s77 offen offset:0 sc1\n" \
    "v_mfma_f32_16x16x32_bf16 v[36:39], v[160:163], v[92:95], v[36:39]\nv_max3_u32 v22, v22, v92, v93\nv_max3_u32 v22, v22, v94, v95\ns_waitcnt vmcnt(10)\n" \
    "v_mfma_f32_16x16x32_bf16 v[24:27], v[132:135], v[96:99], v[24:27]\nbuffer_load_dwordx4 v[92:95], v0, s[64:67], s77 offen offset:1024 sc1\n" \
    "v_mfma_f32_16x16x32_bf16 v[28:31], v[164:167], v[96:99], v[28:31]\nv_max3_u32 v22, v22, v96, v97\nv_max3_u32 v22, v22, v98, v99\ns_waitcnt vmcnt(10)\n" \
    "v_mfma_f32_16x16x32_bf16 v[32:35], v[132:135], v[100:103], v[32:35]\nbuffer_load_dwordx4 v[96:99], v0, s[64:67], s77 offen offset:2048 sc1\n" \
    "v_mfma_f32_16x16x32_bf16 v[36:39], v[164:167], v[100:103], v[36:39]\nv_max3_u32 v22, v22, v100, v101\nv_max3_u32 v22, v22, v102, v103\ns_waitcnt vmcnt(10)\n" \
    "v_mfma_f32_16x16x32_bf16 v[24:27], v[136:139], v[104:107], v[24:27]\nbuffer_load_dwordx4 v[100:103], v0, s[64:67], s77 offen offset:3072 sc1\n" \
    "v_mfma_f32_16x16x32_bf16 v[28:31], v[168:171], v[104:107], v[28:31]\nv_max3_u32 v22, v22, v104, v105\nv_max3_u32 v22, v22, v106, v107\ns_waitcnt vmcnt(10)\n" \
    "v_mfma_f32_16x16x32_bf16 v[32:35], v[136:139], v[108:111], v[32:35]\nv_mfma_f32_16x16x32_bf16 v[36:39], v[168:171], v[108:111], v[36:39]\nv_max3_u32 v22, v22, v108, v109\n" \
    "v_max3_u32 v22, v22, v110, v111\ns_waitcnt vmcnt(9)\nv_mfma_f32_16x16x32_bf16 v[24:27], v[140:143], v[112:115], v[24:27]\n" \
    "v_mfma_f32_16x16x32_bf16 v[28:31], v[172:175], v[112:115], v[28:31]\nv_max3_u32 v22, v22, v112, v113\nv_max3_u32 v22, v22, v114, v115\ns_waitcnt vmcnt(8)\n" \
    "v_mfma_f32_16x16x32_bf16 v[32:35], v[140:143], v[116:119], v[32:35]\nv_mfma_f32_16x16x32_bf16 v[36:39], v[172:175], v[116:119], v[36:39]\nv_max3_u32 v22, v22, v116, v117\n" \
    "v_max3_u32 v22, v22, v118, v119\ns_waitcnt vmcnt(7)\nv_mfma_f32_16x16x32_bf16 v[24:27], v[144:147], v[72:75], v[24:27]\n" \
    "v_mfma_f32_16x16x32_bf16 v[28:31], v[176:179], v[72:75], v[28:31]\nv_max3_u32 v22, v22, v72, v73\nv_max3_u32 v22, v22, v74, v75\ns_waitcnt vmcnt(6)\n" \
    "v_mfma_f32_16x16x32_bf16 v[32:35], v[144:147], v[76:79], v[32:35]\nv_mfma_f32_16x16x32_bf16 v[36:39], v[176:179], v[76:79], v[36:39]\nv_max3_u32 v22, v22, v76, v77\n" \
    "v_max3_u32 v22, v22, v78, v79\ns_waitcnt vmcnt(5)\nv_mfma_f32_16x16x32_bf16 v[24:27], v[148:151], v[80:83], v[24:27]\n" \
    "v_mfma_f32_16x16x32_bf16 v[28:31], v[180:183], v[80:83], v[28:31]\nv_max3_u32 v22, v22, v80, v81\nv_max3_u32 v22, v22, v82, v83\ns_waitcnt vmcnt(4)\n" \
    "v_mfma_f32_16x16x32_bf16 v[32:35], v[148:151], v[84:87], v[32:35]\nv_mfma_f32_16x16x32_bf16 v[36:39], v[180:183], v[84:87], v[36:39]\nv_max3_u32 v22, v22, v84, v85\n" \
    "v_max3_u32 v22, v22, v86, v87\ns_waitcnt vmcnt(3)\nv_mfma_f32_16x16x32_bf16 v[24:27], v[40:43], v[88:91], v[24:27]\n" \
    "v_mfma_f32_16x16x32_bf16 v[28:31], v[48:51], v[88:91], v[28:31]\nv_max3_u32 v22, v22, v88, v89\nv_max3_u32 v22, v22, v90, v91\ns_waitcnt vmcnt(2)\n" \
    "v_mfma_f32_16x16x32_bf16 v[32:35], v[40:43], v[92:95], v[32:35]\nv_mfma_f32_16x16x32_bf16 v[36:39], v[48:51], v[92:95], v[36:39]\nv_max3_u32 v22, v22, v92, v93\n" \
    "v_max3_u32 v22, v22, v94, v95\ns_waitcnt vmcnt(1)\nv_mfma_f32_16x16x32_bf16 v[24:27], v[44:47], v[96:99], v[24:27]\n" \
    "v_mfma_f32_16x16x32_bf16 v[28:31], v[52:55], v[96:99], v[28:31]\nv_max3_u32 v22, v22, v96, v97\nv_max3_u32 v22, v22, v98, v99\ns_waitcnt vmcnt(0)\n" \
    "v_mfma_f32_16x16x32_bf16 v[32:35], v[44:47], v[100:103], v[32:35]\nv_mfma_f32_16x16x32_bf16 v[36:39], v[52:55], v[100:103], v[36:39]\nv_max3_u32 v22, v22, v100, v101\n" \
    "v_max3_u32 v22, v22, v102, v103\nv_cmp_eq_u32 vcc, -1, v22\ns_cbranch_vccnz .Lr141_%=\nds_write_b128 v9, v[24:27] offset:0\nds_write_b128 v9, v[28:31] offset:64\ns_nop 0\n" \
    "ds_write_b128 v9, v[32:35] offset:2304\nds_write_b128 v9, v[36:39] offset:2368\nv_add_u32 v252, s80, v3\nds_read_b64 v[252:253], v252\nds_read_b128 v[18:21], v16\n" \
    "s_waitcnt lgkmcnt(0)\ns_barrier\nds_read_b128 v[40:43], v10 offset:0\nds_read_b128 v[44:47], v10 offset:4608\nds_read_b128 v[48:51], v10 offset:9216\n" \
    "ds_read_b128 v[52:55], v10 offset:13824\nv_lshlrev_b32 v248, 16, v252\nv_and_b32 v249, 0xffff0000, v252\nv_lshlrev_b32 v250, 16, v253\nv_and_b32 v251, 0xffff0000, v253\n" \
    "s_waitcnt lgkmcnt(0)\nv_add_f32 v40, v40, v44\nv_add_f32 v41, v41, v45\nv_add_f32 v42, v42, v46\nv_add_f32 v43, v43, v47\nv_add_f32 v48, v48, v52\nv_add_f32 v49, v49, v53\n" \
    "v_add_f32 v50, v50, v54\nv_add_f32 v51, v51, v55\nv_add_f32 v40, v40, v48\nv_add_f32 v41, v41, v49\nv_add_f32 v42, v42, v50\nv_add_f32 v43, v43, v51\n" \
    "v_fma_f32 v248, v40, v18, v248\nv_fma_f32 v249, v41, v19, v249\nv_fma_f32 v250, v42, v20, v250\nv_fma_f32 v251, v43, v21, v251\nv_cvt_pk_bf16_f32 v40, v248, v249\n" \
    "v_cvt_pk_bf16_f32 v41, v250, v251\nv_lshlrev_b32 v248, 16, v40\nv_and_b32 v249, 0xffff0000, v40\nv_lshlrev_b32 v250, 16, v41\nv_and_b32 v251, 0xffff0000, v41\n" \
    "v_add_f32 v18, v248, v249\nv_add_f32 v19, v250, v251\nv_min_u32 v40, -2, v40\nv_min_u32 v41, -2, v41\nv_add_f32 v18, v18, v19\nv_permlane32_swap_b32 v40, v42\n" \
    "v_permlane32_swap_b32 v41, v43\nbuffer_store_dwordx4 v[40:43], v12, s[64:67], s75 offen sc1\nv_add_f32_dpp v19, v18, v18 row_ror:8 row_mask:0xf bank_mask:0xf\n" \
    "v_add_f32_dpp v20, v18, v18 row_ror:8 row_mask:0xf bank_mask:0xf\ns_nop 1\nv_permlane16_swap_b32 v19, v20\nv_add_f32 v18, v19, v20\nv_add_f32 v19, v19, v20\ns_nop 1\n" \
    "v_permlane32_swap_b32 v18, v19\nv_add_f32 v18, v18, v19\nv_mul_f32 v18, 0x3d000000, v18\nv_sub_f32 v44, v248, v18\nv_sub_f32 v45, v249, v18\nv_sub_f32 v46, v250, v18\n" \
    "v_sub_f32 v47, v251, v18\nv_mul_f32 v19, v44, v44\nv_fmac_f32 v19, v45, v45\nv_fmac_f32 v19, v46, v46\nv_fmac_f32 v19, v47, v47\nv_min_u32 v18, -2, v18\ns_nop 0\n" \
    "v_add_f32_dpp v20, v19, v19 row_ror:8 row_mask:0xf bank_mask:0xf\nv_add_f32_dpp v21, v19, v19 row_ror:8 row_mask:0xf bank_mask:0xf\ns_nop 1\n" \
    "v_permlane16_swap_b32 v20, v21\nv_add_f32 v19, v20, v21\nv_add_f32 v20, v20, v21\ns_nop 1\nv_permlane32_swap_b32 v19, v20\nv_add_f32 v19, v19, v20\nv_min_u32 v19, -2, v19\n" \
    "v_mov_b32 v22, 1\nbuffer_store_dwordx2 v[18:19], v13, s[64:67], s75 offen sc1\ns_barrier\ns_cmp_eq_u32 s56, 3\ns_cselect_b64 exec, 1, 0\n" \
    "buffer_atomic_add v22, off, s[64:67], s82 sc0\ns_mov_b64 exec, -1\ns_waitcnt vmcnt(0)\nv_readfirstlane_b32 s69, v22\ns_cmp_eq_u32 s69, 31\ns_cbranch_scc1 .Lr144_%=\n" \
    ".Lr145_%=:\ns_add_u32 s69, s50, 0\ns_and_b32 s69, s69, s41\ns_lshl_b32 s69, s69, 20\ns_add_u32 s69, s69, s32\ns_add_u32 s74, s48, s69\ns_lshr_b32 s74, s74, 8\n" \
    "s_xor_b32 s74, s74, s49\ns_and_b32 s74, s74, 0x2000\ns_add_u32 s74, s74, s69\ns_add_u32 s69, s50, 0\ns_and_b32 s69, s69, s41\ns_lshl_b32 s69, s69, 19\n" \
    "s_add_u32 s69, s69, s33\ns_add_u32 s87, s48, s69\ns_lshr_b32 s87, s87, 8\ns_xor_b32 s87, s87, s49\ns_and_b32 s87, s87, 0x2000\ns_add_u32 s87, s87, s69\n" \
    "s_add_u32 s69, s50, 1\ns_and_b32 s69, s69, s41\ns_lshl_b32 s69, s69, 18\ns_add_u32 s69, s69, s34\ns_add_u32 s88, s48, s69\ns_lshr_b32 s88, s88, 8\ns_xor_b32 s88, s88, s49\n" \
    "s_and_b32 s88, s88, 0x2000\ns_add_u32 s88, s88, s69\ns_xor_b32 s89, s88, 0x1000\ns_add_u32 s78, s87, s38\ns_mov_b32 s68, 0\n.Lr7_%=:\n" \
    "buffer_load_dword v20, off, s[64:67], s78 sc1\ns_waitcnt vmcnt(0)\nv_readfirstlane_b32 s70, v20\ns_memrealtime s[72:73]\ns_cmp_eq_u32 s70, -1\ns_cbranch_scc1 .Lr8_%=\n" \
    "s_lshr_b32 s71, s70, 8\ns_waitcnt lgkmcnt(0)\ns_sub_u32 s71, s72, s71\ns_and_b32 s71, s71, 0xffffff\ns_sub_u32 s70, s72, s71\ns_cmp_eq_u32 s50, 63\n" \
    "s_cselect_b32 s71, s44, s43\ns_add_u32 s70, s70, s71\n.Lr146_%=:\ns_sleep 1\ns_memrealtime s[72:73]\ns_waitcnt lgkmcnt(0)\ns_sub_u32 s72, s72, s70\ns_cmp_lt_i32 s72, 0\n" \
    "s_cbranch_scc1 .Lr146_%=\ns_mov_b32 s68, 0\ns_add_u32 s69, s50, 0\ns_lshl_b32 s69, s69, 20\ns_add_u32 s69, s69, s36\ns_add_u32 s75, s48, s69\ns_lshr_b32 s75, s75, 8\n" \
    "s_xor_b32 s75, s75, s49\ns_and_b32 s75, s75, 0x2000\ns_add_u32 s75, s75, s69\ns_add_u32 s76, s75, 0x4000\ns_add_u32 s77, s75, s37\ns_add_u32 s82, s50, 0\n" \
    "s_lshl_b32 s80, s82, 10\ns_lshl_b32 s82, s82, 2\ns_add_u32 s82, s82, s40\ns_add_u32 s69, s50, 0\ns_and_b32 s69, s69, s41\ns_lshl_b32 s69, s69, 20\ns_add_u32 s69, s69, s32\n" \
    "s_add_u32 s74, s48, s69\ns_lshr_b32 s74, s74, 8\ns_xor_b32 s74, s74, s49\ns_and_b32 s74, s74, 0x2000\ns_add_u32 s74, s74, s69\n.Lr147_%=:\n" \
    "buffer_load_dwordx4 v[56:59], v0, s[64:67], s74 offen offset:0 sc1\nbuffer_load_dwordx4 v[60:63], v0, s[64:67], s74 offen offset:1024 sc1\ns_add_u32 s69, s74, 0x4000\n" \
    "buffer_load_dwordx4 v[64:67], v0, s[64:67], s69 offen offset:0 sc1\nbuffer_load_dwordx4 v[68:71], v0, s[64:67], s69 offen offset:1024 sc1\n" \
    "buffer_load_dwordx4 v[72:75], v0, s[64:67], s75 offen offset:0 sc1\nbuffer_load_dwordx4 v[76:79], v0, s[64:67], s75 offen offset:1024 sc1\n" \
    "buffer_load_dwordx4 v[80:83], v0, s[64:67], s75 offen offset:2048 sc1\nbuffer_load_dwordx4 v[84:87], v0, s[64:67], s75 offen offset:3072 sc1\n" \
    "buffer_load_dwordx4 v[88:91], v1, s[64:67], s75 offen offset:0 sc1\nbuffer_load_dwordx4 v[92:95], v1, s[64:67], s75 offen offset:1024 sc1\n" \
    "buffer_load_dwordx4 v[96:99], v1, s[64:67], s75 offen offset:2048 sc1\nbuffer_load_dwordx4 v[100:103], v1, s[64:67], s75 offen offset:3072 sc1\n" \
    "buffer_load_dwordx4 v[104:107], v0, s[64:67], s76 offen offset:0 sc1\nbuffer_load_dwordx4 v[108:111], v0, s[64:67], s76 offen offset:1024 sc1\n" \
    "buffer_load_dwordx4 v[112:115], v0, s[64:67], s76 offen offset:2048 sc1\n.Lr148_%=:\nv_mov_b32 v22, 0\ns_waitcnt vmcnt(10)\n" \
    "v_mfma_f32_16x16x32_bf16 v[24:27], v[184:187], v[72:75], 0\nbuffer_load_dwordx4 v[116:119], v0, s[64:67], s76 offen offset:3072 sc1\n" \
    "v_mfma_f32_16x16x32_bf16 v[28:31], v[216:219], v[72:75], 0\nv_max3_u32 v22, v22, v72, v73\nv_max3_u32 v22, v22, v74, v75\ns_waitcnt vmcnt(10)\n" \
    "v_mfma_f32_16x16x32_bf16 v[32:35], v[184:187], v[76:79], 0\nbuffer_load_dwordx4 v[72:75], v1, s[64:67], s76 offen offset:0 sc1\n" \
    "v_mfma_f32_16x16x32_bf16 v[36:39], v[216:219], v[76:79], 0\nv_max3_u32 v22, v22, v76, v77\nv_max3_u32 v22, v22, v78, v79\ns_waitcnt vmcnt(10)\n" \
    "v_mfma_f32_16x16x32_bf16 v[24:27], v[188:191], v[80:83], v[24:27]\nbuffer_load_dwordx4 v[76:79], v1, s[64:67], s76 offen offset:1024 sc1\n" \
    "v_mfma_f32_16x16x32_bf16 v[28:31], v[220:223], v[80:83], v[28:31]\nv_max3_u32 v22, v22, v80, v81\nv_max3_u32 v22, v22, v82, v83\ns_waitcnt vmcnt(10)\n" \
    "v_mfma_f32_16x16x32_bf16 v[32:35], v[188:191], v[84:87], v[32:35]\nbuffer_load_dwordx4 v[80:83], v1, s[64:67], s76 offen offset:2048 sc1\n" \
    "v_mfma_f32_16x16x32_bf16 v[36:39], v[220:223], v[84:87], v[36:39]\nv_max3_u32 v22, v22, v84, v85\nv_max3_u32 v22, v22, v86, v87\ns_waitcnt vmcnt(10)\n" \
    "v_mfma_f32_16x16x32_bf16 v[24:27], v[192:195], v[88:91], v[24:27]\nbuffer_load_dwordx4 v[84:87], v1, s[64:67], s76 offen offset:3072 sc1\n" \
    "v_mfma_f32_16x16x32_bf16 v[28:31], v[224:227], v[88:91], v[28:31]\nv_max3_u32 v22, v22, v88, v89\nv_max3_u32 v22, v22, v90, v91\ns_waitcnt vmcnt(10)\n" \
    "v_mfma_f32_16x16x32_bf16 v[32:35], v[192:195], v[92:95], v[32:35]\nbuffer_load_dwordx4 v[88:91], v0, s[64:67], s77 offen offset:0 sc1\n" \
    "v_mfma_f32_16x16x32_bf16 v[36:39], v[224:227], v[92:95], v[36:39]\nv_max3_u32 v22, v22, v92, v93\nv_max3_u32 v22, v22, v94, v95\ns_waitcnt vmcnt(10)\n" \
    "v_mfma_f32_16x16x32_bf16 v[24:27], v[196:199], v[96:99], v[24:27]\nbuffer_load_dwordx4 v[92:95], v0, s[64:67], s77 offen offset:1024 sc1\n" \
    "v_mfma_f32_16x16x32_bf16 v[28:31], v[228:231], v[96:99], v[28:31]\nv_max3_u32 v22, v22, v96, v97\nv_max3_u32 v22, v22, v98, v99\ns_waitcnt vmcnt(10)\n" \
    "v_mfma_f32_16x16x32_bf16 v[32:35], v[196:199], v[100:103], v[32:35]\nbuffer_load_dwordx4 v[96:99], v0, s[64:67], s77 offen offset:2048 sc1\n" \
    "v_mfma_f32_16x16x32_bf16 v[36:39], v[228:231], v[100:103], v[36:39]\nv_max3_u32 v22, v22, v100, v101\nv_max3_u32 v22, v22, v102, v103\ns_waitcnt vmcnt(10)\n" \
    "v_mfma_f32_16x16x32_bf16 v[24:27], v[200:203], v[104:107], v[24:27]\nbuffer_load_dwordx4 v[100:103], v0, s[64:67], s77 offen offset:3072 sc1\n" \
    "v_mfma_f32_16x16x32_bf16 v[28:31], v[232:235], v[104:107], v[28:31]\nv_max3_u32 v22, v22, v104, v105\nv_max3_u32 v22, v22, v106, v107\ns_waitcnt vmcnt(10)\n" \
    "v_mfma_f32_16x16x32_bf16 v[32:35], v[200:203], v[108:111], v[32:35]\nv_mfma_f32_16x16x32_bf16 v[36:39], v[232:235], v[108:111], v[36:39]\nv_max3_u32 v22, v22, v108, v109\n" \
    "v_max3_u32 v22, v22, v110, v111\ns_waitcnt vmcnt(9)\nv_mfma_f32_16x16x32_bf16 v[24:27], v[204:207], v[112:115], v[24:27]\n" \
    "v_mfma_f32_16x16x32_bf16 v[28:31], v[236:239], v[112:115], v[28:31]\nv_max3_u32 v22, v22, v112, v113\nv_max3_u32 v22, v22, v114, v115\ns_waitcnt vmcnt(8)\n" \
    "v_mfma_f32_16x16x32_bf16 v[32:35], v[204:207], v[116:119], v[32:35]\nv_mfma_f32_16x16x32_bf16 v[36:39], v[236:239], v[116:119], v[36:39]\nv_max3_u32 v22, v22, v116, v117\n" \
    "v_max3_u32 v22, v22, v118, v119\ns_waitcnt vmcnt(7)\nv_mfma_f32_16x16x32_bf16 v[24:27], v[208:211], v[72:75], v[24:27]\n" \
    "v_mfma_f32_16x16x32_bf16 v[28:31], v[240:243], v[72:75], v[28:31]\nv_max3_u32 v22, v22, v72, v73\nv_max3_u32 v22, v22, v74, v75\ns_waitcnt vmcnt(6)\n" \
    "v_mfma_f32_16x16x32_bf16 v[32:35], v[208:211], v[76:79], v[32:35]\nv_mfma_f32_16x16x32_bf16 v[36:39], v[240:243], v[76:79], v[36:39]\nv_max3_u32 v22, v22, v76, v77\n" \
    "v_max3_u32 v22, v22, v78, v79\ns_waitcnt vmcnt(5)\nv_mfma_f32_16x16x32_bf16 v[24:27], v[212:215], v[80:83], v[24:27]\n" \
    "v_mfma_f32_16x16x32_bf16 v[28:31], v[244:247], v[80:83], v[28:31]\nv_max3_u32 v22, v22, v80, v81\nv_max3_u32 v22, v22, v82, v83\ns_waitcnt vmcnt(4)\n" \
    "v_mfma_f32_16x16x32_bf16 v[32:35], v[212:215], v[84:87], v[32:35]\nv_mfma_f32_16x16x32_bf16 v[36:39], v[244:247], v[84:87], v[36:39]\nv_max3_u32 v22, v22, v84, v85\n" \
    "v_max3_u32 v22, v22, v86, v87\nv_max3_u32 v22, v22, v56, v57\nv_max3_u32 v22, v22, v58, v59\nv_max3_u32 v22, v22, v60, v61\nv_max3_u32 v22, v22, v62, v63\n" \
    "v_max3_u32 v22, v22, v64, v65\nv_max3_u32 v22, v22, v66, v67\nv_max3_u32 v22, v22, v68, v69\nv_max3_u32 v22, v22, v70, v71\ns_waitcnt vmcnt(3)\n" \
    "v_mfma_f32_16x16x32_bf16 v[24:27], v[56:59], v[88:91], v[24:27]\nv_mfma_f32_16x16x32_bf16 v[28:31], v[64:67], v[88:91], v[28:31]\nv_max3_u32 v22, v22, v88, v89\n" \
    "v_max3_u32 v22, v22, v90, v91\ns_waitcnt vmcnt(2)\nv_mfma_f32_16x16x32_bf16 v[32:35], v[56:59], v[92:95], v[32:35]\n" \
    "v_mfma_f32_16x16x32_bf16 v[36:39], v[64:67], v[92:95], v[36:39]\nv_max3_u32 v22, v22, v92, v93\nv_max3_u32 v22, v22, v94, v95\ns_waitcnt vmcnt(1)\n" \
    "v_mfma_f32_16x16x32_bf16 v[24:27], v[60:63], v[96:99], v[24:27]\nv_mfma_f32_16x16x32_bf16 v[28:31], v[68:71], v[96:99], v[28:31]\nv_max3_u32 v22, v22, v96, v97\n" \
    "v_max3_u32 v22, v22, v98, v99\ns_waitcnt vmcnt(0)\nv_mfma_f32_16x16x32_bf16 v[32:35], v[60:63], v[100:103], v[32:35]\n" \
    "v_mfma_f32_16x16x32_bf16 v[36:39], v[68:71], v[100:103], v[36:39]\nv_max3_u32 v22, v22, v100, v101\nv_max3_u32 v22, v22, v102, v103\nv_cmp_eq_u32 vcc, -1, v22\n" \
    "s_cbranch_vccnz .Lr149_%=\nds_write_b128 v9, v[24:27] offset:0\nds_write_b128 v9, v[28:31] offset:64\ns_nop 0\nds_write_b128 v9, v[32:35] offset:2304\n" \
    "ds_write_b128 v9, v[36:39] offset:2368\nv_add_u32 v252, s80, v3\nds_read_b64 v[252:253], v252\nds_read_b128 v[18:21], v16\ns_waitcnt lgkmcnt(0)\ns_barrier\n" \
    "ds_read_b128 v[56:59], v10 offset:0\nds_read_b128 v[60:63], v10 offset:4608\nds_read_b128 v[64:67], v10 offset:9216\nds_read_b128 v[68:71], v10 offset:13824\n" \
    "v_lshlrev_b32 v248, 16, v252\nv_and_b32 v249, 0xffff0000, v252\nv_lshlrev_b32 v250, 16, v253\nv_and_b32 v251, 0xffff0000, v253\ns_waitcnt lgkmcnt(0)\n" \
    "v_add_f32 v56, v56, v60\nv_add_f32 v57, v57, v61\nv_add_f32 v58, v58, v62\nv_add_f32 v59, v59, v63\nv_add_f32 v64, v64, v68\nv_add_f32 v65, v65, v69\n" \
    "v_add_f32 v66, v66, v70\nv_add_f32 v67, v67, v71\nv_add_f32 v56, v56, v64\nv_add_f32 v57, v57, v65\nv_add_f32 v58, v58, v66\nv_add_f32 v59, v59, v67\n" \
    "v_fma_f32 v248, v56, v18, v248\nv_fma_f32 v249, v57, v19, v249\nv_fma_f32 v250, v58, v20, v250\nv_fma_f32 v251, v59, v21, v251\nv_cvt_pk_bf16_f32 v56, v248, v249\n" \
    "v_cvt_pk_bf16_f32 v57, v250, v251\nv_lshlrev_b32 v248, 16, v56\nv_and_b32 v249, 0xffff0000, v56\nv_lshlrev_b32 v250, 16, v57\nv_and_b32 v251, 0xffff0000, v57\n" \
    "v_add_f32 v18, v248, v249\nv_add_f32 v19, v250, v251\nv_min_u32 v56, -2, v56\nv_min_u32 v57, -2, v57\nv_add_f32 v18, v18, v19\nv_permlane32_swap_b32 v56, v58\n" \
    "v_permlane32_swap_b32 v57, v59\nbuffer_store_dwordx4 v[56:59], v12, s[64:67], s75 offen sc1\nv_add_f32_dpp v19, v18, v18 row_ror:8 row_mask:0xf bank_mask:0xf\n" \
    "v_add_f32_dpp v20, v18, v18 row_ror:8 row_mask:0xf bank_mask:0xf\ns_nop 1\nv_permlane16_swap_b32 v19, v20\nv_add_f32 v18, v19, v20\nv_add_f32 v19, v19, v20\ns_nop 1\n" \
    "v_permlane32_swap_b32 v18, v19\nv_add_f32 v18, v18, v19\nv_mul_f32 v18, 0x3d000000, v18\nv_sub_f32 v60, v248, v18\nv_sub_f32 v61, v249, v18\nv_sub_f32 v62, v250, v18\n" \
    "v_sub_f32 v63, v251, v18\nv_mul_f32 v19, v60, v60\nv_fmac_f32 v19, v61, v61\nv_fmac_f32 v19, v62, v62\nv_fmac_f32 v19, v63, v63\nv_min_u32 v18, -2, v18\ns_nop 0\n" \
    "v_add_f32_dpp v20, v19, v19 row_ror:8 row_mask:0xf bank_mask:0xf\nv_add_f32_dpp v21, v19, v19 row_ror:8 row_mask:0xf bank_mask:0xf\ns_nop 1\n" \
    "v_permlane16_swap_b32 v20, v21\nv_add_f32 v19, v20, v21\nv_add_f32 v20, v20, v21\ns_nop 1\nv_permlane32_swap_b32 v19, v20\nv_add_f32 v19, v19, v20\nv_min_u32 v19, -2, v19\n" \
    "v_mov_b32 v22, 1\nbuffer_store_dwordx2 v[18:19], v13, s[64:67], s75 offen sc1\ns_barrier\ns_cmp_eq_u32 s56, 3\ns_cselect_b64 exec, 1, 0\n" \
    "buffer_atomic_add v22, off, s[64:67], s82 sc0\ns_mov_b64 exec, -1\ns_waitcnt vmcnt(0)\nv_readfirstlane_b32 s69, v22\ns_cmp_eq_u32 s69, 31\ns_cbranch_scc1 .Lr152_%=\n" \
    ".Lr153_%=:\ns_branch .Lr2_%=\n.Lr4_%=:\ns_sleep 8\ns_add_u32 s68, s68, 1\ns_and_b32 s69, s68, 63\ns_cbranch_scc1 .Lr3_%=\ns_cmp_gt_u32 s68, 0x200000\n" \
    "s_cbranch_scc1 .Lr1_%=\nbuffer_load_dword v21, off, s[64:67], 0 sc1\ns_waitcnt vmcnt(0)\nv_readfirstlane_b32 s69, v21\ns_cmp_eq_u32 s69, 1\ns_cbranch_scc0 .Lr3_%=\n" \
    "s_endpgm\n.Lr12_%=:\ns_sleep 3\ns_add_u32 s68, s68, 1\ns_and_b32 s69, s68, 63\ns_cbranch_scc1 .Lr10_%=\ns_cmp_gt_u32 s68, 0x200000\ns_cbranch_scc1 .Lr1_%=\n" \
    "buffer_load_dword v21, off, s[64:67], 0 sc1\ns_waitcnt vmcnt(0)\nv_readfirstlane_b32 s69, v21\ns_cmp_eq_u32 s69, 1\ns_cbranch_scc0 .Lr10_%=\ns_endpgm\n.Lr21_%=:\n" \
    "s_waitcnt lgkmcnt(0)\n.Lr22_%=:\ns_add_u32 s51, s51, 20\ns_sub_u32 s70, s51, s72\ns_cmp_lt_i32 s70, 52\ns_cbranch_scc1 .Lr22_%=\ns_add_u32 s68, s68, 1\n" \
    "s_and_b32 s69, s68, 63\ns_cbranch_scc1 .Lr20_%=\ns_cmp_gt_u32 s68, 0x200000\ns_cbranch_scc1 .Lr1_%=\nbuffer_load_dword v21, off, s[64:67], 0 sc1\ns_waitcnt vmcnt(0)\n" \
    "v_readfirstlane_b32 s69, v21\ns_cmp_eq_u32 s69, 1\ns_cbranch_scc0 .Lr20_%=\ns_endpgm\n.Lr25_%=:\nv_max3_u32 v22, v40, v41, v42\nv_max3_u32 v22, v22, v43, v44\n" \
    "v_max3_u32 v22, v22, v45, v46\nv_max3_u32 v22, v22, v47, v48\nv_max3_u32 v22, v22, v49, v50\nv_max3_u32 v22, v22, v51, v52\nv_max3_u32 v22, v22, v53, v54\n" \
    "v_max_u32 v22, v22, v55\nv_cmp_eq_u32 vcc, -1, v22\ns_add_u32 s51, s51, s53\ns_xor_b32 s87, s87, 0x1000\ns_add_u32 s68, s68, 1\ns_and_b32 s69, s68, 63\n" \
    "s_cbranch_scc1 .Lr26_%=\ns_cmp_gt_u32 s68, 0x200000\ns_cbranch_scc1 .Lr1_%=\nbuffer_load_dword v21, off, s[64:67], 0 sc1\ns_waitcnt vmcnt(0)\nv_readfirstlane_b32 s69, v21\n" \
    "s_cmp_eq_u32 s69, 1\ns_cbranch_scc0 .Lr26_%=\ns_endpgm\n.Lr26_%=:\ns_cbranch_vccz .Lr27_%=\ns_sub_u32 s70, s51, s54\n.Lr33_%=:\ns_memrealtime s[72:73]\n" \
    "s_waitcnt lgkmcnt(0)\ns_sub_u32 s72, s72, s70\ns_cmp_lt_i32 s72, 0\ns_cbranch_scc1 .Lr33_%=\nbuffer_load_dwordx4 v[40:43], v0, s[64:67], s74 offen offset:0 sc1\n" \
    "buffer_load_dwordx4 v[44:47], v0, s[64:67], s74 offen offset:1024 sc1\ns_add_u32 s69, s74, 0x4000\nbuffer_load_dwordx4 v[48:51], v0, s[64:67], s69 offen offset:0 sc1\n" \
    "buffer_load_dwordx4 v[52:55], v0, s[64:67], s69 offen offset:1024 sc1\n.Lr27_%=:\n.Lr34_%=:\ns_memrealtime s[72:73]\ns_waitcnt lgkmcnt(0)\ns_sub_u32 s72, s72, s51\n" \
    "s_cmp_lt_i32 s72, -3\ns_cbranch_scc1 .Lr34_%=\ns_cmp_ge_i32 s72, 0\ns_cbranch_scc1 .Lr35_%=\ns_nop 2\ns_cmp_ge_i32 s72, -1\ns_cbranch_scc1 .Lr35_%=\ns_nop 2\n" \
    "s_cmp_ge_i32 s72, -2\ns_cbranch_scc1 .Lr35_%=\ns_nop 2\n.Lr35_%=:\nbuffer_load_dwordx4 v[100:103], v0, s[64:67], s87 offen offset:0 sc1\n" \
    "buffer_load_dwordx4 v[104:107], v0, s[64:67], s87 offen offset:1024 sc1\nbuffer_load_dwordx4 v[108:111], v0, s[64:67], s87 offen offset:2048 sc1\n" \
    "buffer_load_dwordx4 v[112:115], v0, s[64:67], s87 offen offset:3072 sc1\nbuffer_load_dwordx2 v[116:117], v5, s[64:67], s87 offen offset:0 sc1\n" \
    "buffer_load_dwordx2 v[118:119], v5, s[64:67], s87 offen offset:128 sc1\ns_branch .Lr24_%=\n.Lr38_%=:\ns_sleep 3\ns_add_u32 s68, s68, 1\ns_and_b32 s69, s68, 63\n" \
    "s_cbranch_scc1 .Lr36_%=\ns_cmp_gt_u32 s68, 0x200000\ns_cbranch_scc1 .Lr1_%=\nbuffer_load_dword v21, off, s[64:67], 0 sc1\ns_waitcnt vmcnt(0)\nv_readfirstlane_b32 s69, v21\n" \
    "s_cmp_eq_u32 s69, 1\ns_cbranch_scc0 .Lr36_%=\ns_endpgm\n.Lr47_%=:\ns_waitcnt lgkmcnt(0)\n.Lr48_%=:\ns_add_u32 s51, s51, 20\ns_sub_u32 s70, s51, s72\ns_cmp_lt_i32 s70, 52\n" \
    "s_cbranch_scc1 .Lr48_%=\ns_add_u32 s68, s68, 1\ns_and_b32 s69, s68, 63\ns_cbranch_scc1 .Lr46_%=\ns_cmp_gt_u32 s68, 0x200000\ns_cbranch_scc1 .Lr1_%=\n" \
    "buffer_load_dword v21, off, s[64:67], 0 sc1\ns_waitcnt vmcnt(0)\nv_readfirstlane_b32 s69, v21\ns_cmp_eq_u32 s69, 1\ns_cbranch_scc0 .Lr46_%=\ns_endpgm\n.Lr51_%=:\n" \
    "v_max3_u32 v22, v56, v57, v58\nv_max3_u32 v22, v22, v59, v60\nv_max3_u32 v22, v22, v61, v62\nv_max3_u32 v22, v22, v63, v64\nv_max3_u32 v22, v22, v65, v66\n" \
    "v_max3_u32 v22, v22, v67, v68\nv_max3_u32 v22, v22, v69, v70\nv_max_u32 v22, v22, v71\nv_cmp_eq_u32 vcc, -1, v22\ns_add_u32 s51, s51, s53\ns_xor_b32 s87, s87, 0x1000\n" \
    "s_add_u32 s68, s68, 1\ns_and_b32 s69, s68, 63\ns_cbranch_scc1 .Lr52_%=\ns_cmp_gt_u32 s68, 0x200000\ns_cbranch_scc1 .Lr1_%=\nbuffer_load_dword v21, off, s[64:67], 0 sc1\n" \
    "s_waitcnt vmcnt(0)\nv_readfirstlane_b32 s69, v21\ns_cmp_eq_u32 s69, 1\ns_cbranch_scc0 .Lr52_%=\ns_endpgm\n.Lr52_%=:\ns_cbranch_vccz .Lr53_%=\ns_sub_u32 s70, s51, s54\n" \
    ".Lr59_%=:\ns_memrealtime s[72:73]\ns_waitcnt lgkmcnt(0)\ns_sub_u32 s72, s72, s70\ns_cmp_lt_i32 s72, 0\ns_cbranch_scc1 .Lr59_%=\n" \
    "buffer_load_dwordx4 v[56:59], v0, s[64:67], s74 offen offset:0 sc1\nbuffer_load_dwordx4 v[60:63], v0, s[64:67], s74 offen offset:1024 sc1\ns_add_u32 s69, s74, 0x4000\n" \
    "buffer_load_dwordx4 v[64:67], v0, s[64:67], s69 offen offset:0 sc1\nbuffer_load_dwordx4 v[68:71], v0, s[64:67], s69 offen offset:1024 sc1\n.Lr53_%=:\n.Lr60_%=:\n" \
    "s_memrealtime s[72:73]\ns_waitcnt lgkmcnt(0)\ns_sub_u32 s72, s72, s51\ns_cmp_lt_i32 s72, -3\ns_cbranch_scc1 .Lr60_%=\ns_cmp_ge_i32 s72, 0\ns_cbranch_scc1 .Lr61_%=\n" \
    "s_nop 2\ns_cmp_ge_i32 s72, -1\ns_cbranch_scc1 .Lr61_%=\ns_nop 2\ns_cmp_ge_i32 s72, -2\ns_cbranch_scc1 .Lr61_%=\ns_nop 2\n.Lr61_%=:\n" \
    "buffer_load_dwordx4 v[100:103], v0, s[64:67], s87 offen offset:0 sc1\nbuffer_load_dwordx4 v[104:107], v0, s[64:67], s87 offen offset:1024 sc1\n" \
    "buffer_load_dwordx4 v[108:111], v0, s[64:67], s87 offen offset:2048 sc1\nbuffer_load_dwordx4 v[112:115], v0, s[64:67], s87 offen offset:3072 sc1\n" \
    "buffer_load_dwordx2 v[116:117], v5, s[64:67], s87 offen offset:0 sc1\nbuffer_load_dwordx2 v[118:119], v5, s[64:67], s87 offen offset:128 sc1\ns_branch .Lr50_%=\n.Lr63_%=:\n" \
    "s_waitcnt lgkmcnt(0)\n.Lr64_%=:\ns_add_u32 s51, s51, s53\ns_sub_u32 s70, s51, s72\ns_cmp_lt_i32 s70, 52\ns_cbranch_scc1 .Lr64_%=\ns_add_u32 s68, s68, 1\n" \
    "s_and_b32 s69, s68, 63\ns_cbranch_scc1 .Lr62_%=\ns_cmp_gt_u32 s68, 0x200000\ns_cbranch_scc1 .Lr1_%=\nbuffer_load_dword v21, off, s[64:67], 0 sc1\ns_waitcnt vmcnt(0)\n" \
    "v_readfirstlane_b32 s69, v21\ns_cmp_eq_u32 s69, 1\ns_cbranch_scc0 .Lr62_%=\ns_endpgm\n.Lr67_%=:\nv_max3_u32 v22, v56, v57, v58\nv_max3_u32 v22, v22, v59, v60\n" \
    "v_max3_u32 v22, v22, v61, v62\nv_max3_u32 v22, v22, v63, v64\nv_max3_u32 v22, v22, v65, v66\nv_max3_u32 v22, v22, v67, v68\nv_max3_u32 v22, v22, v69, v70\n" \
    "v_max_u32 v22, v22, v71\nv_cmp_eq_u32 vcc, -1, v22\ns_add_u32 s51, s51, s53\ns_xor_b32 s87, s87, 0x1000\ns_add_u32 s68, s68, 1\ns_and_b32 s69, s68, 63\n" \
    "s_cbranch_scc1 .Lr68_%=\ns_cmp_gt_u32 s68, 0x200000\ns_cbranch_scc1 .Lr1_%=\nbuffer_load_dword v21, off, s[64:67], 0 sc1\ns_waitcnt vmcnt(0)\nv_readfirstlane_b32 s69, v21\n" \
    "s_cmp_eq_u32 s69, 1\ns_cbranch_scc0 .Lr68_%=\ns_endpgm\n.Lr68_%=:\ns_cbranch_vccz .Lr69_%=\ns_sub_u32 s70, s51, s54\n.Lr75_%=:\ns_memrealtime s[72:73]\n" \
    "s_waitcnt lgkmcnt(0)\ns_sub_u32 s72, s72, s70\ns_cmp_lt_i32 s72, 0\ns_cbranch_scc1 .Lr75_%=\nbuffer_load_dwordx4 v[56:59], v0, s[64:67], s74 offen offset:0 sc1\n" \
    "buffer_load_dwordx4 v[60:63], v0, s[64:67], s74 offen offset:1024 sc1\ns_add_u32 s69, s74, 0x4000\nbuffer_load_dwordx4 v[64:67], v0, s[64:67], s69 offen offset:0 sc1\n" \
    "buffer_load_dwordx4 v[68:71], v0, s[64:67], s69 offen offset:1024 sc1\n.Lr69_%=:\n.Lr76_%=:\ns_memrealtime s[72:73]\ns_waitcnt lgkmcnt(0)\ns_sub_u32 s72, s72, s51\n" \
    "s_cmp_lt_i32 s72, -3\ns_cbranch_scc1 .Lr76_%=\ns_cmp_ge_i32 s72, 0\ns_cbranch_scc1 .Lr77_%=\ns_nop 2\ns_cmp_ge_i32 s72, -1\ns_cbranch_scc1 .Lr77_%=\ns_nop 2\n" \
    "s_cmp_ge_i32 s72, -2\ns_cbranch_scc1 .Lr77_%=\ns_nop 2\n.Lr77_%=:\nbuffer_load_dwordx4 v[100:103], v0, s[64:67], s87 offen offset:0 sc1\n" \
    "buffer_load_dwordx4 v[104:107], v0, s[64:67], s87 offen offset:1024 sc1\nbuffer_load_dwordx4 v[108:111], v0, s[64:67], s87 offen offset:2048 sc1\n" \
    "buffer_load_dwordx4 v[112:115], v0, s[64:67], s87 offen offset:3072 sc1\nbuffer_load_dwordx2 v[116:117], v5, s[64:67], s87 offen offset:0 sc1\n" \
    "buffer_load_dwordx2 v[118:119], v5, s[64:67], s87 offen offset:128 sc1\ns_branch .Lr66_%=\n.Lr87_%=:\ns_sleep 3\ns_add_u32 s68, s68, 1\ns_and_b32 s69, s68, 63\n" \
    "s_cbranch_scc1 .Lr85_%=\ns_cmp_gt_u32 s68, 0x200000\ns_cbranch_scc1 .Lr1_%=\nbuffer_load_dword v21, off, s[64:67], 0 sc1\ns_waitcnt vmcnt(0)\nv_readfirstlane_b32 s69, v21\n" \
    "s_cmp_eq_u32 s69, 1\ns_cbranch_scc0 .Lr85_%=\ns_endpgm\n.Lr80_%=:\ns_waitcnt vmcnt(0)\ns_sleep 4\ns_add_u32 s68, s68, 1\ns_and_b32 s69, s68, 63\ns_cbranch_scc1 .Lr78_%=\n" \
    "s_cmp_gt_u32 s68, 0x200000\ns_cbranch_scc1 .Lr1_%=\nbuffer_load_dword v21, off, s[64:67], 0 sc1\ns_waitcnt vmcnt(0)\nv_readfirstlane_b32 s69, v21\ns_cmp_eq_u32 s69, 1\n" \
    "s_cbranch_scc0 .Lr78_%=\ns_endpgm\n.Lr92_%=:\ns_waitcnt lgkmcnt(0)\n.Lr93_%=:\ns_add_u32 s51, s51, s53\ns_sub_u32 s70, s51, s72\ns_cmp_lt_i32 s70, 52\n" \
    "s_cbranch_scc1 .Lr93_%=\ns_add_u32 s68, s68, 1\ns_and_b32 s69, s68, 63\ns_cbranch_scc1 .Lr91_%=\ns_cmp_gt_u32 s68, 0x200000\ns_cbranch_scc1 .Lr1_%=\n" \
    "buffer_load_dword v21, off, s[64:67], 0 sc1\ns_waitcnt vmcnt(0)\nv_readfirstlane_b32 s69, v21\ns_cmp_eq_u32 s69, 1\ns_cbranch_scc0 .Lr91_%=\ns_endpgm\n.Lr96_%=:\n" \
    "v_max3_u32 v22, v40, v41, v42\nv_max3_u32 v22, v22, v43, v44\nv_max3_u32 v22, v22, v45, v46\nv_max3_u32 v22, v22, v47, v48\nv_max3_u32 v22, v22, v49, v50\n" \
    "v_max3_u32 v22, v22, v51, v52\nv_max3_u32 v22, v22, v53, v54\nv_max_u32 v22, v22, v55\nv_cmp_eq_u32 vcc, -1, v22\ns_add_u32 s51, s51, s53\ns_xor_b32 s87, s87, 0x1000\n" \
    "s_add_u32 s68, s68, 1\ns_and_b32 s69, s68, 63\ns_cbranch_scc1 .Lr97_%=\ns_cmp_gt_u32 s68, 0x200000\ns_cbranch_scc1 .Lr1_%=\nbuffer_load_dword v21, off, s[64:67], 0 sc1\n" \
    "s_waitcnt vmcnt(0)\nv_readfirstlane_b32 s69, v21\ns_cmp_eq_u32 s69, 1\ns_cbranch_scc0 .Lr97_%=\ns_endpgm\n.Lr97_%=:\ns_cbranch_vccz .Lr98_%=\ns_sub_u32 s70, s51, s54\n" \
    ".Lr104_%=:\ns_memrealtime s[72:73]\ns_waitcnt lgkmcnt(0)\ns_sub_u32 s72, s72, s70\ns_cmp_lt_i32 s72, 0\ns_cbranch_scc1 .Lr104_%=\n" \
    "buffer_load_dwordx4 v[40:43], v0, s[64:67], s74 offen offset:0 sc1\nbuffer_load_dwordx4 v[44:47], v0, s[64:67], s74 offen offset:1024 sc1\ns_add_u32 s69, s74, 0x4000\n" \
    "buffer_load_dwordx4 v[48:51], v0, s[64:67], s69 offen offset:0 sc1\nbuffer_load_dwordx4 v[52:55], v0, s[64:67], s69 offen offset:1024 sc1\n.Lr98_%=:\n.Lr105_%=:\n" \
    "s_memrealtime s[72:73]\ns_waitcnt lgkmcnt(0)\ns_sub_u32 s72, s72, s51\ns_cmp_lt_i32 s72, -3\ns_cbranch_scc1 .Lr105_%=\ns_cmp_ge_i32 s72, 0\ns_cbranch_scc1 .Lr106_%=\n" \
    "s_nop 2\ns_cmp_ge_i32 s72, -1\ns_cbranch_scc1 .Lr106_%=\ns_nop 2\ns_cmp_ge_i32 s72, -2\ns_cbranch_scc1 .Lr106_%=\ns_nop 2\n.Lr106_%=:\n" \
    "buffer_load_dwordx4 v[100:103], v0, s[64:67], s87 offen offset:0 sc1\nbuffer_load_dwordx4 v[104:107], v0, s[64:67], s87 offen offset:1024 sc1\n" \
    "buffer_load_dwordx4 v[108:111], v0, s[64:67], s87 offen offset:2048 sc1\nbuffer_load_dwordx4 v[112:115], v0, s[64:67], s87 offen offset:3072 sc1\n" \
    "buffer_load_dwordx2 v[116:117], v5, s[64:67], s87 offen offset:0 sc1\nbuffer_load_dwordx2 v[118:119], v5, s[64:67], s87 offen offset:128 sc1\ns_branch .Lr95_%=\n" \
    ".Lr116_%=:\ns_sleep 3\ns_add_u32 s68, s68, 1\ns_and_b32 s69, s68, 63\ns_cbranch_scc1 .Lr114_%=\ns_cmp_gt_u32 s68, 0x200000\ns_cbranch_scc1 .Lr1_%=\n" \
    "buffer_load_dword v21, off, s[64:67], 0 sc1\ns_waitcnt vmcnt(0)\nv_readfirstlane_b32 s69, v21\ns_cmp_eq_u32 s69, 1\ns_cbranch_scc0 .Lr114_%=\ns_endpgm\n.Lr109_%=:\n" \
    "s_waitcnt vmcnt(0)\ns_sleep 4\ns_add_u32 s68, s68, 1\ns_and_b32 s69, s68, 63\ns_cbranch_scc1 .Lr107_%=\ns_cmp_gt_u32 s68, 0x200000\ns_cbranch_scc1 .Lr1_%=\n" \
    "buffer_load_dword v21, off, s[64:67], 0 sc1\ns_waitcnt vmcnt(0)\nv_readfirstlane_b32 s69, v21\ns_cmp_eq_u32 s69, 1\ns_cbranch_scc0 .Lr107_%=\ns_endpgm\n.Lr121_%=:\n" \
    "s_waitcnt lgkmcnt(0)\n.Lr122_%=:\ns_add_u32 s51, s51, 20\ns_sub_u32 s70, s51, s72\ns_cmp_lt_i32 s70, 52\ns_cbranch_scc1 .Lr122_%=\ns_add_u32 s68, s68, 1\n" \
    "s_and_b32 s69, s68, 63\ns_cbranch_scc1 .Lr120_%=\ns_cmp_gt_u32 s68, 0x200000\ns_cbranch_scc1 .Lr1_%=\nbuffer_load_dword v21, off, s[64:67], 0 sc1\ns_waitcnt vmcnt(0)\n" \
    "v_readfirstlane_b32 s69, v21\ns_cmp_eq_u32 s69, 1\ns_cbranch_scc0 .Lr120_%=\ns_endpgm\n.Lr125_%=:\nv_max3_u32 v22, v56, v57, v58\nv_max3_u32 v22, v22, v59, v60\n" \
    "v_max3_u32 v22, v22, v61, v62\nv_max3_u32 v22, v22, v63, v64\nv_max3_u32 v22, v22, v65, v66\nv_max3_u32 v22, v22, v67, v68\nv_max3_u32 v22, v22, v69, v70\n" \
    "v_max_u32 v22, v22, v71\nv_cmp_eq_u32 vcc, -1, v22\ns_add_u32 s51, s51, s53\ns_xor_b32 s87, s87, 0x1000\ns_add_u32 s68, s68, 1\ns_and_b32 s69, s68, 63\n" \
    "s_cbranch_scc1 .Lr126_%=\ns_cmp_gt_u32 s68, 0x200000\ns_cbranch_scc1 .Lr1_%=\nbuffer_load_dword v21, off, s[64:67], 0 sc1\ns_waitcnt vmcnt(0)\n" \
    "v_readfirstlane_b32 s69, v21\ns_cmp_eq_u32 s69, 1\ns_cbranch_scc0 .Lr126_%=\ns_endpgm\n.Lr126_%=:\ns_cbranch_vccz .Lr127_%=\ns_sub_u32 s70, s51, s54\n.Lr133_%=:\n" \
    "s_memrealtime s[72:73]\ns_waitcnt lgkmcnt(0)\ns_sub_u32 s72, s72, s70\ns_cmp_lt_i32 s72, 0\ns_cbranch_scc1 .Lr133_%=\n" \
    "buffer_load_dwordx4 v[56:59], v0, s[64:67], s74 offen offset:0 sc1\nbuffer_load_dwordx4 v[60:63], v0, s[64:67], s74 offen offset:1024 sc1\ns_add_u32 s69, s74, 0x4000\n" \
    "buffer_load_dwordx4 v[64:67], v0, s[64:67], s69 offen offset:0 sc1\nbuffer_load_dwordx4 v[68:71], v0, s[64:67], s69 offen offset:1024 sc1\n.Lr127_%=:\n.Lr134_%=:\n" \
    "s_memrealtime s[72:73]\ns_waitcnt lgkmcnt(0)\ns_sub_u32 s72, s72, s51\ns_cmp_lt_i32 s72, -3\ns_cbranch_scc1 .Lr134_%=\ns_cmp_ge_i32 s72, 0\ns_cbranch_scc1 .Lr135_%=\n" \
    "s_nop 2\ns_cmp_ge_i32 s72, -1\ns_cbranch_scc1 .Lr135_%=\ns_nop 2\ns_cmp_ge_i32 s72, -2\ns_cbranch_scc1 .Lr135_%=\ns_nop 2\n.Lr135_%=:\n" \
    "buffer_load_dwordx4 v[100:103], v0, s[64:67], s87 offen offset:0 sc1\nbuffer_load_dwordx4 v[104:107], v0, s[64:67], s87 offen offset:1024 sc1\n" \
    "buffer_load_dwordx4 v[108:111], v0, s[64:67], s87 offen offset:2048 sc1\nbuffer_load_dwordx4 v[112:115], v0, s[64:67], s87 offen offset:3072 sc1\n" \
    "buffer_load_dwordx2 v[116:117], v5, s[64:67], s87 offen offset:0 sc1\nbuffer_load_dwordx2 v[118:119], v5, s[64:67], s87 offen offset:128 sc1\ns_branch .Lr124_%=\n" \
    ".Lr138_%=:\ns_sleep 3\ns_add_u32 s68, s68, 1\ns_and_b32 s69, s68, 63\ns_cbranch_scc1 .Lr136_%=\ns_cmp_gt_u32 s68, 0x200000\ns_cbranch_scc1 .Lr1_%=\n" \
    "buffer_load_dword v21, off, s[64:67], 0 sc1\ns_waitcnt vmcnt(0)\nv_readfirstlane_b32 s69, v21\ns_cmp_eq_u32 s69, 1\ns_cbranch_scc0 .Lr136_%=\ns_endpgm\n.Lr141_%=:\n" \
    "s_waitcnt vmcnt(0)\ns_sleep 4\ns_add_u32 s68, s68, 1\ns_and_b32 s69, s68, 63\ns_cbranch_scc1 .Lr139_%=\ns_cmp_gt_u32 s68, 0x200000\ns_cbranch_scc1 .Lr1_%=\n" \
    "buffer_load_dword v21, off, s[64:67], 0 sc1\ns_waitcnt vmcnt(0)\nv_readfirstlane_b32 s69, v21\ns_cmp_eq_u32 s69, 1\ns_cbranch_scc0 .Lr139_%=\ns_endpgm\n.Lr144_%=:\n" \
    "s_add_u32 s69, s50, -1\nv_mov_b32 v22, s69\nbuffer_atomic_umax v22, v15, s[64:67], 0 offen sc1\ns_branch .Lr145_%=\n.Lr149_%=:\ns_waitcnt vmcnt(0)\ns_sleep 4\n" \
    "s_add_u32 s68, s68, 1\ns_and_b32 s69, s68, 63\ns_cbranch_scc1 .Lr147_%=\ns_cmp_gt_u32 s68, 0x200000\ns_cbranch_scc1 .Lr1_%=\nbuffer_load_dword v21, off, s[64:67], 0 sc1\n" \
    "s_waitcnt vmcnt(0)\nv_readfirstlane_b32 s69, v21\ns_cmp_eq_u32 s69, 1\ns_cbranch_scc0 .Lr147_%=\ns_endpgm\n.Lr152_%=:\ns_add_u32 s69, s50, 1\nv_mov_b32 v22, s69\n" \
    "buffer_atomic_umax v22, v15, s[64:67], 0 offen sc1\ns_branch .Lr153_%=\n.Lr8_%=:\ns_sleep 3\ns_add_u32 s68, s68, 1\ns_and_b32 s69, s68, 63\ns_cbranch_scc1 .Lr7_%=\n" \
    "s_cmp_gt_u32 s68, 0x200000\ns_cbranch_scc1 .Lr1_%=\nbuffer_load_dword v21, off, s[64:67], 0 sc1\ns_waitcnt vmcnt(0)\nv_readfirstlane_b32 s69, v21\ns_cmp_eq_u32 s69, 1\n" \
    "s_cbranch_scc0 .Lr7_%=\ns_endpgm\n.Lr1_%=:\nv_mov_b32 v21, 1\nbuffer_store_dword v21, off, s[64:67], 0 sc1\ns_endpgm\n.Lr2_%=:\n"
#define ROLE_F1 \
    "s_mov_b32 s80, 0\ns_lshl_b32 s79, s54, 12\nv_mov_b32 v10, 0\n.Lf7_%=:\ns_mov_b32 s68, 0\n.Lf6_%=:\ns_mov_b32 s71, s50\ns_and_b32 s69, s71, 1\ns_lshl_b32 s69, s69, 2\n" \
    "buffer_load_dword v9, v7, s[64:67], s69 offen sc1\ns_waitcnt vmcnt(0)\nv_readfirstlane_b32 s78, v9\nv_readfirstlane_b32 s81, v10\ns_cmp_eq_u32 s81, 13\n" \
    "s_cbranch_scc1 .Lf22_%=\n.Lf23_%=:\ns_cmp_gt_i32 s78, s50\ns_cbranch_scc0 .Lf8_%=\n.Lf3_%=:\ns_add_u32 s72, s52, s51\ns_add_u32 m0, s72, 0\ns_add_u32 s70, s53, s42\n" \
    "buffer_load_dwordx4 v0, s[64:67], s70 offen offset:0 sc1 lds\nbuffer_load_dwordx4 v0, s[64:67], s70 offen offset:1024 sc1 lds\n" \
    "buffer_load_dwordx4 v0, s[64:67], s70 offen offset:2048 sc1 lds\nbuffer_load_dwordx4 v0, s[64:67], s70 offen offset:3072 sc1 lds\ns_add_u32 m0, s72, 16384\n" \
    "s_add_u32 s70, s53, s43\nbuffer_load_dwordx4 v0, s[64:67], s70 offen offset:0 sc1 lds\nbuffer_load_dwordx4 v0, s[64:67], s70 offen offset:1024 sc1 lds\n" \
    "buffer_load_dwordx4 v0, s[64:67], s70 offen offset:2048 sc1 lds\nbuffer_load_dwordx4 v0, s[64:67], s70 offen offset:3072 sc1 lds\nv_add_u32 v8, s51, v1\n" \
    "s_waitcnt vmcnt(4)\ns_barrier\nds_read_b128 v[24:27], v8 offset:0\nds_read_b128 v[28:31], v8 offset:512\nds_read_b128 v[32:35], v8 offset:2048\n" \
    "ds_read_b128 v[36:39], v8 offset:2560\nds_read_b128 v[40:43], v8 offset:4096\nds_read_b128 v[44:47], v8 offset:4608\nds_read_b128 v[48:51], v8 offset:6144\n" \
    "ds_read_b128 v[52:55], v8 offset:6656\ns_waitcnt lgkmcnt(6)\nv_mfma_f32_32x32x16_bf16 v[56:71], a[0:3], v[24:27], 0\nds_read_b128 v[24:27], v8 offset:8192\n" \
    "s_add_u32 m0, s72, 32768\ns_add_u32 s70, s53, s44\nbuffer_load_dwordx4 v0, s[64:67], s70 offen offset:0 sc1 lds\n" \
    "v_mfma_f32_32x32x16_bf16 v[56:71], a[4:7], v[28:31], v[56:71]\ns_waitcnt lgkmcnt(5)\nds_read_b128 v[28:31], v8 offset:8704\nv_readfirstlane_b32 s81, v10\n" \
    "v_mfma_f32_32x32x16_bf16 v[56:71], a[8:11], v[32:35], v[56:71]\nds_read_b128 v[32:35], v8 offset:10240\nbuffer_load_dwordx4 v0, s[64:67], s70 offen offset:1024 sc1 lds\n" \
    "s_cmp_eq_u32 s81, 13\ns_cbranch_scc1 .Lf15_%=\n.Lf16_%=:\nv_mfma_f32_32x32x16_bf16 v[56:71], a[12:15], v[36:39], v[56:71]\ns_waitcnt lgkmcnt(5)\n" \
    "ds_read_b128 v[36:39], v8 offset:10752\ns_add_u32 s71, s50, 1\ns_min_u32 s73, s71, 63\ns_lshl_b32 s73, s73, 20\ns_add_u32 s73, s73, s40\n" \
    "v_mfma_f32_32x32x16_bf16 v[56:71], a[16:19], v[40:43], v[56:71]\nds_read_b128 v[40:43], v8 offset:12288\nbuffer_load_dwordx4 v0, s[64:67], s70 offen offset:2048 sc1 lds\n" \
    "s_add_u32 s74, s48, s73\ns_lshr_b32 s74, s74, 8\ns_xor_b32 s74, s74, s49\nv_mfma_f32_32x32x16_bf16 v[56:71], a[20:23], v[44:47], v[56:71]\ns_waitcnt lgkmcnt(5)\n" \
    "ds_read_b128 v[44:47], v8 offset:12800\ns_and_b32 s74, s74, 0x2000\ns_add_u32 s74, s74, s73\ns_xor_b32 s75, s51, 0x10000\ns_add_u32 s75, s75, s52\n" \
    "v_mfma_f32_32x32x16_bf16 v[56:71], a[24:27], v[48:51], v[56:71]\nds_read_b128 v[48:51], v8 offset:14336\nbuffer_load_dwordx4 v0, s[64:67], s70 offen offset:3072 sc1 lds\n" \
    "v_mfma_f32_32x32x16_bf16 v[56:71], a[28:31], v[52:55], v[56:71]\ns_waitcnt lgkmcnt(5)\ns_waitcnt vmcnt(4)\ns_barrier\nds_read_b128 v[52:55], v8 offset:14848\n" \
    "v_mfma_f32_32x32x16_bf16 v[56:71], a[32:35], v[24:27], v[56:71]\nds_read_b128 v[24:27], v8 offset:16384\n" \
    "buffer_load_dwordx2 v[104:105], v2, s[64:67], s53 offen offset:0 sc1\nv_mfma_f32_32x32x16_bf16 v[56:71], a[36:39], v[28:31], v[56:71]\ns_waitcnt lgkmcnt(5)\n" \
    "ds_read_b128 v[28:31], v8 offset:16896\nbuffer_load_dwordx2 v[106:107], v2, s[64:67], s53 offen offset:256 sc1\n" \
    "v_mfma_f32_32x32x16_bf16 v[56:71], a[40:43], v[32:35], v[56:71]\nds_read_b128 v[32:35], v8 offset:18432\n" \
    "buffer_load_dwordx2 v[108:109], v2, s[64:67], s53 offen offset:512 sc1\nv_mfma_f32_32x32x16_bf16 v[56:71], a[44:47], v[36:39], v[56:71]\ns_waitcnt lgkmcnt(5)\n" \
    "ds_read_b128 v[36:39], v8 offset:18944\nbuffer_load_dwordx2 v[110:111], v2, s[64:67], s53 offen offset:768 sc1\n" \
    "v_mfma_f32_32x32x16_bf16 v[56:71], a[48:51], v[40:43], v[56:71]\nds_read_b128 v[40:43], v8 offset:20480\ns_and_b32 s69, s71, 1\ns_lshl_b32 s69, s69, 2\n" \
    "buffer_load_dword v9, v7, s[64:67], s69 offen sc1\nv_mfma_f32_32x32x16_bf16 v[56:71], a[52:55], v[44:47], v[56:71]\ns_waitcnt lgkmcnt(5)\n" \
    "ds_read_b128 v[44:47], v8 offset:20992\ns_add_u32 m0, s72, 49152\ns_add_u32 s70, s53, s45\nv_mfma_f32_32x32x16_bf16 v[56:71], a[56:59], v[48:51], v[56:71]\n" \
    "ds_read_b128 v[48:51], v8 offset:22528\nbuffer_load_dwordx4 v0, s[64:67], s70 offen offset:0 sc1 lds\nv_mfma_f32_32x32x16_bf16 v[56:71], a[60:63], v[52:55], v[56:71]\n" \
    "s_waitcnt lgkmcnt(5)\nds_read_b128 v[52:55], v8 offset:23040\nv_mfma_f32_32x32x16_bf16 v[56:71], a[64:67], v[24:27], v[56:71]\nds_read_b128 v[24:27], v8 offset:24576\n" \
    "buffer_load_dwordx4 v0, s[64:67], s70 offen offset:1024 sc1 lds\nv_mfma_f32_32x32x16_bf16 v[56:71], a[68:71], v[28:31], v[56:71]\ns_waitcnt lgkmcnt(5)\n" \
    "ds_read_b128 v[28:31], v8 offset:25088\nv_mfma_f32_32x32x16_bf16 v[56:71], a[72:75], v[32:35], v[56:71]\nds_read_b128 v[32:35], v8 offset:26624\n" \
    "buffer_load_dwordx4 v0, s[64:67], s70 offen offset:2048 sc1 lds\nv_mfma_f32_32x32x16_bf16 v[56:71], a[76:79], v[36:39], v[56:71]\ns_waitcnt lgkmcnt(5)\n" \
    "ds_read_b128 v[36:39], v8 offset:27136\nv_mfma_f32_32x32x16_bf16 v[56:71], a[80:83], v[40:43], v[56:71]\nds_read_b128 v[40:43], v8 offset:28672\n" \
    "buffer_load_dwordx4 v0, s[64:67], s70 offen offset:3072 sc1 lds\nv_mfma_f32_32x32x16_bf16 v[56:71], a[84:87], v[44:47], v[56:71]\ns_waitcnt lgkmcnt(5)\n" \
    "ds_read_b128 v[44:47], v8 offset:29184\nv_mfma_f32_32x32x16_bf16 v[56:71], a[88:91], v[48:51], v[56:71]\nds_read_b128 v[48:51], v8 offset:30720\ns_waitcnt vmcnt(5)\n" \
    "v_max3_u32 v12, v104, v105, v106\nv_max3_u32 v12, v12, v107, v108\nv_max3_u32 v12, v12, v109, v110\nv_max_u32 v12, v12, v111\n" \
    "v_mfma_f32_32x32x16_bf16 v[56:71], a[92:95], v[52:55], v[56:71]\ns_waitcnt lgkmcnt(5)\ns_barrier\nds_read_b128 v[52:55], v8 offset:31232\n" \
    "v_mfma_f32_32x32x16_bf16 v[56:71], a[96:99], v[24:27], v[56:71]\nds_read_b128 v[24:27], v8 offset:32768\nv_cmp_eq_u32 vcc, -1, v12\n" \
    "v_mfma_f32_32x32x16_bf16 v[56:71], a[100:103], v[28:31], v[56:71]\ns_waitcnt lgkmcnt(5)\nds_read_b128 v[28:31], v8 offset:33280\ns_cbranch_vccnz .Lf12_%=\n.Lf13_%=:\n" \
    "v_add_f32 v12, v104, v106\nv_add_f32 v13, v108, v110\nv_mfma_f32_32x32x16_bf16 v[56:71], a[104:107], v[32:35], v[56:71]\nds_read_b128 v[32:35], v8 offset:34816\n" \
    "s_waitcnt vmcnt(4)\nv_add_f32 v12, v12, v13\nv_add_f32 v105, v105, v107\nv_add_f32 v109, v109, v111\nv_readfirstlane_b32 s76, v9\n" \
    "v_mfma_f32_32x32x16_bf16 v[56:71], a[108:111], v[36:39], v[56:71]\ns_waitcnt lgkmcnt(5)\nds_read_b128 v[36:39], v8 offset:35328\ns_cmp_gt_i32 s76, s71\n" \
    "s_cselect_b32 s76, 0, 0x7f000000\nv_add_f32_dpp v13, v12, v12 row_ror:8 row_mask:0xf bank_mask:0xf\nv_add_f32_dpp v14, v12, v12 row_ror:8 row_mask:0xf bank_mask:0xf\n" \
    "v_mfma_f32_32x32x16_bf16 v[56:71], a[112:115], v[40:43], v[56:71]\nds_read_b128 v[40:43], v8 offset:36864\nv_permlane16_swap_b32 v13, v14\nv_add_f32 v12, v13, v14\n" \
    "v_add_f32 v13, v13, v14\nv_or_b32 v11, s76, v0\nv_mfma_f32_32x32x16_bf16 v[56:71], a[116:119], v[44:47], v[56:71]\ns_waitcnt lgkmcnt(5)\n" \
    "ds_read_b128 v[44:47], v8 offset:37376\nv_permlane32_swap_b32 v12, v13\nv_add_f32 v12, v12, v13\nv_mul_f32 v12, 0x3d000000, v12\n" \
    "v_mfma_f32_32x32x16_bf16 v[56:71], a[120:123], v[48:51], v[56:71]\nds_read_b128 v[48:51], v8 offset:38912\nv_sub_f32 v104, v104, v12\nv_sub_f32 v106, v106, v12\n" \
    "v_sub_f32 v108, v108, v12\nv_sub_f32 v110, v110, v12\nv_mul_f32 v13, v104, v104\nv_mfma_f32_32x32x16_bf16 v[56:71], a[124:127], v[52:55], v[56:71]\ns_waitcnt lgkmcnt(5)\n" \
    "ds_read_b128 v[52:55], v8 offset:39424\nv_fmac_f32 v13, v106, v106\nv_fmac_f32 v13, v108, v108\nv_fmac_f32 v13, v110, v110\nv_add_f32 v14, v105, v109\n" \
    "v_mfma_f32_32x32x16_bf16 v[56:71], a[128:131], v[24:27], v[56:71]\nds_read_b128 v[24:27], v8 offset:40960\ns_add_u32 m0, s75, 0\ns_add_u32 s70, s74, s42\n" \
    "buffer_load_dwordx4 v11, s[64:67], s70 offen offset:0 sc1 lds\nv_fmamk_f32 v13, v13, 0x42000000, v14\nv_mfma_f32_32x32x16_bf16 v[56:71], a[132:135], v[28:31], v[56:71]\n" \
    "s_waitcnt lgkmcnt(5)\nds_read_b128 v[28:31], v8 offset:41472\nbuffer_load_dwordx4 v11, s[64:67], s70 offen offset:1024 sc1 lds\n" \
    "v_add_f32_dpp v14, v13, v13 row_ror:8 row_mask:0xf bank_mask:0xf\nv_add_f32_dpp v15, v13, v13 row_ror:8 row_mask:0xf bank_mask:0xf\n" \
    "v_mfma_f32_32x32x16_bf16 v[56:71], a[136:139], v[32:35], v[56:71]\nds_read_b128 v[32:35], v8 offset:43008\nv_permlane16_swap_b32 v14, v15\nv_add_f32 v13, v14, v15\n" \
    "v_add_f32 v14, v14, v15\nv_mfma_f32_32x32x16_bf16 v[56:71], a[140:143], v[36:39], v[56:71]\ns_waitcnt lgkmcnt(5)\nds_read_b128 v[36:39], v8 offset:43520\n" \
    "v_permlane32_swap_b32 v13, v14\nv_add_f32 v13, v13, v14\nv_mov_b32 v14, 0x3727c5ac\nv_mfma_f32_32x32x16_bf16 v[56:71], a[144:147], v[40:43], v[56:71]\n" \
    "ds_read_b128 v[40:43], v8 offset:45056\nbuffer_load_dwordx4 v11, s[64:67], s70 offen offset:2048 sc1 lds\nv_fmamk_f32 v13, v13, 0x3a800000, v14\nv_rsq_f32 v13, v13\n" \
    "v_mfma_f32_32x32x16_bf16 v[56:71], a[148:151], v[44:47], v[56:71]\ns_waitcnt lgkmcnt(5)\nds_read_b128 v[44:47], v8 offset:45568\nds_write_b64 v3, v[12:13]\n" \
    "v_mfma_f32_32x32x16_bf16 v[56:71], a[152:155], v[48:51], v[56:71]\nds_read_b128 v[48:51], v8 offset:47104\n" \
    "buffer_load_dwordx4 v11, s[64:67], s70 offen offset:3072 sc1 lds\ns_add_u32 m0, s75, 16384\ns_add_u32 s70, s74, s43\n" \
    "v_mfma_f32_32x32x16_bf16 v[56:71], a[156:159], v[52:55], v[56:71]\ns_waitcnt lgkmcnt(6)\ns_waitcnt vmcnt(4) lgkmcnt(1)\ns_barrier\nds_read_b128 v[52:55], v8 offset:47616\n" \
    "v_mfma_f32_32x32x16_bf16 v[56:71], a[160:163], v[24:27], v[56:71]\nds_read_b128 v[24:27], v8 offset:49152\nbuffer_load_dwordx4 v11, s[64:67], s70 offen offset:0 sc1 lds\n" \
    "v_mfma_f32_32x32x16_bf16 v[56:71], a[164:167], v[28:31], v[56:71]\nds_read_b128 v[28:31], v8 offset:49664\n" \
    "buffer_load_dwordx4 v11, s[64:67], s70 offen offset:1024 sc1 lds\nv_mfma_f32_32x32x16_bf16 v[56:71], a[168:171], v[32:35], v[56:71]\n" \
    "ds_read_b128 v[32:35], v8 offset:51200\nbuffer_load_dwordx4 v11, s[64:67], s70 offen offset:2048 sc1 lds\n" \
    "v_mfma_f32_32x32x16_bf16 v[56:71], a[172:175], v[36:39], v[56:71]\nds_read_b128 v[36:39], v8 offset:51712\n" \
    "v_mfma_f32_32x32x16_bf16 v[56:71], a[176:179], v[40:43], v[56:71]\nds_read_b128 v[40:43], v8 offset:53248\nds_read_b64 v[14:15], v4\n" \
    "buffer_load_dwordx4 v11, s[64:67], s70 offen offset:3072 sc1 lds\nv_mfma_f32_32x32x16_bf16 v[56:71], a[180:183], v[44:47], v[56:71]\ns_waitcnt lgkmcnt(6)\n" \
    "ds_read_b128 v[44:47], v8 offset:53760\nv_mfma_f32_32x32x16_bf16 v[56:71], a[184:187], v[48:51], v[56:71]\nds_read_b128 v[48:51], v8 offset:55296\n" \
    "s_add_u32 m0, s75, 32768\ns_add_u32 s70, s74, s44\nbuffer_load_dwordx4 v11, s[64:67], s70 offen offset:0 sc1 lds\n" \
    "v_mfma_f32_32x32x16_bf16 v[56:71], a[188:191], v[52:55], v[56:71]\ns_waitcnt lgkmcnt(6)\nds_read_b128 v[52:55], v8 offset:55808\n" \
    "v_mfma_f32_32x32x16_bf16 v[56:71], a[192:195], v[24:27], v[56:71]\nds_read_b128 v[24:27], v8 offset:57344\n" \
    "buffer_load_dwordx4 v11, s[64:67], s70 offen offset:1024 sc1 lds\nv_mfma_f32_32x32x16_bf16 v[56:71], a[196:199], v[28:31], v[56:71]\ns_waitcnt lgkmcnt(6)\n" \
    "ds_read_b128 v[28:31], v8 offset:57856\nv_mfma_f32_32x32x16_bf16 v[56:71], a[200:203], v[32:35], v[56:71]\nds_read_b128 v[32:35], v8 offset:59392\n" \
    "buffer_load_dwordx4 v11, s[64:67], s70 offen offset:2048 sc1 lds\nv_mfma_f32_32x32x16_bf16 v[56:71], a[204:207], v[36:39], v[56:71]\ns_waitcnt lgkmcnt(5)\n" \
    "ds_read_b128 v[36:39], v8 offset:59904\nv_mfma_f32_32x32x16_bf16 v[56:71], a[208:211], v[40:43], v[56:71]\nds_read_b128 v[40:43], v8 offset:61440\n" \
    "buffer_load_dwordx4 v11, s[64:67], s70 offen offset:3072 sc1 lds\nv_mfma_f32_32x32x16_bf16 v[56:71], a[212:215], v[44:47], v[56:71]\ns_waitcnt lgkmcnt(5)\n" \
    "ds_read_b128 v[44:47], v8 offset:61952\nv_mfma_f32_32x32x16_bf16 v[56:71], a[216:219], v[48:51], v[56:71]\nds_read_b128 v[48:51], v8 offset:63488\n" \
    "s_add_u32 m0, s75, 49152\ns_add_u32 s70, s74, s45\nbuffer_load_dwordx4 v11, s[64:67], s70 offen offset:0 sc1 lds\n" \
    "v_mfma_f32_32x32x16_bf16 v[56:71], a[220:223], v[52:55], v[56:71]\ns_waitcnt lgkmcnt(5)\nds_read_b128 v[52:55], v8 offset:64000\n" \
    "v_mfma_f32_32x32x16_bf16 v[56:71], a[224:227], v[24:27], v[56:71]\nbuffer_load_dwordx4 v11, s[64:67], s70 offen offset:1024 sc1 lds\n" \
    "v_mfma_f32_32x32x16_bf16 v[56:71], a[228:231], v[28:31], v[56:71]\ns_waitcnt lgkmcnt(4)\nv_mfma_f32_32x32x16_bf16 v[56:71], a[232:235], v[32:35], v[56:71]\n" \
    "buffer_load_dwordx4 v11, s[64:67], s70 offen offset:2048 sc1 lds\nv_mfma_f32_32x32x16_bf16 v[56:71], a[236:239], v[36:39], v[56:71]\ns_waitcnt lgkmcnt(2)\n" \
    "v_mfma_f32_32x32x16_bf16 v[56:71], a[240:243], v[40:43], v[56:71]\nbuffer_load_dwordx4 v11, s[64:67], s70 offen offset:3072 sc1 lds\n" \
    "v_mfma_f32_32x32x16_bf16 v[56:71], a[244:247], v[44:47], v[56:71]\ns_waitcnt lgkmcnt(0)\nv_mfma_f32_32x32x16_bf16 v[56:71], a[248:251], v[48:51], v[56:71]\n" \
    "v_mfma_f32_32x32x16_bf16 v[56:71], a[252:255], v[52:55], v[56:71]\ns_and_b32 s69, s71, 1\ns_lshl_b32 s69, s69, 2\ns_branch .Lf5_%=\n.Lf4_%=:\ns_add_u32 s72, s52, s51\n" \
    "v_add_u32 v8, s51, v1\ns_waitcnt vmcnt(15)\ns_barrier\nds_read_b128 v[24:27], v8 offset:0\nds_read_b128 v[28:31], v8 offset:512\nds_read_b128 v[32:35], v8 offset:2048\n" \
    "ds_read_b128 v[36:39], v8 offset:2560\nds_read_b128 v[40:43], v8 offset:4096\nds_read_b128 v[44:47], v8 offset:4608\nds_read_b128 v[48:51], v8 offset:6144\n" \
    "ds_read_b128 v[52:55], v8 offset:6656\ns_waitcnt lgkmcnt(6)\nv_mfma_f32_32x32x16_bf16 v[56:71], a[0:3], v[24:27], 0\nds_read_b128 v[24:27], v8 offset:8192\n" \
    "buffer_load_dwordx2 v[104:105], v2, s[64:67], s53 offen offset:0 sc1\nv_mfma_f32_32x32x16_bf16 v[56:71], a[4:7], v[28:31], v[56:71]\ns_waitcnt lgkmcnt(5)\n" \
    "ds_read_b128 v[28:31], v8 offset:8704\nbuffer_load_dwordx2 v[106:107], v2, s[64:67], s53 offen offset:256 sc1\n" \
    "v_mfma_f32_32x32x16_bf16 v[56:71], a[8:11], v[32:35], v[56:71]\nds_read_b128 v[32:35], v8 offset:10240\n" \
    "buffer_load_dwordx2 v[108:109], v2, s[64:67], s53 offen offset:512 sc1\ns_add_u32 s71, s50, 1\ns_min_u32 s73, s71, 63\ns_lshl_b32 s73, s73, 20\n" \
    "v_mfma_f32_32x32x16_bf16 v[56:71], a[12:15], v[36:39], v[56:71]\ns_waitcnt lgkmcnt(5)\nds_read_b128 v[36:39], v8 offset:10752\n" \
    "buffer_load_dwordx2 v[110:111], v2, s[64:67], s53 offen offset:768 sc1\ns_add_u32 s73, s73, s40\ns_add_u32 s74, s48, s73\n" \
    "v_mfma_f32_32x32x16_bf16 v[56:71], a[16:19], v[40:43], v[56:71]\nds_read_b128 v[40:43], v8 offset:12288\ns_lshr_b32 s74, s74, 8\ns_xor_b32 s74, s74, s49\n" \
    "s_and_b32 s74, s74, 0x2000\ns_add_u32 s74, s74, s73\ns_xor_b32 s75, s51, 0x10000\nv_mfma_f32_32x32x16_bf16 v[56:71], a[20:23], v[44:47], v[56:71]\ns_waitcnt lgkmcnt(5)\n" \
    "ds_read_b128 v[44:47], v8 offset:12800\ns_add_u32 s75, s75, s52\nv_mfma_f32_32x32x16_bf16 v[56:71], a[24:27], v[48:51], v[56:71]\nds_read_b128 v[48:51], v8 offset:14336\n" \
    "v_mfma_f32_32x32x16_bf16 v[56:71], a[28:31], v[52:55], v[56:71]\ns_waitcnt lgkmcnt(5)\ns_waitcnt vmcnt(15)\ns_barrier\nds_read_b128 v[52:55], v8 offset:14848\n" \
    "v_mfma_f32_32x32x16_bf16 v[56:71], a[32:35], v[24:27], v[56:71]\nds_read_b128 v[24:27], v8 offset:16384\nv_mfma_f32_32x32x16_bf16 v[56:71], a[36:39], v[28:31], v[56:71]\n" \
    "s_waitcnt lgkmcnt(5)\nds_read_b128 v[28:31], v8 offset:16896\nv_mfma_f32_32x32x16_bf16 v[56:71], a[40:43], v[32:35], v[56:71]\nds_read_b128 v[32:35], v8 offset:18432\n" \
    "v_mfma_f32_32x32x16_bf16 v[56:71], a[44:47], v[36:39], v[56:71]\ns_waitcnt lgkmcnt(5)\nds_read_b128 v[36:39], v8 offset:18944\n" \
    "v_mfma_f32_32x32x16_bf16 v[56:71], a[48:51], v[40:43], v[56:71]\nds_read_b128 v[40:43], v8 offset:20480\ns_and_b32 s69, s71, 1\ns_lshl_b32 s69, s69, 2\n" \
    "buffer_load_dword v9, v7, s[64:67], s69 offen sc1\nv_mfma_f32_32x32x16_bf16 v[56:71], a[52:55], v[44:47], v[56:71]\ns_waitcnt lgkmcnt(5)\n" \
    "ds_read_b128 v[44:47], v8 offset:20992\nv_mfma_f32_32x32x16_bf16 v[56:71], a[56:59], v[48:51], v[56:71]\nds_read_b128 v[48:51], v8 offset:22528\n" \
    "v_mfma_f32_32x32x16_bf16 v[56:71], a[60:63], v[52:55], v[56:71]\ns_waitcnt lgkmcnt(5)\nds_read_b128 v[52:55], v8 offset:23040\n" \
    "v_mfma_f32_32x32x16_bf16 v[56:71], a[64:67], v[24:27], v[56:71]\nds_read_b128 v[24:27], v8 offset:24576\ns_waitcnt vmcnt(1)\nv_readfirstlane_b32 s81, v10\n" \
    "v_mfma_f32_32x32x16_bf16 v[56:71], a[68:71], v[28:31], v[56:71]\ns_waitcnt lgkmcnt(5)\nds_read_b128 v[28:31], v8 offset:25088\ns_cmp_eq_u32 s81, 13\n" \
    "s_cbranch_scc1 .Lf20_%=\n.Lf21_%=:\nv_mfma_f32_32x32x16_bf16 v[56:71], a[72:75], v[32:35], v[56:71]\nds_read_b128 v[32:35], v8 offset:26624\n" \
    "v_mfma_f32_32x32x16_bf16 v[56:71], a[76:79], v[36:39], v[56:71]\ns_waitcnt lgkmcnt(5)\nds_read_b128 v[36:39], v8 offset:27136\n" \
    "v_mfma_f32_32x32x16_bf16 v[56:71], a[80:83], v[40:43], v[56:71]\nds_read_b128 v[40:43], v8 offset:28672\nv_mfma_f32_32x32x16_bf16 v[56:71], a[84:87], v[44:47], v[56:71]\n" \
    "s_waitcnt lgkmcnt(5)\nds_read_b128 v[44:47], v8 offset:29184\nv_mfma_f32_32x32x16_bf16 v[56:71], a[88:91], v[48:51], v[56:71]\nds_read_b128 v[48:51], v8 offset:30720\n" \
    "v_max3_u32 v12, v104, v105, v106\nv_max3_u32 v12, v12, v107, v108\nv_max3_u32 v12, v12, v109, v110\nv_max_u32 v12, v12, v111\n" \
    "v_mfma_f32_32x32x16_bf16 v[56:71], a[92:95], v[52:55], v[56:71]\ns_waitcnt lgkmcnt(5)\ns_barrier\nds_read_b128 v[52:55], v8 offset:31232\n" \
    "v_mfma_f32_32x32x16_bf16 v[56:71], a[96:99], v[24:27], v[56:71]\nds_read_b128 v[24:27], v8 offset:32768\nv_cmp_eq_u32 vcc, -1, v12\n" \
    "v_mfma_f32_32x32x16_bf16 v[56:71], a[100:103], v[28:31], v[56:71]\ns_waitcnt lgkmcnt(5)\nds_read_b128 v[28:31], v8 offset:33280\ns_cbranch_vccnz .Lf17_%=\n.Lf18_%=:\n" \
    "v_add_f32 v12, v104, v106\nv_add_f32 v13, v108, v110\nv_mfma_f32_32x32x16_bf16 v[56:71], a[104:107], v[32:35], v[56:71]\nds_read_b128 v[32:35], v8 offset:34816\n" \
    "s_waitcnt vmcnt(0)\nv_add_f32 v12, v12, v13\nv_add_f32 v105, v105, v107\nv_add_f32 v109, v109, v111\nv_readfirstlane_b32 s76, v9\n" \
    "v_mfma_f32_32x32x16_bf16 v[56:71], a[108:111], v[36:39], v[56:71]\ns_waitcnt lgkmcnt(5)\nds_read_b128 v[36:39], v8 offset:35328\ns_cmp_gt_i32 s76, s71\n" \
    "s_cselect_b32 s76, 0, 0x7f000000\nv_add_f32_dpp v13, v12, v12 row_ror:8 row_mask:0xf bank_mask:0xf\nv_add_f32_dpp v14, v12, v12 row_ror:8 row_mask:0xf bank_mask:0xf\n" \
    "v_mfma_f32_32x32x16_bf16 v[56:71], a[112:115], v[40:43], v[56:71]\nds_read_b128 v[40:43], v8 offset:36864\nv_permlane16_swap_b32 v13, v14\nv_add_f32 v12, v13, v14\n" \
    "v_add_f32 v13, v13, v14\nv_or_b32 v11, s76, v0\nv_mfma_f32_32x32x16_bf16 v[56:71], a[116:119], v[44:47], v[56:71]\ns_waitcnt lgkmcnt(5)\n" \
    "ds_read_b128 v[44:47], v8 offset:37376\nv_permlane32_swap_b32 v12, v13\nv_add_f32 v12, v12, v13\nv_mul_f32 v12, 0x3d000000, v12\n" \
    "v_mfma_f32_32x32x16_bf16 v[56:71], a[120:123], v[48:51], v[56:71]\nds_read_b128 v[48:51], v8 offset:38912\nv_sub_f32 v104, v104, v12\nv_sub_f32 v106, v106, v12\n" \
    "v_sub_f32 v108, v108, v12\nv_sub_f32 v110, v110, v12\nv_mul_f32 v13, v104, v104\nv_mfma_f32_32x32x16_bf16 v[56:71], a[124:127], v[52:55], v[56:71]\ns_waitcnt lgkmcnt(5)\n" \
    "ds_read_b128 v[52:55], v8 offset:39424\nv_fmac_f32 v13, v106, v106\nv_fmac_f32 v13, v108, v108\nv_fmac_f32 v13, v110, v110\nv_add_f32 v14, v105, v109\n" \
    "v_mfma_f32_32x32x16_bf16 v[56:71], a[128:131], v[24:27], v[56:71]\nds_read_b128 v[24:27], v8 offset:40960\ns_add_u32 m0, s75, 0\ns_add_u32 s70, s74, s42\n" \
    "buffer_load_dwordx4 v11, s[64:67], s70 offen offset:0 sc1 lds\nv_fmamk_f32 v13, v13, 0x42000000, v14\nv_mfma_f32_32x32x16_bf16 v[56:71], a[132:135], v[28:31], v[56:71]\n" \
    "s_waitcnt lgkmcnt(5)\nds_read_b128 v[28:31], v8 offset:41472\nbuffer_load_dwordx4 v11, s[64:67], s70 offen offset:1024 sc1 lds\n" \
    "v_add_f32_dpp v14, v13, v13 row_ror:8 row_mask:0xf bank_mask:0xf\nv_add_f32_dpp v15, v13, v13 row_ror:8 row_mask:0xf bank_mask:0xf\n" \
    "v_mfma_f32_32x32x16_bf16 v[56:71], a[136:139], v[32:35], v[56:71]\nds_read_b128 v[32:35], v8 offset:43008\nv_permlane16_swap_b32 v14, v15\nv_add_f32 v13, v14, v15\n" \
    "v_add_f32 v14, v14, v15\nv_mfma_f32_32x32x16_bf16 v[56:71], a[140:143], v[36:39], v[56:71]\ns_waitcnt lgkmcnt(5)\nds_read_b128 v[36:39], v8 offset:43520\n" \
    "v_permlane32_swap_b32 v13, v14\nv_add_f32 v13, v13, v14\nv_mov_b32 v14, 0x3727c5ac\nv_mfma_f32_32x32x16_bf16 v[56:71], a[144:147], v[40:43], v[56:71]\n" \
    "ds_read_b128 v[40:43], v8 offset:45056\nbuffer_load_dwordx4 v11, s[64:67], s70 offen offset:2048 sc1 lds\nv_fmamk_f32 v13, v13, 0x3a800000, v14\nv_rsq_f32 v13, v13\n" \
    "v_mfma_f32_32x32x16_bf16 v[56:71], a[148:151], v[44:47], v[56:71]\ns_waitcnt lgkmcnt(5)\nds_read_b128 v[44:47], v8 offset:45568\nds_write_b64 v3, v[12:13]\n" \
    "v_mfma_f32_32x32x16_bf16 v[56:71], a[152:155], v[48:51], v[56:71]\nds_read_b128 v[48:51], v8 offset:47104\n" \
    "buffer_load_dwordx4 v11, s[64:67], s70 offen offset:3072 sc1 lds\ns_add_u32 m0, s75, 16384\ns_add_u32 s70, s74, s43\n" \
    "v_mfma_f32_32x32x16_bf16 v[56:71], a[156:159], v[52:55], v[56:71]\ns_waitcnt lgkmcnt(6)\ns_waitcnt lgkmcnt(1)\ns_barrier\nds_read_b128 v[52:55], v8 offset:47616\n" \
    "v_mfma_f32_32x32x16_bf16 v[56:71], a[160:163], v[24:27], v[56:71]\nds_read_b128 v[24:27], v8 offset:49152\nbuffer_load_dwordx4 v11, s[64:67], s70 offen offset:0 sc1 lds\n" \
    "v_mfma_f32_32x32x16_bf16 v[56:71], a[164:167], v[28:31], v[56:71]\nds_read_b128 v[28:31], v8 offset:49664\n" \
    "buffer_load_dwordx4 v11, s[64:67], s70 offen offset:1024 sc1 lds\nv_mfma_f32_32x32x16_bf16 v[56:71], a[168:171], v[32:35], v[56:71]\n" \
    "ds_read_b128 v[32:35], v8 offset:51200\nbuffer_load_dwordx4 v11, s[64:67], s70 offen offset:2048 sc1 lds\n" \
    "v_mfma_f32_32x32x16_bf16 v[56:71], a[172:175], v[36:39], v[56:71]\nds_read_b128 v[36:39], v8 offset:51712\n" \
    "v_mfma_f32_32x32x16_bf16 v[56:71], a[176:179], v[40:43], v[56:71]\nds_read_b128 v[40:43], v8 offset:53248\nds_read_b64 v[14:15], v4\n" \
    "buffer_load_dwordx4 v11, s[64:67], s70 offen offset:3072 sc1 lds\nv_mfma_f32_32x32x16_bf16 v[56:71], a[180:183], v[44:47], v[56:71]\ns_waitcnt lgkmcnt(6)\n" \
    "ds_read_b128 v[44:47], v8 offset:53760\nv_mfma_f32_32x32x16_bf16 v[56:71], a[184:187], v[48:51], v[56:71]\nds_read_b128 v[48:51], v8 offset:55296\n" \
    "s_add_u32 m0, s75, 32768\ns_add_u32 s70, s74, s44\nbuffer_load_dwordx4 v11, s[64:67], s70 offen offset:0 sc1 lds\n" \
    "v_mfma_f32_32x32x16_bf16 v[56:71], a[188:191], v[52:55], v[56:71]\ns_waitcnt lgkmcnt(6)\nds_read_b128 v[52:55], v8 offset:55808\n" \
    "v_mfma_f32_32x32x16_bf16 v[56:71], a[192:195], v[24:27], v[56:71]\nds_read_b128 v[24:27], v8 offset:57344\n" \
    "buffer_load_dwordx4 v11, s[64:67], s70 offen offset:1024 sc1 lds\nv_mfma_f32_32x32x16_bf16 v[56:71], a[196:199], v[28:31], v[56:71]\ns_waitcnt lgkmcnt(6)\n" \
    "ds_read_b128 v[28:31], v8 offset:57856\nv_mfma_f32_32x32x16_bf16 v[56:71], a[200:203], v[32:35], v[56:71]\nds_read_b128 v[32:35], v8 offset:59392\n" \
    "buffer_load_dwordx4 v11, s[64:67], s70 offen offset:2048 sc1 lds\nv_mfma_f32_32x32x16_bf16 v[56:71], a[204:207], v[36:39], v[56:71]\ns_waitcnt lgkmcnt(5)\n" \
    "ds_read_b128 v[36:39], v8 offset:59904\nv_mfma_f32_32x32x16_bf16 v[56:71], a[208:211], v[40:43], v[56:71]\nds_read_b128 v[40:43], v8 offset:61440\n" \
    "buffer_load_dwordx4 v11, s[64:67], s70 offen offset:3072 sc1 lds\nv_mfma_f32_32x32x16_bf16 v[56:71], a[212:215], v[44:47], v[56:71]\ns_waitcnt lgkmcnt(5)\n" \
    "ds_read_b128 v[44:47], v8 offset:61952\nv_mfma_f32_32x32x16_bf16 v[56:71], a[216:219], v[48:51], v[56:71]\nds_read_b128 v[48:51], v8 offset:63488\n" \
    "s_add_u32 m0, s75, 49152\ns_add_u32 s70, s74, s45\nbuffer_load_dwordx4 v11, s[64:67], s70 offen offset:0 sc1 lds\n" \
    "v_mfma_f32_32x32x16_bf16 v[56:71], a[220:223], v[52:55], v[56:71]\ns_waitcnt lgkmcnt(5)\nds_read_b128 v[52:55], v8 offset:64000\n" \
    "v_mfma_f32_32x32x16_bf16 v[56:71], a[224:227], v[24:27], v[56:71]\nbuffer_load_dwordx4 v11, s[64:67], s70 offen offset:1024 sc1 lds\n" \
    "v_mfma_f32_32x32x16_bf16 v[56:71], a[228:231], v[28:31], v[56:71]\ns_waitcnt lgkmcnt(4)\nv_mfma_f32_32x32x16_bf16 v[56:71], a[232:235], v[32:35], v[56:71]\n" \
    "buffer_load_dwordx4 v11, s[64:67], s70 offen offset:2048 sc1 lds\nv_mfma_f32_32x32x16_bf16 v[56:71], a[236:239], v[36:39], v[56:71]\ns_waitcnt lgkmcnt(2)\n" \
    "v_mfma_f32_32x32x16_bf16 v[56:71], a[240:243], v[40:43], v[56:71]\nbuffer_load_dwordx4 v11, s[64:67], s70 offen offset:3072 sc1 lds\n" \
    "v_mfma_f32_32x32x16_bf16 v[56:71], a[244:247], v[44:47], v[56:71]\ns_waitcnt lgkmcnt(0)\nv_mfma_f32_32x32x16_bf16 v[56:71], a[248:251], v[48:51], v[56:71]\n" \
    "v_mfma_f32_32x32x16_bf16 v[56:71], a[252:255], v[52:55], v[56:71]\ns_and_b32 s69, s71, 1\ns_lshl_b32 s69, s69, 2\n.Lf5_%=:\n" \
    "buffer_load_dword v9, v7, s[64:67], s69 offen sc1\ns_nop 8\nv_cmp_u_f32 vcc, v56, v56\ns_xor_b32 s77, s53, 0x2000\ns_add_u32 s77, s77, s41\ns_cbranch_vccnz .Lf9_%=\n" \
    ".Lf11_%=:\nv_pk_fma_f32 v[56:57], v[72:73], v[14:15], v[56:57] op_sel_hi:[1,0,1] neg_lo:[1,0,0] neg_hi:[1,0,0]\n" \
    "v_pk_fma_f32 v[58:59], v[74:75], v[14:15], v[58:59] op_sel_hi:[1,0,1] neg_lo:[1,0,0] neg_hi:[1,0,0]\n" \
    "v_pk_fma_f32 v[60:61], v[76:77], v[14:15], v[60:61] op_sel_hi:[1,0,1] neg_lo:[1,0,0] neg_hi:[1,0,0]\n" \
    "v_pk_fma_f32 v[62:63], v[78:79], v[14:15], v[62:63] op_sel_hi:[1,0,1] neg_lo:[1,0,0] neg_hi:[1,0,0]\nv_pk_fma_f32 v[56:57], v[56:57], v[14:15], v[88:89] op_sel:[0,1,0]\n" \
    "v_pk_fma_f32 v[58:59], v[58:59], v[14:15], v[90:91] op_sel:[0,1,0]\nv_pk_fma_f32 v[60:61], v[60:61], v[14:15], v[92:93] op_sel:[0,1,0]\n" \
    "v_pk_fma_f32 v[62:63], v[62:63], v[14:15], v[94:95] op_sel:[0,1,0]\nv_pk_mul_f32 v[112:113], v[56:57], v[56:57]\nv_pk_mul_f32 v[114:115], v[58:59], v[58:59]\n" \
    "v_pk_mul_f32 v[116:117], v[60:61], v[60:61]\nv_pk_mul_f32 v[118:119], v[62:63], v[62:63]\n" \
    "v_pk_fma_f32 v[112:113], v[112:113], s[46:47], s[46:47] op_sel:[0,0,1] op_sel_hi:[1,0,1]\n" \
    "v_pk_fma_f32 v[114:115], v[114:115], s[46:47], s[46:47] op_sel:[0,0,1] op_sel_hi:[1,0,1]\n" \
    "v_pk_fma_f32 v[116:117], v[116:117], s[46:47], s[46:47] op_sel:[0,0,1] op_sel_hi:[1,0,1]\n" \
    "v_pk_fma_f32 v[118:119], v[118:119], s[46:47], s[46:47] op_sel:[0,0,1] op_sel_hi:[1,0,1]\nv_pk_mul_f32 v[112:113], v[112:113], v[56:57]\n" \
    "v_pk_mul_f32 v[114:115], v[114:115], v[58:59]\nv_pk_mul_f32 v[116:117], v[116:117], v[60:61]\nv_pk_mul_f32 v[118:119], v[118:119], v[62:63]\nv_exp_f32 v112, v112\n" \
    "v_exp_f32 v113, v113\nv_exp_f32 v114, v114\nv_exp_f32 v115, v115\nv_exp_f32 v116, v116\nv_exp_f32 v117, v117\nv_exp_f32 v118, v118\nv_exp_f32 v119, v119\n" \
    "v_pk_add_f32 v[112:113], v[112:113], 1.0 op_sel_hi:[1,0]\nv_pk_add_f32 v[114:115], v[114:115], 1.0 op_sel_hi:[1,0]\n" \
    "v_pk_add_f32 v[116:117], v[116:117], 1.0 op_sel_hi:[1,0]\nv_pk_add_f32 v[118:119], v[118:119], 1.0 op_sel_hi:[1,0]\nv_rcp_f32 v112, v112\nv_rcp_f32 v113, v113\n" \
    "v_rcp_f32 v114, v114\nv_rcp_f32 v115, v115\nv_rcp_f32 v116, v116\nv_rcp_f32 v117, v117\nv_rcp_f32 v118, v118\nv_rcp_f32 v119, v119\n" \
    "v_pk_mul_f32 v[56:57], v[56:57], v[112:113]\nv_pk_mul_f32 v[58:59], v[58:59], v[114:115]\nv_pk_mul_f32 v[60:61], v[60:61], v[116:117]\n" \
    "v_pk_mul_f32 v[62:63], v[62:63], v[118:119]\nv_cvt_pk_bf16_f32 v56, v56, v57\nv_cvt_pk_bf16_f32 v57, v58, v59\nv_cvt_pk_bf16_f32 v58, v60, v61\n" \
    "v_cvt_pk_bf16_f32 v59, v62, v63\nv_min_u32 v56, -2, v56\nv_min_u32 v57, -2, v57\nv_min_u32 v58, -2, v58\nv_min_u32 v59, -2, v59\ns_nop 0\nv_permlane32_swap_b32 v56, v58\n" \
    "v_permlane32_swap_b32 v57, v59\nbuffer_store_dwordx4 v[56:59], v5, s[64:67], s77 offen offset:0 sc1\n" \
    "v_pk_fma_f32 v[64:65], v[80:81], v[14:15], v[64:65] op_sel_hi:[1,0,1] neg_lo:[1,0,0] neg_hi:[1,0,0]\n" \
    "v_pk_fma_f32 v[66:67], v[82:83], v[14:15], v[66:67] op_sel_hi:[1,0,1] neg_lo:[1,0,0] neg_hi:[1,0,0]\n" \
    "v_pk_fma_f32 v[68:69], v[84:85], v[14:15], v[68:69] op_sel_hi:[1,0,1] neg_lo:[1,0,0] neg_hi:[1,0,0]\n" \
    "v_pk_fma_f32 v[70:71], v[86:87], v[14:15], v[70:71] op_sel_hi:[1,0,1] neg_lo:[1,0,0] neg_hi:[1,0,0]\nv_pk_fma_f32 v[64:65], v[64:65], v[14:15], v[96:97] op_sel:[0,1,0]\n" \
    "v_pk_fma_f32 v[66:67], v[66:67], v[14:15], v[98:99] op_sel:[0,1,0]\nv_pk_fma_f32 v[68:69], v[68:69], v[14:15], v[100:101] op_sel:[0,1,0]\n" \
    "v_pk_fma_f32 v[70:71], v[70:71], v[14:15], v[102:103] op_sel:[0,1,0]\nv_pk_mul_f32 v[112:113], v[64:65], v[64:65]\nv_pk_mul_f32 v[114:115], v[66:67], v[66:67]\n" \
    "v_pk_mul_f32 v[116:117], v[68:69], v[68:69]\nv_pk_mul_f32 v[118:119], v[70:71], v[70:71]\n" \
    "v_pk_fma_f32 v[112:113], v[112:113], s[46:47], s[46:47] op_sel:[0,0,1] op_sel_hi:[1,0,1]\n" \
    "v_pk_fma_f32 v[114:115], v[114:115], s[46:47], s[46:47] op_sel:[0,0,1] op_sel_hi:[1,0,1]\n" \
    "v_pk_fma_f32 v[116:117], v[116:117], s[46:47], s[46:47] op_sel:[0,0,1] op_sel_hi:[1,0,1]\n" \
    "v_pk_fma_f32 v[118:119], v[118:119], s[46:47], s[46:47] op_sel:[0,0,1] op_sel_hi:[1,0,1]\nv_pk_mul_f32 v[112:113], v[112:113], v[64:65]\n" \
    "v_pk_mul_f32 v[114:115], v[114:115], v[66:67]\nv_pk_mul_f32 v[116:117], v[116:117], v[68:69]\nv_pk_mul_f32 v[118:119], v[118:119], v[70:71]\nv_exp_f32 v112, v112\n" \
    "v_exp_f32 v113, v113\nv_exp_f32 v114, v114\nv_exp_f32 v115, v115\nv_exp_f32 v116, v116\nv_exp_f32 v117, v117\nv_exp_f32 v118, v118\nv_exp_f32 v119, v119\n" \
    "v_pk_add_f32 v[112:113], v[112:113], 1.0 op_sel_hi:[1,0]\nv_pk_add_f32 v[114:115], v[114:115], 1.0 op_sel_hi:[1,0]\n" \
    "v_pk_add_f32 v[116:117], v[116:117], 1.0 op_sel_hi:[1,0]\nv_pk_add_f32 v[118:119], v[118:119], 1.0 op_sel_hi:[1,0]\nv_rcp_f32 v112, v112\nv_rcp_f32 v113, v113\n" \
    "v_rcp_f32 v114, v114\nv_rcp_f32 v115, v115\nv_rcp_f32 v116, v116\nv_rcp_f32 v117, v117\nv_rcp_f32 v118, v118\nv_rcp_f32 v119, v119\n" \
    "v_pk_mul_f32 v[64:65], v[64:65], v[112:113]\nv_pk_mul_f32 v[66:67], v[66:67], v[114:115]\nv_pk_mul_f32 v[68:69], v[68:69], v[116:117]\n" \
    "v_pk_mul_f32 v[70:71], v[70:71], v[118:119]\nv_cvt_pk_bf16_f32 v64, v64, v65\nv_cvt_pk_bf16_f32 v65, v66, v67\nv_cvt_pk_bf16_f32 v66, v68, v69\n" \
    "v_cvt_pk_bf16_f32 v67, v70, v71\nv_min_u32 v64, -2, v64\nv_min_u32 v65, -2, v65\nv_min_u32 v66, -2, v66\nv_min_u32 v67, -2, v67\ns_nop 0\nv_permlane32_swap_b32 v64, v66\n" \
    "v_permlane32_swap_b32 v65, v67\nbuffer_store_dwordx4 v[64:67], v5, s[64:67], s77 offen offset:512 sc1\nv_mov_b32 v10, 1\ns_waitcnt vmcnt(2)\nv_readfirstlane_b32 s78, v9\n" \
    "s_lshl_b32 s69, s50, 2\ns_add_u32 s69, s69, s55\ns_barrier\ns_cmp_eq_u32 s54, 3\ns_cselect_b64 exec, 1, 0\nbuffer_atomic_add v10, off, s[64:67], s69 sc0\n" \
    "s_mov_b64 exec, -1\ns_mov_b32 s50, s71\ns_mov_b32 s53, s74\ns_xor_b32 s51, s51, 0x10000\ns_cmp_eq_u32 s50, 64\ns_cbranch_scc1 .Lf2_%=\ns_cmp_eq_u32 s76, 0\n" \
    "s_cbranch_scc1 .Lf4_%=\ns_cmp_gt_i32 s78, s50\ns_cbranch_scc1 .Lf3_%=\ns_branch .Lf7_%=\n.Lf8_%=:\ns_sleep 3\ns_add_u32 s68, s68, 1\ns_and_b32 s69, s68, 63\n" \
    "s_cbranch_scc1 .Lf6_%=\ns_cmp_gt_u32 s68, 0x200000\ns_cbranch_scc1 .Lf1_%=\nbuffer_load_dword v12, off, s[64:67], 0 sc1\ns_waitcnt vmcnt(0)\nv_readfirstlane_b32 s69, v12\n" \
    "s_cmp_eq_u32 s69, 1\ns_cbranch_scc0 .Lf6_%=\ns_endpgm\n.Lf9_%=:\nv_mov_b32 v12, 0\ns_sub_u32 s69, s52, s79\ns_add_u32 s69, s69, s51\nv_add_u32 v13, s69, v0\n" \
    "s_mov_b32 s69, 64\n.Lf10_%=:\nds_read_b128 v[112:115], v13\nv_add_u32 v13, 0x400, v13\ns_waitcnt lgkmcnt(0)\nv_max3_u32 v12, v12, v112, v113\n" \
    "v_max3_u32 v12, v12, v114, v115\ns_sub_u32 s69, s69, 1\ns_cmp_lg_u32 s69, 0\ns_cbranch_scc1 .Lf10_%=\nv_cmp_eq_u32 vcc, -1, v12\ns_cbranch_vccz .Lf11_%=\n" \
    "s_waitcnt vmcnt(0)\ns_barrier\ns_sleep 4\ns_mov_b32 s68, s80\ns_add_u32 s80, s80, 1\ns_add_u32 s68, s68, 1\ns_and_b32 s69, s68, 63\ns_cbranch_scc1 .Lf3_%=\n" \
    "s_cmp_gt_u32 s68, 0x200000\ns_cbranch_scc1 .Lf1_%=\nbuffer_load_dword v12, off, s[64:67], 0 sc1\ns_waitcnt vmcnt(0)\nv_readfirstlane_b32 s69, v12\ns_cmp_eq_u32 s69, 1\n" \
    "s_cbranch_scc0 .Lf3_%=\ns_endpgm\n.Lf15_%=:\nv_mov_b32 v10, 0\nv_mov_b32 v16, s50\ns_not_b32 s81, s50\ns_and_b32 s81, s81, 1\ns_lshl_b32 s81, s81, 2\n" \
    "buffer_atomic_umax v16, v6, s[64:67], s81 offen sc1\ns_branch .Lf16_%=\n.Lf12_%=:\ns_mov_b32 s68, 0\n.Lf14_%=:\ns_sleep 1\n" \
    "buffer_load_dwordx2 v[104:105], v2, s[64:67], s53 offen offset:0 sc1\nbuffer_load_dwordx2 v[106:107], v2, s[64:67], s53 offen offset:256 sc1\n" \
    "buffer_load_dwordx2 v[108:109], v2, s[64:67], s53 offen offset:512 sc1\nbuffer_load_dwordx2 v[110:111], v2, s[64:67], s53 offen offset:768 sc1\ns_waitcnt vmcnt(0)\n" \
    "v_max3_u32 v12, v104, v105, v106\nv_max3_u32 v12, v12, v107, v108\nv_max3_u32 v12, v12, v109, v110\nv_max_u32 v12, v12, v111\nv_cmp_eq_u32 vcc, -1, v12\n" \
    "s_cbranch_vccz .Lf13_%=\ns_add_u32 s68, s68, 1\ns_and_b32 s69, s68, 63\ns_cbranch_scc1 .Lf14_%=\ns_cmp_gt_u32 s68, 0x200000\ns_cbranch_scc1 .Lf1_%=\n" \
    "buffer_load_dword v12, off, s[64:67], 0 sc1\ns_waitcnt vmcnt(0)\nv_readfirstlane_b32 s69, v12\ns_cmp_eq_u32 s69, 1\ns_cbranch_scc0 .Lf14_%=\ns_endpgm\n.Lf20_%=:\n" \
    "v_mov_b32 v10, 0\nv_mov_b32 v16, s50\ns_not_b32 s81, s50\ns_and_b32 s81, s81, 1\ns_lshl_b32 s81, s81, 2\nbuffer_atomic_umax v16, v6, s[64:67], s81 offen sc1\n" \
    "s_branch .Lf21_%=\n.Lf17_%=:\ns_mov_b32 s68, 0\n.Lf19_%=:\ns_sleep 1\nbuffer_load_dwordx2 v[104:105], v2, s[64:67], s53 offen offset:0 sc1\n" \
    "buffer_load_dwordx2 v[106:107], v2, s[64:67], s53 offen offset:256 sc1\nbuffer_load_dwordx2 v[108:109], v2, s[64:67], s53 offen offset:512 sc1\n" \
    "buffer_load_dwordx2 v[110:111], v2, s[64:67], s53 offen offset:768 sc1\ns_waitcnt vmcnt(0)\nv_max3_u32 v12, v104, v105, v106\nv_max3_u32 v12, v12, v107, v108\n" \
    "v_max3_u32 v12, v12, v109, v110\nv_max_u32 v12, v12, v111\nv_cmp_eq_u32 vcc, -1, v12\ns_cbranch_vccz .Lf18_%=\ns_add_u32 s68, s68, 1\ns_and_b32 s69, s68, 63\n" \
    "s_cbranch_scc1 .Lf19_%=\ns_cmp_gt_u32 s68, 0x200000\ns_cbranch_scc1 .Lf1_%=\nbuffer_load_dword v12, off, s[64:67], 0 sc1\ns_waitcnt vmcnt(0)\nv_readfirstlane_b32 s69, v12\n" \
    "s_cmp_eq_u32 s69, 1\ns_cbranch_scc0 .Lf19_%=\ns_endpgm\n.Lf22_%=:\nv_mov_b32 v10, 0\nv_mov_b32 v16, s50\ns_not_b32 s81, s50\ns_and_b32 s81, s81, 1\ns_lshl_b32 s81, s81, 2\n" \
    "buffer_atomic_umax v16, v6, s[64:67], s81 offen sc1\ns_branch .Lf23_%=\n.Lf1_%=:\nv_mov_b32 v12, 1\nbuffer_store_dword v12, off, s[64:67], 0 sc1\ns_endpgm\n.Lf24_%=:\n" \
    "v_mov_b32 v10, 0\nv_mov_b32 v16, s50\ns_not_b32 s81, s50\ns_and_b32 s81, s81, 1\ns_lshl_b32 s81, s81, 2\nbuffer_atomic_umax v16, v6, s[64:67], s81 offen sc1\n" \
    "s_branch .Lf25_%=\n.Lf2_%=:\ns_waitcnt vmcnt(0)\nv_readfirstlane_b32 s81, v10\ns_cmp_eq_u32 s81, 13\ns_cbranch_scc1 .Lf24_%=\n.Lf25_%=:\n"
#define TD_F 57
#define TD_G 155
constexpr int DQ = LQ + 1, NR = 4, NQ = 8, L_Q = NR * 16384, L_KP = L_Q + NQ * 8192;
static_assert(DQ < NQ, "q slots");
__device__ __forceinline__ int keyof(int gg, int r, int t) { return 16 * (gg & 1) + 8 * (gg >> 1) + (r & 1) + 4 * (r >> 1) + 2 * t; }
template <class C8> __device__ __forceinline__ void qk_tail(const Args& a, rsrc_t rs, unsigned char* lds, int head, unsigned* ctl, C8 c, f32x2 ms) {
    const int lane = lane_id(), wave = a.pad0, l15 = lane & 15, g = lane >> 4;
    const unsigned lb = (unsigned)(size_t)(LAS unsigned char*)lds, q = l15 >> 2, p = lane & 3, l16 = lane * 16;
    const unsigned pol = __builtin_amdgcn_readfirstlane(*(const unsigned*)(a.ws + O_POL));
    const int qt = wave & 1, mk = (l15 & 1) + 4 * ((l15 >> 1) & 1) + 2 * ((l15 >> 2) & 1);
    const unsigned kxr = g * 512 + keyof(q, p, 0) * 16, kmr = mk * 64 + 16 * g;
    const unsigned po1 = (g >> 1) * 0x7F000000u + (8 + (head >> 4)) * 16384 + (((head >> 2) & 3) * 2 + qt) * 1024 + ((head & 3) * 16 + l15) * 16 + 8 * (g & 1);
    constexpr float SC2 = 0.08838834764831845f * 1.4426950408889634f;
    if (!wave_wait<8>(PW_V(head), 1, 1, NSEG, ctl + C_ABORT)) asm volatile("s_endpgm");
    v4u rA = mkrs(a.ws, WS_END - 16);
    asm volatile("s_nop 4" : "+s"(rA));
    for (int sg = NSEG - DQ + 1; sg < NSEG; ++sg) {
        set_m0(lb + (sg & 7) * 8192 + wave * 2048); asm volatile("s_nop 0");
        const unsigned kb = nearg(a.ws, pol, O_KV + RI(sg) * KV_SLOT + head * 16384, 1) + wave * 2048;
        dma1<0>(rA, l16, kb); dma1<1024>(rA, l16, kb);
    }
    asm volatile("s_waitcnt vmcnt(0)\n\ts_barrier" ::: "memory");
    v4u qf[4], kf[4];
    f32x4 sc;
    float mx = ms[0], sum = ms[1];
    auto smm = [&]() {
#pragma unroll
        for (int ks = 0; ks < 4; ++ks) { if (ks == 0) MFMA_V0(sc, kf[0], qf[0]); else MFMA_V(sc, kf[ks], qf[ks]); }
        asm volatile("s_nop 7" : "+v"(sc));
    };
    for (int ps = NSEG - DQ + (((NSEG - DQ) ^ (wave >> 1)) & 1); ps < NSEG; ps += 2) {
#pragma unroll
        for (int ks = 0; ks < 4; ++ks) qf[ks] = lds16(lds + L_Q + (ps & (NQ - 1)) * 8192 + (qt * 4 + ks) * 1024 + l16);
        if (ps > NSEG - DQ) {
            mx = -3e38f;
#pragma unroll
            for (int t = 0; t < 2; ++t) {
#pragma unroll
                for (int ks = 0; ks < 4; ++ks) kf[ks] = lds16(lds + (ps & 7) * 8192 + kxr + 32 * t + ks * 2048);
                smm();
#pragma unroll
                for (int r = 0; r < 4; ++r) { c[4 * t + r] = keyof(g, r, t) <= 16 * qt + l15 ? sc[r] : -INFINITY; mx = fmaxf(mx, c[4 * t + r]); }
            }
            sum = 0.f;
#pragma unroll
            for (int i = 0; i < 8; ++i) { c[i] = __builtin_amdgcn_exp2f(fmaf(c[i], SC2, -SC2 * mx)); sum += c[i]; }
        }
        const unsigned pb = nearg(a.ws, pol, O_D + (unsigned)ps * D_SLOT, 0), kb = kmof(a.ws, pol, ps, head);
        unsigned spins = 0;
        for (;;) {
            const unsigned cw = __builtin_amdgcn_readfirstlane(__hip_atomic_load((gu32*)(ctl + C_C + 32 * (head >> 2)), RLX_AGENT));
            if ((int)(cw & 127) > ps) { if ((int)(cw & 127) == ps + 1) cp_delay(ps == NSEG - 1 ? TD_P - TD_L : TD_P, cw); break; }
            __builtin_amdgcn_s_sleep(5);
            if (spin_fail(spins, ctl + C_ABORT)) asm volatile("s_endpgm");
        }
        RSTAMP(ps + DQ, 0);
        for (;;) {
#pragma unroll
            for (int ks = 0; ks < 4; ++ks) kf[ks] = sld16(rs, kmr + 512 * ks, kb);
            if (!__any(max(max(umax4(kf[0]), umax4(kf[1])), max(umax4(kf[2]), umax4(kf[3]))) == SENT)) break;
            __builtin_amdgcn_s_sleep(2);
            if (spin_fail(spins, ctl + C_ABORT)) asm volatile("s_endpgm");
        }
        RSTAMP(ps + DQ, 1);
        asm volatile("s_nop 1" ::: "memory");
        smm();
        float t1 = fmaxf(fmaxf(fmaxf(sc[0], sc[1]), fmaxf(sc[2], sc[3])), mx);
        { auto r = __builtin_amdgcn_permlane16_swap(__float_as_uint(t1), __float_as_uint(t1), false, false); t1 = fmaxf(__uint_as_float(r[0]), __uint_as_float(r[1])); }
        { auto r = __builtin_amdgcn_permlane32_swap(__float_as_uint(t1), __float_as_uint(t1), false, false); t1 = -SC2 * fmaxf(__uint_as_float(r[0]), __uint_as_float(r[1])); }
        const float ax = __builtin_amdgcn_exp2f(fmaf(mx, SC2, t1));
#pragma unroll
        for (int r = 0; r < 4; ++r) sc[r] = __builtin_amdgcn_exp2f(fmaf(sc[r], SC2, t1));
        const float rc = __builtin_amdgcn_rcpf(red32_add(red16_add(fmaf(0.5f, (sc[0] + sc[1]) + (sc[2] + sc[3]), sum * ax)))), fx = ax * rc;
        sst16((v4u){pack2(c[0] * fx, c[1] * fx), pack2(c[2] * fx, c[3] * fx), pack2(c[4] * fx, c[5] * fx), pack2(c[6] * fx, c[7] * fx)}, rs, l16, pb + (head >> 2) * 16384 + ((head & 3) * 2 + qt) * 1024, 16);
        __builtin_amdgcn_raw_buffer_store_b64((v2u){pack2(sc[0] * rc, sc[1] * rc), pack2(sc[2] * rc, sc[3] * rc)}, rs, po1, pb, 16);
        unsigned old = arrive(a.ws, 0, ps);
        asm volatile("s_waitcnt vmcnt(0)" : "+v"(old) :: "memory");
        tell<0>((unsigned*)(a.ws + nearg(a.ws, pol, C_P * 4, 0)) - GO(0, 0), ps, old, 64);
        RSTAMP(ps + DQ, 2);
    }
}
__device__ __forceinline__ v16u cat(v4u x0, v4u x1, v4u x2, v4u x3) { return __builtin_shufflevector(__builtin_shufflevector(x0, x1, 0, 1, 2, 3, 4, 5, 6, 7), __builtin_shufflevector(x2, x3, 0, 1, 2, 3, 4, 5, 6, 7), 0, 1, 2, 3, 4, 5, 6, 7, 8, 9, 10, 11, 12, 13, 14, 15); }
__device__ __forceinline__ v32u cat2(v16u x, v16u y) { return __builtin_shufflevector(x, y, 0, 1, 2, 3, 4, 5, 6, 7, 8, 9, 10, 11, 12, 13, 14, 15, 16, 17, 18, 19, 20, 21, 22, 23, 24, 25, 26, 27, 28, 29, 30, 31); }
template <bool isV> __device__ __forceinline__ void proj_role(const Args& a, rsrc_t rs, unsigned char* lds, int head, unsigned* ctl) {
    const int tid = otid(), lane = tid & 63, wave = a.pad0, l15 = lane & 15, g = lane >> 4;
    const unsigned lb = (unsigned)(size_t)(LAS unsigned char*)lds, q = l15 >> 2, p = lane & 3;
    const unsigned pol = __builtin_amdgcn_readfirstlane(*(const unsigned*)(a.ws + O_POL));
    v4u rA = mkrs(a.ws, WS_END);
    auto prm = [](unsigned x) { return isV ? ((x & 1) << 1) | (x >> 1) : x; };
    const unsigned ar = 8 * wave + (lane >> 3), ao = (2 * prm((ar & 15) >> 2) + (ar >> 4)) * 16384 + (ar & 3) * 2048 + 16 * ((lane & 7) ^ ((ar >> 1) & 7));
    const unsigned h0 = nearg(a.ws, pol, O_D + D_H1, 1);
    auto stage0 = [&](int t, unsigned oob) { set_m0(lb + wave * 4096 + t * 16384); asm volatile("s_nop 0"); const unsigned so = h0 + t * 512;
        dma1<0>(rA, ao | oob, so); dma1<1024>(rA, ao | oob, so - 896); dma1<2048>(rA, ao | oob, so - 1792); dma1<3072>(rA, ao | oob, so - 2688); };
    int hr = 0;
    v4u w[32][3];
    {
        v4u rX = mkrs(isV ? a.Wv : a.Wq, DM * HD * 2), rK = mkrs(a.Wk, DM * HD * 2);
        unsigned bo[2], ko[2];
#pragma unroll
        for (int i = 0; i < 2; ++i) {
            bo[i] = (g * HD + 16 * ((l15 >> 1) ^ (g | i << 2)) + 8 * (lane & 1)) * 2;
            ko[i] = ((lane >> 3) * HD + 16 * (((lane >> 1) & 3) ^ ((g & 1) | i << 1)) + 8 * (lane & 1)) * 2;
        }
        asm volatile("s_nop 4" : "+s"(rX), "+s"(rK), "+s"(rA));
        unsigned hc;
        auto issue = [&](int G) {
            const unsigned r0 = (4 * GO_(G) + wave) * 32, d = lb + ((G + 1) % 3) * 49152;
#pragma unroll
            for (int j = 0; j < 8; ++j) bdma<(NTW & 1)>(rX, bo[(j >> 1) & 1], ((r0 + 4 * j) * HD + head * DH) * 2, d + wave * 8192 + j * 1024);
#pragma unroll
            for (int j = 0; j < 4; ++j) bdma<(NTW & 1)>(rK, ko[j & 1], ((r0 + 8 * j) * HD + head * DH + (isV ? 64 : 0)) * 2, d + 32768 + wave * 4096 + j * 1024);
        };
        issue(0); issue(1); issue(2);
        const unsigned ox = (8 * g + q) * 256 + 8 * p, sx = q | (g & 1) << 2, ok = 32768 + (8 * g + q) * 128 + 32 * (wave ^ ((q >> 1) | (g & 1) << 1)) + 8 * p;
        sfor<0, 8>([&](auto gg) {
            constexpr int G = decltype(gg)::v;
            asm volatile("s_waitcnt vmcnt(%0)\n\ts_barrier" :: "n"(G < 7 ? 24 : 12) : "memory");
            if constexpr (G == 5) { asm volatile("" : "+v"(hc)); const u64 m = __ballot(hc < 32u); if (wave == 0) *(volatile LAS unsigned*)(lds + L_FLAG) = m ? __builtin_ctzll(m) : NSEG; }
#pragma unroll
            for (int k = 0; k < 4; ++k) {
#pragma unroll
                for (int n = 0; n < 3; ++n) {
                    const unsigned o = ((G + 1) % 3) * 49152 + (n < 2 ? k * 8192 + ox + 32 * ((2 * wave + n) ^ sx) : k * 4096 + ok);
                    const v2u lo = tr_rd(lds, o), hi = tr_rd(lds, o + (n < 2 ? 1024 : 512));
                    w[4 * GO_(G) + k][n] = (v4u){lo.x, lo.y, hi.x, hi.y};
                }
            }
#pragma unroll
            for (int k = 4 * GO_(G); k < 4 * GO_(G) + 4; ++k) asm volatile("" : "+a"(w[k][0]), "+a"(w[k][1]), "+v"(w[k][2]));
            lds_barrier();
            if constexpr (G == 3) asm volatile("buffer_load_dword %0, %1, %2, 0 offen sc1" : "=v"(hc) : "v"((C_H + 32 * lane) * 4), "s"(rA));
            if (G + 3 < 8) issue(G + 3);
            if constexpr (G == 5) hr = __builtin_amdgcn_readfirstlane(*(volatile LAS unsigned*)(lds + L_FLAG));
            if constexpr (G == 5) { for (int t = 0; t < 3; ++t) stage0(t, hr ? 0u : 0x7F000000u); }
        });
    }
    RSTAMP(74, 0);
    unsigned ra[2];
#pragma unroll
    for (int u = 0; u < 2; ++u) ra[u] = (l15 >> 3) * 4096 + (l15 & 7) * 128 + 16 * ((4 * u + g) ^ (l15 >> 1));
    if (!hr) {
        if (!wave_wait<5>(ctl + C_H, 1, 1, 32, ctl + C_ABORT)) asm volatile("s_endpgm");
        for (int t = 0; t < 3; ++t) stage0(t, 0u);
    }
    asm volatile("s_waitcnt vmcnt(0)\n\ts_barrier" ::: "memory");
    const unsigned tok = 8 * prm(q) + p, l16 = lane * 16;
    const int qt = wave & 1, mk = (l15 & 1) + 4 * ((l15 >> 1) & 1) + 2 * ((l15 >> 2) & 1);
    const unsigned kxr = g * 512 + keyof(q, p, 0) * 16, kmr = mk * 64 + 16 * g;
    const unsigned po1 = (g >> 1) * 0x7F000000u + (8 + (head >> 4)) * 16384 + (((head >> 2) & 3) * 2 + qt) * 1024 + ((head & 3) * 16 + l15) * 16 + 8 * (g & 1);
    constexpr float SC2 = 0.08838834764831845f * 1.4426950408889634f;
    constexpr unsigned OOB = 0x7F000000u;
    typedef float f32x8 __attribute__((ext_vector_type(8)));
    f32x8 c = {0.f, 0.f, 0.f, 0.f, 0.f, 0.f, 0.f, 0.f};
    f32x2 ms = {0.f, 0.f};
    {
        auto qwa = [&](int m) { return lb + L_Q + (((tok >> 4) * 4 + wave) * 64 + (2 * (g & 1) + (g >> 1)) * 16 + ((tok + 4 * m) & 15)) * 16; };
        v16u c0 = {l16, ao, lb + ra[0], lb + ra[1], ((isV ? 8 : 0) + 2 * wave + (g >> 1)) * 512 + (tok + 4 * (g & 1)) * 16, tid ? OOB : (C_X + 32 * (head + (isV ? 0 : 64))) * 4u,
                   tid > 1 ? OOB : (tid == 0 ? C_C + 32 * (head >> 2) : isV ? GO(1, head >> 2) : C_X + 32 * head) * 4u, lb + L_FLAG + (tid < 3 ? 4 * tid : 16),
                   isV ? tid * 16u : lb + L_KP + kxr, kmr, po1, qwa(0), qwa(1), lb + L_Q + qt * 4096 + l16, isV ? lb + L_FLAG : lane < 8 ? nearg(a.ws, pol, C_P * 4, 0) + 128 * lane : OOB, isV ? (lane & 1) * 4u : lb + L_FLAG};
        auto u = [](unsigned x) { return (unsigned)__builtin_amdgcn_readfirstlane(x); };
        v16u sv = {(unsigned)(size_t)a.ws, pol, 0u, (unsigned)hr, u(lb + wave * 4096), u(O_KV + head * 16384), h0, (unsigned)wave,
                   u(isV ? O_UM + head * 32768 : lb + L_KP + wave * 2048), u(isV ? (head < 22 ? head * 32768 : (head - 22) * 32768 + 8192) : (head >> 2) * 16384 + (head & 3) * 2048),
                   u(isV ? (head - 21u < 8u ? (head - 21) * 16384 + 8192 : head * 16384) : (head >> 2) * 16384 + ((head & 3) * 2 + qt) * 1024), u(isV ? O_E1 + (head >> 1) * 16384 : 0u), u(isV ? (head & 1) * 4096 : wave * 2048), 0u, 0u, 0u};
        const v16u sk = {RG - 1, ROFF & 1 ? 64 : RG, O_D + D_H1, D_H1, O_EP, O_D, C_N * 4, DQ, DQ - 1, (NQ - DQ + 1) * 8192, (NQ - DQ) * 8192, LVS, LV0, LV, TD_F << 8, TD_G << 8};
        v16u mk8;
#pragma unroll
        for (int i = 0; i < 8; ++i) { const u64 m = __ballot(keyof(g, i & 3, i >> 2) <= 16 * qt + l15); mk8[2 * i] = (unsigned)m; mk8[2 * i + 1] = (unsigned)(m >> 32); }
        v32u ww[12];
#pragma unroll
        for (int i = 0; i < 8; ++i) ww[i] = cat2(cat(w[4 * i][0], w[4 * i][1], w[4 * i + 1][0], w[4 * i + 1][1]), cat(w[4 * i + 2][0], w[4 * i + 2][1], w[4 * i + 3][0], w[4 * i + 3][1]));
#pragma unroll
        for (int i = 0; i < 4; ++i) ww[8 + i] = cat2(cat(w[8 * i][2], w[8 * i + 1][2], w[8 * i + 2][2], w[8 * i + 3][2]), cat(w[8 * i + 4][2], w[8 * i + 5][2], w[8 * i + 6][2], w[8 * i + 7][2]));
        v4u sn = sentv();
        const unsigned* dp = nullptr;
#define PROJ_CALL(TXT) asm volatile(TXT : "+{s[48:63]}"(sv), "+{v[0:15]}"(c0), "+{v[16:19]}"(sn), "+{v[112:119]}"(c), "+{v[120:121]}"(ms) \
            : "{s[16:31]}"(sk), "{s[64:67]}"(rA), "{s[80:95]}"(mk8), "{s32}"(SC2), "{s33}"(-3e38f), "{s[34:35]}"(dp), "{s36}"(C_H * 4), "{v22}"(-INFINITY), [sm] "n"(SM), [sv] "n"(SV), \
              "{a[0:31]}"(ww[0]), "{a[32:63]}"(ww[1]), "{a[64:95]}"(ww[2]), "{a[96:127]}"(ww[3]), "{a[128:159]}"(ww[4]), "{a[160:191]}"(ww[5]), "{a[192:223]}"(ww[6]), "{a[224:255]}"(ww[7]), \
              "{v[124:155]}"(ww[8]), "{v[156:187]}"(ww[9]), "{v[188:219]}"(ww[10]), "{v[220:251]}"(ww[11]) \
            : "memory", "vcc", "scc", "m0", "v20", "v21", "v24", "v25", "v26", "v27", "v28", "v29", "v30", "v31", "v32", "v33", "v34", "v35", "v36", "v37", "v38", "v39", "v40", "v41", "v42", "v43", "v44", "v45", "v46", "v47", "v48", "v49", "v50", "v51", "v52", "v53", "v54", "v55", "v56", "v57", "v58", "v59", "v60", "v61", "v62", "v63", "v64", "v65", "v66", "v67", "v68", "v69", "v70", "v71", "v72", "v73", "v74", "v75", "v76", "v77", "v78", "v79", "v80", "v81", "v82", "v83", "v84", "v85", "v86", "v87", "v88", "v89", "v90", "v91", "v92", "v93", "v94", "v95", "v96", "v97", "v98", "v99", "v100", "v101", "v102", "v103", "v104", "v105", "v106", "v107", "v108", "v109", "v110", "v111", "v122", "v123", "s37", "s38", "s39", "s40", "s41", "s42", "s43", "s68", "s69", "s70", "s71", "s72", "s73", "s74", "s75", "s76", "s77", "s78", "s79", "s96", "s97", "s98", "s99")
        if constexpr (isV) PROJ_CALL(PROJ_V); else PROJ_CALL(PROJ_Q);
    }
    auto signal = [&](int n) { if (otid() == 0) __hip_atomic_store((gu32*)(isV ? PW_V(head) : PW_Q(head)), (unsigned)n, RLX_AGENT); };
    asm volatile("s_waitcnt vmcnt(0) lgkmcnt(0)\n\ts_barrier" ::: "memory");
    signal(NSEG + 1);
    if constexpr (!isV) qk_tail(a, rs, lds, head, ctl, c, ms);
}

#define TD_T 24
__device__ __forceinline__ void r_role(const Args& a, unsigned char* lds, int xg, int cg, int set, unsigned par, unsigned ev) {
    const int tid = otid(), lane = tid & 63, wave = a.pad0, l15 = lane & 15, g = lane >> 4;
    const unsigned q = l15 >> 2, p = lane & 3, lb0 = (unsigned)(size_t)(LAS unsigned char*)lds;
    const unsigned pol = __builtin_amdgcn_readfirstlane(*(const unsigned*)(a.ws + O_POL));
    v4u w[8][8];
    {
        const unsigned lb = lb0 + wave * 32768;
        v4u rW = mkrs(a.Wo, DM * HD * 2);
        const unsigned bo = ((lane >> 2) * DM + 16 * (((lane >> 1) & 1) ^ (g & 1)) + 8 * (lane & 1)) * 2;
        asm volatile("s_nop 4" : "+s"(rW));
        auto issue = [&](int c) {
#pragma unroll
            for (int i = 0; i < 8; ++i) bdma<(NTW & 32)>(rW, bo, (((8 * wave + c) * DH + 16 * i) * DM + 32 * cg) * 2, lb + (c & 3) * 8192 + i * 1024);
        };
        issue(0); issue(1); issue(2); issue(3);
        sfor<0, 8>([&](auto cc) {
            constexpr int c = decltype(cc)::v;
            asm volatile("s_waitcnt vmcnt(%0)" :: "n"(c < 5 ? 24 : 8 * (7 - c)) : "memory");
#pragma unroll
            for (int n = 0; n < 8; ++n) {
                const unsigned o = wave * 32768 + (c & 3) * 8192 + (n & 3) * 2048 + (4 * g + q) * 64 + 32 * ((n >> 2) ^ (g & 1)) + 8 * p;
                const v2u lo = tr_rd(lds, o), hi = tr_rd(lds, o + 1024);
                w[c][n] = (v4u){lo.x, lo.y, hi.x, hi.y};
            }
#pragma unroll
            for (int n = 0; n < 8; n += 4) asm volatile("" : "+a"(w[c][n]), "+a"(w[c][n + 1]), "+a"(w[c][n + 2]), "+a"(w[c][n + 3]));
            asm volatile("s_waitcnt lgkmcnt(0)" ::: "memory");
            if (c + 4 < 8) issue(c + 4);
        });
    }
    lds_barrier();
    constexpr unsigned OOB = 0x7F000000u, L_R = 0, L_O = 4608, L_M = L_O + 4 * 4608, L_X = 65536;
    RSTAMP(74, 0);
    const unsigned l16 = lane * 16, r = 8 * wave + (lane & 7), c4 = 8 * ((lane >> 3) & 3) + 4 * (lane >> 5), cr = xg >> 1, cb = 2 * cg;
    const v16u c0 = {l16, l16 + 4096, ((lane >> 2) * DM + 8 * (lane & 3)) * 2u, lb0 + L_X - set * 1024 + r * 64 + c4 * 2, (lane & 8) ? 0xEEEEu : 0x4444u, (16 - 3 * cr - wave) * 16384 + wave * 1024 + 2 * g * 128 + (l15 & 7) * 16 + (l15 >> 3) * 8,
                     lb0 + L_R + wave * 1152 + 4 * (g & 1) * 144 + (16 * (g >> 1) + l15) * 4, lb0 + L_R + (l15 >> 1) * 144 + (16 * (g & 1) + 8 * (lane & 1) + 4 * (g >> 1)) * 4,
                     lane < 8 ? (C_X + 32 * (8 * wave + lane)) * 4u : lane - 9u < 2u ? nearg(a.ws, pol, (C_P + 32 * xg) * 4, 0) + 4 * (set ^ (lane & 1) ^ 1) : OOB,
                     lb0 + L_O + wave * 4608 + l15 * 144 + g * 16, lb0 + L_O + r * 144 + c4 * 4, (32 * cg + c4) * 4,
                     lane < 32 ? D_X2 - wave * 32768 + (cg >> 2) * 16384 + ((cg & 3) * 2 + (r >> 4)) * 1024 + ((lane >> 3) * 16 + (r & 15)) * 16 : OOB,
                     lane < 8 ? D_ST - wave * 32768 + cg * 256 + r * 8 : OOB, tid ? OOB : (C_X + 32 * (32 + cg) + set) * 4u, lane < 8 ? (GO(1, lane) + set) * 4u : OOB};
    auto u = [](unsigned x) { return (unsigned)__builtin_amdgcn_readfirstlane(x); };
    const u64 dp = 0;
    const v16u sk = {u(O_UM + cb * 16384 + wave * 2048), u(O_EP + (cr * 4 + wave) * 16384), u(O_E1 + (wave * 4 + ((cb >> 2) & 3)) * 16384 + (cb >> 4) * 1024 + (cb & 3) * 256), u(O_KV + wave * 131072),
                     u(O_D + wave * 32768), u((8 + (wave >> 1) - 2 * wave) * 16384 + (wave & 1) * 4096), u((20 - cr * 4 - wave) * 16384 + xg * 128), nearg(a.ws, pol, (C_R + 32 * xg + 1) * 4, 0), (C_N + 64) * 4, RG - 1, O_MOD + 8192, TD_O, TD_O - TD_L, TD_T, (unsigned)dp, (unsigned)(dp >> 32)};
    v8u sv = {(unsigned)(size_t)a.ws, pol, u(set - 2), 0u, 0u, par >> 26, u(cg & 4 ? (par >> 16) & 31 : ev), (par & 255) + ((par >> 8) & 255)};
    v32u ww[8];
#pragma unroll
    for (int i = 0; i < 8; ++i) ww[i] = cat2(cat(w[i][0], w[i][1], w[i][2], w[i][3]), cat(w[i][4], w[i][5], w[i][6], w[i][7]));
    asm volatile("s_nop 4\n" ROLE_R : "+{s[48:55]}"(sv)
        : "{v[0:15]}"(c0), "{v16}"(lb0 + L_M + wave * 3072 + l16), "{s[32:47]}"(sk), "{s56}"(wave), "{s58}"(u(((set + 16 * wave) * SEG * DM + 32 * cg) * 2)), "{s59}"(u(lb0 + L_X + wave * 16384)), "{s[60:63]}"(mkrs(a.x, S_TOK * DM * 2)), "{s[64:67]}"(mkrs(a.ws, WS_END)),
          "{a[0:31]}"(ww[0]), "{a[32:63]}"(ww[1]), "{a[64:95]}"(ww[2]), "{a[96:127]}"(ww[3]), "{a[128:159]}"(ww[4]), "{a[160:191]}"(ww[5]), "{a[192:223]}"(ww[6]), "{a[224:255]}"(ww[7])
        : "memory", "vcc", "scc", "m0", "v17", "v18", "v19", "v20", "v21", "v22", "v24", "v25", "v26", "v27", "v28", "v29", "v30", "v31", "v32", "v33", "v34", "v35", "v36", "v37", "v38", "v39", "v40", "v41", "v42", "v43", "v44", "v45", "v46", "v47", "v48", "v49", "v50", "v51", "v52", "v53", "v54", "v55", "v56", "v57", "v58", "v59", "v60", "v61", "v62", "v63", "v64", "v65", "v66", "v67", "v68", "v69", "v70", "v71", "v72", "v73", "v74", "v75", "v76", "v77", "v78", "v79", "v80", "v81", "v82", "v83", "v84", "v85", "v86", "v87", "v88", "v89", "v90", "v91", "v92", "v93", "v94", "v95", "v96", "v97", "v98", "v99", "v100", "v101", "v102", "v103", "v104", "v105", "v106", "v107", "v108", "v109", "v110", "v111", "v112", "v113", "v114", "v115", "v116", "v117", "v118", "v119", "v120", "v121", "v122", "v123", "v124", "v125", "v126", "v127", "v128", "v129", "v130", "v131", "v132", "v133", "v134", "v135", "v136", "v137", "v138", "v139", "v140", "v141", "v142", "v143", "v144", "v145", "v146", "v147", "v148", "v149", "v150", "v151", "v152", "v153", "v154", "v155", "v156", "v157", "v158", "v159", "v160", "v161", "v162", "v163", "v164", "v165", "v166", "v167", "v168", "v169", "v170", "v171", "v172", "v173", "v174", "v175", "v176", "v177", "v178", "v179", "v180", "v181", "v182", "v183", "v184", "v185", "v186", "v187", "v188", "v189", "v190", "v191", "v192", "v193", "v194", "v195", "v196", "v197", "v198", "v199", "v200", "v201", "v202", "v203", "v204", "v205", "v206", "v207", "v208", "v209", "v210", "v211", "v212", "v213", "v214", "v215", "v216", "v217", "v218", "v219", "v220", "v221", "v222", "v223", "v224", "v225", "v226", "v227", "v228", "v229", "v230", "v231", "v232", "v233", "v234", "v235", "v236", "v237", "v238", "v239", "v240", "v241", "v242", "v243", "v244", "v245", "v246", "v247", "v248", "v249", "v250", "v251", "v252", "v253", "s68", "s69", "s70", "s71", "s72", "s73", "s74", "s75", "s76", "s77", "s78", "s79", "s80", "s81", "s82", "s83", "s84", "s85", "s86", "s87", "s88", "s89");
}

template <int OFF> __device__ __forceinline__ void ld2(v4u (&d)[2], v4u r, unsigned vo, unsigned so) {
    asm volatile("buffer_load_dwordx4 %0, %2, %3, %4 offen offset:%5 sc1\n\tbuffer_load_dwordx4 %1, %2, %3, %4 offen offset:%6 sc1"
                 : "+v"(d[0]), "+v"(d[1]) : "v"(vo), "s"(r), "s"(so), "n"(OFF), "n"(OFF + 1024));
}
__device__ __forceinline__ v4u ld16w(v4u r, unsigned vo, unsigned so) {
    v4u d; asm volatile("buffer_load_dwordx4 %0, %1, %2, %3 offen sc1\n\ts_waitcnt vmcnt(0)" : "=&v"(d) : "v"(vo), "s"(r), "s"(so)); return d;
}
template <int OFF> __device__ __forceinline__ void ld2n(v4u (&d)[2], v4u r, unsigned vo, unsigned so) {
    asm volatile("buffer_load_dwordx4 %0, %2, %3, %4 offen offset:%5 sc1\n\tbuffer_load_dwordx4 %1, %2, %3, %4 offen offset:%6 sc1"
                 : "=&v"(d[0]), "=&v"(d[1]) : "v"(vo), "s"(r), "s"(so), "n"(OFF), "n"(OFF + 1024));
}
typedef __attribute__((ext_vector_type(16))) float f32x16;
__device__ __forceinline__ void mm32(f32x16& c, v4u x, v4u y) { asm volatile("s_nop 1\n\tv_mfma_f32_32x32x16_bf16 %0, %1, %2, %0" : "+v"(c) : "v"(x), "v"(y)); }
__device__ __forceinline__ void f1_role(const Args& a, rsrc_t rs, unsigned char* lds, int arg, int s0, unsigned* ctl) {
    const int tid = otid(), lane = tid & 63, wave = a.pad0, l15 = lane & 15, g = lane >> 4;
    const unsigned lb = (unsigned)(size_t)(LAS unsigned char*)lds, q = l15 >> 2, p = lane & 3, l16 = lane * 16;
    const unsigned pol = __builtin_amdgcn_readfirstlane(*(const unsigned*)(a.ws + O_POL));
    const int kh = arg >= 14, ia = arg - 14 * kh, rot = ia & 3;
    lds_barrier();
    v4u w[32][2];
    f32x16 bv, cs = {};
#pragma unroll
    for (int h = 0; h < 4; ++h) {
        const v2u br = *(const v2u*)(a.b1 + 128 * arg + 32 * wave + 8 * h + 4 * (g >> 1));
        bv[4 * h] = lo16f(br.x); bv[4 * h + 1] = hi16f(br.x); bv[4 * h + 2] = lo16f(br.y); bv[4 * h + 3] = hi16f(br.y);
    }
    asm volatile("" : "+v"(bv));
    {
        v4u rW = mkrs(a.w1, DM * FF * 2);
        unsigned bo[2];
#pragma unroll
        for (int i = 0; i < 2; ++i) bo[i] = (g * FF + 16 * ((l15 >> 1) ^ (g | i << 2)) + 8 * (lane & 1)) * 2;
        asm volatile("s_nop 4" : "+s"(rW));
        auto issue = [&](int G) {
            const unsigned r0 = ((4 * G + wave + 8 * rot) & 31) * 32;
#pragma unroll
            for (int j = 0; j < 8; ++j) bdma<(NTW & 8)>(rW, bo[(j >> 1) & 1], ((r0 + 4 * j) * FF + 128 * arg) * 2, lb + (G & 3) * 32768 + wave * 8192 + j * 1024);
        };
        {
            f32x4 m[2];
            ld_mod<2>(rs, ctl, {4 * DM + 4 * tid, 3 * DM + 4 * tid}, m);
            *(LAS f32x4*)(lds + 131072 + 16 * tid) = m[0]; *(LAS v2u*)(lds + 135168 + 8 * tid) = pack4(m[1]);
            asm volatile("s_waitcnt lgkmcnt(0)" ::: "memory");
        }
        issue(0); issue(1); issue(2); issue(3);
        const unsigned ox = (8 * (g >> 1) + q) * 256 + 32 * ((2 * wave + (g & 1)) ^ (q | (g >> 1) << 2)) + 8 * p;
        sfor<0, 8>([&](auto gg) {
            constexpr int G = decltype(gg)::v;
            asm volatile("s_waitcnt vmcnt(%0)\n\ts_barrier" :: "n"(G < 5 ? 24 : 8 * (7 - G)) : "memory");
#pragma unroll
            for (int k = 0; k < 4; ++k)
#pragma unroll
                for (int n = 0; n < 2; ++n) {
                    const unsigned o = (G & 3) * 32768 + k * 8192 + n * 4096 + ox;
                    const v2u lo = tr_rd(lds, o), hi = tr_rd(lds, o + 1024);
                    w[4 * G + k][n] = (v4u){lo.x, lo.y, hi.x, hi.y};
                }
            lds_barrier();
            if (G + 4 < 8) issue(G + 4);
#pragma unroll
            for (int i = 0; i < 8; ++i) {
                v4u& x = w[4 * G + (i >> 1)][i & 1];
                const unsigned k0 = ((4 * G + (i >> 1) + 8 * rot) & 31) * 32 + 16 * (i & 1) + 8 * (g >> 1);
                mm32(bv, x, lds16(lds + 135168 + 2 * k0));
#pragma unroll
                for (int j = 0; j < 4; ++j) { const f32x2 c1 = *(const LAS f32x2*)(lds + 131072 + 4 * k0 + 8 * j); x[j] = pack2(lo16f(x[j]) * c1[0], hi16f(x[j]) * c1[1]); }
                mm32(cs, x, (v4u){0x3F803F80u, 0x3F803F80u, 0x3F803F80u, 0x3F803F80u});
                asm volatile("" : "+a"(x));
            }
        });
    }
    asm volatile("s_nop 11" : "+v"(bv), "+v"(cs));
    RSTAMP(74, 0);
    constexpr unsigned OOB = 0x7F000000u, L_S = 139264;
    auto u = [](unsigned x) { return (unsigned)__builtin_amdgcn_readfirstlane(x); };
    auto st = [&](int t) { return u(D_X2 + (((t + rot) & 3) * 2 + (wave >> 1)) * 16384 + (wave & 1) * 4096); };
    const v8u c0 = {l16, lb + (g & 1) * 1024 + (g >> 1) * 256 + l15 * 16, D_ST + (lane >> 3) * 1024 + (8 * wave + (lane & 7)) * 8, lb + L_S + (8 * wave + (lane & 7)) * 8, lb + L_S + (lane & 31) * 8,
                    (g & 1) * 1024u + (g >> 1) * 256 + l15 * 16, lane < 4 ? GO(2, 4 * kh + lane) * 4u : OOB, lane ? OOB : GO(1, 4 * kh + (ia >> 2)) * 4u};
    v8u sv = {(unsigned)(size_t)a.ws, pol, 0u, 0u, u(lb + wave * 4096), nearg(a.ws, pol, O_D, 0), (unsigned)wave, u((C_N + 64 * (2 + kh)) * 4)};
    const v8u sk = {O_D, u(arg * 16384 + wave * 2048), st(0), st(1), st(2), st(3), __float_as_uint(-0.1029432397f), __float_as_uint(-2.302208198f)};
    const unsigned* dp = nullptr;
    v32u ww[8];
#pragma unroll
    for (int i = 0; i < 8; ++i) ww[i] = cat2(cat(w[4 * i][0], w[4 * i][1], w[4 * i + 1][0], w[4 * i + 1][1]), cat(w[4 * i + 2][0], w[4 * i + 2][1], w[4 * i + 3][0], w[4 * i + 3][1]));
    asm volatile("s_nop 11\n" ROLE_F1 : "+{s[48:55]}"(sv)
        : "{v[0:7]}"(c0), "{v[72:87]}"(cs), "{v[88:103]}"(bv), "{s[40:47]}"(sk), "{s[38:39]}"(dp), "{s[64:67]}"(mkrs(a.ws, WS_END)), [sm] "n"(SM), [sv] "n"(SV),
          "{a[0:31]}"(ww[0]), "{a[32:63]}"(ww[1]), "{a[64:95]}"(ww[2]), "{a[96:127]}"(ww[3]), "{a[128:159]}"(ww[4]), "{a[160:191]}"(ww[5]), "{a[192:223]}"(ww[6]), "{a[224:255]}"(ww[7])
        : "memory", "vcc", "scc", "m0", "v8", "v9", "v10", "v11", "v12", "v13", "v14", "v15", "v16", "v24", "v25", "v26", "v27", "v28", "v29", "v30", "v31", "v32", "v33", "v34", "v35", "v36", "v37", "v38", "v39", "v40", "v41", "v42", "v43", "v44", "v45", "v46", "v47", "v48", "v49", "v50", "v51", "v52", "v53", "v54", "v55", "v56", "v57", "v58", "v59", "v60", "v61", "v62", "v63", "v64", "v65", "v66", "v67", "v68", "v69", "v70", "v71", "v104", "v105", "v106", "v107", "v108", "v109", "v110", "v111", "v112", "v113", "v114", "v115", "v116", "v117", "v118", "v119", "s68", "s69", "s70", "s71", "s72", "s73", "s74", "s75", "s76", "s77", "s78", "s79", "s80", "s81");
}

static_assert(LV < 16 && LV + 2 < RG && DQ + 2 < RG, "rings");
__device__ __forceinline__ void f2_role(const Args& a, rsrc_t rs, unsigned char* lds, int kh, int cg, int s0, unsigned* ctl) {
    constexpr int KS = 14;
    const int tid = otid(), lane = tid & 63, wave = a.pad0, l15 = lane & 15, g = lane >> 4;
    const int ks0 = (kh * 4 + wave) * KS, rot = (cg & 3) * 2;
    auto kp = [&](int k) { const int x = (k >> 1) + rot; return x >= 7 ? x - 7 : x; };
    auto ko = [&](int k) { const int pi = wave * 7 + kp(k); return (pi >> 1) * 16384 + (pi & 1) * 4096; };
    const unsigned pol = __builtin_amdgcn_readfirstlane(*(const unsigned*)(a.ws + O_POL));
    lds_barrier();
    v4u w[KS][4];
    {
        v4u rW = mkrs(a.w2, DM * FF * 2);
        const unsigned lb = (unsigned)(size_t)(LAS unsigned char*)lds + wave * 32768, q = l15 >> 2, p = lane & 3;
        unsigned bo[2];
#pragma unroll
        for (int i = 0; i < 2; ++i) bo[i] = ((lane >> 3) * DM + 16 * (((lane >> 1) & 3) ^ ((g & 1) | i << 1)) + 8 * (lane & 1)) * 2;
        asm volatile("s_nop 4" : "+s"(rW));
        sfor<0, 2>([&](auto hh) {
            constexpr int h0 = 7 * decltype(hh)::v;
#pragma unroll
            for (int k = h0; k < h0 + 7; ++k)
#pragma unroll
                for (int j = 0; j < 4; ++j) bdma<(NTW & 8)>(rW, bo[j & 1], (unsigned)(((ks0 + 2 * kp(k) + (k & 1)) * 32 + 8 * j) * DM + 64 * cg) * 2, lb + (k - h0) * 4096 + j * 1024);
            wait_vm0();
#pragma unroll
            for (int k = h0; k < h0 + 7; ++k)
#pragma unroll
                for (int n = 0; n < 4; ++n) {
                    const unsigned o = wave * 32768 + (k - h0) * 4096 + (8 * g + q) * 128 + 32 * (n ^ ((q >> 1) | (g & 1) << 1)) + 8 * p;
                    const v2u lo = tr_rd(lds, o), hi = tr_rd(lds, o + 512);
                    w[k][n] = (v4u){lo.x, lo.y, hi.x, hi.y};
                }
#pragma unroll
            for (int k = h0; k < h0 + 7; ++k) asm volatile("" : "+a"(w[k][0]), "+a"(w[k][1]), "+a"(w[k][2]), "+a"(w[k][3]));
            asm volatile("s_waitcnt lgkmcnt(0)" ::: "memory");
        });
    }
    const int colf = 64 * cg + 32 * kh + 16 * (wave & 1) + 4 * g;
    const v2u br = *(const v2u*)(a.b2 + colf);
    f32x4 bv = {lo16f(br.x), hi16f(br.x), lo16f(br.y), hi16f(br.y)};
    f32x4 gv, fin;
    ld_mod<1>(rs, ctl, {5 * DM + colf}, &gv);
    asm volatile("" : "+v"(gv), "+v"(bv));
    lds_barrier();
    RSTAMP(74, 0);
    const unsigned pwo = (GO(2, kh * 4 + (cg >> 2)) + (lane & 1)) * 4, l16 = lane * 16, OOB = 0x7FFF0000u;
    const unsigned orow = (unsigned)((16 * (wave >> 1) + l15) * DM + colf) * 2, sr = D_SL + cg * 16384 + (kh * 4 + wave) * 1024;
    auto slab = [&](int sg) { return nearg(a.ws, pol, O_D + (unsigned)sg * D_SLOT, 1); };
    const unsigned xo = D_X2 + (colf >> 7) * 16384 + (((colf >> 5) & 3) * 2 + (wave >> 1)) * 1024 + (((colf >> 3) & 3) * 16 + l15) * 16 + (colf & 4) * 2;
    v4u rA = mkrs(a.ws, WS_END);
    v4u af[KS][2];
#pragma unroll
    for (int k = 0; k < KS; ++k) asm volatile("" : "=v"(af[k][0]), "=v"(af[k][1]));
    auto fetch = [&](auto kk, unsigned vo, unsigned so) { constexpr int k = decltype(kk)::v; ld2<(k & 1) * 2048>(af[k], rA, vo, so + ko(k)); };
    unsigned pv = 0, spins = 0;
    v4u pp; v2u xr;
    auto rdy = [&](int sg) { return (int)__builtin_amdgcn_readlane(pv, sg & 1) > sg; };
    auto finish = [&](int sg) {
        while (__any(umax4(pp) == SENT)) {
            __builtin_amdgcn_s_sleep(1);
            pp = ld16w(rA, l16, slab(sg) + sr);
            if (spin_fail(spins, ctl + C_ABORT)) asm volatile("s_endpgm");
        }
        float y[4];
#pragma unroll
        for (int i = 0; i < 4; ++i) { const unsigned xx = xr[i >> 1]; const float v = rb1(rb1(fin[i] + __uint_as_float(pp[i])) + bv[i]); y[i] = ((i & 1) ? hi16f(xx) : lo16f(xx)) + rb1(gv[i] * v); }
        *(v2u*)((char*)a.out + (size_t)sg * 65536 + orow) = (v2u){pack2(y[0], y[1]), pack2(y[2], y[3])};
    };
    bool have = false, pend = false;
    asm volatile("s_nop 4" : "+s"(rA));
    auto finish_now = [&](int sg) {
        xr = bld8(rs, (slab(sg) ^ 8192) + xo);
        rt_delay(TD_S);
        pp = sentv();
        finish(sg);
        RSTAMP(sg, 2);
    };
    unsigned buf = 0;
    int prev = 0;
    for (int seg = s0; seg < NSEG;) {
        const int nxt = seg + 1 + (seg + 1 >= XF2);
        const unsigned ab = slab(seg) + kh * 14 * 16384, an = slab(min(nxt, NSEG - 1)) + kh * 14 * 16384;
        if (!have) {
            asm volatile("s_waitcnt vmcnt(1)" :: "v"(af[0][0]), "v"(af[0][1]), "v"(af[1][0]), "v"(af[1][1]), "v"(af[2][0]), "v"(af[2][1]), "v"(af[3][0]), "v"(af[3][1]), "v"(af[4][0]), "v"(af[4][1]), "v"(af[5][0]), "v"(af[5][1]), "v"(af[6][0]), "v"(af[6][1]), "v"(af[7][0]), "v"(af[7][1]), "v"(af[8][0]), "v"(af[8][1]), "v"(af[9][0]), "v"(af[9][1]), "v"(af[10][0]), "v"(af[10][1]), "v"(af[11][0]), "v"(af[11][1]), "v"(af[12][0]), "v"(af[12][1]), "v"(af[13][0]), "v"(af[13][1]));
            if (pend && !rdy(seg)) { finish_now(prev); pend = false; }
            while (!rdy(seg)) {
                pv = ldg4_sc1(a.ws, pwo);
                if (rdy(seg)) break;
                __builtin_amdgcn_s_sleep(4);
                if (spin_fail(spins, ctl + C_ABORT)) asm volatile("s_endpgm");
            }
            sfor<0, KS>([&](auto kk) { constexpr int k = decltype(kk)::v; ld2n<(k & 1) * 2048>(af[k], rA, l16, ab + ko(k)); });
        }
        RSTAMP(seg, 0); RSTAMP(seg, 3);
        have = rdy(nxt);
        const unsigned vl = have ? l16 : OOB;
        f32x4 acc[2][4];
        unsigned mx = 0;
        SB();
        sfor<0, KS>([&](auto kk) {
            constexpr int k = decltype(kk)::v;
            asm volatile("s_waitcnt vmcnt(%2)" : "+v"(af[k][0]), "+v"(af[k][1]) : "n"(2 * KS - 2));
#pragma unroll
            for (int n = 0; n < 4; ++n)
#pragma unroll
                for (int m = 0; m < 2; ++m) { if (k == 0) MFMA_A0(acc[m][n], w[k][n], af[k][m]); else MFMA_A(acc[m][n], w[k][n], af[k][m]); }
            mx = max(mx, max(umax4(af[k][0]), umax4(af[k][1])));
            asm volatile("" : "+v"(mx));
            SB();
            if constexpr (k == 6) asm volatile("buffer_load_dword %0, %3, %6, 0 offen sc1\n\tbuffer_load_dwordx2 %1, %4, %6, %7 offen sc1\n\tbuffer_load_dwordx4 %2, %5, %6, %8 offen sc1"
                                               : "=&v"(pv), "=&v"(xr), "=&v"(pp) : "v"(pwo), "v"(pend ? xo : OOB), "v"(pend ? l16 : OOB), "s"(rA), "s"(slab(prev) ^ 8192), "s"(slab(prev) + sr));
            fetch(kk, vl, an);
            SB();
        });
        asm volatile("s_waitcnt vmcnt(16)" : "+v"(pv), "+v"(xr), "+v"(pp));
        if (pend) { finish(prev); RSTAMP(prev, 2); pend = false; }
        if (__any(mx == SENT)) {
            have = false;
            wait_vm0();
            __builtin_amdgcn_s_sleep(4);
            if (spin_fail(spins, ctl + C_ABORT)) asm volatile("s_endpgm");
            continue;
        }
        if (!have && rdy(nxt)) { sfor<0, KS>([&](auto kk) { fetch(kk, l16, an); }); have = true; }
        RSTAMP(seg, 1);
        asm volatile("s_nop 7" ::: "memory");
        unsigned char* rb = lds + buf + l16;
#pragma unroll
        for (int n = 0; n < 4; ++n)
#pragma unroll
            for (int m = 0; m < 2; ++m) { asm volatile("" : "+v"(acc[m][n])); *(LAS f32x4*)(rb + wave * 8192 + (n * 2 + m) * 1024) = acc[m][n]; }
        lds_barrier();
        auto sum4 = [&](int t) { const unsigned char* p = rb + t * 1024; return (*(const LAS f32x4*)p + *(const LAS f32x4*)(p + 8192)) + (*(const LAS f32x4*)(p + 16384) + *(const LAS f32x4*)(p + 24576)); };
        const int t0 = (wave & 1) * 2 + (wave >> 1);
        sst16(unsent4(__builtin_bit_cast(v4u, sum4(4 * (kh ^ 1) + t0))), rs, l16, slab(seg) + (sr ^ 4096), 16);
        fin = sum4(4 * kh + t0);
        pend = true;
        prev = seg; seg = nxt; spins = 0; buf ^= 32768;
    }
    asm volatile("s_waitcnt vmcnt(1)" :: "v"(af[0][0]), "v"(af[0][1]), "v"(af[1][0]), "v"(af[1][1]), "v"(af[2][0]), "v"(af[2][1]), "v"(af[3][0]), "v"(af[3][1]), "v"(af[4][0]), "v"(af[4][1]), "v"(af[5][0]), "v"(af[5][1]), "v"(af[6][0]), "v"(af[6][1]), "v"(af[7][0]), "v"(af[7][1]), "v"(af[8][0]), "v"(af[8][1]), "v"(af[9][0]), "v"(af[9][1]), "v"(af[10][0]), "v"(af[10][1]), "v"(af[11][0]), "v"(af[11][1]), "v"(af[12][0]), "v"(af[12][1]), "v"(af[13][0]), "v"(af[13][1]));
    finish_now(prev);
}

#define GM 0x76325410u
__global__ void __launch_bounds__(256, 1) k_main(Args a) {
    extern __shared__ __attribute__((aligned(16))) unsigned char lds[];
    const int px = blockIdx.x & 7, by = blockIdx.x >> 3;
    const int xg = 2 * (px & 3) + (by & 1), b2 = by >> 1;
    const int ii = ((GM >> (4 * ((b2 >> 2) + (px & 4)))) & 7) * 4 + (b2 & 3);
    int wv = __builtin_amdgcn_readfirstlane(threadIdx.x >> 6); asm volatile("" : "+s"(wv));
    if (blockIdx.x >= 224) code_touch(lds + LDS_BYTES - 1024, ((blockIdx.x >> 3) - 28) * 4 + wv);
    if (ii < 8) {
        if (ii & 1) chain_main<true>(a, lds, xg, ii, wv); else chain_main<false>(a, lds, xg, ii, wv);
        return;
    }
    if ((a.mode & 255) == 2) return;
    const unsigned par = a.pad0, ev = a.mode >> 8;
    a.pad0 = wv; a.mode = ii * 8 + xg;
    const rsrc_t rs = __builtin_amdgcn_make_buffer_rsrc((void*)a.ws, (short)0, (int)WS_END, 0x00020000);
    unsigned* const ctl = (unsigned*)(a.ws + O_CTL);
    const int role = (ii >> 2) - 2, i4 = ii & 3;
    RSTAMP(75, 0);
    if (role == 0) proj_role<true>(a, rs, lds, 4 * xg + i4, ctl);
    else if (role == 1) proj_role<false>(a, rs, lds, 4 * xg + i4, ctl);
    else if (role < 4) { r_role(a, lds, xg, 4 * xg + i4, role & 1, par, ev); return; }
    else {
        const unsigned t0 = rt_now();
        ln1_rows(a, rs, lds, (ii - 24) * 8 + xg);
        if (role > 3 && (ii < 30 || (xg & 3) < 3)) rt_delay(TD_W, t0);
        RSTAMP(75, 1);
    }
    if (ROFF & 2) return;
    else if (role == 4 || (role == 0 && XF2 < NSEG)) f2_role(a, rs, lds, xg >> 2, 4 * (xg & 3) + i4, role ? 0 : XF2, ctl);
    else if (role < 3) return;
    else if (4 * (xg & 3) + i4 < 14) f1_role(a, rs, lds, 14 * (xg >> 2) + 4 * (xg & 3) + i4, 0, ctl);
}

extern "C" void kernel_launch(void* const* d_in, const int* in_sizes, int n_in,
                              void* d_out, int out_size, void* d_ws, size_t ws_size,
                              hipStream_t stream) {
    Args a;
    a.x = (const u16*)d_in[0]; a.mem0 = (const u16*)d_in[1]; a.Wq = (const u16*)d_in[2]; a.Wk = (const u16*)d_in[3];
    a.Wv = (const u16*)d_in[4]; a.Wo = (const u16*)d_in[5];
    a.w1 = (const u16*)d_in[8]; a.b1 = (const u16*)d_in[9]; a.w2 = (const u16*)d_in[10]; a.b2 = (const u16*)d_in[11];
    a.out = (u16*)d_out; a.ws = (char*)d_ws + (size_t)WSB * MiB; a.mode = CH_EV << 8; a.pad0 = CH_PAR;
    char* ws = a.ws;
    a.c = (const u16*)d_in[12]; a.w_mod = (const u16*)d_in[13];
    k_pre<<<280, 256, 0, stream>>>(a.c, a.w_mod, a.mem0, ws);
    hipFuncSetAttribute((const void*)k_main, hipFuncAttributeMaxDynamicSharedMemorySize, LDS_BYTES);
    k_main<<<256, 256, LDS_BYTES, stream>>>(a);
}
```

```cpp
#include <hip/hip_runtime.h>
#include <hip/hip_bf16.h>
#include <stdint.h>

typedef unsigned short u16;
typedef unsigned long long u64;
typedef __attribute__((ext_vector_type(8))) short bf16x8;
typedef __attribute__((ext_vector_type(4))) short bf16x4;
typedef __attribute__((ext_vector_type(4))) __bf16 bf16x4_t;
typedef __attribute__((ext_vector_type(8))) __bf16 v8bf;
typedef __attribute__((ext_vector_type(16))) __bf16 v16bf;
typedef __attribute__((ext_vector_type(4))) float f32x4;
typedef __attribute__((ext_vector_type(2))) float f32x2;
typedef unsigned v8u __attribute__((ext_vector_type(8)));
typedef unsigned v4u __attribute__((ext_vector_type(4)));
typedef unsigned v2u __attribute__((ext_vector_type(2)));
#define GAS __attribute__((address_space(1)))
#define LAS __attribute__((address_space(3)))
typedef GAS unsigned gu32;
#define RLX_AGENT __ATOMIC_RELAXED, __HIP_MEMORY_SCOPE_AGENT

constexpr int S_TOK = 2048, DM = 1024, NH = 32, DH = 128, MEMR = 8, SEG = 32, NSEG = 64;
constexpr int HD = NH * DH;
constexpr int QKN = 2 * HD;
constexpr int FF = 3584;

#define NTW 111
#define HXM 0
#define RG 16
#define RI(s) ((unsigned)(s) & (RG - 1))
#define PLACE 9
constexpr unsigned MiB = 1u << 20;
constexpr unsigned O_CTL = 0;
constexpr unsigned CTL_BYTES = 65536;
constexpr unsigned O_MOD = 65536;
constexpr unsigned O_POL = O_MOD + 32768;
constexpr unsigned O_MODP = 131072;
constexpr unsigned O_EP = 4 * MiB, EP_SLOT = 524288;
constexpr unsigned O_UM = O_EP + RG * EP_SLOT;
constexpr unsigned UM_SLOT = 64 * 16384;
constexpr unsigned O_E1 = O_UM + RG * UM_SLOT;
constexpr unsigned E1_SLOT = 262144;
constexpr unsigned O_KV = O_E1 + RG * E1_SLOT, KV_SLOT = 524288;
constexpr unsigned O_D = O_KV + RG * KV_SLOT, D_SLOT = MiB, D_SL = 28 * 16384, D_H1 = 54 * 16384, D_X2 = 44 * 16384, D_ST = 52 * 16384;
constexpr unsigned WS_END = O_D + 64 * D_SLOT;
#define WSB 0
static_assert(WSB * MiB + WS_END <= (RG == 64 ? 512 : 256) * MiB && O_D % (2 * MiB) == 0, "ws");
__device__ __forceinline__ unsigned nearg(const char* ws, unsigned pol, unsigned off, unsigned h) {
    unsigned r;
    asm("s_add_u32 %0, %1, %2\n\ts_lshr_b32 %0, %0, 8\n\ts_xor_b32 %0, %0, %3\n\ts_and_b32 %0, %0, 0x2000\n\ts_add_u32 %0, %0, %2" : "=&s"(r) : "s"((unsigned)(size_t)ws), "s"(__builtin_amdgcn_readfirstlane(off)), "s"(pol ^ h << 13) : "scc");
    return r;
}
__device__ __forceinline__ unsigned kmof(const char* ws, unsigned pol, int seg, int h) { return nearg(ws, pol, O_EP + RI(seg) * EP_SLOT, 1) + (h >> 2) * 16384 + (h & 3) * 2048; }
constexpr int C_ABORT = 0;
constexpr int C_X = 64;
#define PW_V(h) (ctl + C_X + 32 * (h))
#define PW_U(i) (ctl + C_X + 32 * (32 + (i)))
#define PW_Q(h) (ctl + C_X + 32 * (64 + (h)))
constexpr int C_H = C_X + 5120;
constexpr int C_R = 12288, C_P = C_R + 256;
constexpr int C_C = C_H + 2048;
constexpr int C_N = C_C + 288;
constexpr int C_G = C_N + 512;

__device__ __forceinline__ float bf2f(u16 v) { return __uint_as_float(((unsigned)v) << 16); }
__device__ __forceinline__ u16 f2bf(float f) {
    __hip_bfloat16 h = __float2bfloat16(f);
    return __builtin_bit_cast(u16, h);
}
__device__ __forceinline__ float rbf(float f) { return bf2f(f2bf(f)); }

#define NPR 6
#define CTC 3
__device__ __forceinline__ void code_touch(unsigned char* lds, int part) {
    u64 pc; const unsigned sz = 104 * 1024;
    asm volatile("s_getpc_b64 %0" : "=s"(pc));
    const v4u r = {(unsigned)pc, (unsigned)(pc >> 32) & 0xFFFFu, sz, 0x00020000u};
    asm volatile("s_mov_b32 m0, %2\n\ts_nop 4\n\tbuffer_load_dword %0, %1, 0 offen lds" :: "v"(((threadIdx.x & 63) + 64 * part) * 128), "s"(r), "s"((unsigned)(size_t)(LAS unsigned char*)lds) : "m0");
}
__global__ __launch_bounds__(256) void k_pre(const u16* __restrict__ c, const u16* __restrict__ w_mod, const u16* __restrict__ mem0, char* __restrict__ ws) {
    __shared__ float red[32][65];
    const int tid = threadIdx.x;
    const unsigned bid = blockIdx.x;
    if (bid >= 24) {
        const int cg = (bid - 24) % 48, kq = (bid - 24) / 48, c8 = tid & 7, kr = 128 * kq + (tid >> 3);
        v4u wv[4];
#pragma unroll
        for (int i = 0; i < 4; ++i) wv[i] = NTW & 4 ? __builtin_nontemporal_load((const v4u*)(w_mod + (size_t)(kr + 32 * i) * 6144 + cg * 64 + 8 * c8)) : *(const v4u*)(w_mod + (size_t)(kr + 32 * i) * 6144 + cg * 64 + 8 * c8);
        float acc[8] = {0.f, 0.f, 0.f, 0.f, 0.f, 0.f, 0.f, 0.f};
#pragma unroll
        for (int i = 0; i < 4; ++i) {
            const float v = bf2f(c[kr + 32 * i]), sv = rbf(v / (1.f + __expf(-v)));
#pragma unroll
            for (int j = 0; j < 4; ++j) { acc[2 * j] += sv * __uint_as_float(wv[i][j] << 16); acc[2 * j + 1] += sv * __uint_as_float(wv[i][j] & 0xFFFF0000u); }
        }
#pragma unroll
        for (int j = 0; j < 8; ++j) red[tid >> 3][8 * c8 + j] = acc[j];
        __syncthreads();
        if (tid < 64) {
            float v = 0.f;
#pragma unroll
            for (int i = 0; i < 32; ++i) v += red[i][tid];
            ((float*)(ws + O_MODP))[kq * 3072 + cg * 64 + tid] = v;
        }
        return;
    }
    if (bid >= 16) {
        if (bid < 22) *(uint4*)(ws + O_MOD + (bid - 16) * 4096 + tid * 16) = make_uint4(~0u, ~0u, ~0u, ~0u);
        if (bid == 23) { for (int i = 0; i < 4; ++i) code_touch((unsigned char*)red, i * 4 + (tid >> 6)); asm volatile("s_waitcnt vmcnt(0)"); }
        if (bid != 22 || tid) return;
        unsigned best[2] = {~0u, ~0u};
        for (int r = 0; r < NPR; ++r) {
            const u64 t0 = __builtin_readcyclecounter();
            asm volatile("global_store_dword %0, %1, %2 sc1\n\ts_waitcnt vmcnt(0)" :: "v"(O_POL + 128 + (r & 1) * 8192 + (r & 2) * 2048), "v"(0u), "s"(ws) : "memory");
            best[r & 1] = min(best[r & 1], (unsigned)(__builtin_readcyclecounter() - t0));
        }
        const unsigned xcc = __builtin_amdgcn_s_getreg((3 << 11) | 20);
        *(unsigned*)(ws + O_POL) = (((best[1] < best[0]) ^ (xcc >> 2) ^ (((unsigned)(size_t)ws + O_POL) >> 21)) & 1) << 13;
        return;
    }
    ((uint4*)(ws + O_CTL))[bid * 256 + tid] = make_uint4(0u, 0u, 0u, 0u);
    const unsigned u = (tid >> 6) * 256 + (bid & 3) * 64 + (tid & 63);
    uint4 m = *(const uint4*)(mem0 + ((u >> 1) & 7) * DM + (u >> 4) * 16 + (u & 1) * 8);
    m.x = min(m.x, 0xFFFFFFFEu); m.y = min(m.y, 0xFFFFFFFEu); m.z = min(m.z, 0xFFFFFFFEu); m.w = min(m.w, 0xFFFFFFFEu);
    for (int i = 0; i < 4; ++i) *(uint4*)(ws + O_E1 + bid * 16384 + i * 4096 + tid * 16) = m;
}

struct Args {
    const u16* Wq; const u16* Wk; const u16* Wv; const u16* Wo; const u16* mem0;
    const u16* c; const u16* w_mod;
    const u16* x; const u16* w1; const u16* b1; const u16* w2; const u16* b2;
    u16* out; char* ws;
    unsigned mode, pad0;
};
#define RSTAMP(sg, i) do { } while (0)
#define STAMP(i) do { } while (0)
#define XSTAMP(i) do { } while (0)
#define CSTAMP(i, v) do { } while (0)
#define STAMP_WAVE 0
constexpr unsigned SPIN_CAP = 1u << 22;
constexpr unsigned SENT = 0xFFFFFFFFu;

__device__ __forceinline__ void lds_barrier() { asm volatile("s_waitcnt lgkmcnt(0)\n\ts_barrier" ::: "memory"); }
__device__ __forceinline__ void wait_vm0() { asm volatile("s_waitcnt vmcnt(0)" ::: "memory"); }
typedef __bf16 bf16x2_t __attribute__((ext_vector_type(2)));
__device__ __forceinline__ unsigned pack2(float a, float b) { const f32x2 v = {a, b}; return __builtin_bit_cast(unsigned, __builtin_convertvector(v, bf16x2_t)); }
__device__ __forceinline__ float lo16f(unsigned u) { return __uint_as_float(u << 16); }
__device__ __forceinline__ float hi16f(unsigned u) { return __uint_as_float(u & 0xFFFF0000u); }
__device__ __forceinline__ unsigned umax4(v4u x) { return max(max(x.x, x.y), max(x.z, x.w)); }
__device__ __forceinline__ unsigned unsent(unsigned x) { return min(x, 0xFFFFFFFEu); }

__device__ __forceinline__ unsigned ldg4_sc1(const void* sb, unsigned vo) {
    unsigned r;
    asm volatile("global_load_dword %0, %1, %2 sc1\n\ts_waitcnt vmcnt(0)" : "=&v"(r) : "v"(vo), "s"(sb) : "memory");
    return r;
}
__device__ __forceinline__ bool spin_fail(unsigned& spins, unsigned* abortw) {
    if (++spins > SPIN_CAP) { __hip_atomic_store((gu32*)abortw, 1u, RLX_AGENT); return true; }
    if ((spins & 63u) == 0 && __builtin_amdgcn_readfirstlane(__hip_atomic_load((gu32*)abortw, RLX_AGENT)) == 1u) return true;
    return false;
}
__device__ __forceinline__ float red16_add(float x) { auto r = __builtin_amdgcn_permlane16_swap(__float_as_uint(x), __float_as_uint(x), false, false); return __uint_as_float(r[0]) + __uint_as_float(r[1]); }
__device__ __forceinline__ float red32_add(float x) { auto r = __builtin_amdgcn_permlane32_swap(__float_as_uint(x), __float_as_uint(x), false, false); return __uint_as_float(r[0]) + __uint_as_float(r[1]); }
__device__ __forceinline__ v2u tr_rd(const unsigned char* lds_base, unsigned off) {
    return __builtin_bit_cast(v2u, __builtin_amdgcn_ds_read_tr16_b64_v4bf16((LAS bf16x4_t*)(lds_base + off)));
}
__device__ __forceinline__ v4u lds16(const unsigned char* p) { return *(const LAS v4u*)p; }
__device__ __forceinline__ void dma16(const void* src, unsigned char* dst, int aux) {
    if (aux) __builtin_amdgcn_global_load_lds((const unsigned*)src, (unsigned*)dst, 16, 0, 2);
    else __builtin_amdgcn_global_load_lds((const unsigned*)src, (unsigned*)dst, 16, 0, 0);
}

__device__ __forceinline__ v4u mkrs(const void* p, unsigned bytes) { return (v4u){(unsigned)(u64)p, (unsigned)((u64)p >> 32) & 0xFFFFu, bytes, 0x00020000u}; }
template <int AUX> __device__ __forceinline__ void bdma(v4u r, unsigned vo, unsigned so, unsigned ldsaddr) {
    if (AUX == 16) asm volatile("s_mov_b32 m0, %0\n\ts_nop 0\n\tbuffer_load_dwordx4 %1, %2, %3 offen sc1 lds" :: "s"(ldsaddr), "v"(vo), "s"(r), "s"(so) : "m0");
    else if (AUX) asm volatile("s_mov_b32 m0, %0\n\ts_nop 0\n\tbuffer_load_dwordx4 %1, %2, %3 offen nt lds" :: "s"(ldsaddr), "v"(vo), "s"(r), "s"(so) : "m0");
    else asm volatile("s_mov_b32 m0, %0\n\ts_nop 0\n\tbuffer_load_dwordx4 %1, %2, %3 offen lds" :: "s"(ldsaddr), "v"(vo), "s"(r), "s"(so) : "m0");
}

constexpr int L_W = 0;
constexpr int L_KX = 131072;
constexpr int L_MEM = L_KX + 8192;
constexpr int L_T = L_MEM + 16384;
constexpr int L_RED = L_T + 5120;
constexpr int LDS_BYTES = 163840;

#define CH_A 141
#define CH_B 83
#define CH_EU 12
#define CH_EV 22
#define CH_DMIN 0
#define CH_R 60
#define CH_S1 11
#define CH_A1 0
#define CH_B1 0
#define CH_S2 54
#define CH_A2 0
#define CH_B2 0
#define FINE_PAR
constexpr unsigned CH_PAR = CH_A | CH_B << 8 | CH_EU << 16 | CH_DMIN << 21 | (unsigned)CH_R << 26;

#define KRV 0
#define WOF 0
constexpr bool iswo(bool isV, int u) { return isV && (u >= 8) != (WOF != 0); }
constexpr int frag(bool isV, int u) { return isV ? 8 * (u & 1) + (((u >> 1) & 3) ^ KRV) : 8 * ((u >> 1) & 1) + ((u >> 2) ^ KRV); }
constexpr int tile(bool isV, int u, int n) {
    if (iswo(isV, u)) return 4 * (8 + (u & 7)) + n;
    const int i = frag(isV, u) + 4 * (n >> 1);
    return isV ? 2 * i + (n & 1) : 4 * i + 2 * ((u & 1) ^ (i > 3 && (i & 3) == 3)) + (n & 1);
}
constexpr int flat(bool isV, int j) { int u = 0; while (tile(isV, u >> 2, u & 3) != 4 * j + 3) ++u; u >>= 2; return u > 12 + (j >> 2) ? u : 12 + (j >> 2); }
template <int I> struct IC { static constexpr int v = I; };
template <int B, int E, class F> __device__ __forceinline__ void sfor(F&& f) { if constexpr (B < E) { f(IC<B>{}); sfor<B + 1, E>(f); } }
#define SB() __builtin_amdgcn_sched_barrier(0)

__device__ __forceinline__ int lane_id() { int l; asm volatile("v_mbcnt_lo_u32_b32 %0, -1, 0\n\tv_mbcnt_hi_u32_b32 %0, -1, %0" : "=v"(l)); return l; }
#define CHAIN_QK \
    "s_mov_b32 m0, s58\n.Lchq1_%=:\ns_mov_b32 s68, 0\nds_read_b128 v[220:223], v7 offset:0\nds_read_b128 v[224:227], v7 offset:1024\nds_read_b128 v[228:231], v7 offset:2048\n" \
    "ds_read_b128 v[232:235], v7 offset:3072\nds_read_b128 v[236:239], v7 offset:4096\nds_read_b128 v[240:243], v7 offset:5120\nds_read_b128 v[244:247], v7 offset:6144\n" \
    "ds_read_b128 v[248:251], v7 offset:7168\nds_read_b128 v[84:87], v7 offset:8192\nds_read_b128 v[88:91], v7 offset:9216\nds_read_b128 v[60:63], v7 offset:10240\n" \
    "ds_read_b128 v[64:67], v7 offset:11264\nds_read_b128 v[68:71], v7 offset:16384\nds_read_b128 v[72:75], v7 offset:17408\ns_waitcnt lgkmcnt(12)\n" \
    "ds_read_b128 v[76:79], v7 offset:18432\nds_read_b128 v[80:83], v7 offset:19456\n.Lchq10_%=:\ns_memrealtime s[72:73]\ns_waitcnt lgkmcnt(0)\n.Lchq11_%=:\n" \
    "s_sub_u32 s72, s72, s63\ns_cmp_lt_i32 s72, -3\ns_cbranch_scc1 .Lchq10_%=\ns_cmp_ge_i32 s72, 0\ns_cbranch_scc1 .Lchq12_%=\ns_nop 2\ns_cmp_ge_i32 s72, -1\n" \
    "s_cbranch_scc1 .Lchq12_%=\ns_nop 2\ns_cmp_ge_i32 s72, -2\ns_cbranch_scc1 .Lchq12_%=\ns_nop 2\n.Lchq12_%=:\n" \
    "buffer_load_dwordx4 v[40:43], v2, s[64:67], s48 offen offset:0 sc1\ns_nop 12\nbuffer_load_dwordx4 v[44:47], v2, s[64:67], s48 offen offset:1024 sc1\ns_nop 12\n" \
    "buffer_load_dwordx4 v[48:51], v2, s[64:67], s48 offen offset:2048 sc1\ns_nop 12\nbuffer_load_dwordx4 v[52:55], v2, s[64:67], s48 offen offset:3072 sc1\n.Lchq9_%=:\n" \
    "v_mov_b64 v[38:39], 0\nv_mov_b64 v[36:37], 0\nv_mov_b64 v[34:35], 0\nv_mov_b64 v[32:33], 0\nv_mov_b64 v[30:31], 0\nv_mov_b64 v[28:29], 0\nv_mov_b64 v[26:27], 0\n" \
    "v_mov_b64 v[24:25], 0\ns_waitcnt vmcnt(3)\nv_smfmac_f32_16x16x64_bf16 v[36:39], v[40:43], v[220:227], v0\nds_write_b128 v3, v[40:43] offset:0\n" \
    "v_smfmac_f32_16x16x64_bf16 v[32:35], v[40:43], a[16:23], v0\nds_read_b128 v[220:223], v7 offset:24576\nv_max3_u32 v252, v40, v41, v41\n" \
    "v_smfmac_f32_16x16x64_bf16 v[28:31], v[40:43], a[8:15], v0\nds_read_b128 v[224:227], v7 offset:25600\nv_max3_u32 v252, v252, v42, v43\n" \
    "v_smfmac_f32_16x16x64_bf16 v[24:27], v[40:43], a[0:7], v0\ns_waitcnt vmcnt(2)\nv_smfmac_f32_16x16x64_bf16 v[36:39], v[44:47], v[228:235], v0\n" \
    "ds_write_b128 v3, v[44:47] offset:1024\nv_smfmac_f32_16x16x64_bf16 v[32:35], v[44:47], a[40:47], v0\nds_read_b128 v[228:231], v7 offset:26624\n" \
    "v_max3_u32 v252, v252, v44, v45\nv_smfmac_f32_16x16x64_bf16 v[28:31], v[44:47], a[32:39], v0\nds_read_b128 v[232:235], v7 offset:27648\nv_max3_u32 v252, v252, v46, v47\n" \
    "v_smfmac_f32_16x16x64_bf16 v[24:27], v[44:47], a[24:31], v0\ns_waitcnt vmcnt(1)\nv_smfmac_f32_16x16x64_bf16 v[36:39], v[48:51], v[236:243], v0\n" \
    "ds_write_b128 v3, v[48:51] offset:2048\nv_smfmac_f32_16x16x64_bf16 v[32:35], v[48:51], a[64:71], v0\ns_waitcnt lgkmcnt(3)\ns_barrier\nv_max3_u32 v252, v252, v48, v49\n" \
    "v_smfmac_f32_16x16x64_bf16 v[28:31], v[48:51], a[56:63], v0\nds_read_b128 v[56:59], v4 offset:0\nv_max3_u32 v252, v252, v50, v51\n" \
    "v_smfmac_f32_16x16x64_bf16 v[24:27], v[48:51], a[48:55], v0\nds_read_b128 v[40:43], v4 offset:1024\ns_waitcnt vmcnt(0)\n" \
    "v_smfmac_f32_16x16x64_bf16 v[36:39], v[52:55], v[244:251], v0\nds_write_b128 v3, v[52:55] offset:3072\nv_smfmac_f32_16x16x64_bf16 v[32:35], v[52:55], a[88:95], v0\n" \
    "ds_read_b128 v[44:47], v5 offset:0\nv_max3_u32 v252, v252, v52, v53\nv_smfmac_f32_16x16x64_bf16 v[28:31], v[52:55], a[80:87], v0\nds_read_b128 v[48:51], v5 offset:1024\n" \
    "v_max3_u32 v252, v252, v54, v55\nv_smfmac_f32_16x16x64_bf16 v[24:27], v[52:55], a[72:79], v0\ns_waitcnt lgkmcnt(4)\nds_write_b32 v16, v252\n" \
    "v_smfmac_f32_16x16x64_bf16 v[36:39], v[56:59], v[84:91], v0\nds_read_b128 v[52:55], v6 offset:0\nv_smfmac_f32_16x16x64_bf16 v[32:35], v[56:59], a[112:119], v0\n" \
    "ds_read_b128 v[84:87], v6 offset:1024\ns_waitcnt lgkmcnt(6)\nv_smfmac_f32_16x16x64_bf16 v[36:39], v[40:43], v[60:67], v0\nds_read_b128 v[236:239], v7 offset:12288\n" \
    "v_smfmac_f32_16x16x64_bf16 v[32:35], v[40:43], a[136:143], v0\ns_waitcnt lgkmcnt(5)\ns_waitcnt lgkmcnt(3)\ns_barrier\n" \
    "v_smfmac_f32_16x16x64_bf16 v[36:39], v[44:47], v[68:75], v0\nds_read_b128 v[18:21], v17\nv_smfmac_f32_16x16x64_bf16 v[32:35], v[44:47], a[208:215], v0\n" \
    "ds_read_b128 v[88:91], v4 offset:2048\nv_smfmac_f32_16x16x64_bf16 v[36:39], v[48:51], v[76:83], v0\nds_read_b128 v[240:243], v7 offset:13312\n" \
    "v_smfmac_f32_16x16x64_bf16 v[32:35], v[48:51], a[232:239], v0\nds_read_b128 v[56:59], v4 offset:3072\ns_waitcnt lgkmcnt(6)\n" \
    "v_smfmac_f32_16x16x64_bf16 v[36:39], v[52:55], v[220:227], v0\nds_read_b128 v[40:43], v5 offset:2048\nv_smfmac_f32_16x16x64_bf16 v[32:35], v[52:55], v[140:147], v0\n" \
    "s_waitcnt lgkmcnt(6)\ns_waitcnt lgkmcnt(4)\nv_max3_u32 v18, v18, v19, v20\nv_smfmac_f32_16x16x64_bf16 v[36:39], v[84:87], v[228:235], v0\n" \
    "ds_read_b128 v[244:247], v7 offset:20480\nv_max_u32 v18, v18, v21\nv_smfmac_f32_16x16x64_bf16 v[32:35], v[84:87], v[164:171], v0\nds_read_b128 v[248:251], v7 offset:21504\n" \
    "s_waitcnt lgkmcnt(4)\nv_cmp_eq_u32 vcc, -1, v18\nv_smfmac_f32_16x16x64_bf16 v[36:39], v[88:91], v[236:243], v0\ns_cbranch_vccnz .Lchq4_%=\n" \
    "v_smfmac_f32_16x16x64_bf16 v[32:35], v[88:91], a[160:167], v0\nds_read_b128 v[44:47], v5 offset:3072\ns_waitcnt lgkmcnt(4)\n" \
    "v_smfmac_f32_16x16x64_bf16 v[36:39], v[56:59], a[176:183], v0\nds_read_b128 v[48:51], v6 offset:2048\nv_smfmac_f32_16x16x64_bf16 v[32:35], v[56:59], a[168:175], v0\n" \
    "ds_read_b128 v[60:63], v7 offset:28672\ns_waitcnt lgkmcnt(3)\nds_read_b128 v[64:67], v7 offset:29696\nv_smfmac_f32_16x16x64_bf16 v[36:39], v[40:43], v[244:251], v0\n" \
    "ds_read_b128 v[52:55], v6 offset:3072\nv_smfmac_f32_16x16x64_bf16 v[32:35], v[40:43], v[92:99], v0\nds_read_b128 v[76:79], v7 offset:14336\ns_waitcnt lgkmcnt(5)\n" \
    "v_smfmac_f32_16x16x64_bf16 v[36:39], v[44:47], v[108:115], v0\nds_read_b128 v[80:83], v7 offset:15360\nv_smfmac_f32_16x16x64_bf16 v[32:35], v[44:47], v[100:107], v0\n" \
    "ds_read_b128 v[68:71], v7 offset:22528\ns_waitcnt lgkmcnt(4)\nv_smfmac_f32_16x16x64_bf16 v[36:39], v[48:51], v[60:67], v0\nds_read_b128 v[72:75], v7 offset:23552\n" \
    "v_smfmac_f32_16x16x64_bf16 v[32:35], v[48:51], v[188:195], v0\nds_read_b128 v[60:63], v7 offset:30720\ns_waitcnt lgkmcnt(5)\n" \
    "v_smfmac_f32_16x16x64_bf16 v[36:39], v[52:55], v[204:211], v0\nds_read_b128 v[64:67], v7 offset:31744\nv_smfmac_f32_16x16x64_bf16 v[32:35], v[52:55], v[196:203], v0\n" \
    "ds_read_b128 v[220:223], v8 offset:0\nv_smfmac_f32_16x16x64_bf16 v[24:27], v[84:87], v[148:155], v0\nds_read_b128 v[224:227], v8 offset:256\ns_nop 0\n" \
    "v_smfmac_f32_16x16x64_bf16 v[28:31], v[84:87], v[156:163], v0\nds_read_b128 v[84:87], v4 offset:0\ns_nop 0\nv_smfmac_f32_16x16x64_bf16 v[24:27], v[88:91], a[144:151], v0\n" \
    "ds_read_b128 v[228:231], v8 offset:2048\nv_permlane32_swap_b32 v32, v36\nv_smfmac_f32_16x16x64_bf16 v[28:31], v[88:91], a[152:159], v0\n" \
    "ds_read_b128 v[88:91], v4 offset:1024\nv_permlane32_swap_b32 v33, v37\nv_smfmac_f32_16x16x64_bf16 v[24:27], v[56:59], a[184:191], v0\ns_waitcnt lgkmcnt(9)\n" \
    "v_add_f32 v32, v32, v36\nv_smfmac_f32_16x16x64_bf16 v[28:31], v[56:59], v[76:83], v0\nds_read_b128 v[56:59], v5 offset:0\nv_add_f32 v33, v33, v37\n" \
    "v_smfmac_f32_16x16x64_bf16 v[24:27], v[40:43], a[240:247], v0\nds_read_b128 v[232:235], v8 offset:2304\nv_permlane32_swap_b32 v34, v38\n" \
    "v_smfmac_f32_16x16x64_bf16 v[28:31], v[40:43], a[248:255], v0\nds_read_b128 v[40:43], v5 offset:1024\nv_permlane32_swap_b32 v35, v39\n" \
    "v_smfmac_f32_16x16x64_bf16 v[24:27], v[44:47], v[116:123], v0\ns_waitcnt lgkmcnt(10)\nv_add_f32 v34, v34, v38\nv_smfmac_f32_16x16x64_bf16 v[28:31], v[44:47], v[68:75], v0\n" \
    "ds_read_b128 v[44:47], v6 offset:0\nv_add_f32 v35, v35, v39\nv_smfmac_f32_16x16x64_bf16 v[24:27], v[48:51], v[172:179], v0\nds_read_b128 v[236:239], v8 offset:4096\n" \
    "v_cvt_pk_bf16_f32 v32, v32, v33\nv_smfmac_f32_16x16x64_bf16 v[28:31], v[48:51], v[180:187], v0\nds_read_b128 v[240:243], v8 offset:4352\nv_cvt_pk_bf16_f32 v33, v34, v35\n" \
    "v_smfmac_f32_16x16x64_bf16 v[24:27], v[52:55], v[212:219], v0\ns_waitcnt lgkmcnt(11)\nds_write_b64 v9, v[32:33]\n" \
    "v_smfmac_f32_16x16x64_bf16 v[28:31], v[52:55], v[60:67], v0\nds_read_b128 v[244:247], v8 offset:6144\ns_waitcnt lgkmcnt(8)\n" \
    "v_smfmac_f32_16x16x64_bf16 v[24:27], v[84:87], a[96:103], v0\nds_read_b128 v[248:251], v8 offset:6400\nv_smfmac_f32_16x16x64_bf16 v[28:31], v[84:87], a[104:111], v0\n" \
    "v_smfmac_f32_16x16x64_bf16 v[24:27], v[88:91], a[120:127], v0\ns_waitcnt lgkmcnt(2)\ns_barrier\nv_smfmac_f32_16x16x64_bf16 v[28:31], v[88:91], a[128:135], v0\n" \
    "ds_read_b64_tr_b16 v[60:61], v10 offset:0\nv_smfmac_f32_16x16x64_bf16 v[24:27], v[56:59], a[192:199], v0\nds_read_b64_tr_b16 v[62:63], v10 offset:32\n" \
    "v_smfmac_f32_16x16x64_bf16 v[28:31], v[56:59], a[200:207], v0\nds_read_b64_tr_b16 v[64:65], v10 offset:256\nv_smfmac_f32_16x16x64_bf16 v[24:27], v[40:43], a[216:223], v0\n" \
    "ds_read_b64_tr_b16 v[66:67], v10 offset:288\nv_smfmac_f32_16x16x64_bf16 v[28:31], v[40:43], a[224:231], v0\nds_read_b64_tr_b16 v[68:69], v10 offset:512\n" \
    "v_smfmac_f32_16x16x64_bf16 v[24:27], v[44:47], v[124:131], v0\nds_read_b64_tr_b16 v[70:71], v10 offset:544\nv_smfmac_f32_16x16x64_bf16 v[28:31], v[44:47], v[132:139], v0\n" \
    "ds_read_b64_tr_b16 v[72:73], v10 offset:768\ns_waitcnt lgkmcnt(5)\nv_mfma_f32_16x16x32_bf16 v[32:35], v[220:223], v[60:63], 0\nds_read_b64_tr_b16 v[74:75], v10 offset:800\n" \
    "s_nop 0\nv_mfma_f32_16x16x32_bf16 v[36:39], v[224:227], v[60:63], 0\ns_nop 1\nv_permlane32_swap_b32 v24, v28\nv_permlane32_swap_b32 v25, v29\n" \
    "v_permlane32_swap_b32 v26, v30\nv_permlane32_swap_b32 v27, v31\nv_pk_add_f32 v[24:25], v[24:25], v[28:29]\nv_pk_add_f32 v[26:27], v[26:27], v[30:31]\n" \
    "v_cvt_pk_bf16_f32 v24, v24, v25\nv_cvt_pk_bf16_f32 v25, v26, v27\nds_write_b64 v9, v[24:25] offset:2560\ns_waitcnt lgkmcnt(5)\n" \
    "v_mfma_f32_16x16x32_bf16 v[32:35], v[228:231], v[64:67], v[32:35]\nv_mfma_f32_16x16x32_bf16 v[36:39], v[232:235], v[64:67], v[36:39]\ns_waitcnt lgkmcnt(3)\n" \
    "v_mfma_f32_16x16x32_bf16 v[32:35], v[236:239], v[68:71], v[32:35]\ns_xor_b32 s74, s50, 0x1000\ns_cmp_eq_u32 s61, 63\ns_waitcnt lgkmcnt(0)\ns_barrier\n" \
    "ds_read_b64_tr_b16 v[40:41], v10 offset:2560\nds_read_b64_tr_b16 v[42:43], v10 offset:2592\nds_read_b64_tr_b16 v[44:45], v10 offset:2816\n" \
    "v_mfma_f32_16x16x32_bf16 v[36:39], v[240:243], v[68:71], v[36:39]\nds_read_b64_tr_b16 v[46:47], v10 offset:2848\nds_read_b64_tr_b16 v[48:49], v10 offset:3072\n" \
    "ds_read_b64_tr_b16 v[50:51], v10 offset:3104\nv_mfma_f32_16x16x32_bf16 v[32:35], v[244:247], v[72:75], v[32:35]\nds_read_b64_tr_b16 v[52:53], v10 offset:3328\n" \
    "ds_read_b64_tr_b16 v[54:55], v10 offset:3360\nv_mfma_f32_16x16x32_bf16 v[36:39], v[248:251], v[72:75], v[36:39]\ns_cbranch_scc1 .Lchq7_%=\ns_waitcnt lgkmcnt(6)\n" \
    "v_mfma_f32_16x16x32_bf16 v[28:31], v[40:43], v[60:63], 0\ns_waitcnt lgkmcnt(4)\nv_mfma_f32_16x16x32_bf16 v[28:31], v[44:47], v[64:67], v[28:31]\ns_waitcnt lgkmcnt(2)\n" \
    "s_nop 1\nv_mov_b32_dpp v32, v36 quad_perm:[0,1,2,3] row_mask:0xf bank_mask:0xc\nv_mov_b32_dpp v33, v37 quad_perm:[0,1,2,3] row_mask:0xf bank_mask:0xc\n" \
    "v_mfma_f32_16x16x32_bf16 v[28:31], v[48:51], v[68:71], v[28:31]\ns_waitcnt lgkmcnt(0)\nv_mov_b32_dpp v34, v38 quad_perm:[0,1,2,3] row_mask:0xf bank_mask:0xc\n" \
    "v_mov_b32_dpp v35, v39 quad_perm:[0,1,2,3] row_mask:0xf bank_mask:0xc\nv_mfma_f32_16x16x32_bf16 v[28:31], v[52:55], v[72:75], v[28:31]\nv_max3_f32 v36, v32, v33, v34\n" \
    "v_max_f32 v36, v36, v35\ns_nop 1\nv_max_f32_dpp v37, v36, v36 row_ror:8 row_mask:0xf bank_mask:0xf\nv_max_f32_dpp v38, v36, v36 row_ror:8 row_mask:0xf bank_mask:0xf\n" \
    "s_nop 1\nv_permlane32_swap_b32 v37, v38\nv_max3_f32 v36, v28, v29, v30\nv_max3_f32 v39, v31, v37, v38\nv_max_f32 v37, v36, v39\nv_max_f32 v36, v36, v39\ns_nop 1\n" \
    "v_permlane16_swap_b32 v36, v37\nv_max_f32 v36, v36, v37\nv_mul_f32_e64 v36, -s32, v36\nv_pk_fma_f32 v[32:33], v[32:33], s[32:33], v[36:37] op_sel_hi:[1,0,0]\n" \
    "v_pk_fma_f32 v[34:35], v[34:35], s[32:33], v[36:37] op_sel_hi:[1,0,0]\nv_pk_fma_f32 v[28:29], v[28:29], s[32:33], v[36:37] op_sel_hi:[1,0,0]\n" \
    "v_pk_fma_f32 v[30:31], v[30:31], s[32:33], v[36:37] op_sel_hi:[1,0,0]\nv_exp_f32 v32, v32\nv_exp_f32 v33, v33\nv_exp_f32 v34, v34\nv_exp_f32 v35, v35\nv_exp_f32 v28, v28\n" \
    "v_pk_add_f32 v[36:37], v[32:33], v[34:35]\nv_add_f32 v36, v36, v37\nv_exp_f32 v29, v29\nv_exp_f32 v30, v30\n" \
    "v_add_f32_dpp v37, v36, v36 row_ror:8 row_mask:0xf bank_mask:0xf\nv_add_f32_dpp v38, v36, v36 row_ror:8 row_mask:0xf bank_mask:0xf\nv_exp_f32 v31, v31\n" \
    "v_add_f32 v36, v28, v29\nv_permlane32_swap_b32 v37, v38\nv_add_f32 v39, v30, v31\nv_pk_add_f32 v[36:37], v[36:37], v[38:39] op_sel:[0,1] op_sel_hi:[1,0]\n" \
    "v_add_f32 v38, v36, v37\nv_add_f32 v36, v36, v37\ns_nop 1\nv_permlane16_swap_b32 v36, v38\nv_add_f32 v36, v36, v38\nv_rcp_f32 v36, v36\ns_nop 0\n" \
    "v_pk_mul_f32 v[28:29], v[28:29], v[36:37] op_sel_hi:[1,0]\nv_pk_mul_f32 v[30:31], v[30:31], v[36:37] op_sel_hi:[1,0]\nv_cvt_pk_bf16_f32 v76, v28, v29\n" \
    "v_cvt_pk_bf16_f32 v77, v30, v31\nv_pk_mul_f32 v[32:33], v[32:33], v[36:37] op_sel_hi:[1,0]\nv_pk_mul_f32 v[34:35], v[34:35], v[36:37] op_sel_hi:[1,0]\n" \
    "v_permlane16_swap_b32 v76, v78\nv_cvt_pk_bf16_f32 v32, v32, v33\nv_permlane16_swap_b32 v77, v79\nv_cvt_pk_bf16_f32 v33, v34, v35\n" \
    "v_mov_b32_dpp v76, v32 quad_perm:[0,1,2,3] row_mask:0xf bank_mask:0x3\nv_mov_b32_dpp v78, v32 row_ror:8 row_mask:0xf bank_mask:0x3\n" \
    "v_mov_b32_dpp v77, v33 quad_perm:[0,1,2,3] row_mask:0xf bank_mask:0x3\nv_mov_b32_dpp v79, v33 row_ror:8 row_mask:0xf bank_mask:0x3\ns_mov_b64 exec, s[46:47]\n" \
    "buffer_store_dwordx4 v[76:79], v11, s[64:67], s50 offen sc1\nbuffer_store_dwordx4 v[76:79], v11, s[64:67], s74 offen sc1\ns_mov_b64 exec, -1\ns_memrealtime s[72:73]\n" \
    "s_add_u32 s63, s63, s44\ns_waitcnt lgkmcnt(0)\ns_sub_u32 s69, s63, s72\ns_cmp_lt_i32 s69, s37\ns_cbranch_scc1 .Lchq13_%=\n.Lchq14_%=:\nds_read_b64_tr_b16 v[56:57], v13\n" \
    "ds_read_b64_tr_b16 v[58:59], v13 offset:32\ns_waitcnt lgkmcnt(0)\ns_mov_b32 exec_lo, 0x00ff00ff\ns_mov_b32 exec_hi, 0x00ff00ff\n" \
    "buffer_store_dwordx4 v[56:59], v14, s[64:67], s51 offen sc1\ns_mov_b64 exec, -1\ns_add_u32 s63, s63, s45\ns_mov_b32 s68, 0\ns_cmp_gt_i32 s60, s61\n" \
    "s_cbranch_scc0 .Lchq5_%=\n.Lchq6_%=:\nbuffer_load_dwordx4 v1, s[64:67], s56 offen offset:0 sc1 lds\nbuffer_load_dwordx4 v1, s[64:67], s56 offen offset:1024 sc1 lds\n" \
    "s_add_u32 s61, s61, 1\ns_and_b32 s69, s61, %[rm]\ns_cselect_b32 s78, 1, %[rw]\ns_cmp_lg_u32 s69, %[rm]\ns_cselect_b32 s79, 1, %[rw]\ns_lshl_b32 s77, s78, 18\n" \
    "s_add_u32 s74, s40, s77\ns_xor_b32 s69, s74, s40\ns_mov_b32 s40, s74\ns_lshr_b32 s69, s69, 8\ns_and_b32 s69, s69, 0x2000\ns_add_u32 s48, s48, s77\ns_xor_b32 s48, s48, s69\n" \
    "s_add_u32 s69, s61, 2\ns_and_b32 s69, s69, %[rm]\ns_lshl_b32 s69, s69, 19\ns_add_u32 s69, s69, s52\ns_add_u32 s74, s53, s69\ns_lshr_b32 s74, s74, 8\n" \
    "s_xor_b32 s74, s74, s54\ns_and_b32 s74, s74, 0x2000\ns_add_u32 s49, s74, s69\ns_lshl_b32 s77, s78, 19\ns_add_u32 s74, s41, s77\ns_xor_b32 s69, s74, s41\n" \
    "s_mov_b32 s41, s74\ns_lshr_b32 s69, s69, 8\ns_and_b32 s69, s69, 0x2000\ns_add_u32 s50, s50, s77\ns_xor_b32 s50, s50, s69\ns_add_u32 s51, s51, s77\ns_xor_b32 s51, s51, s69\n" \
    "s_lshl_b32 s77, s79, 19\ns_add_u32 s74, s35, s77\ns_xor_b32 s69, s74, s35\ns_mov_b32 s35, s74\ns_lshr_b32 s69, s69, 8\ns_and_b32 s69, s69, 0x2000\ns_add_u32 s56, s56, s77\n" \
    "s_xor_b32 s56, s56, s69\ns_cmp_eq_u32 s61, %[s1]\ns_cselect_b32 s69, %[a1], 0\ns_cselect_b32 s74, %[b1], 0\ns_sub_u32 s44, s44, s69\ns_sub_u32 s45, s45, s74\n" \
    "s_cmp_eq_u32 s61, %[s2]\ns_cselect_b32 s69, %[a2], 0\ns_cselect_b32 s74, %[b2], 0\ns_sub_u32 s44, s44, s69\ns_sub_u32 s45, s45, s74\ns_branch .Lchq1_%=\n.Lchq4_%=:\n" \
    "s_waitcnt lgkmcnt(0)\ns_barrier\ns_add_u32 s63, s63, s38\ns_xor_b32 s48, s48, 0x1000\ns_xor_b32 s50, s50, 0x1000\ns_add_u32 s68, s68, 1\ns_and_b32 s69, s68, 63\n" \
    "s_cbranch_scc1 .Lchq3_%=\ns_cmp_gt_u32 s68, 0x100000\ns_cbranch_scc1 .Lchq8_%=\nv_mov_b32 v252, 0\nbuffer_load_dword v253, v252, s[64:67], 0 offen sc1\ns_waitcnt vmcnt(0)\n" \
    "v_readfirstlane_b32 s69, v253\ns_cmp_eq_u32 s69, 1\ns_cbranch_scc0 .Lchq3_%=\ns_endpgm\n.Lchq3_%=:\n.Lchq15_%=:\ns_memrealtime s[72:73]\ns_waitcnt lgkmcnt(0)\n.Lchq16_%=:\n" \
    "s_sub_u32 s72, s72, s63\ns_cmp_lt_i32 s72, -3\ns_cbranch_scc1 .Lchq15_%=\ns_cmp_ge_i32 s72, 0\ns_cbranch_scc1 .Lchq17_%=\ns_nop 2\ns_cmp_ge_i32 s72, -1\n" \
    "s_cbranch_scc1 .Lchq17_%=\ns_nop 2\ns_cmp_ge_i32 s72, -2\ns_cbranch_scc1 .Lchq17_%=\ns_nop 2\n.Lchq17_%=:\n" \
    "buffer_load_dwordx4 v[40:43], v2, s[64:67], s48 offen offset:0 sc1\ns_nop 12\nbuffer_load_dwordx4 v[44:47], v2, s[64:67], s48 offen offset:1024 sc1\ns_nop 12\n" \
    "buffer_load_dwordx4 v[48:51], v2, s[64:67], s48 offen offset:2048 sc1\ns_nop 12\nbuffer_load_dwordx4 v[52:55], v2, s[64:67], s48 offen offset:3072 sc1\n" \
    "ds_read_b128 v[220:223], v7 offset:0\nds_read_b128 v[224:227], v7 offset:1024\nds_read_b128 v[228:231], v7 offset:2048\nds_read_b128 v[232:235], v7 offset:3072\n" \
    "ds_read_b128 v[236:239], v7 offset:4096\nds_read_b128 v[240:243], v7 offset:5120\nds_read_b128 v[244:247], v7 offset:6144\nds_read_b128 v[248:251], v7 offset:7168\n" \
    "ds_read_b128 v[84:87], v7 offset:8192\nds_read_b128 v[88:91], v7 offset:9216\nds_read_b128 v[60:63], v7 offset:10240\nds_read_b128 v[64:67], v7 offset:11264\n" \
    "ds_read_b128 v[68:71], v7 offset:16384\nds_read_b128 v[72:75], v7 offset:17408\ns_waitcnt lgkmcnt(12)\nds_read_b128 v[76:79], v7 offset:18432\n" \
    "ds_read_b128 v[80:83], v7 offset:19456\ns_waitcnt lgkmcnt(0)\ns_branch .Lchq9_%=\n.Lchq5_%=:\nbuffer_load_dword v22, v12, s[64:67], 0 offen sc1\ns_waitcnt vmcnt(0)\n" \
    "v_readfirstlane_b32 s74, v22\ns_max_i32 s60, s60, s74\ns_cmp_gt_i32 s60, s61\ns_cbranch_scc1 .Lchq18_%=\ns_sleep 4\ns_add_u32 s68, s68, 1\ns_and_b32 s69, s68, 63\n" \
    "s_cbranch_scc1 .Lchq5_%=\ns_cmp_gt_u32 s68, 0x100000\ns_cbranch_scc1 .Lchq8_%=\nv_mov_b32 v252, 0\nbuffer_load_dword v253, v252, s[64:67], 0 offen sc1\ns_waitcnt vmcnt(0)\n" \
    "v_readfirstlane_b32 s69, v253\ns_cmp_eq_u32 s69, 1\ns_cbranch_scc0 .Lchq5_%=\ns_endpgm\n.Lchq18_%=:\ns_memrealtime s[72:73]\ns_waitcnt lgkmcnt(0)\n.Lchq19_%=:\n" \
    "s_sub_u32 s69, s63, s72\ns_cmp_ge_i32 s69, 20\ns_cbranch_scc1 .Lchq6_%=\ns_add_u32 s63, s63, s38\ns_xor_b32 s48, s48, 0x1000\ns_xor_b32 s50, s50, 0x1000\n" \
    "s_branch .Lchq19_%=\n.Lchq13_%=:\ns_add_u32 s63, s63, s38\ns_xor_b32 s48, s48, 0x1000\ns_xor_b32 s50, s50, 0x1000\ns_sub_u32 s69, s63, s72\ns_cmp_lt_i32 s69, s37\n" \
    "s_cbranch_scc1 .Lchq13_%=\ns_branch .Lchq14_%=\n.Lchq8_%=:\nv_mov_b32 v252, 0\nv_mov_b32 v253, 1\nbuffer_store_dword v253, v252, s[64:67], 0 offen sc1\ns_endpgm\n" \
    ".Lchq7_%=:\nds_read_b64_tr_b16 v[56:57], v13\nds_read_b64_tr_b16 v[58:59], v13 offset:32\ns_waitcnt lgkmcnt(0)\ns_mov_b32 exec_lo, 0x00ff00ff\n" \
    "s_mov_b32 exec_hi, 0x00ff00ff\nbuffer_store_dwordx4 v[56:59], v14, s[64:67], s51 offen sc1\ns_mov_b64 exec, -1\n.Lchq2_%=:\ns_waitcnt vmcnt(0)\n"
#define CHAIN_V \
    "s_mov_b32 m0, s58\n.Lchv1_%=:\ns_mov_b32 s68, 0\nds_read_b128 v[220:223], v7 offset:0\nds_read_b128 v[224:227], v7 offset:1024\nds_read_b128 v[228:231], v7 offset:2048\n" \
    "ds_read_b128 v[232:235], v7 offset:3072\nds_read_b128 v[236:239], v7 offset:4096\nds_read_b128 v[240:243], v7 offset:5120\nds_read_b128 v[244:247], v7 offset:8192\n" \
    "ds_read_b128 v[248:251], v7 offset:9216\nds_read_b128 v[60:63], v7 offset:12288\nds_read_b128 v[64:67], v7 offset:13312\nds_read_b128 v[68:71], v7 offset:6144\n" \
    "ds_read_b128 v[72:75], v7 offset:7168\nds_read_b128 v[76:79], v7 offset:10240\nds_read_b128 v[80:83], v7 offset:11264\ns_waitcnt lgkmcnt(12)\n" \
    "ds_read_b128 v[84:87], v7 offset:14336\nds_read_b128 v[88:91], v7 offset:15360\n.Lchv10_%=:\ns_memrealtime s[72:73]\ns_waitcnt lgkmcnt(0)\n.Lchv11_%=:\n" \
    "s_sub_u32 s72, s72, s63\ns_cmp_lt_i32 s72, -3\ns_cbranch_scc1 .Lchv10_%=\ns_cmp_ge_i32 s72, 0\ns_cbranch_scc1 .Lchv12_%=\ns_nop 2\ns_cmp_ge_i32 s72, -1\n" \
    "s_cbranch_scc1 .Lchv12_%=\ns_nop 2\ns_cmp_ge_i32 s72, -2\ns_cbranch_scc1 .Lchv12_%=\ns_nop 2\n.Lchv12_%=:\n" \
    "buffer_load_dwordx4 v[32:35], v2, s[64:67], s48 offen offset:0 sc1\ns_nop 12\nbuffer_load_dwordx4 v[36:39], v2, s[64:67], s48 offen offset:1024 sc1\ns_nop 12\n" \
    "buffer_load_dwordx4 v[40:43], v2, s[64:67], s48 offen offset:2048 sc1\ns_nop 12\nbuffer_load_dwordx4 v[44:47], v2, s[64:67], s48 offen offset:3072 sc1\n.Lchv9_%=:\n" \
    "v_mov_b64 v[30:31], 0\nv_mov_b64 v[28:29], 0\nv_mov_b64 v[26:27], 0\nv_mov_b64 v[24:25], 0\ns_add_u32 s77, s63, s44\ns_lshl_b32 s77, s77, 8\ns_add_u32 s77, s77, s61\n" \
    "s_sub_u32 s77, s77, 45055\ns_lshr_b32 s74, s48, 5\ns_and_b32 s74, s74, 128\ns_or_b32 s77, s77, s74\nv_mov_b32 v56, s77\ns_waitcnt vmcnt(3)\n" \
    "v_smfmac_f32_16x16x64_bf16 v[24:27], v[32:35], a[0:7], v0\nds_write_b128 v3, v[32:35] offset:0\nv_max3_u32 v252, v32, v33, v33\n" \
    "v_smfmac_f32_16x16x64_bf16 v[28:31], v[32:35], a[8:15], v0\ns_waitcnt vmcnt(2)\nv_max3_u32 v252, v252, v34, v35\n" \
    "v_smfmac_f32_16x16x64_bf16 v[24:27], v[36:39], a[16:23], v0\nds_write_b128 v3, v[36:39] offset:1024\nv_max3_u32 v252, v252, v36, v37\n" \
    "v_smfmac_f32_16x16x64_bf16 v[28:31], v[36:39], v[220:227], v0\ns_waitcnt vmcnt(1)\nv_max3_u32 v252, v252, v38, v39\n" \
    "v_smfmac_f32_16x16x64_bf16 v[24:27], v[40:43], a[24:31], v0\nds_write_b128 v3, v[40:43] offset:2048\nv_max3_u32 v252, v252, v40, v41\n" \
    "v_smfmac_f32_16x16x64_bf16 v[28:31], v[40:43], a[32:39], v0\ns_waitcnt lgkmcnt(1)\ns_barrier\nds_read_b128 v[48:51], v4 offset:0\nds_read_b128 v[52:55], v4 offset:1024\n" \
    "v_max3_u32 v252, v252, v42, v43\ns_waitcnt vmcnt(0)\nv_smfmac_f32_16x16x64_bf16 v[24:27], v[44:47], a[40:47], v0\nds_write_b128 v3, v[44:47] offset:3072\n" \
    "v_max3_u32 v252, v252, v44, v45\nv_smfmac_f32_16x16x64_bf16 v[28:31], v[44:47], v[228:235], v0\nds_read_b128 v[32:35], v5 offset:0\ns_waitcnt lgkmcnt(3)\n" \
    "v_max3_u32 v252, v252, v46, v47\nv_smfmac_f32_16x16x64_bf16 v[24:27], v[48:51], a[48:55], v0\nds_write_b32 v16, v252\n" \
    "v_smfmac_f32_16x16x64_bf16 v[28:31], v[48:51], a[56:63], v0\nds_read_b128 v[36:39], v5 offset:1024\ns_waitcnt lgkmcnt(4)\n" \
    "v_smfmac_f32_16x16x64_bf16 v[24:27], v[52:55], a[64:71], v0\nds_read_b128 v[40:43], v6 offset:0\nv_smfmac_f32_16x16x64_bf16 v[28:31], v[52:55], v[236:243], v0\n" \
    "ds_read_b128 v[44:47], v6 offset:1024\ns_waitcnt lgkmcnt(4)\nv_smfmac_f32_16x16x64_bf16 v[24:27], v[32:35], a[96:103], v0\n" \
    "v_smfmac_f32_16x16x64_bf16 v[28:31], v[32:35], a[104:111], v0\ns_waitcnt lgkmcnt(2)\ns_barrier\nv_smfmac_f32_16x16x64_bf16 v[24:27], v[36:39], a[112:119], v0\n" \
    "ds_read_b128 v[18:21], v17\nv_smfmac_f32_16x16x64_bf16 v[28:31], v[36:39], v[244:251], v0\nds_read_b128 v[48:51], v4 offset:2048\ns_waitcnt lgkmcnt(3)\n" \
    "v_smfmac_f32_16x16x64_bf16 v[24:27], v[40:43], a[144:151], v0\nds_read_b128 v[52:55], v4 offset:3072\nv_smfmac_f32_16x16x64_bf16 v[28:31], v[40:43], a[152:159], v0\n" \
    "ds_read_b128 v[32:35], v5 offset:2048\ns_waitcnt lgkmcnt(4)\nv_smfmac_f32_16x16x64_bf16 v[24:27], v[44:47], a[160:167], v0\ns_waitcnt lgkmcnt(3)\n" \
    "v_max3_u32 v18, v18, v19, v20\nv_smfmac_f32_16x16x64_bf16 v[28:31], v[44:47], v[60:67], v0\ns_waitcnt lgkmcnt(2)\nv_max_u32 v18, v18, v21\n" \
    "v_smfmac_f32_16x16x64_bf16 v[24:27], v[48:51], a[72:79], v0\nds_read_b128 v[36:39], v5 offset:3072\nv_cmp_eq_u32 vcc, -1, v18\n" \
    "v_smfmac_f32_16x16x64_bf16 v[28:31], v[48:51], a[80:87], v0\ns_waitcnt lgkmcnt(2)\ns_cbranch_vccnz .Lchv4_%=\nv_smfmac_f32_16x16x64_bf16 v[24:27], v[52:55], a[88:95], v0\n" \
    "buffer_store_dword v56, v14, s[64:67], s51 offen sc1\nv_smfmac_f32_16x16x64_bf16 v[28:31], v[52:55], v[68:75], v0\nds_read_b128 v[40:43], v6 offset:2048\n" \
    "s_waitcnt lgkmcnt(2)\nv_smfmac_f32_16x16x64_bf16 v[24:27], v[32:35], a[120:127], v0\nds_read_b128 v[44:47], v6 offset:3072\n" \
    "v_smfmac_f32_16x16x64_bf16 v[28:31], v[32:35], a[128:135], v0\nds_read_b128 v[60:63], v7 offset:16384\ns_waitcnt lgkmcnt(3)\n" \
    "v_smfmac_f32_16x16x64_bf16 v[24:27], v[36:39], a[136:143], v0\nbuffer_store_dword v56, v8, s[64:67], 0 offen sc1\n" \
    "v_smfmac_f32_16x16x64_bf16 v[28:31], v[36:39], v[76:83], v0\nds_read_b128 v[64:67], v7 offset:17408\ns_waitcnt lgkmcnt(3)\n" \
    "v_smfmac_f32_16x16x64_bf16 v[24:27], v[40:43], a[168:175], v0\nds_read_b128 v[68:71], v7 offset:18432\nv_smfmac_f32_16x16x64_bf16 v[28:31], v[40:43], a[176:183], v0\n" \
    "ds_read_b128 v[72:75], v7 offset:19456\ns_waitcnt lgkmcnt(4)\nv_smfmac_f32_16x16x64_bf16 v[24:27], v[44:47], a[184:191], v0\nds_read_b128 v[76:79], v7 offset:20480\n" \
    "v_smfmac_f32_16x16x64_bf16 v[28:31], v[44:47], v[84:91], v0\nds_read_b128 v[80:83], v7 offset:21504\nds_read_b128 v[84:87], v7 offset:22528\n" \
    "ds_read_b128 v[88:91], v7 offset:23552\nv_mov_b64 v[48:49], 0\nv_mov_b64 v[50:51], 0\nv_mov_b64 v[52:53], 0\nv_mov_b64 v[54:55], 0\ns_nop 0\n" \
    "v_permlane32_swap_b32 v24, v28\nv_permlane32_swap_b32 v25, v29\nv_permlane32_swap_b32 v26, v30\nv_permlane32_swap_b32 v27, v31\nv_pk_add_f32 v[24:25], v[24:25], v[28:29]\n" \
    "v_pk_add_f32 v[26:27], v[26:27], v[30:31]\nv_cvt_pk_bf16_f32 v24, v24, v25\nv_cvt_pk_bf16_f32 v25, v26, v27\nds_write_b64 v9, v[24:25]\ns_xor_b32 s74, s50, 0x10000\n" \
    "s_xor_b32 s75, s50, 0x20000\ns_xor_b32 s76, s50, 0x30000\ns_waitcnt lgkmcnt(0)\ns_barrier\nds_read_b64_tr_b16 v[32:33], v10 offset:0\n" \
    "ds_read_b64_tr_b16 v[34:35], v10 offset:32\nds_read_b64_tr_b16 v[36:37], v10 offset:256\nds_read_b64_tr_b16 v[38:39], v10 offset:288\n" \
    "ds_read_b64_tr_b16 v[40:41], v10 offset:512\nds_read_b64_tr_b16 v[42:43], v10 offset:544\nds_read_b64_tr_b16 v[44:45], v10 offset:768\n" \
    "ds_read_b64_tr_b16 v[46:47], v10 offset:800\nv_mov_b64 v[24:25], 0\nv_mov_b64 v[26:27], 0\nv_mov_b64 v[28:29], 0\nv_mov_b64 v[30:31], 0\ns_waitcnt lgkmcnt(6)\n" \
    "v_smfmac_f32_16x16x64_bf16 v[48:51], v[32:35], a[192:199], v0\nv_smfmac_f32_16x16x64_bf16 v[52:55], v[32:35], a[208:215], v0\ns_waitcnt lgkmcnt(4)\n" \
    "v_smfmac_f32_16x16x64_bf16 v[48:51], v[36:39], a[200:207], v0\nv_smfmac_f32_16x16x64_bf16 v[52:55], v[36:39], v[60:67], v0\ns_waitcnt lgkmcnt(2)\nv_mov_b64 v[60:61], 0\n" \
    "v_smfmac_f32_16x16x64_bf16 v[48:51], v[40:43], a[216:223], v0\nv_mov_b64 v[62:63], 0\nv_smfmac_f32_16x16x64_bf16 v[52:55], v[40:43], a[232:239], v0\ns_waitcnt lgkmcnt(0)\n" \
    "v_mov_b64 v[64:65], 0\nv_smfmac_f32_16x16x64_bf16 v[48:51], v[44:47], a[224:231], v0\nv_mov_b64 v[66:67], 0\nv_smfmac_f32_16x16x64_bf16 v[52:55], v[44:47], v[68:75], v0\n" \
    "v_mov_b64 v[68:69], 0\nv_smfmac_f32_16x16x64_bf16 v[24:27], v[32:35], a[240:247], v0\ns_nop 1\nv_mov_b64 v[70:71], 0\n" \
    "v_smfmac_f32_16x16x64_bf16 v[28:31], v[32:35], v[92:99], v0\ns_nop 1\nv_mov_b64 v[72:73], 0\nv_smfmac_f32_16x16x64_bf16 v[24:27], v[36:39], a[248:255], v0\n" \
    "v_cvt_pk_bf16_f32 v56, v48, v49\nv_smfmac_f32_16x16x64_bf16 v[28:31], v[36:39], v[76:83], v0\nds_read_b128 v[76:79], v7 offset:24576\nv_cvt_pk_bf16_f32 v57, v52, v53\n" \
    "v_smfmac_f32_16x16x64_bf16 v[24:27], v[40:43], v[100:107], v0\nds_read_b128 v[80:83], v7 offset:25600\nv_cvt_pk_bf16_f32 v58, v50, v51\n" \
    "v_smfmac_f32_16x16x64_bf16 v[28:31], v[40:43], v[116:123], v0\nv_cvt_pk_bf16_f32 v59, v54, v55\nv_smfmac_f32_16x16x64_bf16 v[24:27], v[44:47], v[108:115], v0\n" \
    "v_permlane16_swap_b32 v56, v57\nv_smfmac_f32_16x16x64_bf16 v[28:31], v[44:47], v[84:91], v0\nds_read_b128 v[84:87], v7 offset:26624\nv_permlane16_swap_b32 v58, v59\n" \
    "v_smfmac_f32_16x16x64_bf16 v[60:63], v[32:35], v[124:131], v0\nbuffer_store_dwordx4 v[56:59], v11, s[64:67], s50 offen sc1\n" \
    "v_smfmac_f32_16x16x64_bf16 v[64:67], v[32:35], v[140:147], v0\ns_nop 1\nv_mov_b64 v[74:75], 0\nv_smfmac_f32_16x16x64_bf16 v[60:63], v[36:39], v[132:139], v0\n" \
    "s_waitcnt lgkmcnt(1)\nv_cvt_pk_bf16_f32 v56, v24, v25\nv_smfmac_f32_16x16x64_bf16 v[64:67], v[36:39], v[76:83], v0\nds_read_b128 v[88:91], v7 offset:27648\n" \
    "v_cvt_pk_bf16_f32 v57, v28, v29\nv_smfmac_f32_16x16x64_bf16 v[60:63], v[40:43], v[148:155], v0\nds_read_b128 v[76:79], v7 offset:28672\nv_cvt_pk_bf16_f32 v58, v26, v27\n" \
    "v_smfmac_f32_16x16x64_bf16 v[64:67], v[40:43], v[164:171], v0\nds_read_b128 v[80:83], v7 offset:29696\nv_cvt_pk_bf16_f32 v59, v30, v31\n" \
    "v_smfmac_f32_16x16x64_bf16 v[60:63], v[44:47], v[156:163], v0\ns_waitcnt lgkmcnt(2)\nv_permlane16_swap_b32 v56, v57\n" \
    "v_smfmac_f32_16x16x64_bf16 v[64:67], v[44:47], v[84:91], v0\nds_read_b128 v[84:87], v7 offset:30720\nv_permlane16_swap_b32 v58, v59\n" \
    "v_smfmac_f32_16x16x64_bf16 v[68:71], v[32:35], v[172:179], v0\nbuffer_store_dwordx4 v[56:59], v11, s[64:67], s74 offen sc1\n" \
    "v_smfmac_f32_16x16x64_bf16 v[72:75], v[32:35], v[188:195], v0\ns_nop 1\nv_smfmac_f32_16x16x64_bf16 v[68:71], v[36:39], v[180:187], v0\ns_waitcnt lgkmcnt(1)\n" \
    "v_cvt_pk_bf16_f32 v56, v60, v61\nv_smfmac_f32_16x16x64_bf16 v[72:75], v[36:39], v[76:83], v0\nds_read_b128 v[88:91], v7 offset:31744\nv_cvt_pk_bf16_f32 v57, v64, v65\n" \
    "v_smfmac_f32_16x16x64_bf16 v[68:71], v[40:43], v[196:203], v0\nv_cvt_pk_bf16_f32 v58, v62, v63\nv_smfmac_f32_16x16x64_bf16 v[72:75], v[40:43], v[212:219], v0\n" \
    "v_cvt_pk_bf16_f32 v59, v66, v67\nv_smfmac_f32_16x16x64_bf16 v[68:71], v[44:47], v[204:211], v0\ns_waitcnt lgkmcnt(0)\nv_permlane16_swap_b32 v56, v57\n" \
    "v_smfmac_f32_16x16x64_bf16 v[72:75], v[44:47], v[84:91], v0\nv_permlane16_swap_b32 v58, v59\nbuffer_store_dwordx4 v[56:59], v11, s[64:67], s75 offen sc1\ns_nop 2\n" \
    "v_cvt_pk_bf16_f32 v48, v68, v69\nv_cvt_pk_bf16_f32 v50, v70, v71\ns_nop 0\nv_cvt_pk_bf16_f32 v49, v72, v73\nv_cvt_pk_bf16_f32 v51, v74, v75\ns_nop 0\n" \
    "v_permlane16_swap_b32 v48, v49\nv_permlane16_swap_b32 v50, v51\nbuffer_store_dwordx4 v[48:51], v11, s[64:67], s76 offen sc1\ns_cmp_eq_u32 s61, 63\n" \
    "s_cbranch_scc1 .Lchv2_%=\ns_memrealtime s[72:73]\ns_add_u32 s63, s63, s44\ns_waitcnt lgkmcnt(0)\ns_sub_u32 s69, s63, s72\ns_cmp_lt_i32 s69, s37\ns_cbranch_scc1 .Lchv13_%=\n" \
    ".Lchv14_%=:\ns_add_u32 s63, s63, s45\ns_mov_b32 s68, 0\ns_cmp_gt_i32 s60, s61\ns_cbranch_scc0 .Lchv5_%=\n.Lchv6_%=:\ns_add_u32 s61, s61, 1\ns_and_b32 s69, s61, %[rm]\n" \
    "s_cselect_b32 s78, 1, %[rw]\ns_cmp_lg_u32 s69, %[rm]\ns_cselect_b32 s79, 1, %[rw]\ns_lshl_b32 s77, s78, 18\ns_add_u32 s74, s40, s77\ns_xor_b32 s69, s74, s40\n" \
    "s_mov_b32 s40, s74\ns_lshr_b32 s69, s69, 8\ns_and_b32 s69, s69, 0x2000\ns_add_u32 s48, s48, s77\ns_xor_b32 s48, s48, s69\ns_sub_u32 s69, s61, 2\ns_max_i32 s69, s69, 0\n" \
    "s_lshl_b32 s69, s69, 20\ns_add_u32 s69, s69, s52\ns_add_u32 s74, s53, s69\ns_lshr_b32 s74, s74, 8\ns_xor_b32 s74, s74, s54\ns_and_b32 s74, s74, 0x2000\n" \
    "s_add_u32 s49, s74, s69\ns_lshl_b32 s77, s78, 20\ns_add_u32 s74, s42, s77\ns_xor_b32 s69, s74, s42\ns_mov_b32 s42, s74\ns_lshr_b32 s69, s69, 8\ns_and_b32 s69, s69, 0x2000\n" \
    "s_add_u32 s50, s50, s77\ns_xor_b32 s50, s50, s69\ns_lshl_b32 s77, s78, 19\ns_add_u32 s74, s41, s77\ns_xor_b32 s69, s74, s41\ns_mov_b32 s41, s74\ns_lshr_b32 s69, s69, 8\n" \
    "s_and_b32 s69, s69, 0x2000\ns_add_u32 s51, s51, s77\ns_xor_b32 s51, s51, s69\ns_cmp_eq_u32 s61, %[s1]\ns_cselect_b32 s69, %[a1], 0\ns_cselect_b32 s74, %[b1], 0\n" \
    "s_sub_u32 s44, s44, s69\ns_sub_u32 s45, s45, s74\ns_cmp_eq_u32 s61, %[s2]\ns_cselect_b32 s69, %[a2], 0\ns_cselect_b32 s74, %[b2], 0\ns_sub_u32 s44, s44, s69\n" \
    "s_sub_u32 s45, s45, s74\ns_branch .Lchv1_%=\n.Lchv4_%=:\ns_waitcnt lgkmcnt(0)\ns_barrier\ns_add_u32 s63, s63, s38\ns_xor_b32 s48, s48, 0x1000\ns_add_u32 s68, s68, 1\n" \
    "s_and_b32 s69, s68, 63\ns_cbranch_scc1 .Lchv3_%=\ns_cmp_gt_u32 s68, 0x100000\ns_cbranch_scc1 .Lchv8_%=\nv_mov_b32 v252, 0\n" \
    "buffer_load_dword v253, v252, s[64:67], 0 offen sc1\ns_waitcnt vmcnt(0)\nv_readfirstlane_b32 s69, v253\ns_cmp_eq_u32 s69, 1\ns_cbranch_scc0 .Lchv3_%=\ns_endpgm\n" \
    ".Lchv3_%=:\n.Lchv15_%=:\ns_memrealtime s[72:73]\ns_waitcnt lgkmcnt(0)\n.Lchv16_%=:\ns_sub_u32 s72, s72, s63\ns_cmp_lt_i32 s72, -3\ns_cbranch_scc1 .Lchv15_%=\n" \
    "s_cmp_ge_i32 s72, 0\ns_cbranch_scc1 .Lchv17_%=\ns_nop 2\ns_cmp_ge_i32 s72, -1\ns_cbranch_scc1 .Lchv17_%=\ns_nop 2\ns_cmp_ge_i32 s72, -2\ns_cbranch_scc1 .Lchv17_%=\n" \
    "s_nop 2\n.Lchv17_%=:\nbuffer_load_dwordx4 v[32:35], v2, s[64:67], s48 offen offset:0 sc1\ns_nop 12\nbuffer_load_dwordx4 v[36:39], v2, s[64:67], s48 offen offset:1024 sc1\n" \
    "s_nop 12\nbuffer_load_dwordx4 v[40:43], v2, s[64:67], s48 offen offset:2048 sc1\ns_nop 12\nbuffer_load_dwordx4 v[44:47], v2, s[64:67], s48 offen offset:3072 sc1\n" \
    "ds_read_b128 v[220:223], v7 offset:0\nds_read_b128 v[224:227], v7 offset:1024\nds_read_b128 v[228:231], v7 offset:2048\nds_read_b128 v[232:235], v7 offset:3072\n" \
    "ds_read_b128 v[236:239], v7 offset:4096\nds_read_b128 v[240:243], v7 offset:5120\nds_read_b128 v[244:247], v7 offset:8192\nds_read_b128 v[248:251], v7 offset:9216\n" \
    "ds_read_b128 v[60:63], v7 offset:12288\nds_read_b128 v[64:67], v7 offset:13312\nds_read_b128 v[68:71], v7 offset:6144\nds_read_b128 v[72:75], v7 offset:7168\n" \
    "ds_read_b128 v[76:79], v7 offset:10240\nds_read_b128 v[80:83], v7 offset:11264\ns_waitcnt lgkmcnt(12)\nds_read_b128 v[84:87], v7 offset:14336\n" \
    "ds_read_b128 v[88:91], v7 offset:15360\ns_waitcnt lgkmcnt(0)\ns_branch .Lchv9_%=\n.Lchv5_%=:\nbuffer_load_dword v22, v12, s[64:67], 0 offen sc1\ns_waitcnt vmcnt(0)\n" \
    "v_readfirstlane_b32 s74, v22\ns_max_i32 s60, s60, s74\ns_cmp_gt_i32 s60, s61\ns_cbranch_scc1 .Lchv18_%=\ns_sleep 4\ns_add_u32 s68, s68, 1\ns_and_b32 s69, s68, 63\n" \
    "s_cbranch_scc1 .Lchv5_%=\ns_cmp_gt_u32 s68, 0x100000\ns_cbranch_scc1 .Lchv8_%=\nv_mov_b32 v252, 0\nbuffer_load_dword v253, v252, s[64:67], 0 offen sc1\ns_waitcnt vmcnt(0)\n" \
    "v_readfirstlane_b32 s69, v253\ns_cmp_eq_u32 s69, 1\ns_cbranch_scc0 .Lchv5_%=\ns_endpgm\n.Lchv18_%=:\ns_memrealtime s[72:73]\ns_waitcnt lgkmcnt(0)\n.Lchv19_%=:\n" \
    "s_sub_u32 s69, s63, s72\ns_cmp_ge_i32 s69, 20\ns_cbranch_scc1 .Lchv6_%=\ns_add_u32 s63, s63, s38\ns_xor_b32 s48, s48, 0x1000\ns_branch .Lchv19_%=\n.Lchv13_%=:\n" \
    "s_add_u32 s63, s63, s38\ns_xor_b32 s48, s48, 0x1000\ns_sub_u32 s69, s63, s72\ns_cmp_lt_i32 s69, s37\ns_cbranch_scc1 .Lchv13_%=\ns_branch .Lchv14_%=\n.Lchv8_%=:\n" \
    "v_mov_b32 v252, 0\nv_mov_b32 v253, 1\nbuffer_store_dword v253, v252, s[64:67], 0 offen sc1\ns_endpgm\n.Lchv2_%=:\ns_waitcnt vmcnt(0)\n"
typedef unsigned v32u __attribute__((ext_vector_type(32)));
typedef unsigned v16u __attribute__((ext_vector_type(16)));
template <bool isV>
__device__ __forceinline__ void chain_main(const Args& a, unsigned char* lds, const int xg, const int ii, int wave) {
    asm volatile("" : "+s"(wave));
    int tid = wave * 64 + lane_id(); asm volatile("" : "+v"(tid));
    const int head = xg * 4 + (ii >> 1);
    char* const ws = a.ws;
    unsigned* const ctl = (unsigned*)(ws + O_CTL);
    v16bf w[48];
    {
        const int lane = tid & 63, l15 = lane & 15, gb = lane >> 4;
        auto bufp = [&](int u, int wv) { const int b = (u + 4) % 5; return lds + (b < 4 ? L_W + wv * 32768 + b * 8192 : L_KX + wv * 8192); };
        v4u rw[2] = {mkrs(isV ? a.Wv : a.Wk, DM * HD * 2), mkrs(isV ? a.Wo : a.Wq, DM * HD * 2)};
        asm volatile("s_nop 4" : "+s"(rw[0]), "+s"(rw[1]));
        auto issue = [&](int u) {
            const unsigned buf = (unsigned)(size_t)(LAS unsigned char*)bufp(u, wave);
            const int c = u & 7;
#pragma unroll
            for (int r8 = 0; r8 < 8; ++r8) {
                const unsigned ch = (lane & 7) ^ (((2 * r8 + (lane >> 5)) & 3) << 1);
                if (!iswo(isV, u)) bdma<(NTW & (isV ? 64 : 2))>(rw[!isV && (u & 1)], ((lane >> 3) * HD + 8 * ch) * 2, ((64 * (4 * (frag(isV, u) & 3) + (wave ^ (frag(isV, u) >> 2))) + 8 * r8) * HD + head * DH + 64 * (wave >> 1)) * 2, buf + r8 * 1024);
                else bdma<(NTW & 32)>(rw[1], ((lane >> 3) * DM + 8 * ch) * 2, ((head * DH + 64 * (c & 1) + 8 * r8) * DM + 256 * wave + 64 * (c >> 1)) * 2, buf + r8 * 1024);
            }
        };
        const unsigned q = l15 >> 2, p = lane & 3;
        const unsigned row0 = 16 * (gb >> 1) + 4 * (gb & 1) + (q & 1) + 8 * (q >> 1);
        const unsigned sw = ((gb & 1) | ((q >> 1) << 1)) << 1;
        v16bf pend[16];
        auto flush = [&](int j) {
            const v8u v = __builtin_bit_cast(v8u, pend[j]);
            unsigned char* d = lds + L_W + wave * 32768 + j * 2048 + lane * 16;
            *(LAS v4u*)d = (v4u){v[0], v[1], v[2], v[3]};
            *(LAS v4u*)(d + 1024) = (v4u){v[4], v[5], v[6], v[7]};
        };
        issue(0); issue(1); issue(2); issue(3);
        sfor<0, 17>([&](auto cc) {
            constexpr int u = decltype(cc)::v;
            asm volatile("s_waitcnt vmcnt(%0)\n\ts_barrier" :: "n"(u < 13 ? 24 : u < 16 ? 8 * (15 - u) : 0) : "memory");
            if (u + 4 < 16) issue(u + 4);
            if constexpr (u < 16) {
                v16bf t[4];
#pragma unroll
                for (int n = 0; n < 4; ++n) {
                    constexpr bool pk = iswo(isV, u);
                    unsigned char* buf = bufp(u, pk ? wave : wave ^ (n >> 1));
                    const unsigned o = pk ? (32 * (n & 1) + 4 * gb + (q & 1)) * 128 + (((4 * (n >> 1) + 2 * (q >> 1) + (p >> 1)) ^ (gb << 1)) << 4) + (p & 1) * 8
                                          : row0 * 128 + (((4 * (wave & 1) + 2 * (n & 1) + (p >> 1)) ^ sw) << 4) + (p & 1) * 8;
                    const v2u r0 = tr_rd(buf, o), r1 = tr_rd(buf, o + 2 * 128), r2 = tr_rd(buf, o + (pk ? 16 : 32) * 128), r3 = tr_rd(buf, o + (pk ? 18 : 34) * 128);
                    const v8u v = {r0.x, r0.y, r1.x, r1.y, r2.x, r2.y, r3.x, r3.y};
                    t[n] = __builtin_bit_cast(v16bf, v);
                }
                asm volatile("s_waitcnt lgkmcnt(0)" ::: "memory");
                sfor<0, 4>([&](auto nn) {
                    constexpr int n = decltype(nn)::v, c = tile(isV, u, n) >> 2, r = 3 * c + (tile(isV, u, n) & 3);
                    if constexpr ((tile(isV, u, n) & 3) == 3) { pend[c] = t[n]; asm volatile("" : "+v"(pend[c])); }
                    else {
                        w[r] = t[n];
                        if constexpr (r < 32) asm volatile("" : "+a"(w[r])); else asm volatile("" : "+v"(w[r]));
                    }
                });
            }
            sfor<0, 16>([&](auto jj) { if constexpr (flat(isV, decltype(jj)::v) == u) flush(decltype(jj)::v); });
        });
        asm volatile("s_waitcnt vmcnt(0) lgkmcnt(0)\n\ts_barrier" ::: "memory");
    }
    { const int s = 0; STAMP(8); }
    auto olane = [&]() { int l = tid & 63; asm volatile("" : "+v"(l)); return l; };
    const unsigned pol = __builtin_amdgcn_readfirstlane(*(const unsigned*)(ws + O_POL));
    const v4u rws = mkrs(ws, WS_END);
    const unsigned lb0 = (unsigned)(size_t)(LAS unsigned char*)lds;
    auto pwo = [&](int l) { return (C_X + 32 * (l < 32 ? l : 64 + head)) * 4; };
    int rdy = 0;
    if (wave == 0) {
        const int l = olane();
        for (unsigned spins = 0;;) {
            const int c = (int)ldg4_sc1(ws, pwo(l));
            if (!__any(c < 1)) { rdy = 1; break; }
            if (spin_fail(spins, ctl + C_ABORT)) break;
            __builtin_amdgcn_s_sleep(8);
        }
        *(volatile LAS int*)(lds + L_RED) = rdy;
    }
    lds_barrier();
    rdy = __builtin_amdgcn_readfirstlane(*(volatile LAS int*)(lds + L_RED));
    if (!rdy) asm volatile("s_endpgm");
    { const int s = 0; STAMP(9); }
    const unsigned kx0 = O_KV + head * 16384;
    if (!isV) { const int l = olane(); for (int i = 0; i < 2; ++i) bdma<16>(rws, l * 16, nearg(ws, pol, kx0, 1) + wave * 2048 + i * 1024, lb0 + L_KX + wave * 2048 + i * 1024); }
    {
        const unsigned lane = olane(), l15 = lane & 15, g = lane >> 4, q = l15 >> 2, p = lane & 3, l16 = lane * 16, to = (8 * g + q) * 8 + (p & 1) * 1152;
        const v16u c0 = {(lane & 8) ? 0xEEEEu : 0x4444u, l16, g * 256 + (l15 & 7) * 32 + (l15 >> 3) * 16, lb0 + L_MEM + wave * 4096 + l16,
                         lb0 + L_MEM + (wave ^ 1) * 4096 + l16, lb0 + L_MEM + (wave ^ 2) * 4096 + l16, lb0 + L_MEM + (wave ^ 3) * 4096 + l16,
                         lb0 + L_W + wave * 32768 + l16, !isV ? lb0 + L_KX + g * 512 + l15 * 16 : ii == 1 && wave == 0 && lane == 0 ? (C_C + 32 * xg) * 4 : 0x7F000000u, lb0 + L_T + (32 * wave + 16 * (g >> 1) + l15) * 8 + (g & 1) * 1152,
                         lb0 + L_T + to,
                         isV ? (((g & 1) << 1) | (g >> 1)) * 16384 + l15 * 16 : l15 < 8 ? (4 * wave + (head >> 3)) * 16384 + (head & 7) * 512 + g * 128 + l15 * 16 : (16 + wave) * 16384 + head * 128 + (l15 & 7) * 16,
                         nearg(ws, pol, (C_R + 32 * xg) * 4, 0), lb0 + L_T + 2560 + to + wave * 256, !isV ? l15 * 64 + g * 16 : ii == 1 && wave == 1 && lane == 0 ? xg * 128 : 0x7F000000u, isV && ((head & 7) * 4 + wave >= 20 || lane >= 32) ? 0x7F000000u : lane * 128};
        const unsigned par = a.pad0, R = par >> 26, blo = (unsigned)(size_t)ws;
        const unsigned n0 = __builtin_amdgcn_readfirstlane((unsigned)__builtin_amdgcn_s_memrealtime() / R), tc = n0 & 1, cr = xg >> 1;
        v16u sv = {nearg(ws, pol, O_E1 + (cr * 4 + wave) * 16384, 0) + tc * 4096, MiB,
                   isV ? nearg(ws, pol, O_UM + wave * 262144, 0) + head * 256 : nearg(ws, pol, O_EP, 0) + tc * 4096,
                   isV ? nearg(ws, pol, O_EP + 20 * 16384, 0) : kmof(ws, pol, 0, head) + wave * 512, isV ? O_D + ((head & 7) * 2 + (wave >> 1)) * 16384 + (wave & 1) * 4096 : O_KV + ((head & 7) * 4 + wave) * 16384, blo, pol, 384 * MiB + ((xg * 8 + ii) * 4 + wave) * 262144u,
                   nearg(ws, pol, kx0 + KV_SLOT, 1) + wave * 2048, 0u, lb0 + L_KX + wave * 2048,
                   (unsigned)wave, 0u, 0u, lb0 + L_RED + 1024 + (wave == STAMP_WAVE ? 0 : MiB), n0 * R};
        v4u sa = {0u, (par >> 21) & 31, R, 1u};
        unsigned sk = blo + O_KV + KV_SLOT;
        v8u sb = {blo + O_E1, blo + O_EP, blo + O_UM, 0u, par & 255, (par >> 8) & 255, 0x00FFFFFFu, 0x00FF00FFu};
        v32u ww[12];
#pragma unroll
        for (int i = 0; i < 12; ++i) {
            const v8u x0 = __builtin_bit_cast(v8u, w[4 * i]), x1 = __builtin_bit_cast(v8u, w[4 * i + 1]), x2 = __builtin_bit_cast(v8u, w[4 * i + 2]), x3 = __builtin_bit_cast(v8u, w[4 * i + 3]);
            ww[i] = __builtin_shufflevector(__builtin_shufflevector(x0, x1, 0, 1, 2, 3, 4, 5, 6, 7, 8, 9, 10, 11, 12, 13, 14, 15), __builtin_shufflevector(x2, x3, 0, 1, 2, 3, 4, 5, 6, 7, 8, 9, 10, 11, 12, 13, 14, 15),
                                            0, 1, 2, 3, 4, 5, 6, 7, 8, 9, 10, 11, 12, 13, 14, 15, 16, 17, 18, 19, 20, 21, 22, 23, 24, 25, 26, 27, 28, 29, 30, 31);
        }
#define CHAIN_CALL(TXT) asm volatile(TXT : "+{s[48:63]}"(sv), "+{s[36:39]}"(sa), "+{s[40:47]}"(sb), "+{s35}"(sk) \
            : "{v[0:15]}"(c0), "{v16}"(lb0 + L_RED + l16 + 4 * wave), "{v17}"(lb0 + L_RED + l16), "{s[64:67]}"(rws), [s1] "n"(CH_S1), [a1] "n"(CH_A1), [b1] "n"(CH_B1), [s2] "n"(CH_S2), [a2] "n"(CH_A2), [b2] "n"(CH_B2), [rm] "n"(RG - 1), [rw] "n"(1 - RG), "{s[32:33]}"(0x3e0293eeull), \
              "{a[0:31]}"(ww[0]), "{a[32:63]}"(ww[1]), "{a[64:95]}"(ww[2]), "{a[96:127]}"(ww[3]), "{a[128:159]}"(ww[4]), "{a[160:191]}"(ww[5]), "{a[192:223]}"(ww[6]), "{a[224:255]}"(ww[7]), \
              "{v[92:123]}"(ww[8]), "{v[124:155]}"(ww[9]), "{v[156:187]}"(ww[10]), "{v[188:219]}"(ww[11]) \
            : "memory", "vcc", "scc", "m0", "v18", "v19", "v20", "v21", "v22", "v24", "v25", "v26", "v27", "v28", "v29", "v30", "v31", "v32", "v33", "v34", "v35", "v36", "v37", "v38", "v39", "v40", "v41", "v42", "v43", "v44", "v45", "v46", "v47", "v48", "v49", "v50", "v51", "v52", "v53", "v54", "v55", "v56", "v57", "v58", "v59", "v60", "v61", "v62", "v63", "v64", "v65", "v66", "v67", "v68", "v69", "v70", "v71", "v72", "v73", "v74", "v75", "v76", "v77", "v78", "v79", "v80", "v81", "v82", "v83", "v84", "v85", "v86", "v87", "v88", "v89", "v90", "v91", "v220", "v221", "v222", "v223", "v224", "v225", "v226", "v227", "v228", "v229", "v230", "v231", "v232", "v233", "v234", "v235", "v236", "v237", "v238", "v239", "v240", "v241", "v242", "v243", "v244", "v245", "v246", "v247", "v248", "v249", "v250", "v251", "v252", "v253", "s68", "s69", "s70", "s71", "s72", "s73", "s74", "s75", "s76", "s77", "s78", "s79")
        if constexpr (isV) CHAIN_CALL(CHAIN_V); else CHAIN_CALL(CHAIN_QK);
    }
    tid = wave * 64 + lane_id();
    asm volatile("s_waitcnt vmcnt(0)\n\ts_barrier" ::: "memory");
}

typedef __amdgpu_buffer_rsrc_t rsrc_t;
__device__ __forceinline__ v4u bld16(rsrc_t r, unsigned off) { return __builtin_bit_cast(v4u, __builtin_amdgcn_raw_buffer_load_b128(r, off, 0, 16)); }
__device__ __forceinline__ v2u bld8(rsrc_t r, unsigned off) { return __builtin_bit_cast(v2u, __builtin_amdgcn_raw_buffer_load_b64(r, off, 0, 16)); }
__device__ __forceinline__ void bst16(rsrc_t r, unsigned off, v4u v) { __builtin_amdgcn_raw_buffer_store_b128(__builtin_bit_cast(__attribute__((ext_vector_type(4))) unsigned, v), r, off, 0, 16); }
__device__ __forceinline__ void bst8(rsrc_t r, unsigned off, v2u v) { __builtin_amdgcn_raw_buffer_store_b64(__builtin_bit_cast(__attribute__((ext_vector_type(2))) unsigned, v), r, off, 0, 16); }

constexpr int L_FLAG = LDS_BYTES - 64;
#define otid() (a.pad0 * 64 + lane_id())
__device__ __forceinline__ v4u sld16(rsrc_t r, unsigned vo, unsigned so) { return __builtin_bit_cast(v4u, __builtin_amdgcn_raw_buffer_load_b128(r, vo, so, 16)); }
__device__ __forceinline__ v4u sentv() { unsigned x = SENT; asm volatile("" : "+v"(x)); return (v4u){x, x, x, x}; }
#define BW_SLEEP 16
template <int SLP = BW_SLEEP> __device__ __forceinline__ bool wave_wait(const unsigned* p, int stride, int n, unsigned target, unsigned* abortw, unsigned char* lds = nullptr) {
    const int lane = lane_id();
    for (unsigned spins = 0;;) {
        const unsigned v = lane < n ? __hip_atomic_load((gu32*)(p + lane * stride), RLX_AGENT) : target;
        if (!__any(v < target)) { if (lds && lane == 0) *(volatile LAS unsigned*)(lds + L_FLAG + 32) = v; return true; }
        __builtin_amdgcn_s_sleep(SLP);
        if (++spins > (SPIN_CAP << 1)) { __hip_atomic_store((gu32*)abortw, 1u, RLX_AGENT); return false; }
        if ((spins & 63u) == 0 && __builtin_amdgcn_readfirstlane(__hip_atomic_load((gu32*)abortw, RLX_AGENT)) == 1u) return false;
    }
}
__device__ __forceinline__ float gelu_tanh(float v) {
    const float e = v * (-2.302208198f - 0.1029432397f * (v * v));
    return v * __builtin_amdgcn_rcpf(1.f + __builtin_amdgcn_exp2f(e));
}
__device__ __forceinline__ v4u widen(v2u x, v2u y) {
    const auto s0 = __builtin_amdgcn_permlane16_swap(x.x, y.x, false, false);
    const auto s1 = __builtin_amdgcn_permlane16_swap(x.y, y.y, false, false);
    return (v4u){s0[0], s1[0], s0[1], s1[1]};
}
#define TD_P 190
#define TD_O 290
__device__ __forceinline__ v4u unsent4(v4u v) { return (v4u){unsent(v.x), unsent(v.y), unsent(v.z), unsent(v.w)}; }
__device__ __forceinline__ unsigned rt_now() { return (unsigned)__builtin_amdgcn_s_memrealtime(); }
__device__ __forceinline__ void rt_delay(unsigned ticks  , unsigned t0 = rt_now()) {
    while ((unsigned)__builtin_amdgcn_s_memrealtime() - t0 < ticks) __builtin_amdgcn_s_sleep(2);
}
__device__ __forceinline__ float wave_sum(float x) {
    x += __uint_as_float(__builtin_amdgcn_update_dpp(0, __float_as_uint(x), 0xB1, 0xF, 0xF, false));
    x += __uint_as_float(__builtin_amdgcn_update_dpp(0, __float_as_uint(x), 0x4E, 0xF, 0xF, false));
    x += __uint_as_float(__builtin_amdgcn_update_dpp(0, __float_as_uint(x), 0x124, 0xF, 0xF, false));
    x += __uint_as_float(__builtin_amdgcn_update_dpp(0, __float_as_uint(x), 0x128, 0xF, 0xF, false));
    return red32_add(red16_add(x));
}
__device__ __forceinline__ void ln_row(const v4u (&xr)[2], const f32x4 (&c1)[4], const f32x4 (&sh)[4], v4u (&o)[2]) {
    f32x2 v[8];
    f32x2 s2 = {0.f, 0.f};
#pragma unroll
    for (int j = 0; j < 8; ++j) { v[j] = (f32x2){lo16f(xr[j >> 2][j & 3]), hi16f(xr[j >> 2][j & 3])}; s2 += v[j]; }
    const float mean = wave_sum(s2[0] + s2[1]) * (1.f / DM);
    f32x2 q2 = {0.f, 0.f};
#pragma unroll
    for (int j = 0; j < 8; ++j) { const f32x2 d = v[j] - mean; q2 += d * d; }
    const float vs = wave_sum(q2[0] + q2[1]);
    const float mb = rbf(mean), vb = rbf(vs * (1.f / DM));
    const float rsd = 1.f / rbf(sqrtf(rbf(vb + 1e-5f)));
    auto r2 = [](f32x2 z) { const unsigned u = pack2(z[0], z[1]); return (f32x2){lo16f(u), hi16f(u)}; };
#pragma unroll
    for (int j = 0; j < 8; ++j) {
        const f32x2 cc = {c1[j >> 1][2 * (j & 1)], c1[j >> 1][2 * (j & 1) + 1]}, ss = {sh[j >> 1][2 * (j & 1)], sh[j >> 1][2 * (j & 1) + 1]};
        const f32x2 y = r2(r2(r2(v[j] - mb) * rsd) * cc) + ss;
        o[j >> 2][j & 3] = pack2(y[0], y[1]);
    }
}
template <int N> __device__ __forceinline__ void ld_mod(rsrc_t rs, unsigned* ctl, const int (&col)[N], f32x4* m) {
    for (unsigned spins = 0;;) {
        unsigned mx = 0;
#pragma unroll
        for (int i = 0; i < N; ++i) { const v4u v = bld16(rs, O_MOD + col[i] * 4); mx = max(mx, umax4(v)); m[i] = __builtin_bit_cast(f32x4, v); }
        if (!__any(mx == SENT)) break;
        __builtin_amdgcn_s_sleep(6);
        if (spin_fail(spins, ctl + C_ABORT)) asm volatile("s_endpgm");
    }
}
__device__ __forceinline__ void cp_delay(unsigned ticks, unsigned cw) {
    while ((((unsigned)__builtin_amdgcn_s_memrealtime() - (cw >> 8)) & 0xFFFFFFu) < ticks) __builtin_amdgcn_s_sleep(1);
}
__device__ __forceinline__ void set_m0(unsigned v) { asm volatile("s_mov_b32 m0, %0" :: "s"(v) : "m0"); }
__device__ __forceinline__ float sum8(float x) {
    x += __uint_as_float(__builtin_amdgcn_update_dpp(0, __float_as_uint(x), 0x128, 0xF, 0xF, false));
    return red32_add(red16_add(x));
}
__device__ __forceinline__ void mod8(const Args& a, int col, f32x4 (&m)[2]) {
    f32x4 p[8][2];
#pragma unroll
    for (int k = 0; k < 16; ++k) p[k >> 1][k & 1] = *(const f32x4*)(a.ws + O_MODP + ((k >> 1) * 3072 + col + 4 * (k & 1)) * 4);
#pragma unroll
    for (int h = 0; h < 2; ++h) {
        const f32x4 t = ((p[0][h] + p[1][h]) + (p[2][h] + p[3][h])) + ((p[4][h] + p[5][h]) + (p[6][h] + p[7][h]));
#pragma unroll
        for (int i = 0; i < 4; ++i) { float v = rbf(t[i]); if ((col >> 10) == 1) v = rbf(1.f + v); m[h][i] = v; }
    }
}
__device__ __forceinline__ void mod_cols(const Args& a, rsrc_t rs, unsigned char* lds, int cg) {
    const int tid = otid();
    v4u wv[32];
#pragma unroll
    for (int i = 0; i < 32; ++i) wv[i] = __builtin_nontemporal_load((const v4u*)(a.w_mod + (size_t)((tid >> 3) + 32 * i) * 6144 + cg * 64 + 8 * (tid & 7)));
    f32x2 acc[4] = {{0.f, 0.f}, {0.f, 0.f}, {0.f, 0.f}, {0.f, 0.f}};
#pragma unroll
    for (int i = 0; i < 32; ++i) {
        const float sv = *(const LAS float*)(lds + 16384 + ((tid >> 3) + 32 * i) * 4);
#pragma unroll
        for (int j = 0; j < 4; ++j) acc[j] += sv * (f32x2){lo16f(wv[i][j]), hi16f(wv[i][j])};
    }
    unsigned char* red = lds + 20480;
    lds_barrier();
    *(LAS f32x4*)(red + (tid >> 3) * 272 + (tid & 7) * 32) = (f32x4){acc[0][0], acc[0][1], acc[1][0], acc[1][1]};
    *(LAS f32x4*)(red + (tid >> 3) * 272 + (tid & 7) * 32 + 16) = (f32x4){acc[2][0], acc[2][1], acc[3][0], acc[3][1]};
    lds_barrier();
    if (tid < 64) {
        f32x4 t = {0.f, 0.f, 0.f, 0.f};
#pragma unroll
        for (int i = 0; i < 8; ++i) t += *(const LAS f32x4*)(red + ((tid >> 4) * 8 + i) * 272 + (tid & 15) * 16);
#pragma unroll
        for (int i = 0; i < 4; ++i) { float v = rbf(red32_add(red16_add(t[i]))); if ((cg >> 4) == 4) v = rbf(1.f + v); t[i] = v; }
        if (tid < 16) bst16(rs, O_MOD + (cg * 64 + 4 * tid) * 4, unsent4(__builtin_bit_cast(v4u, t)));
    }
}
#define TCH 1
#define TOF 0
#define TK0 512
__device__ __forceinline__ void touch(const Args& a, unsigned char* lds, int xg, int wi) {
    const int lane = lane_id();
    constexpr int NU = (1024 - TK0) / 8 * 3;
    set_m0((unsigned)(size_t)(LAS unsigned char*)lds + LDS_BYTES - 1024);
    for (int v = wi; v < NU; v += 32) {
        const int u = TOF ? (v < 128 ? NU + v : v - 128) : v;
        const int m = u < NU ? u % 3 : 3;
        v4u r = mkrs(m == 0 ? a.Wv : m == 1 ? a.Wk : m == 2 ? a.Wq : a.Wo, DM * HD * 2);
        const unsigned vo = m < 3 ? (lane >> 3) * 8192 + (lane & 7) * 128 : (lane >> 1) * 2048 + (lane & 1) * 1024;
        const unsigned so = m < 3 ? (TK0 + 8 * (u / 3)) * 8192 + xg * 1024 : (512 * xg + 32 * ((u - NU) & 15)) * 2048 + (((u - NU) >> 4) & 1) * 512 + ((u - NU) >> 5) * 128;
        if (TCH == 1) asm volatile("s_nop 4\n\tbuffer_load_dword %0, %1, %2 offen lds" :: "v"(vo), "s"(r), "s"(so));
        else asm volatile("s_nop 4\n\tbuffer_load_dword %0, %1, %2 offen nt lds" :: "v"(vo), "s"(r), "s"(so));
    }
}
#define GO(kind, x) (C_G + ((kind) * 8 + (x)) * 32)
__device__ __forceinline__ void monitor(const Args& a, unsigned* ctl) {
    const int lane = lane_id();
    unsigned* const cr = (unsigned*)(a.ws + nearg(a.ws, __builtin_amdgcn_readfirstlane(*(const unsigned*)(a.ws + O_POL)), C_R * 4, 0));
    int last = 0, lv = 0;
    for (unsigned spins = 0; last < NSEG;) {
        const int v = (int)__hip_atomic_load((gu32*)PW_V(lane + (lane & 32)), RLX_AGENT) - 1;
        int r = last, rv = lv;
        while (r < NSEG && !__any(v <= r)) ++r;
        while (rv <= NSEG && !__any(lane < 32 && v < rv)) ++rv;
        if (r > last || rv > lv) { last = r; lv = rv; spins = 0; if (lane < 8) __hip_atomic_store((GAS u64*)(cr + 32 * lane), (u64)rv << 32 | (unsigned)r, RLX_AGENT); }
        if (last < 6) __builtin_amdgcn_s_sleep(2); else __builtin_amdgcn_s_sleep(12);
        if (spin_fail(spins, ctl + C_ABORT)) return;
    }
}
#define TD_N 1300
#define M2D 150
__device__ __forceinline__ void monitor2(unsigned* ctl) {
    const int lane = lane_id();
    for (int sg = 0; sg < NSEG - 4; ++sg) {
        unsigned spins = 0;
        while (__any((int)__hip_atomic_load((gu32*)(PW_U(lane & 31) + 1024 * 3 * (sg & 1)), RLX_AGENT) <= sg)) {
            __builtin_amdgcn_s_sleep(1);
            if (spin_fail(spins, ctl + C_ABORT)) return;
        }
        if (lane < 8) __hip_atomic_fetch_max((gu32*)(ctl + GO(1, lane) + (sg & 1)), (unsigned)sg + 1, RLX_AGENT);
        if (spins) rt_delay(M2D);
    }
}

__device__ __forceinline__ void ln1_rows(const Args& a, rsrc_t rs, unsigned char* lds, int idx  ) {
    const int tid = otid(), lane = tid & 63, wave = a.pad0;
    unsigned* const ctl = (unsigned*)(a.ws + O_CTL);
    const unsigned pol = __builtin_amdgcn_readfirstlane(*(const unsigned*)(a.ws + O_POL));
    const unsigned t0 = rt_now();
    const int r0 = 4 * idx + wave, nrow = 8;
    v4u xr[2];
#pragma unroll
    for (int c = 0; c < 2; ++c) xr[c] = *(const v4u*)(a.x + (size_t)r0 * DM + 512 * c + 8 * lane);
    {
        f32x4 m[2];
        mod8(a, 8 * tid, m);
        *(LAS f32x4*)(lds + 32 * tid) = m[0]; *(LAS f32x4*)(lds + 32 * tid + 16) = m[1];
        const v2u cr = *(const v2u*)(a.c + 4 * tid);
        f32x4 sv = {lo16f(cr.x), hi16f(cr.x), lo16f(cr.y), hi16f(cr.y)};
#pragma unroll
        for (int i = 0; i < 4; ++i) sv[i] = rbf(sv[i] / (1.f + __expf(-sv[i])));
        *(LAS f32x4*)(lds + 16384 + tid * 16) = sv;
    }
    lds_barrier();
    f32x4 c1[4], sh[4];
#pragma unroll
    for (int i = 0; i < 4; ++i) { const int col = 512 * (i >> 1) + 8 * lane + 4 * (i & 1); sh[i] = *(const LAS f32x4*)(lds + col * 4); c1[i] = *(const LAS f32x4*)(lds + (DM + col) * 4); }
    constexpr int MON = 51;
    const int nn = r0 == 4 * MON + 1 ? 2 * nrow - 1 : nrow;
    auto rowof = [&](int k) { return k < nrow ? r0 + 256 * k : r0 - 1 + 256 * (k - nrow + 1); };
    for (int k = 0; k < nn; ++k) {
        const int row = rowof(k);
        v4u o[2], xn[2];
#pragma unroll
        for (int c = 0; c < 2; ++c) xn[c] = *(const v4u*)(a.x + (size_t)min(rowof(k + 1), S_TOK - 1) * DM + 512 * c + 8 * lane);
        ln_row(xr, c1, sh, o);
#pragma unroll
        for (int c = 0; c < 2; ++c) { bst16(rs, nearg(a.ws, pol, O_D + (unsigned)(row >> 5) * D_SLOT + D_H1 + ((row >> 2) & 7) * 16384, 1) + (row & 3) * 2048 + 1024 * c + 16 * lane, o[c]); xr[c] = xn[c]; }
        if (k == 0) {
            wait_vm0();
            if (lane == 0) __hip_atomic_fetch_add((gu32*)(ctl + C_H + 32 * (r0 >> 5)), 1u, RLX_AGENT);
            RSTAMP(75, 2);
            if (TCH) touch(a, lds, idx & 7, (idx >> 3) * 4 + wave);
            if (r0 == 4 * MON) { monitor(a, ctl); asm volatile("s_endpgm"); }
            rt_delay(TD_N, t0);
        }
    }
    wait_vm0();
    if (lane >= 1 && lane < nn) __hip_atomic_fetch_add((gu32*)(ctl + C_H + 32 * (rowof(lane) >> 5)), 1u, RLX_AGENT);
    if (r0 == 4 * MON + 2) { monitor2(ctl); asm volatile("s_endpgm"); }
    if (idx < 48) mod_cols(a, rs, lds, idx + 48);
    lds_barrier();
}
__device__ __forceinline__ void sst16(v4u v, rsrc_t r, unsigned vo, unsigned so, int) { __builtin_amdgcn_raw_buffer_store_b128(v, r, vo, so, 16); asm volatile("s_nop 1" :: "v"(v) : "memory"); }
#define MFMA_A(acc, wv, xv) asm volatile("v_mfma_f32_16x16x32_bf16 %0, %1, %2, %0" : "+v"(acc) : "a"(wv), "v"(xv))
#define MFMA_V(acc, wv, xv) asm volatile("v_mfma_f32_16x16x32_bf16 %0, %1, %2, %0" : "+v"(acc) : "v"(wv), "v"(xv))
#define MFMA_A0(acc, wv, xv) asm volatile("v_mfma_f32_16x16x32_bf16 %0, %1, %2, 0" : "=&v"(acc) : "a"(wv), "v"(xv))
#define MFMA_V0(acc, wv, xv) asm volatile("v_mfma_f32_16x16x32_bf16 %0, %1, %2, 0" : "=&v"(acc) : "v"(wv), "v"(xv))
__device__ __forceinline__ float rb1(float v) { return __uint_as_float(pack2(0.f, v)); }
#define TD_L 24
#define TD_W 1000
#define XF1 55
#define XF2 64
#define XO 64
#define TLF 2
#define TD_S 16
__device__ __forceinline__ unsigned arrive(const char* ws, int cnt, int seg, int wave = 3) {
    unsigned old = 1u;
    asm volatile("s_cmp_eq_u32 %3, 3\n\ts_cselect_b64 exec, 1, 0\n\tglobal_atomic_add %0, %1, %0, %2 sc0\n\ts_mov_b64 exec, -1" : "+v"(old) : "v"((C_N + 64 * cnt + seg) * 4), "s"(ws), "s"(wave) : "memory", "scc");
    return old;
}
template <int N> __device__ __forceinline__ unsigned rdw(unsigned v) { unsigned r; asm volatile("s_waitcnt vmcnt(%2)\n\tv_readfirstlane_b32 %0, %1" : "=s"(r) : "v"(v), "n"(N) : "memory"); return r; }
template <int KIND> __device__ __forceinline__ void tell(unsigned* ctl, int seg, unsigned old, unsigned total, int n = 8, int x0 = 0) {
    const int lane = lane_id();
    if (__builtin_amdgcn_readfirstlane(old) == total - 1 && lane < n) __hip_atomic_fetch_max((gu32*)(ctl + GO(KIND, x0 + lane) + (seg & 1)), (unsigned)seg + 1, RLX_AGENT);
}
template <int KIND> __device__ __forceinline__ void tell_now(const Args& a, unsigned* ctl, int cnt, int seg, unsigned total, int n = 8, int x0 = 0) {
    if (a.pad0 == 3) { unsigned old = arrive(a.ws, cnt, seg); asm volatile("s_waitcnt vmcnt(0)" : "+v"(old)); tell<KIND>(ctl, seg, old, total, n, x0); }
}
__device__ __forceinline__ v2u pack4(f32x4 v) { return (v2u){pack2(v[0], v[1]), pack2(v[2], v[3])}; }

template <int OFF> __device__ __forceinline__ void dma1(v4u r, unsigned vo, unsigned so) { asm volatile("buffer_load_dwordx4 %0, %1, %2 offen offset:%3 sc1 lds" :: "v"(vo), "s"(r), "s"(so), "n"(OFF)); }
template <int O0, int ST> __device__ __forceinline__ void ld4(v4u (&d)[4], v4u r, unsigned vo, unsigned so) {
    asm volatile("buffer_load_dwordx4 %0, %4, %5, %6 offen offset:%7 sc1\n\tbuffer_load_dwordx4 %1, %4, %5, %6 offen offset:%8 sc1\n\t"
                 "buffer_load_dwordx4 %2, %4, %5, %6 offen offset:%9 sc1\n\tbuffer_load_dwordx4 %3, %4, %5, %6 offen offset:%10 sc1"
                 : "=&v"(d[0]), "=&v"(d[1]), "=&v"(d[2]), "=&v"(d[3]) : "v"(vo), "s"(r), "s"(so), "n"(O0), "n"(O0 + ST), "n"(O0 + 2 * ST), "n"(O0 + 3 * ST));
}
#define ROFF 0
#define XB 0
#define GO_(G) (G)
#define UPF 0
#define UPO 1
#define AFD(v) ((v) ? 2 : 1)
#define LV0 7
#define LVS 3
#define KTL 29
#define LQ 4
#define LV 12
constexpr int SM = 0, SV = 0;
#define PROJ_Q \
    "ds_read_b128 v[48:51], v2 offset:0\nds_read_b128 v[52:55], v2 offset:8192\nds_read_b128 v[56:59], v3 offset:0\nds_read_b128 v[60:63], v3 offset:8192\ns_mov_b32 s72, 0\n" \
    "s_mov_b32 s73, 0\nv_mov_b32 v21, 0\ns_add_u32 s97, s50, 1\ns_min_u32 s97, s97, 63\ns_sub_u32 s98, s50, s23\ns_add_u32 s99, s98, 1\ns_branch .Lprqslow_%=\n.Lprqgo_%=:\n" \
    "s_lshr_b32 s68, s55, 1\ns_xor_b32 s68, s68, s98\ns_bitcmp1_b32 s68, 0\ns_cbranch_scc0 .Lprqb2_%=\n.Lprqb1_%=:\ns_waitcnt lgkmcnt(4)\n" \
    "v_mfma_f32_16x16x32_bf16 v[24:27], a[0:3], v[48:51], 0\nds_read_b128 v[64:67], v2 offset:1024\ns_sub_u32 s38, s50, s24\n" \
    "v_mfma_f32_16x16x32_bf16 v[36:39], a[0:3], v[52:55], 0\nds_read_b128 v[68:71], v2 offset:9216\ns_max_i32 s38, s38, 0\n" \
    "v_mfma_f32_16x16x32_bf16 v[28:31], a[4:7], v[48:51], 0\ns_add_u32 m0, s52, 49152\ns_add_u32 s69, s54, 1536\ns_and_b32 s38, s38, s16\n" \
    "v_mfma_f32_16x16x32_bf16 v[40:43], a[4:7], v[52:55], 0\nbuffer_load_dwordx4 v1, s[64:67], s69 offen offset:0 sc1 lds\ns_lshl_b32 s38, s38, 19\n" \
    "v_mfma_f32_16x16x32_bf16 v[32:35], v[124:127], v[48:51], 0\ns_add_u32 s38, s38, s53\ns_add_u32 s74, s48, s38\ns_lshr_b32 s74, s74, 8\n" \
    "v_mfma_f32_16x16x32_bf16 v[44:47], v[124:127], v[52:55], 0\ns_waitcnt lgkmcnt(4)\ns_xnor_b32 s74, s74, s49\ns_and_b32 s74, s74, 0x2000\n" \
    "v_mfma_f32_16x16x32_bf16 v[24:27], a[8:11], v[56:59], v[24:27]\nds_read_b128 v[72:75], v3 offset:1024\ns_add_u32 s74, s74, s38\n" \
    "v_mfma_f32_16x16x32_bf16 v[36:39], a[8:11], v[60:63], v[36:39]\nds_read_b128 v[76:79], v3 offset:9216\ns_add_u32 s74, s74, s60\n" \
    "v_mfma_f32_16x16x32_bf16 v[28:31], a[12:15], v[56:59], v[28:31]\ns_add_u32 s68, s50, 1\ns_min_u32 s68, s68, 63\ns_lshl_b32 s68, s68, 20\n" \
    "v_mfma_f32_16x16x32_bf16 v[40:43], a[12:15], v[60:63], v[40:43]\ns_add_u32 s68, s68, s18\ns_add_u32 s70, s48, s68\ns_lshr_b32 s70, s70, 8\n" \
    "v_mfma_f32_16x16x32_bf16 v[32:35], v[128:131], v[56:59], v[32:35]\ns_xnor_b32 s70, s70, s49\ns_and_b32 s70, s70, 0x2000\ns_add_u32 s70, s70, s68\n" \
    "v_mfma_f32_16x16x32_bf16 v[44:47], v[128:131], v[60:63], v[44:47]\ns_waitcnt lgkmcnt(2)\ns_add_u32 s69, s54, 640\n" \
    "v_mfma_f32_16x16x32_bf16 v[24:27], a[16:19], v[64:67], v[24:27]\nds_read_b128 v[48:51], v2 offset:2048\nv_mfma_f32_16x16x32_bf16 v[36:39], a[16:19], v[68:71], v[36:39]\n" \
    "ds_read_b128 v[52:55], v2 offset:10240\nv_mfma_f32_16x16x32_bf16 v[28:31], a[20:23], v[64:67], v[28:31]\nbuffer_load_dwordx4 v1, s[64:67], s69 offen offset:1024 sc1 lds\n" \
    "s_add_u32 s69, s54, -256\nv_mfma_f32_16x16x32_bf16 v[40:43], a[20:23], v[68:71], v[40:43]\nbuffer_load_dwordx4 v1, s[64:67], s69 offen offset:2048 sc1 lds\n" \
    "v_mfma_f32_16x16x32_bf16 v[32:35], v[132:135], v[64:67], v[32:35]\nv_mfma_f32_16x16x32_bf16 v[44:47], v[132:135], v[68:71], v[44:47]\ns_waitcnt lgkmcnt(2)\n" \
    "v_mfma_f32_16x16x32_bf16 v[24:27], a[24:27], v[72:75], v[24:27]\nds_read_b128 v[56:59], v3 offset:2048\nv_mfma_f32_16x16x32_bf16 v[36:39], a[24:27], v[76:79], v[36:39]\n" \
    "ds_read_b128 v[60:63], v3 offset:10240\nv_mfma_f32_16x16x32_bf16 v[28:31], a[28:31], v[72:75], v[28:31]\ns_add_u32 s69, s54, -1152\n" \
    "buffer_load_dwordx4 v1, s[64:67], s69 offen offset:3072 sc1 lds\nv_mfma_f32_16x16x32_bf16 v[40:43], a[28:31], v[76:79], v[40:43]\ns_waitcnt vmcnt(7)\n" \
    "v_readfirstlane_b32 s42, v21\nv_mfma_f32_16x16x32_bf16 v[32:35], v[136:139], v[72:75], v[32:35]\nv_mfma_f32_16x16x32_bf16 v[44:47], v[136:139], v[76:79], v[44:47]\n" \
    "s_waitcnt lgkmcnt(2)\nv_mfma_f32_16x16x32_bf16 v[24:27], a[32:35], v[48:51], v[24:27]\nds_read_b128 v[64:67], v2 offset:3072\n" \
    "v_mfma_f32_16x16x32_bf16 v[36:39], a[32:35], v[52:55], v[36:39]\nds_read_b128 v[68:71], v2 offset:11264\nv_mfma_f32_16x16x32_bf16 v[28:31], a[36:39], v[48:51], v[28:31]\n" \
    "s_and_b32 s37, s50, s16\ns_lshl_b32 s37, s37, 19\ns_add_u32 s37, s37, s53\nv_mfma_f32_16x16x32_bf16 v[40:43], a[36:39], v[52:55], v[40:43]\ns_add_u32 s71, s48, s37\n" \
    "s_lshr_b32 s71, s71, 8\ns_xnor_b32 s71, s71, s49\nv_mfma_f32_16x16x32_bf16 v[32:35], v[140:143], v[48:51], v[32:35]\ns_and_b32 s71, s71, 0x2000\ns_add_u32 s71, s71, s37\n" \
    "s_mov_b32 m0, s56\nv_mfma_f32_16x16x32_bf16 v[44:47], v[140:143], v[52:55], v[44:47]\ns_waitcnt lgkmcnt(2)\nbuffer_load_dwordx4 v0, s[64:67], s74 offen offset:0 sc1 lds\n" \
    "v_mfma_f32_16x16x32_bf16 v[24:27], a[40:43], v[56:59], v[24:27]\nds_read_b128 v[72:75], v3 offset:3072\nv_mfma_f32_16x16x32_bf16 v[36:39], a[40:43], v[60:63], v[36:39]\n" \
    "ds_read_b128 v[76:79], v3 offset:11264\nv_mfma_f32_16x16x32_bf16 v[28:31], a[44:47], v[56:59], v[28:31]\nbuffer_load_dwordx4 v0, s[64:67], s74 offen offset:1024 sc1 lds\n" \
    "v_mfma_f32_16x16x32_bf16 v[40:43], a[44:47], v[60:63], v[40:43]\ns_cmp_eq_u32 s42, 63\ns_cbranch_scc1 .Lprqtl_%=\n.Lprqtlb_%=:\n" \
    "v_mfma_f32_16x16x32_bf16 v[32:35], v[144:147], v[56:59], v[32:35]\nv_mfma_f32_16x16x32_bf16 v[44:47], v[144:147], v[60:63], v[44:47]\ns_waitcnt lgkmcnt(0)\ns_barrier\n" \
    "v_mfma_f32_16x16x32_bf16 v[24:27], a[48:51], v[64:67], v[24:27]\nds_read_b128 v[48:51], v2 offset:16384\nv_mfma_f32_16x16x32_bf16 v[36:39], a[48:51], v[68:71], v[36:39]\n" \
    "ds_read_b128 v[52:55], v2 offset:24576\nv_mfma_f32_16x16x32_bf16 v[28:31], a[52:55], v[64:67], v[28:31]\ns_add_u32 s39, s50, 1\ns_add_u32 s97, s39, 1\n" \
    "s_min_u32 s97, s97, 63\nv_mfma_f32_16x16x32_bf16 v[40:43], a[52:55], v[68:71], v[40:43]\ns_sub_u32 s98, s39, s23\ns_add_u32 s99, s98, 1\n" \
    "v_mfma_f32_16x16x32_bf16 v[32:35], v[148:151], v[64:67], v[32:35]\ns_cmp_gt_i32 s51, s97\ns_cselect_b32 s96, s98, 0x7fffffff\ns_sub_u32 s39, s39, 2\n" \
    "v_mfma_f32_16x16x32_bf16 v[44:47], v[148:151], v[68:71], v[44:47]\ns_cmp_lt_u32 s39, 62\ns_cselect_b32 s96, s96, 0x7fffffff\n" \
    "v_mfma_f32_16x16x32_bf16 v[24:27], a[56:59], v[72:75], v[24:27]\nds_read_b128 v[56:59], v3 offset:16384\nv_mfma_f32_16x16x32_bf16 v[36:39], a[56:59], v[76:79], v[36:39]\n" \
    "ds_read_b128 v[60:63], v3 offset:24576\nv_mfma_f32_16x16x32_bf16 v[28:31], a[60:63], v[72:75], v[28:31]\nv_mfma_f32_16x16x32_bf16 v[40:43], a[60:63], v[76:79], v[40:43]\n" \
    "v_mfma_f32_16x16x32_bf16 v[32:35], v[152:155], v[72:75], v[32:35]\nv_mfma_f32_16x16x32_bf16 v[44:47], v[152:155], v[76:79], v[44:47]\ns_waitcnt lgkmcnt(2)\n" \
    "v_mfma_f32_16x16x32_bf16 v[24:27], a[64:67], v[48:51], v[24:27]\nds_read_b128 v[64:67], v2 offset:17408\nv_mfma_f32_16x16x32_bf16 v[36:39], a[64:67], v[52:55], v[36:39]\n" \
    "ds_read_b128 v[68:71], v2 offset:25600\nv_mfma_f32_16x16x32_bf16 v[28:31], a[68:71], v[48:51], v[28:31]\ns_add_u32 m0, s52, 0\ns_add_u32 s69, s70, 0\n" \
    "v_mfma_f32_16x16x32_bf16 v[40:43], a[68:71], v[52:55], v[40:43]\nbuffer_load_dwordx4 v1, s[64:67], s69 offen offset:0 sc1 lds\n" \
    "v_mfma_f32_16x16x32_bf16 v[32:35], v[156:159], v[48:51], v[32:35]\nv_mfma_f32_16x16x32_bf16 v[44:47], v[156:159], v[52:55], v[44:47]\ns_waitcnt lgkmcnt(2)\n" \
    "v_mfma_f32_16x16x32_bf16 v[24:27], a[72:75], v[56:59], v[24:27]\nds_read_b128 v[72:75], v3 offset:17408\nv_mfma_f32_16x16x32_bf16 v[36:39], a[72:75], v[60:63], v[36:39]\n" \
    "ds_read_b128 v[76:79], v3 offset:25600\nv_mfma_f32_16x16x32_bf16 v[28:31], a[76:79], v[56:59], v[28:31]\ns_add_u32 s69, s70, -896\n" \
    "buffer_load_dwordx4 v1, s[64:67], s69 offen offset:1024 sc1 lds\nv_mfma_f32_16x16x32_bf16 v[40:43], a[76:79], v[60:63], v[40:43]\n" \
    "v_mfma_f32_16x16x32_bf16 v[32:35], v[160:163], v[56:59], v[32:35]\nv_mfma_f32_16x16x32_bf16 v[44:47], v[160:163], v[60:63], v[44:47]\ns_waitcnt lgkmcnt(2)\n" \
    "v_mfma_f32_16x16x32_bf16 v[24:27], a[80:83], v[64:67], v[24:27]\nds_read_b128 v[48:51], v2 offset:18432\nv_mfma_f32_16x16x32_bf16 v[36:39], a[80:83], v[68:71], v[36:39]\n" \
    "ds_read_b128 v[52:55], v2 offset:26624\nv_mfma_f32_16x16x32_bf16 v[28:31], a[84:87], v[64:67], v[28:31]\ns_add_u32 s69, s70, -1792\n" \
    "buffer_load_dwordx4 v1, s[64:67], s69 offen offset:2048 sc1 lds\nv_mfma_f32_16x16x32_bf16 v[40:43], a[84:87], v[68:71], v[40:43]\ns_add_u32 s68, s59, s25\n" \
    "s_and_b32 s68, s68, 0xffff\nv_add_u32 v122, s68, v13\nv_mfma_f32_16x16x32_bf16 v[32:35], v[164:167], v[64:67], v[32:35]\nds_read_b128 v[80:83], v122 offset:0\n" \
    "v_mfma_f32_16x16x32_bf16 v[44:47], v[164:167], v[68:71], v[44:47]\ns_waitcnt lgkmcnt(3)\nds_read_b128 v[84:87], v122 offset:1024\n" \
    "v_mfma_f32_16x16x32_bf16 v[24:27], a[88:91], v[72:75], v[24:27]\nds_read_b128 v[56:59], v3 offset:18432\nv_mfma_f32_16x16x32_bf16 v[36:39], a[88:91], v[76:79], v[36:39]\n" \
    "ds_read_b128 v[60:63], v3 offset:26624\nv_mfma_f32_16x16x32_bf16 v[28:31], a[92:95], v[72:75], v[28:31]\ns_add_u32 s69, s70, -2688\n" \
    "buffer_load_dwordx4 v1, s[64:67], s69 offen offset:3072 sc1 lds\nv_mfma_f32_16x16x32_bf16 v[40:43], a[92:95], v[76:79], v[40:43]\nds_read_b128 v[88:91], v122 offset:2048\n" \
    "v_mfma_f32_16x16x32_bf16 v[32:35], v[168:171], v[72:75], v[32:35]\nds_read_b128 v[92:95], v122 offset:3072\n" \
    "v_mfma_f32_16x16x32_bf16 v[44:47], v[168:171], v[76:79], v[44:47]\ns_waitcnt lgkmcnt(6)\nv_mfma_f32_16x16x32_bf16 v[24:27], a[96:99], v[48:51], v[24:27]\n" \
    "ds_read_b128 v[64:67], v2 offset:19456\nv_mfma_f32_16x16x32_bf16 v[36:39], a[96:99], v[52:55], v[36:39]\nds_read_b128 v[68:71], v2 offset:27648\n" \
    "v_mfma_f32_16x16x32_bf16 v[28:31], a[100:103], v[48:51], v[28:31]\nbuffer_load_dword v20, v6, s[64:67], 0 offen sc1\n" \
    "v_mfma_f32_16x16x32_bf16 v[40:43], a[100:103], v[52:55], v[40:43]\nv_mfma_f32_16x16x32_bf16 v[32:35], v[172:175], v[48:51], v[32:35]\n" \
    "v_mfma_f32_16x16x32_bf16 v[44:47], v[172:175], v[52:55], v[44:47]\ns_waitcnt lgkmcnt(4)\nv_mfma_f32_16x16x32_bf16 v[24:27], a[104:107], v[56:59], v[24:27]\n" \
    "ds_read_b128 v[72:75], v3 offset:19456\nv_mfma_f32_16x16x32_bf16 v[36:39], a[104:107], v[60:63], v[36:39]\nds_read_b128 v[76:79], v3 offset:27648\n" \
    "v_mfma_f32_16x16x32_bf16 v[28:31], a[108:111], v[56:59], v[28:31]\ns_memrealtime s[72:73]\nv_mfma_f32_16x16x32_bf16 v[40:43], a[108:111], v[60:63], v[40:43]\n" \
    "v_mfma_f32_16x16x32_bf16 v[32:35], v[176:179], v[56:59], v[32:35]\nv_mfma_f32_16x16x32_bf16 v[44:47], v[176:179], v[60:63], v[44:47]\ns_waitcnt vmcnt(5) lgkmcnt(0)\n" \
    "s_barrier\nv_mfma_f32_16x16x32_bf16 v[24:27], a[112:115], v[64:67], v[24:27]\nds_read_b128 v[48:51], v2 offset:32768\nv_mov_b32 v123, s50\n" \
    "v_mfma_f32_16x16x32_bf16 v[36:39], a[112:115], v[68:71], v[36:39]\nds_read_b128 v[52:55], v2 offset:40960\n" \
    "v_mfma_f32_16x16x32_bf16 v[28:31], a[116:119], v[64:67], v[28:31]\nbuffer_store_dword v123, v5, s[64:67], 0 offen sc1\n" \
    "v_mfma_f32_16x16x32_bf16 v[40:43], a[116:119], v[68:71], v[40:43]\nds_read_b128 v[96:99], v8 offset:0\nv_mfma_f32_16x16x32_bf16 v[32:35], v[180:183], v[64:67], v[32:35]\n" \
    "ds_read_b128 v[100:103], v8 offset:2048\nv_mfma_f32_16x16x32_bf16 v[44:47], v[180:183], v[68:71], v[44:47]\nds_read_b128 v[104:107], v8 offset:4096\n" \
    "v_mfma_f32_16x16x32_bf16 v[24:27], a[120:123], v[72:75], v[24:27]\nds_read_b128 v[56:59], v3 offset:32768\n" \
    "v_mfma_f32_16x16x32_bf16 v[36:39], a[120:123], v[76:79], v[36:39]\nds_read_b128 v[60:63], v3 offset:40960\n" \
    "v_mfma_f32_16x16x32_bf16 v[28:31], a[124:127], v[72:75], v[28:31]\nds_read_b128 v[108:111], v8 offset:6144\n" \
    "v_mfma_f32_16x16x32_bf16 v[40:43], a[124:127], v[76:79], v[40:43]\nv_mfma_f32_16x16x32_bf16 v[32:35], v[184:187], v[72:75], v[32:35]\n" \
    "v_mfma_f32_16x16x32_bf16 v[44:47], v[184:187], v[76:79], v[44:47]\ns_waitcnt lgkmcnt(6)\nv_mfma_f32_16x16x32_bf16 v[24:27], a[128:131], v[48:51], v[24:27]\n" \
    "ds_read_b128 v[64:67], v2 offset:33792\nv_mfma_f32_16x16x32_bf16 v[36:39], a[128:131], v[52:55], v[36:39]\nds_read_b128 v[68:71], v2 offset:41984\n" \
    "v_mfma_f32_16x16x32_bf16 v[28:31], a[132:135], v[48:51], v[28:31]\ns_waitcnt lgkmcnt(2)\ns_add_u32 m0, s52, 16384\ns_add_u32 s69, s70, 512\n" \
    "v_mfma_f32_16x16x32_bf16 v[112:115], v[96:99], v[80:83], 0\nbuffer_load_dwordx4 v1, s[64:67], s69 offen offset:0 sc1 lds\n" \
    "v_mfma_f32_16x16x32_bf16 v[40:43], a[132:135], v[52:55], v[40:43]\nds_read_b128 v[96:99], v8 offset:32\n" \
    "v_mfma_f32_16x16x32_bf16 v[112:115], v[100:103], v[84:87], v[112:115]\nds_read_b128 v[100:103], v8 offset:2080\n" \
    "v_mfma_f32_16x16x32_bf16 v[32:35], v[188:191], v[48:51], v[32:35]\nv_mfma_f32_16x16x32_bf16 v[112:115], v[104:107], v[88:91], v[112:115]\n" \
    "ds_read_b128 v[104:107], v8 offset:4128\nv_mfma_f32_16x16x32_bf16 v[44:47], v[188:191], v[52:55], v[44:47]\n" \
    "v_mfma_f32_16x16x32_bf16 v[112:115], v[108:111], v[92:95], v[112:115]\nds_read_b128 v[108:111], v8 offset:6176\n" \
    "v_mfma_f32_16x16x32_bf16 v[24:27], a[136:139], v[56:59], v[24:27]\nds_read_b128 v[72:75], v3 offset:33792\n" \
    "v_mfma_f32_16x16x32_bf16 v[36:39], a[136:139], v[60:63], v[36:39]\nds_read_b128 v[76:79], v3 offset:41984\n" \
    "v_mfma_f32_16x16x32_bf16 v[28:31], a[140:143], v[56:59], v[28:31]\ns_add_u32 s69, s70, -384\nbuffer_load_dwordx4 v1, s[64:67], s69 offen offset:1024 sc1 lds\n" \
    "v_mfma_f32_16x16x32_bf16 v[40:43], a[140:143], v[60:63], v[40:43]\nv_cndmask_b32_e64 v112, v22, v112, s[80:81]\nv_cndmask_b32_e64 v113, v22, v113, s[82:83]\n" \
    "v_mfma_f32_16x16x32_bf16 v[32:35], v[192:195], v[56:59], v[32:35]\nv_cndmask_b32_e64 v114, v22, v114, s[84:85]\nv_cndmask_b32_e64 v115, v22, v115, s[86:87]\n" \
    "v_mfma_f32_16x16x32_bf16 v[44:47], v[192:195], v[60:63], v[44:47]\ns_waitcnt lgkmcnt(6)\nv_max3_f32 v120, v112, v113, v114\nv_max3_f32 v120, v120, v115, s33\n" \
    "v_mfma_f32_16x16x32_bf16 v[24:27], a[144:147], v[64:67], v[24:27]\nds_read_b128 v[48:51], v2 offset:34816\n" \
    "v_mfma_f32_16x16x32_bf16 v[36:39], a[144:147], v[68:71], v[36:39]\nds_read_b128 v[52:55], v2 offset:43008\n" \
    "v_mfma_f32_16x16x32_bf16 v[28:31], a[148:151], v[64:67], v[28:31]\ns_waitcnt lgkmcnt(4)\ns_add_u32 s69, s70, -1280\n" \
    "v_mfma_f32_16x16x32_bf16 v[116:119], v[96:99], v[80:83], 0\nbuffer_load_dwordx4 v1, s[64:67], s69 offen offset:2048 sc1 lds\n" \
    "v_mfma_f32_16x16x32_bf16 v[40:43], a[148:151], v[68:71], v[40:43]\nv_mfma_f32_16x16x32_bf16 v[116:119], v[100:103], v[84:87], v[116:119]\n" \
    "v_mfma_f32_16x16x32_bf16 v[32:35], v[196:199], v[64:67], v[32:35]\nv_mfma_f32_16x16x32_bf16 v[116:119], v[104:107], v[88:91], v[116:119]\n" \
    "v_mfma_f32_16x16x32_bf16 v[44:47], v[196:199], v[68:71], v[44:47]\nv_mfma_f32_16x16x32_bf16 v[116:119], v[108:111], v[92:95], v[116:119]\ns_waitcnt lgkmcnt(2)\n" \
    "v_mfma_f32_16x16x32_bf16 v[24:27], a[152:155], v[72:75], v[24:27]\nds_read_b128 v[56:59], v3 offset:34816\n" \
    "v_mfma_f32_16x16x32_bf16 v[36:39], a[152:155], v[76:79], v[36:39]\nds_read_b128 v[60:63], v3 offset:43008\n" \
    "v_mfma_f32_16x16x32_bf16 v[28:31], a[156:159], v[72:75], v[28:31]\ns_add_u32 s69, s70, -2176\nbuffer_load_dwordx4 v1, s[64:67], s69 offen offset:3072 sc1 lds\n" \
    "v_mfma_f32_16x16x32_bf16 v[40:43], a[156:159], v[76:79], v[40:43]\nv_cndmask_b32_e64 v116, v22, v116, s[88:89]\nv_cndmask_b32_e64 v117, v22, v117, s[90:91]\n" \
    "v_mfma_f32_16x16x32_bf16 v[32:35], v[200:203], v[72:75], v[32:35]\nv_cndmask_b32_e64 v118, v22, v118, s[92:93]\nv_cndmask_b32_e64 v119, v22, v119, s[94:95]\n" \
    "v_mfma_f32_16x16x32_bf16 v[44:47], v[200:203], v[76:79], v[44:47]\ns_waitcnt lgkmcnt(2)\nv_max3_f32 v120, v120, v116, v117\nv_max3_f32 v120, v120, v118, v119\n" \
    "v_mfma_f32_16x16x32_bf16 v[24:27], a[160:163], v[48:51], v[24:27]\nds_read_b128 v[64:67], v2 offset:35840\nv_mul_f32_e64 v122, -s32, v120\n" \
    "v_mfma_f32_16x16x32_bf16 v[36:39], a[160:163], v[52:55], v[36:39]\nds_read_b128 v[68:71], v2 offset:44032\nv_fma_f32 v112, v112, s32, v122\n" \
    "v_mfma_f32_16x16x32_bf16 v[28:31], a[164:167], v[48:51], v[28:31]\ns_waitcnt vmcnt(5)\nv_writelane_b32 v20, s72, 2\nv_fma_f32 v113, v113, s32, v122\n" \
    "v_mfma_f32_16x16x32_bf16 v[40:43], a[164:167], v[52:55], v[40:43]\nds_write_b32 v7, v20\nv_mfma_f32_16x16x32_bf16 v[32:35], v[204:207], v[48:51], v[32:35]\n" \
    "v_fma_f32 v114, v114, s32, v122\nv_fma_f32 v115, v115, s32, v122\nv_mfma_f32_16x16x32_bf16 v[44:47], v[204:207], v[52:55], v[44:47]\ns_waitcnt lgkmcnt(3)\n" \
    "v_fma_f32 v116, v116, s32, v122\nv_fma_f32 v117, v117, s32, v122\nv_mfma_f32_16x16x32_bf16 v[24:27], a[168:171], v[56:59], v[24:27]\n" \
    "ds_read_b128 v[72:75], v3 offset:35840\nv_fma_f32 v118, v118, s32, v122\nv_mfma_f32_16x16x32_bf16 v[36:39], a[168:171], v[60:63], v[36:39]\n" \
    "ds_read_b128 v[76:79], v3 offset:44032\nv_fma_f32 v119, v119, s32, v122\nv_mfma_f32_16x16x32_bf16 v[28:31], a[172:175], v[56:59], v[28:31]\nv_exp_f32 v112, v112\n" \
    "v_mfma_f32_16x16x32_bf16 v[40:43], a[172:175], v[60:63], v[40:43]\nv_exp_f32 v113, v113\nv_mfma_f32_16x16x32_bf16 v[32:35], v[208:211], v[56:59], v[32:35]\n" \
    "v_exp_f32 v114, v114\nv_mfma_f32_16x16x32_bf16 v[44:47], v[208:211], v[60:63], v[44:47]\ns_waitcnt lgkmcnt(0)\ns_barrier\n" \
    "v_mfma_f32_16x16x32_bf16 v[24:27], a[176:179], v[64:67], v[24:27]\nds_read_b128 v[48:51], v2 offset:49152\n" \
    "v_mfma_f32_16x16x32_bf16 v[36:39], a[176:179], v[68:71], v[36:39]\nds_read_b128 v[52:55], v2 offset:57344\n" \
    "v_mfma_f32_16x16x32_bf16 v[28:31], a[180:183], v[64:67], v[28:31]\nv_exp_f32 v115, v115\nv_mfma_f32_16x16x32_bf16 v[40:43], a[180:183], v[68:71], v[40:43]\n" \
    "v_exp_f32 v116, v116\nv_mfma_f32_16x16x32_bf16 v[32:35], v[212:215], v[64:67], v[32:35]\nv_exp_f32 v117, v117\n" \
    "v_mfma_f32_16x16x32_bf16 v[44:47], v[212:215], v[68:71], v[44:47]\nv_exp_f32 v118, v118\nv_mfma_f32_16x16x32_bf16 v[24:27], a[184:187], v[72:75], v[24:27]\n" \
    "ds_read_b128 v[56:59], v3 offset:49152\nv_mfma_f32_16x16x32_bf16 v[36:39], a[184:187], v[76:79], v[36:39]\nds_read_b128 v[60:63], v3 offset:57344\n" \
    "v_mfma_f32_16x16x32_bf16 v[28:31], a[188:191], v[72:75], v[28:31]\nv_exp_f32 v119, v119\nv_mfma_f32_16x16x32_bf16 v[40:43], a[188:191], v[76:79], v[40:43]\n" \
    "v_add_f32 v121, v112, v113\nv_add_f32 v121, v121, v114\nv_mfma_f32_16x16x32_bf16 v[32:35], v[216:219], v[72:75], v[32:35]\nv_add_f32 v121, v121, v115\n" \
    "v_add_f32 v121, v121, v116\nv_mfma_f32_16x16x32_bf16 v[44:47], v[216:219], v[76:79], v[44:47]\ns_waitcnt lgkmcnt(2)\nv_add_f32 v121, v121, v117\n" \
    "v_add_f32 v121, v121, v118\nv_mfma_f32_16x16x32_bf16 v[24:27], a[192:195], v[48:51], v[24:27]\nds_read_b128 v[64:67], v2 offset:50176\nv_add_f32 v121, v121, v119\n" \
    "v_mfma_f32_16x16x32_bf16 v[36:39], a[192:195], v[52:55], v[36:39]\nds_read_b128 v[68:71], v2 offset:58368\n" \
    "v_mfma_f32_16x16x32_bf16 v[28:31], a[196:199], v[48:51], v[28:31]\ns_add_u32 m0, s52, 32768\ns_add_u32 s69, s70, 1024\n" \
    "v_mfma_f32_16x16x32_bf16 v[40:43], a[196:199], v[52:55], v[40:43]\nbuffer_load_dwordx4 v1, s[64:67], s69 offen offset:0 sc1 lds\n" \
    "v_mfma_f32_16x16x32_bf16 v[32:35], v[220:223], v[48:51], v[32:35]\nds_read_b96 v[108:110], v15\nv_mfma_f32_16x16x32_bf16 v[44:47], v[220:223], v[52:55], v[44:47]\n" \
    "s_waitcnt lgkmcnt(3)\nv_mfma_f32_16x16x32_bf16 v[24:27], a[200:203], v[56:59], v[24:27]\nds_read_b128 v[72:75], v3 offset:50176\n" \
    "v_mfma_f32_16x16x32_bf16 v[36:39], a[200:203], v[60:63], v[36:39]\nds_read_b128 v[76:79], v3 offset:58368\n" \
    "v_mfma_f32_16x16x32_bf16 v[28:31], a[204:207], v[56:59], v[28:31]\ns_add_u32 s69, s70, 128\nbuffer_load_dwordx4 v1, s[64:67], s69 offen offset:1024 sc1 lds\n" \
    "v_mfma_f32_16x16x32_bf16 v[40:43], a[204:207], v[60:63], v[40:43]\nv_mfma_f32_16x16x32_bf16 v[32:35], v[224:227], v[56:59], v[32:35]\ns_waitcnt lgkmcnt(2)\n" \
    "v_readfirstlane_b32 s72, v108\nv_readfirstlane_b32 s73, v109\nv_mfma_f32_16x16x32_bf16 v[44:47], v[224:227], v[60:63], v[44:47]\nv_readfirstlane_b32 s40, v110\n" \
    "v_mfma_f32_16x16x32_bf16 v[24:27], a[208:211], v[64:67], v[24:27]\nds_read_b128 v[48:51], v2 offset:51200\n" \
    "v_mfma_f32_16x16x32_bf16 v[36:39], a[208:211], v[68:71], v[36:39]\nds_read_b128 v[52:55], v2 offset:59392\ns_lshr_b32 s41, s72, 8\n" \
    "v_mfma_f32_16x16x32_bf16 v[28:31], a[212:215], v[64:67], v[28:31]\ns_add_u32 s69, s70, -768\nbuffer_load_dwordx4 v1, s[64:67], s69 offen offset:2048 sc1 lds\n" \
    "v_mfma_f32_16x16x32_bf16 v[40:43], a[212:215], v[68:71], v[40:43]\ns_sub_u32 s41, s40, s41\ns_lshl_b32 s41, s41, 8\ns_and_b32 s72, s72, 127\n" \
    "v_mfma_f32_16x16x32_bf16 v[32:35], v[228:231], v[64:67], v[32:35]\ns_cmp_lt_i32 s41, s30\ns_subb_u32 s72, s72, 0\ns_max_i32 s72, s72, 0\n" \
    "v_mfma_f32_16x16x32_bf16 v[44:47], v[228:231], v[68:71], v[44:47]\ns_waitcnt lgkmcnt(2)\ns_cmp_gt_i32 s72, s96\ns_cselect_b32 s41, s73, 0x80000000\n" \
    "v_mfma_f32_16x16x32_bf16 v[24:27], a[216:219], v[72:75], v[24:27]\nds_read_b128 v[56:59], v3 offset:51200\n" \
    "v_mfma_f32_16x16x32_bf16 v[36:39], a[216:219], v[76:79], v[36:39]\nds_read_b128 v[60:63], v3 offset:59392\n" \
    "v_mfma_f32_16x16x32_bf16 v[28:31], a[220:223], v[72:75], v[28:31]\ns_add_u32 s69, s70, -1664\nbuffer_load_dwordx4 v1, s[64:67], s69 offen offset:3072 sc1 lds\n" \
    "v_mfma_f32_16x16x32_bf16 v[40:43], a[220:223], v[76:79], v[40:43]\nbuffer_load_dword v20, v6, s[64:67], 0 offen sc1\n" \
    "v_mfma_f32_16x16x32_bf16 v[32:35], v[232:235], v[72:75], v[32:35]\nv_mfma_f32_16x16x32_bf16 v[44:47], v[232:235], v[76:79], v[44:47]\ns_waitcnt lgkmcnt(2)\n" \
    "v_mfma_f32_16x16x32_bf16 v[24:27], a[224:227], v[48:51], v[24:27]\nds_read_b128 v[64:67], v2 offset:52224\n" \
    "v_mfma_f32_16x16x32_bf16 v[36:39], a[224:227], v[52:55], v[36:39]\nds_read_b128 v[68:71], v2 offset:60416\n" \
    "v_mfma_f32_16x16x32_bf16 v[28:31], a[228:231], v[48:51], v[28:31]\nds_read_b128 v[72:75], v3 offset:52224\n" \
    "v_mfma_f32_16x16x32_bf16 v[40:43], a[228:231], v[52:55], v[40:43]\nds_read_b128 v[76:79], v3 offset:60416\n" \
    "v_mfma_f32_16x16x32_bf16 v[32:35], v[236:239], v[48:51], v[32:35]\nv_mfma_f32_16x16x32_bf16 v[44:47], v[236:239], v[52:55], v[44:47]\ns_waitcnt lgkmcnt(4)\n" \
    "v_mfma_f32_16x16x32_bf16 v[32:35], v[240:243], v[56:59], v[32:35]\nv_add_u32 v122, s59, v11\nv_add_u32 v123, s59, v12\n" \
    "v_mfma_f32_16x16x32_bf16 v[44:47], v[240:243], v[60:63], v[44:47]\ns_waitcnt lgkmcnt(2)\ns_add_u32 s59, s59, 0x2000\ns_and_b32 s59, s59, 0xffff\n" \
    "v_mfma_f32_16x16x32_bf16 v[32:35], v[244:247], v[64:67], v[32:35]\ns_waitcnt lgkmcnt(0)\ns_barrier\nv_mfma_f32_16x16x32_bf16 v[44:47], v[244:247], v[68:71], v[44:47]\n" \
    "ds_read_b128 v[48:51], v2 offset:0\nv_mfma_f32_16x16x32_bf16 v[32:35], v[248:251], v[72:75], v[32:35]\nds_read_b128 v[52:55], v2 offset:8192\n" \
    "v_mfma_f32_16x16x32_bf16 v[44:47], v[248:251], v[76:79], v[44:47]\ns_mov_b32 s54, s70\ns_add_u32 s50, s50, 1\n" \
    "v_mfma_f32_16x16x32_bf16 v[24:27], a[232:235], v[56:59], v[24:27]\ns_nop 1\nv_mfma_f32_16x16x32_bf16 v[24:27], a[240:243], v[64:67], v[24:27]\ns_nop 0\n" \
    "v_cvt_pk_bf16_f32 v80, v32, v33\nv_cvt_pk_bf16_f32 v81, v34, v35\nv_mfma_f32_16x16x32_bf16 v[24:27], a[248:251], v[72:75], v[24:27]\nv_cvt_pk_bf16_f32 v82, v44, v45\n" \
    "v_cvt_pk_bf16_f32 v83, v46, v47\nv_mfma_f32_16x16x32_bf16 v[28:31], a[236:239], v[56:59], v[28:31]\nv_permlane16_swap_b32 v80, v82\n" \
    "v_mfma_f32_16x16x32_bf16 v[28:31], a[244:247], v[64:67], v[28:31]\nds_read_b128 v[56:59], v3 offset:0\nv_mfma_f32_16x16x32_bf16 v[28:31], a[252:255], v[72:75], v[28:31]\n" \
    "v_permlane16_swap_b32 v81, v83\nv_mfma_f32_16x16x32_bf16 v[36:39], a[232:235], v[60:63], v[36:39]\nbuffer_store_dwordx4 v[80:83], v4, s[64:67], s71 offen sc1\n" \
    "v_mfma_f32_16x16x32_bf16 v[36:39], a[240:243], v[68:71], v[36:39]\nv_cvt_pk_bf16_f32 v84, v24, v25\nv_cvt_pk_bf16_f32 v85, v26, v27\n" \
    "v_mfma_f32_16x16x32_bf16 v[36:39], a[248:251], v[76:79], v[36:39]\ns_nop 0\nv_cvt_pk_bf16_f32 v86, v28, v29\nv_cvt_pk_bf16_f32 v87, v30, v31\n" \
    "v_mfma_f32_16x16x32_bf16 v[40:43], a[236:239], v[60:63], v[40:43]\nv_permlane16_swap_b32 v84, v86\nv_mfma_f32_16x16x32_bf16 v[40:43], a[244:247], v[68:71], v[40:43]\n" \
    "ds_read_b128 v[60:63], v3 offset:8192\nv_mfma_f32_16x16x32_bf16 v[40:43], a[252:255], v[76:79], v[40:43]\nv_permlane16_swap_b32 v85, v87\nds_write_b128 v122, v[84:87]\n" \
    "s_cmp_gt_i32 s41, s99\nv_cvt_pk_bf16_f32 v88, v36, v37\nv_cvt_pk_bf16_f32 v89, v38, v39\ns_nop 2\nv_cvt_pk_bf16_f32 v90, v40, v41\nv_cvt_pk_bf16_f32 v91, v42, v43\n" \
    "s_nop 0\nv_permlane16_swap_b32 v88, v90\nv_permlane16_swap_b32 v89, v91\nds_write_b128 v123, v[88:91]\ns_cbranch_scc0 .Lprqmid_%=\n.Lprqb2_%=:\ns_waitcnt lgkmcnt(4)\n" \
    "v_mfma_f32_16x16x32_bf16 v[24:27], a[0:3], v[48:51], 0\nds_read_b128 v[64:67], v2 offset:1024\ns_sub_u32 s38, s50, s24\n" \
    "v_mfma_f32_16x16x32_bf16 v[36:39], a[0:3], v[52:55], 0\nds_read_b128 v[68:71], v2 offset:9216\ns_max_i32 s38, s38, 0\n" \
    "v_mfma_f32_16x16x32_bf16 v[28:31], a[4:7], v[48:51], 0\ns_add_u32 m0, s52, 49152\ns_add_u32 s69, s54, 1536\ns_and_b32 s38, s38, s16\n" \
    "v_mfma_f32_16x16x32_bf16 v[40:43], a[4:7], v[52:55], 0\nbuffer_load_dwordx4 v1, s[64:67], s69 offen offset:0 sc1 lds\ns_lshl_b32 s38, s38, 19\n" \
    "v_mfma_f32_16x16x32_bf16 v[32:35], v[124:127], v[48:51], 0\ns_add_u32 s38, s38, s53\ns_add_u32 s74, s48, s38\ns_lshr_b32 s74, s74, 8\n" \
    "v_mfma_f32_16x16x32_bf16 v[44:47], v[124:127], v[52:55], 0\ns_waitcnt lgkmcnt(4)\ns_xnor_b32 s74, s74, s49\ns_and_b32 s74, s74, 0x2000\n" \
    "v_mfma_f32_16x16x32_bf16 v[24:27], a[8:11], v[56:59], v[24:27]\nds_read_b128 v[72:75], v3 offset:1024\ns_add_u32 s74, s74, s38\n" \
    "v_mfma_f32_16x16x32_bf16 v[36:39], a[8:11], v[60:63], v[36:39]\nds_read_b128 v[76:79], v3 offset:9216\ns_add_u32 s74, s74, s60\n" \
    "v_mfma_f32_16x16x32_bf16 v[28:31], a[12:15], v[56:59], v[28:31]\ns_add_u32 s68, s50, 1\ns_min_u32 s68, s68, 63\ns_lshl_b32 s68, s68, 20\n" \
    "v_mfma_f32_16x16x32_bf16 v[40:43], a[12:15], v[60:63], v[40:43]\ns_add_u32 s68, s68, s18\ns_add_u32 s70, s48, s68\ns_lshr_b32 s70, s70, 8\n" \
    "v_mfma_f32_16x16x32_bf16 v[32:35], v[128:131], v[56:59], v[32:35]\ns_xnor_b32 s70, s70, s49\ns_and_b32 s70, s70, 0x2000\ns_add_u32 s70, s70, s68\n" \
    "v_mfma_f32_16x16x32_bf16 v[44:47], v[128:131], v[60:63], v[44:47]\ns_waitcnt lgkmcnt(2)\ns_add_u32 s69, s54, 640\n" \
    "v_mfma_f32_16x16x32_bf16 v[24:27], a[16:19], v[64:67], v[24:27]\nds_read_b128 v[48:51], v2 offset:2048\nv_mfma_f32_16x16x32_bf16 v[36:39], a[16:19], v[68:71], v[36:39]\n" \
    "ds_read_b128 v[52:55], v2 offset:10240\nv_mfma_f32_16x16x32_bf16 v[28:31], a[20:23], v[64:67], v[28:31]\nbuffer_load_dwordx4 v1, s[64:67], s69 offen offset:1024 sc1 lds\n" \
    "s_add_u32 s69, s54, -256\nv_mfma_f32_16x16x32_bf16 v[40:43], a[20:23], v[68:71], v[40:43]\nbuffer_load_dwordx4 v1, s[64:67], s69 offen offset:2048 sc1 lds\n" \
    "s_sub_u32 s77, s50, s23\nv_mfma_f32_16x16x32_bf16 v[32:35], v[132:135], v[64:67], v[32:35]\ns_max_i32 s76, s77, 0\ns_and_b32 s76, s76, s16\ns_lshl_b32 s76, s76, 19\n" \
    "v_mfma_f32_16x16x32_bf16 v[44:47], v[132:135], v[68:71], v[44:47]\ns_waitcnt lgkmcnt(2)\ns_add_u32 s76, s76, s20\ns_add_u32 s75, s48, s76\n" \
    "v_mfma_f32_16x16x32_bf16 v[24:27], a[24:27], v[72:75], v[24:27]\nds_read_b128 v[56:59], v3 offset:2048\ns_lshr_b32 s75, s75, 8\n" \
    "v_mfma_f32_16x16x32_bf16 v[36:39], a[24:27], v[76:79], v[36:39]\nds_read_b128 v[60:63], v3 offset:10240\ns_xnor_b32 s75, s75, s49\n" \
    "v_mfma_f32_16x16x32_bf16 v[28:31], a[28:31], v[72:75], v[28:31]\ns_add_u32 s69, s54, -1152\nbuffer_load_dwordx4 v1, s[64:67], s69 offen offset:3072 sc1 lds\n" \
    "v_mfma_f32_16x16x32_bf16 v[40:43], a[28:31], v[76:79], v[40:43]\ns_and_b32 s75, s75, 0x2000\ns_add_u32 s75, s75, s76\ns_add_u32 s75, s75, s57\n" \
    "v_mfma_f32_16x16x32_bf16 v[32:35], v[136:139], v[72:75], v[32:35]\nv_mfma_f32_16x16x32_bf16 v[44:47], v[136:139], v[76:79], v[44:47]\ns_waitcnt lgkmcnt(2)\n" \
    "v_mfma_f32_16x16x32_bf16 v[24:27], a[32:35], v[48:51], v[24:27]\nds_read_b128 v[64:67], v2 offset:3072\nv_mfma_f32_16x16x32_bf16 v[36:39], a[32:35], v[52:55], v[36:39]\n" \
    "ds_read_b128 v[68:71], v2 offset:11264\nv_mfma_f32_16x16x32_bf16 v[28:31], a[36:39], v[48:51], v[28:31]\ns_and_b32 s37, s50, s16\ns_lshl_b32 s37, s37, 19\n" \
    "s_add_u32 s37, s37, s53\nv_mfma_f32_16x16x32_bf16 v[40:43], a[36:39], v[52:55], v[40:43]\ns_add_u32 s71, s48, s37\ns_lshr_b32 s71, s71, 8\ns_xnor_b32 s71, s71, s49\n" \
    "v_mfma_f32_16x16x32_bf16 v[32:35], v[140:143], v[48:51], v[32:35]\ns_and_b32 s71, s71, 0x2000\ns_add_u32 s71, s71, s37\ns_mov_b32 m0, s56\n" \
    "v_mfma_f32_16x16x32_bf16 v[44:47], v[140:143], v[52:55], v[44:47]\ns_waitcnt lgkmcnt(2)\nbuffer_load_dwordx4 v0, s[64:67], s74 offen offset:0 sc1 lds\n" \
    "v_mfma_f32_16x16x32_bf16 v[24:27], a[40:43], v[56:59], v[24:27]\nds_read_b128 v[72:75], v3 offset:3072\nv_mfma_f32_16x16x32_bf16 v[36:39], a[40:43], v[60:63], v[36:39]\n" \
    "ds_read_b128 v[76:79], v3 offset:11264\nv_mfma_f32_16x16x32_bf16 v[28:31], a[44:47], v[56:59], v[28:31]\nbuffer_load_dwordx4 v0, s[64:67], s74 offen offset:1024 sc1 lds\n" \
    "v_mfma_f32_16x16x32_bf16 v[40:43], a[44:47], v[60:63], v[40:43]\nbuffer_load_dwordx4 v[96:99], v9, s[64:67], s75 offen offset:0 sc1\n" \
    "v_mfma_f32_16x16x32_bf16 v[32:35], v[144:147], v[56:59], v[32:35]\nbuffer_load_dwordx4 v[100:103], v9, s[64:67], s75 offen offset:512 sc1\n" \
    "v_mfma_f32_16x16x32_bf16 v[44:47], v[144:147], v[60:63], v[44:47]\ns_waitcnt vmcnt(14) lgkmcnt(0)\ns_barrier\n" \
    "v_mfma_f32_16x16x32_bf16 v[24:27], a[48:51], v[64:67], v[24:27]\nds_read_b128 v[48:51], v2 offset:16384\nv_mfma_f32_16x16x32_bf16 v[36:39], a[48:51], v[68:71], v[36:39]\n" \
    "ds_read_b128 v[52:55], v2 offset:24576\nv_mfma_f32_16x16x32_bf16 v[28:31], a[52:55], v[64:67], v[28:31]\n" \
    "buffer_load_dwordx4 v[104:107], v9, s[64:67], s75 offen offset:1024 sc1\ns_add_u32 s39, s50, 1\nv_mfma_f32_16x16x32_bf16 v[40:43], a[52:55], v[68:71], v[40:43]\n" \
    "buffer_load_dwordx4 v[108:111], v9, s[64:67], s75 offen offset:1536 sc1\ns_add_u32 s97, s39, 1\nv_mfma_f32_16x16x32_bf16 v[32:35], v[148:151], v[64:67], v[32:35]\n" \
    "s_lshl_b32 s78, s77, 20\ns_add_u32 s78, s78, s21\ns_add_u32 s76, s48, s78\nv_mfma_f32_16x16x32_bf16 v[44:47], v[148:151], v[68:71], v[44:47]\ns_lshr_b32 s76, s76, 8\n" \
    "s_xor_b32 s76, s76, s49\ns_and_b32 s76, s76, 0x2000\nv_mfma_f32_16x16x32_bf16 v[24:27], a[56:59], v[72:75], v[24:27]\nds_read_b128 v[56:59], v3 offset:16384\n" \
    "s_add_u32 s76, s76, s78\nv_mfma_f32_16x16x32_bf16 v[36:39], a[56:59], v[76:79], v[36:39]\nds_read_b128 v[60:63], v3 offset:24576\ns_min_u32 s97, s97, 63\n" \
    "v_mfma_f32_16x16x32_bf16 v[28:31], a[60:63], v[72:75], v[28:31]\ns_cmp_lt_i32 s77, 0\ns_cselect_b32 s76, 0x100000, s76\ns_add_u32 s78, s76, s58\n" \
    "v_mfma_f32_16x16x32_bf16 v[40:43], a[60:63], v[76:79], v[40:43]\ns_lshl_b32 s79, s77, 2\ns_add_u32 s79, s79, s22\ns_sub_u32 s98, s39, s23\n" \
    "v_mfma_f32_16x16x32_bf16 v[32:35], v[152:155], v[72:75], v[32:35]\ns_add_u32 s99, s98, 1\ns_cmp_gt_i32 s51, s97\ns_cselect_b32 s96, s98, 0x7fffffff\n" \
    "v_mfma_f32_16x16x32_bf16 v[44:47], v[152:155], v[76:79], v[44:47]\ns_waitcnt lgkmcnt(2)\ns_sub_u32 s39, s39, 2\n" \
    "v_mfma_f32_16x16x32_bf16 v[24:27], a[64:67], v[48:51], v[24:27]\nds_read_b128 v[64:67], v2 offset:17408\nv_mfma_f32_16x16x32_bf16 v[36:39], a[64:67], v[52:55], v[36:39]\n" \
    "ds_read_b128 v[68:71], v2 offset:25600\nv_mfma_f32_16x16x32_bf16 v[28:31], a[68:71], v[48:51], v[28:31]\ns_add_u32 m0, s52, 0\ns_add_u32 s69, s70, 0\n" \
    "v_mfma_f32_16x16x32_bf16 v[40:43], a[68:71], v[52:55], v[40:43]\nbuffer_load_dwordx4 v1, s[64:67], s69 offen offset:0 sc1 lds\n" \
    "v_mfma_f32_16x16x32_bf16 v[32:35], v[156:159], v[48:51], v[32:35]\ns_cmp_lt_u32 s39, 62\ns_cselect_b32 s96, s96, 0x7fffffff\n" \
    "v_mfma_f32_16x16x32_bf16 v[44:47], v[156:159], v[52:55], v[44:47]\ns_waitcnt lgkmcnt(2)\nv_mfma_f32_16x16x32_bf16 v[24:27], a[72:75], v[56:59], v[24:27]\n" \
    "ds_read_b128 v[72:75], v3 offset:17408\nv_mfma_f32_16x16x32_bf16 v[36:39], a[72:75], v[60:63], v[36:39]\nds_read_b128 v[76:79], v3 offset:25600\n" \
    "v_mfma_f32_16x16x32_bf16 v[28:31], a[76:79], v[56:59], v[28:31]\ns_add_u32 s69, s70, -896\nbuffer_load_dwordx4 v1, s[64:67], s69 offen offset:1024 sc1 lds\n" \
    "v_mfma_f32_16x16x32_bf16 v[40:43], a[76:79], v[60:63], v[40:43]\nv_mfma_f32_16x16x32_bf16 v[32:35], v[160:163], v[56:59], v[32:35]\n" \
    "v_mfma_f32_16x16x32_bf16 v[44:47], v[160:163], v[60:63], v[44:47]\ns_waitcnt lgkmcnt(2)\nv_mfma_f32_16x16x32_bf16 v[24:27], a[80:83], v[64:67], v[24:27]\n" \
    "ds_read_b128 v[48:51], v2 offset:18432\nv_mfma_f32_16x16x32_bf16 v[36:39], a[80:83], v[68:71], v[36:39]\nds_read_b128 v[52:55], v2 offset:26624\n" \
    "v_mfma_f32_16x16x32_bf16 v[28:31], a[84:87], v[64:67], v[28:31]\ns_add_u32 s69, s70, -1792\nbuffer_load_dwordx4 v1, s[64:67], s69 offen offset:2048 sc1 lds\n" \
    "v_mfma_f32_16x16x32_bf16 v[40:43], a[84:87], v[68:71], v[40:43]\ns_add_u32 s68, s59, s26\ns_and_b32 s68, s68, 0xffff\nv_add_u32 v122, s68, v13\n" \
    "v_mfma_f32_16x16x32_bf16 v[32:35], v[164:167], v[64:67], v[32:35]\nds_read_b128 v[80:83], v122 offset:0\nv_mfma_f32_16x16x32_bf16 v[44:47], v[164:167], v[68:71], v[44:47]\n" \
    "s_waitcnt lgkmcnt(3)\nds_read_b128 v[84:87], v122 offset:1024\nv_mfma_f32_16x16x32_bf16 v[24:27], a[88:91], v[72:75], v[24:27]\nds_read_b128 v[56:59], v3 offset:18432\n" \
    "v_mfma_f32_16x16x32_bf16 v[36:39], a[88:91], v[76:79], v[36:39]\nds_read_b128 v[60:63], v3 offset:26624\nv_mfma_f32_16x16x32_bf16 v[28:31], a[92:95], v[72:75], v[28:31]\n" \
    "s_add_u32 s69, s70, -2688\nbuffer_load_dwordx4 v1, s[64:67], s69 offen offset:3072 sc1 lds\nv_mfma_f32_16x16x32_bf16 v[40:43], a[92:95], v[76:79], v[40:43]\n" \
    "ds_read_b128 v[88:91], v122 offset:2048\nv_mfma_f32_16x16x32_bf16 v[32:35], v[168:171], v[72:75], v[32:35]\nds_read_b128 v[92:95], v122 offset:3072\n" \
    "v_mfma_f32_16x16x32_bf16 v[44:47], v[168:171], v[76:79], v[44:47]\ns_waitcnt lgkmcnt(6)\nv_mfma_f32_16x16x32_bf16 v[24:27], a[96:99], v[48:51], v[24:27]\n" \
    "ds_read_b128 v[64:67], v2 offset:19456\nv_mfma_f32_16x16x32_bf16 v[36:39], a[96:99], v[52:55], v[36:39]\nds_read_b128 v[68:71], v2 offset:27648\n" \
    "v_mfma_f32_16x16x32_bf16 v[28:31], a[100:103], v[48:51], v[28:31]\nbuffer_load_dword v20, v6, s[64:67], 0 offen sc1\n" \
    "v_mfma_f32_16x16x32_bf16 v[40:43], a[100:103], v[52:55], v[40:43]\nv_mfma_f32_16x16x32_bf16 v[32:35], v[172:175], v[48:51], v[32:35]\ns_waitcnt vmcnt(5)\n" \
    "v_max3_u32 v123, v96, v97, v98\nv_max3_u32 v123, v123, v99, v100\nv_mfma_f32_16x16x32_bf16 v[44:47], v[172:175], v[52:55], v[44:47]\ns_waitcnt lgkmcnt(4)\n" \
    "v_max3_u32 v123, v123, v101, v102\nv_max3_u32 v123, v123, v103, v104\nv_mfma_f32_16x16x32_bf16 v[24:27], a[104:107], v[56:59], v[24:27]\n" \
    "ds_read_b128 v[72:75], v3 offset:19456\nv_max3_u32 v123, v123, v105, v106\nv_mfma_f32_16x16x32_bf16 v[36:39], a[104:107], v[60:63], v[36:39]\n" \
    "ds_read_b128 v[76:79], v3 offset:27648\nv_max3_u32 v123, v123, v107, v108\nv_mfma_f32_16x16x32_bf16 v[28:31], a[108:111], v[56:59], v[28:31]\ns_memrealtime s[72:73]\n" \
    "v_max3_u32 v123, v123, v109, v110\nv_max_u32 v123, v123, v111\nv_mfma_f32_16x16x32_bf16 v[40:43], a[108:111], v[60:63], v[40:43]\nv_cmp_eq_u32 vcc, -1, v123\n" \
    "v_mfma_f32_16x16x32_bf16 v[32:35], v[176:179], v[56:59], v[32:35]\ns_cbranch_vccnz .Lprqkm_%=\n.Lprqkmb_%=:\n" \
    "v_mfma_f32_16x16x32_bf16 v[44:47], v[176:179], v[60:63], v[44:47]\ns_waitcnt lgkmcnt(0)\ns_barrier\nv_mfma_f32_16x16x32_bf16 v[24:27], a[112:115], v[64:67], v[24:27]\n" \
    "ds_read_b128 v[48:51], v2 offset:32768\nv_mov_b32 v123, s50\nv_mfma_f32_16x16x32_bf16 v[36:39], a[112:115], v[68:71], v[36:39]\nds_read_b128 v[52:55], v2 offset:40960\n" \
    "v_mfma_f32_16x16x32_bf16 v[28:31], a[116:119], v[64:67], v[28:31]\nv_mfma_f32_16x16x32_bf16 v[16:19], v[96:99], v[80:83], 0\n" \
    "buffer_store_dword v123, v5, s[64:67], 0 offen sc1\nv_mfma_f32_16x16x32_bf16 v[40:43], a[116:119], v[68:71], v[40:43]\n" \
    "v_mfma_f32_16x16x32_bf16 v[16:19], v[100:103], v[84:87], v[16:19]\nv_mfma_f32_16x16x32_bf16 v[32:35], v[180:183], v[64:67], v[32:35]\n" \
    "v_mfma_f32_16x16x32_bf16 v[16:19], v[104:107], v[88:91], v[16:19]\nv_mfma_f32_16x16x32_bf16 v[44:47], v[180:183], v[68:71], v[44:47]\n" \
    "v_mfma_f32_16x16x32_bf16 v[16:19], v[108:111], v[92:95], v[16:19]\nv_mfma_f32_16x16x32_bf16 v[24:27], a[120:123], v[72:75], v[24:27]\n" \
    "ds_read_b128 v[56:59], v3 offset:32768\nv_mfma_f32_16x16x32_bf16 v[36:39], a[120:123], v[76:79], v[36:39]\nds_read_b128 v[60:63], v3 offset:40960\n" \
    "v_mfma_f32_16x16x32_bf16 v[28:31], a[124:127], v[72:75], v[28:31]\nv_mfma_f32_16x16x32_bf16 v[40:43], a[124:127], v[76:79], v[40:43]\n" \
    "v_mfma_f32_16x16x32_bf16 v[32:35], v[184:187], v[72:75], v[32:35]\ns_nop 0\nv_max3_f32 v122, v120, v16, v17\nv_max3_f32 v122, v122, v18, v19\n" \
    "v_mfma_f32_16x16x32_bf16 v[44:47], v[184:187], v[76:79], v[44:47]\ns_waitcnt lgkmcnt(2)\nv_mov_b32 v123, v122\n" \
    "v_mfma_f32_16x16x32_bf16 v[24:27], a[128:131], v[48:51], v[24:27]\nds_read_b128 v[64:67], v2 offset:33792\n" \
    "v_mfma_f32_16x16x32_bf16 v[36:39], a[128:131], v[52:55], v[36:39]\nds_read_b128 v[68:71], v2 offset:41984\n" \
    "v_mfma_f32_16x16x32_bf16 v[28:31], a[132:135], v[48:51], v[28:31]\ns_add_u32 m0, s52, 16384\ns_add_u32 s69, s70, 512\n" \
    "v_mfma_f32_16x16x32_bf16 v[40:43], a[132:135], v[52:55], v[40:43]\nbuffer_load_dwordx4 v1, s[64:67], s69 offen offset:0 sc1 lds\n" \
    "v_mfma_f32_16x16x32_bf16 v[32:35], v[188:191], v[48:51], v[32:35]\nv_permlane16_swap_b32 v122, v123\nv_mfma_f32_16x16x32_bf16 v[44:47], v[188:191], v[52:55], v[44:47]\n" \
    "s_waitcnt lgkmcnt(2)\nv_max_f32 v122, v122, v123\nv_mov_b32 v123, v122\nv_mfma_f32_16x16x32_bf16 v[24:27], a[136:139], v[56:59], v[24:27]\n" \
    "ds_read_b128 v[72:75], v3 offset:33792\nv_mfma_f32_16x16x32_bf16 v[36:39], a[136:139], v[60:63], v[36:39]\nds_read_b128 v[76:79], v3 offset:41984\n" \
    "v_mfma_f32_16x16x32_bf16 v[28:31], a[140:143], v[56:59], v[28:31]\ns_add_u32 s69, s70, -384\nbuffer_load_dwordx4 v1, s[64:67], s69 offen offset:1024 sc1 lds\n" \
    "v_mfma_f32_16x16x32_bf16 v[40:43], a[140:143], v[60:63], v[40:43]\nv_permlane32_swap_b32 v122, v123\nv_mfma_f32_16x16x32_bf16 v[32:35], v[192:195], v[56:59], v[32:35]\n" \
    "v_max_f32 v122, v122, v123\nv_mul_f32_e64 v122, -s32, v122\nv_mfma_f32_16x16x32_bf16 v[44:47], v[192:195], v[60:63], v[44:47]\ns_waitcnt lgkmcnt(2)\n" \
    "v_fma_f32 v120, v120, s32, v122\nv_fma_f32 v16, v16, s32, v122\nv_mfma_f32_16x16x32_bf16 v[24:27], a[144:147], v[64:67], v[24:27]\nds_read_b128 v[48:51], v2 offset:34816\n" \
    "v_fma_f32 v17, v17, s32, v122\nv_mfma_f32_16x16x32_bf16 v[36:39], a[144:147], v[68:71], v[36:39]\nds_read_b128 v[52:55], v2 offset:43008\nv_fma_f32 v18, v18, s32, v122\n" \
    "v_mfma_f32_16x16x32_bf16 v[28:31], a[148:151], v[64:67], v[28:31]\ns_add_u32 s69, s70, -1280\nbuffer_load_dwordx4 v1, s[64:67], s69 offen offset:2048 sc1 lds\n" \
    "v_mfma_f32_16x16x32_bf16 v[40:43], a[148:151], v[68:71], v[40:43]\nv_fma_f32 v19, v19, s32, v122\nv_mfma_f32_16x16x32_bf16 v[32:35], v[196:199], v[64:67], v[32:35]\n" \
    "v_exp_f32 v120, v120\nv_mfma_f32_16x16x32_bf16 v[44:47], v[196:199], v[68:71], v[44:47]\ns_waitcnt lgkmcnt(2)\nv_exp_f32 v16, v16\n" \
    "v_mfma_f32_16x16x32_bf16 v[24:27], a[152:155], v[72:75], v[24:27]\nds_read_b128 v[56:59], v3 offset:34816\n" \
    "v_mfma_f32_16x16x32_bf16 v[36:39], a[152:155], v[76:79], v[36:39]\nds_read_b128 v[60:63], v3 offset:43008\n" \
    "v_mfma_f32_16x16x32_bf16 v[28:31], a[156:159], v[72:75], v[28:31]\ns_add_u32 s69, s70, -2176\nbuffer_load_dwordx4 v1, s[64:67], s69 offen offset:3072 sc1 lds\n" \
    "v_mfma_f32_16x16x32_bf16 v[40:43], a[156:159], v[76:79], v[40:43]\nv_exp_f32 v17, v17\nv_mfma_f32_16x16x32_bf16 v[32:35], v[200:203], v[72:75], v[32:35]\n" \
    "v_exp_f32 v18, v18\nv_mfma_f32_16x16x32_bf16 v[44:47], v[200:203], v[76:79], v[44:47]\ns_waitcnt lgkmcnt(2)\nv_exp_f32 v19, v19\n" \
    "v_mfma_f32_16x16x32_bf16 v[24:27], a[160:163], v[48:51], v[24:27]\nds_read_b128 v[64:67], v2 offset:35840\nv_add_f32 v122, v16, v17\n" \
    "v_mfma_f32_16x16x32_bf16 v[36:39], a[160:163], v[52:55], v[36:39]\nds_read_b128 v[68:71], v2 offset:44032\nv_add_f32 v123, v18, v19\n" \
    "v_mfma_f32_16x16x32_bf16 v[28:31], a[164:167], v[48:51], v[28:31]\ns_waitcnt vmcnt(5)\nv_writelane_b32 v20, s72, 2\nv_mul_f32 v121, v121, v120\n" \
    "v_mfma_f32_16x16x32_bf16 v[40:43], a[164:167], v[52:55], v[40:43]\nds_write_b32 v7, v20\nv_mfma_f32_16x16x32_bf16 v[32:35], v[204:207], v[48:51], v[32:35]\n" \
    "v_add_f32 v122, v122, v123\nv_fma_f32 v121, 0.5, v122, v121\nv_mfma_f32_16x16x32_bf16 v[44:47], v[204:207], v[52:55], v[44:47]\ns_waitcnt lgkmcnt(3)\nv_mov_b32 v123, v121\n" \
    "v_mfma_f32_16x16x32_bf16 v[24:27], a[168:171], v[56:59], v[24:27]\nds_read_b128 v[72:75], v3 offset:35840\n" \
    "v_mfma_f32_16x16x32_bf16 v[36:39], a[168:171], v[60:63], v[36:39]\nds_read_b128 v[76:79], v3 offset:44032\n" \
    "v_mfma_f32_16x16x32_bf16 v[28:31], a[172:175], v[56:59], v[28:31]\nv_permlane16_swap_b32 v121, v123\nv_mfma_f32_16x16x32_bf16 v[40:43], a[172:175], v[60:63], v[40:43]\n" \
    "v_add_f32 v121, v121, v123\nv_mov_b32 v123, v121\nv_mfma_f32_16x16x32_bf16 v[32:35], v[208:211], v[56:59], v[32:35]\n" \
    "v_mfma_f32_16x16x32_bf16 v[44:47], v[208:211], v[60:63], v[44:47]\ns_waitcnt lgkmcnt(0)\ns_barrier\nv_mfma_f32_16x16x32_bf16 v[24:27], a[176:179], v[64:67], v[24:27]\n" \
    "ds_read_b128 v[48:51], v2 offset:49152\nv_mfma_f32_16x16x32_bf16 v[36:39], a[176:179], v[68:71], v[36:39]\nds_read_b128 v[52:55], v2 offset:57344\n" \
    "v_mfma_f32_16x16x32_bf16 v[28:31], a[180:183], v[64:67], v[28:31]\nv_permlane32_swap_b32 v121, v123\nv_mfma_f32_16x16x32_bf16 v[40:43], a[180:183], v[68:71], v[40:43]\n" \
    "ds_read_b96 v[108:110], v15\nv_add_f32 v121, v121, v123\nv_mfma_f32_16x16x32_bf16 v[32:35], v[212:215], v[64:67], v[32:35]\nv_rcp_f32 v121, v121\n" \
    "v_mfma_f32_16x16x32_bf16 v[44:47], v[212:215], v[68:71], v[44:47]\nv_mul_f32 v120, v120, v121\nv_mul_f32 v16, v16, v121\n" \
    "v_mfma_f32_16x16x32_bf16 v[24:27], a[184:187], v[72:75], v[24:27]\nds_read_b128 v[56:59], v3 offset:49152\nv_mul_f32 v17, v17, v121\n" \
    "v_mfma_f32_16x16x32_bf16 v[36:39], a[184:187], v[76:79], v[36:39]\nds_read_b128 v[60:63], v3 offset:57344\nv_mul_f32 v18, v18, v121\n" \
    "v_mfma_f32_16x16x32_bf16 v[28:31], a[188:191], v[72:75], v[28:31]\nv_mul_f32 v19, v19, v121\nv_mul_f32 v112, v112, v120\n" \
    "v_mfma_f32_16x16x32_bf16 v[40:43], a[188:191], v[76:79], v[40:43]\nv_mul_f32 v113, v113, v120\nv_mul_f32 v114, v114, v120\n" \
    "v_mfma_f32_16x16x32_bf16 v[32:35], v[216:219], v[72:75], v[32:35]\nv_mul_f32 v115, v115, v120\nv_mul_f32 v116, v116, v120\n" \
    "v_mfma_f32_16x16x32_bf16 v[44:47], v[216:219], v[76:79], v[44:47]\ns_waitcnt lgkmcnt(3)\nv_mul_f32 v117, v117, v120\nv_mul_f32 v118, v118, v120\n" \
    "v_mfma_f32_16x16x32_bf16 v[24:27], a[192:195], v[48:51], v[24:27]\nds_read_b128 v[64:67], v2 offset:50176\nv_mul_f32 v119, v119, v120\n" \
    "v_mfma_f32_16x16x32_bf16 v[36:39], a[192:195], v[52:55], v[36:39]\nds_read_b128 v[68:71], v2 offset:58368\nv_cvt_pk_bf16_f32 v16, v16, v17\n" \
    "v_mfma_f32_16x16x32_bf16 v[28:31], a[196:199], v[48:51], v[28:31]\ns_add_u32 m0, s52, 32768\ns_add_u32 s69, s70, 1024\nv_cvt_pk_bf16_f32 v17, v18, v19\n" \
    "v_mfma_f32_16x16x32_bf16 v[40:43], a[196:199], v[52:55], v[40:43]\nbuffer_load_dwordx4 v1, s[64:67], s69 offen offset:0 sc1 lds\nv_cvt_pk_bf16_f32 v112, v112, v113\n" \
    "v_mfma_f32_16x16x32_bf16 v[32:35], v[220:223], v[48:51], v[32:35]\nv_cvt_pk_bf16_f32 v113, v114, v115\nv_cvt_pk_bf16_f32 v114, v116, v117\n" \
    "v_mfma_f32_16x16x32_bf16 v[44:47], v[220:223], v[52:55], v[44:47]\ns_waitcnt lgkmcnt(2)\nv_cvt_pk_bf16_f32 v115, v118, v119\n" \
    "v_mfma_f32_16x16x32_bf16 v[24:27], a[200:203], v[56:59], v[24:27]\nds_read_b128 v[72:75], v3 offset:50176\n" \
    "v_mfma_f32_16x16x32_bf16 v[36:39], a[200:203], v[60:63], v[36:39]\nds_read_b128 v[76:79], v3 offset:58368\n" \
    "v_mfma_f32_16x16x32_bf16 v[28:31], a[204:207], v[56:59], v[28:31]\ns_add_u32 s69, s70, 128\nbuffer_load_dwordx4 v1, s[64:67], s69 offen offset:1024 sc1 lds\n" \
    "v_mfma_f32_16x16x32_bf16 v[40:43], a[204:207], v[60:63], v[40:43]\nbuffer_store_dwordx4 v[112:115], v0, s[64:67], s78 offen sc1\n" \
    "v_mfma_f32_16x16x32_bf16 v[32:35], v[224:227], v[56:59], v[32:35]\nbuffer_store_dwordx2 v[16:17], v10, s[64:67], s76 offen sc1\n" \
    "v_mfma_f32_16x16x32_bf16 v[44:47], v[224:227], v[60:63], v[44:47]\ns_waitcnt lgkmcnt(2)\nv_mov_b32 v21, 1\n" \
    "v_mfma_f32_16x16x32_bf16 v[24:27], a[208:211], v[64:67], v[24:27]\nds_read_b128 v[48:51], v2 offset:51200\n" \
    "v_mfma_f32_16x16x32_bf16 v[36:39], a[208:211], v[68:71], v[36:39]\nds_read_b128 v[52:55], v2 offset:59392\n" \
    "v_mfma_f32_16x16x32_bf16 v[28:31], a[212:215], v[64:67], v[28:31]\ns_add_u32 s69, s70, -768\nbuffer_load_dwordx4 v1, s[64:67], s69 offen offset:2048 sc1 lds\n" \
    "v_mfma_f32_16x16x32_bf16 v[40:43], a[212:215], v[68:71], v[40:43]\ns_mov_b64 exec, 1\nbuffer_atomic_add v21, off, s[64:67], s79 sc0\ns_mov_b64 exec, -1\n" \
    "v_mfma_f32_16x16x32_bf16 v[32:35], v[228:231], v[64:67], v[32:35]\nv_readfirstlane_b32 s72, v108\nv_readfirstlane_b32 s73, v109\n" \
    "v_mfma_f32_16x16x32_bf16 v[44:47], v[228:231], v[68:71], v[44:47]\ns_waitcnt lgkmcnt(2)\nv_readfirstlane_b32 s40, v110\n" \
    "v_mfma_f32_16x16x32_bf16 v[24:27], a[216:219], v[72:75], v[24:27]\nds_read_b128 v[56:59], v3 offset:51200\n" \
    "v_mfma_f32_16x16x32_bf16 v[36:39], a[216:219], v[76:79], v[36:39]\nds_read_b128 v[60:63], v3 offset:59392\ns_lshr_b32 s41, s72, 8\n" \
    "v_mfma_f32_16x16x32_bf16 v[28:31], a[220:223], v[72:75], v[28:31]\ns_add_u32 s69, s70, -1664\nbuffer_load_dwordx4 v1, s[64:67], s69 offen offset:3072 sc1 lds\n" \
    "v_mfma_f32_16x16x32_bf16 v[40:43], a[220:223], v[76:79], v[40:43]\nbuffer_load_dword v20, v6, s[64:67], 0 offen sc1\ns_sub_u32 s41, s40, s41\n" \
    "v_mfma_f32_16x16x32_bf16 v[32:35], v[232:235], v[72:75], v[32:35]\ns_lshl_b32 s41, s41, 8\ns_and_b32 s72, s72, 127\n" \
    "v_mfma_f32_16x16x32_bf16 v[44:47], v[232:235], v[76:79], v[44:47]\ns_waitcnt lgkmcnt(2)\ns_cmp_lt_i32 s41, s30\ns_subb_u32 s72, s72, 0\n" \
    "v_mfma_f32_16x16x32_bf16 v[24:27], a[224:227], v[48:51], v[24:27]\nds_read_b128 v[64:67], v2 offset:52224\ns_max_i32 s72, s72, 0\n" \
    "v_mfma_f32_16x16x32_bf16 v[36:39], a[224:227], v[52:55], v[36:39]\nds_read_b128 v[68:71], v2 offset:60416\n" \
    "v_mfma_f32_16x16x32_bf16 v[28:31], a[228:231], v[48:51], v[28:31]\nds_read_b128 v[72:75], v3 offset:52224\n" \
    "v_mfma_f32_16x16x32_bf16 v[40:43], a[228:231], v[52:55], v[40:43]\nds_read_b128 v[76:79], v3 offset:60416\n" \
    "v_mfma_f32_16x16x32_bf16 v[32:35], v[236:239], v[48:51], v[32:35]\ns_cmp_gt_i32 s72, s96\ns_cselect_b32 s41, s73, 0x80000000\n" \
    "v_mfma_f32_16x16x32_bf16 v[44:47], v[236:239], v[52:55], v[44:47]\ns_waitcnt lgkmcnt(4)\nv_mfma_f32_16x16x32_bf16 v[32:35], v[240:243], v[56:59], v[32:35]\n" \
    "v_add_u32 v122, s59, v11\nv_add_u32 v123, s59, v12\nv_mfma_f32_16x16x32_bf16 v[44:47], v[240:243], v[60:63], v[44:47]\ns_waitcnt lgkmcnt(2)\ns_add_u32 s59, s59, 0x2000\n" \
    "s_and_b32 s59, s59, 0xffff\nv_mfma_f32_16x16x32_bf16 v[32:35], v[244:247], v[64:67], v[32:35]\ns_waitcnt lgkmcnt(0)\ns_barrier\n" \
    "v_mfma_f32_16x16x32_bf16 v[44:47], v[244:247], v[68:71], v[44:47]\nds_read_b128 v[48:51], v2 offset:0\nv_mfma_f32_16x16x32_bf16 v[32:35], v[248:251], v[72:75], v[32:35]\n" \
    "ds_read_b128 v[52:55], v2 offset:8192\nv_mfma_f32_16x16x32_bf16 v[44:47], v[248:251], v[76:79], v[44:47]\ns_mov_b32 s54, s70\ns_add_u32 s50, s50, 1\n" \
    "v_mfma_f32_16x16x32_bf16 v[24:27], a[232:235], v[56:59], v[24:27]\ns_nop 1\nv_mfma_f32_16x16x32_bf16 v[24:27], a[240:243], v[64:67], v[24:27]\ns_nop 0\n" \
    "v_cvt_pk_bf16_f32 v80, v32, v33\nv_cvt_pk_bf16_f32 v81, v34, v35\nv_mfma_f32_16x16x32_bf16 v[24:27], a[248:251], v[72:75], v[24:27]\nv_cvt_pk_bf16_f32 v82, v44, v45\n" \
    "v_cvt_pk_bf16_f32 v83, v46, v47\nv_mfma_f32_16x16x32_bf16 v[28:31], a[236:239], v[56:59], v[28:31]\nv_permlane16_swap_b32 v80, v82\n" \
    "v_mfma_f32_16x16x32_bf16 v[28:31], a[244:247], v[64:67], v[28:31]\nds_read_b128 v[56:59], v3 offset:0\nv_mfma_f32_16x16x32_bf16 v[28:31], a[252:255], v[72:75], v[28:31]\n" \
    "v_permlane16_swap_b32 v81, v83\nv_mfma_f32_16x16x32_bf16 v[36:39], a[232:235], v[60:63], v[36:39]\nbuffer_store_dwordx4 v[80:83], v4, s[64:67], s71 offen sc1\n" \
    "v_mfma_f32_16x16x32_bf16 v[36:39], a[240:243], v[68:71], v[36:39]\nv_cvt_pk_bf16_f32 v84, v24, v25\nv_cvt_pk_bf16_f32 v85, v26, v27\n" \
    "v_mfma_f32_16x16x32_bf16 v[36:39], a[248:251], v[76:79], v[36:39]\ns_nop 0\nv_cvt_pk_bf16_f32 v86, v28, v29\nv_cvt_pk_bf16_f32 v87, v30, v31\n" \
    "v_mfma_f32_16x16x32_bf16 v[40:43], a[236:239], v[60:63], v[40:43]\nv_permlane16_swap_b32 v84, v86\nv_mfma_f32_16x16x32_bf16 v[40:43], a[244:247], v[68:71], v[40:43]\n" \
    "ds_read_b128 v[60:63], v3 offset:8192\nv_mfma_f32_16x16x32_bf16 v[40:43], a[252:255], v[76:79], v[40:43]\nv_permlane16_swap_b32 v85, v87\nds_write_b128 v122, v[84:87]\n" \
    "s_cmp_gt_i32 s41, s99\nv_cvt_pk_bf16_f32 v88, v36, v37\nv_cvt_pk_bf16_f32 v89, v38, v39\ns_nop 2\nv_cvt_pk_bf16_f32 v90, v40, v41\nv_cvt_pk_bf16_f32 v91, v42, v43\n" \
    "s_nop 0\nv_permlane16_swap_b32 v88, v90\nv_permlane16_swap_b32 v89, v91\nds_write_b128 v123, v[88:91]\ns_cbranch_scc1 .Lprqb1_%=\n.Lprqmid_%=:\n" \
    "s_cmp_eq_u32 s96, 0x7fffffff\ns_cbranch_scc1 .Lprqslow_%=\ns_waitcnt vmcnt(1)\nds_write_b32 v7, v20\nv_readfirstlane_b32 s42, v21\ns_cmp_eq_u32 s42, 63\n" \
    "s_cbranch_scc1 .Lprqtn_%=\n.Lprqtnb_%=:\ns_waitcnt lgkmcnt(0)\ns_barrier\nds_read_b64 v[96:97], v15\ns_waitcnt lgkmcnt(0)\nv_readfirstlane_b32 s78, v96\n" \
    "v_readfirstlane_b32 s73, v97\ns_and_b32 s72, s78, 127\ns_cmp_gt_i32 s51, s97\ns_cbranch_scc0 .Lprqslow_%=\ns_cmp_gt_i32 s72, s98\ns_cbranch_scc0 .Lprqslow_%=\n" \
    "s_cmp_gt_i32 s73, s99\ns_cbranch_scc0 .Lprqslow_%=\n.Lprqclk_%=:\ns_memrealtime s[40:41]\ns_mov_b32 s72, s78\ns_waitcnt lgkmcnt(0)\ns_lshr_b32 s41, s72, 8\n" \
    "s_sub_u32 s41, s40, s41\ns_lshl_b32 s41, s41, 8\ns_and_b32 s72, s72, 127\ns_cmp_lt_i32 s41, s31\ns_subb_u32 s72, s72, 0\ns_max_i32 s72, s72, 0\ns_cmp_gt_i32 s72, s98\n" \
    "s_cbranch_scc0 .Lprqclk_%=\ns_branch .Lprqgo_%=\n.Lprqslow_%=:\ns_waitcnt vmcnt(0) lgkmcnt(0)\nv_readfirstlane_b32 s42, v21\ns_cmp_eq_u32 s42, 63\n" \
    "s_cbranch_scc1 .Lprqtm_%=\n.Lprqtmb_%=:\ns_cmp_eq_u32 s50, 64\ns_cbranch_scc1 .Lprqdone_%=\ns_barrier\nv_mov_b32 v96, s50\n" \
    "buffer_store_dword v96, v5, s[64:67], 0 offen sc1\ns_cmp_gt_i32 s51, s97\ns_cbranch_scc0 .Lprqpoll_%=\ns_cmp_gt_i32 s72, s98\ns_cbranch_scc0 .Lprqpoll_%=\n" \
    "s_cmp_gt_i32 s73, s99\ns_cbranch_scc0 .Lprqpoll_%=\ns_branch .Lprqgo_%=\n.Lprqpoll_%=:\ns_mov_b32 s68, 0\ns_cmp_lg_u32 s55, 0\ns_cbranch_scc1 .Lprqjoin_%=\n.Lprqpl2_%=:\n" \
    "s_cmp_gt_i32 s51, s97\ns_cbranch_scc1 .Lprqpl3_%=\nv_lshlrev_b32 v96, 3, v0\nbuffer_load_dword v96, v96, s[64:67], s36 offen sc1\ns_waitcnt vmcnt(0)\n" \
    "v_cmp_gt_u32 vcc, 32, v96\ns_nop 4\ns_ff1_i32_b64 s51, vcc\ns_min_u32 s51, s51, 64\n.Lprqpl3_%=:\nbuffer_load_dword v96, v6, s[64:67], 0 offen sc1\ns_waitcnt vmcnt(0)\n" \
    "v_readlane_b32 s73, v96, 1\n.Lprqpl4_%=:\nv_readlane_b32 s72, v96, 0\ns_memrealtime s[70:71]\ns_and_b32 s78, s72, 127\ns_waitcnt lgkmcnt(0)\ns_mov_b32 s40, s70\n" \
    "s_lshr_b32 s41, s72, 8\ns_sub_u32 s41, s40, s41\ns_lshl_b32 s41, s41, 8\ns_and_b32 s72, s72, 127\ns_cmp_lt_i32 s41, s31\ns_subb_u32 s72, s72, 0\ns_max_i32 s72, s72, 0\n" \
    "s_cmp_gt_i32 s51, s97\ns_cbranch_scc0 .Lprqok_%=\ns_cmp_gt_i32 s72, s98\ns_cbranch_scc0 .Lprqok_%=\ns_cmp_gt_i32 s73, s99\ns_cbranch_scc0 .Lprqok_%=\nv_mov_b32 v96, s51\n" \
    "ds_write_b32 v7, v96\n.Lprqjoin_%=:\ns_waitcnt lgkmcnt(0)\ns_barrier\nds_read_b32 v96, v15\ns_waitcnt lgkmcnt(0)\nv_readfirstlane_b32 s51, v96\ns_branch .Lprqgo_%=\n" \
    ".Lprqok_%=:\ns_sleep 1\ns_cmp_gt_i32 s78, s98\ns_cselect_b32 s69, s73, s99\ns_cmp_gt_i32 s51, s97\ns_cselect_b32 s69, s69, s99\ns_cmp_gt_i32 s69, s99\n" \
    "s_cbranch_scc1 .Lprqpl4_%=\ns_sleep 5\ns_add_u32 s68, s68, 1\ns_and_b32 s43, s68, 63\ns_cbranch_scc1 .Lprqpl2_%=\ns_cmp_gt_u32 s68, 0x200000\ns_cbranch_scc1 .Lprqabort_%=\n" \
    "buffer_load_dword v97, off, s[64:67], 0 sc1\ns_waitcnt vmcnt(0)\nv_readfirstlane_b32 s43, v97\ns_cmp_eq_u32 s43, 1\ns_cbranch_scc0 .Lprqpl2_%=\ns_endpgm\n.Lprqtl_%=:\n" \
    "v_mov_b32 v21, 0\ns_cmp_lt_i32 s77, 0\ns_cbranch_scc1 .Lprqtlb_%=\ns_add_u32 s42, s77, 1\nv_mov_b32 v123, s42\ns_and_b32 s42, s77, 1\ns_lshl_b32 s42, s42, 2\n" \
    "buffer_atomic_umax v123, v14, s[64:67], s42 offen sc1\ns_branch .Lprqtlb_%=\n.Lprqkm_%=:\ns_cmp_lt_i32 s77, 0\ns_cbranch_scc1 .Lprqkmb_%=\ns_mov_b32 s42, 0\n.Lprqkm2_%=:\n" \
    "buffer_load_dwordx4 v[96:99], v9, s[64:67], s75 offen offset:0 sc1\nbuffer_load_dwordx4 v[100:103], v9, s[64:67], s75 offen offset:512 sc1\n" \
    "buffer_load_dwordx4 v[104:107], v9, s[64:67], s75 offen offset:1024 sc1\nbuffer_load_dwordx4 v[108:111], v9, s[64:67], s75 offen offset:1536 sc1\ns_waitcnt vmcnt(0)\n" \
    "v_max3_u32 v123, v96, v97, v98\nv_max3_u32 v123, v123, v99, v100\nv_max3_u32 v123, v123, v101, v102\nv_max3_u32 v123, v123, v103, v104\nv_max3_u32 v123, v123, v105, v106\n" \
    "v_max3_u32 v123, v123, v107, v108\nv_max3_u32 v123, v123, v109, v110\nv_max_u32 v123, v123, v111\nv_cmp_eq_u32 vcc, -1, v123\ns_cbranch_vccz .Lprqkmb_%=\n" \
    "s_add_u32 s42, s42, 1\ns_and_b32 s43, s42, 63\ns_cbranch_scc1 .Lprqkm2_%=\ns_cmp_gt_u32 s42, 0x200000\ns_cbranch_scc1 .Lprqabort_%=\n" \
    "buffer_load_dword v123, off, s[64:67], 0 sc1\ns_waitcnt vmcnt(0)\nv_readfirstlane_b32 s43, v123\ns_cmp_eq_u32 s43, 1\ns_cbranch_scc0 .Lprqkm2_%=\ns_endpgm\n.Lprqtn_%=:\n" \
    "v_mov_b32 v21, 0\ns_cmp_lt_i32 s77, 0\ns_cbranch_scc1 .Lprqtnb_%=\ns_add_u32 s42, s77, 1\nv_mov_b32 v123, s42\ns_and_b32 s42, s77, 1\ns_lshl_b32 s42, s42, 2\n" \
    "buffer_atomic_umax v123, v14, s[64:67], s42 offen sc1\ns_branch .Lprqtnb_%=\n.Lprqtm_%=:\nv_mov_b32 v21, 0\ns_cmp_lt_i32 s77, 0\ns_cbranch_scc1 .Lprqtmb_%=\n" \
    "s_add_u32 s42, s77, 1\nv_mov_b32 v123, s42\ns_and_b32 s42, s77, 1\ns_lshl_b32 s42, s42, 2\nbuffer_atomic_umax v123, v14, s[64:67], s42 offen sc1\ns_branch .Lprqtmb_%=\n" \
    ".Lprqabort_%=:\nv_mov_b32 v96, 1\nbuffer_store_dword v96, off, s[64:67], 0 sc1\ns_endpgm\n.Lprqdone_%=:\n"
#define PROJ_V \
    "ds_read_b128 v[48:51], v2 offset:0\nds_read_b128 v[52:55], v2 offset:8192\nds_read_b128 v[56:59], v3 offset:0\nds_read_b128 v[60:63], v3 offset:8192\ns_mov_b32 s72, 0\n" \
    "s_mov_b32 s73, 0\nv_mov_b32 v21, 0\ns_add_u32 s97, s50, 1\ns_min_u32 s97, s97, 63\ns_lshr_b32 s98, s50, s27\ns_add_u32 s98, s98, s28\ns_min_u32 s98, s98, s29\n" \
    "s_sub_u32 s98, s50, s98\ns_sub_u32 s99, s50, s17\ns_branch .Lprvslow_%=\n.Lprvgo_%=:\ns_waitcnt lgkmcnt(2)\nv_mfma_f32_16x16x32_bf16 v[24:27], a[0:3], v[48:51], 0\n" \
    "ds_read_b128 v[64:67], v2 offset:1024\nv_mfma_f32_16x16x32_bf16 v[36:39], a[0:3], v[52:55], 0\nds_read_b128 v[68:71], v2 offset:9216\n" \
    "v_mfma_f32_16x16x32_bf16 v[28:31], a[4:7], v[48:51], 0\ns_add_u32 m0, s52, 49152\ns_add_u32 s69, s54, 1536\ns_and_b32 s76, s50, s16\n" \
    "v_mfma_f32_16x16x32_bf16 v[40:43], a[4:7], v[52:55], 0\nbuffer_load_dwordx4 v1, s[64:67], s69 offen offset:0 sc1 lds\ns_lshl_b32 s76, s76, 20\n" \
    "v_mfma_f32_16x16x32_bf16 v[32:35], v[124:127], v[48:51], 0\ns_add_u32 s76, s76, s56\ns_add_u32 s74, s48, s76\ns_lshr_b32 s74, s74, 8\n" \
    "v_mfma_f32_16x16x32_bf16 v[44:47], v[124:127], v[52:55], 0\ns_waitcnt lgkmcnt(2)\ns_xor_b32 s74, s74, s49\ns_and_b32 s74, s74, 0x2000\n" \
    "v_mfma_f32_16x16x32_bf16 v[24:27], a[8:11], v[56:59], v[24:27]\nds_read_b128 v[72:75], v3 offset:1024\ns_add_u32 s74, s74, s76\n" \
    "v_mfma_f32_16x16x32_bf16 v[36:39], a[8:11], v[60:63], v[36:39]\nds_read_b128 v[76:79], v3 offset:9216\nv_mfma_f32_16x16x32_bf16 v[28:31], a[12:15], v[56:59], v[28:31]\n" \
    "s_add_u32 s68, s50, 1\ns_min_u32 s68, s68, 63\ns_lshl_b32 s68, s68, 20\nv_mfma_f32_16x16x32_bf16 v[40:43], a[12:15], v[60:63], v[40:43]\ns_add_u32 s68, s68, s18\n" \
    "s_add_u32 s70, s48, s68\ns_lshr_b32 s70, s70, 8\nv_mfma_f32_16x16x32_bf16 v[32:35], v[128:131], v[56:59], v[32:35]\ns_xnor_b32 s70, s70, s49\ns_and_b32 s70, s70, 0x2000\n" \
    "s_add_u32 s70, s70, s68\nv_mfma_f32_16x16x32_bf16 v[44:47], v[128:131], v[60:63], v[44:47]\ns_waitcnt lgkmcnt(2)\ns_add_u32 s69, s54, 640\n" \
    "v_mfma_f32_16x16x32_bf16 v[24:27], a[16:19], v[64:67], v[24:27]\nds_read_b128 v[48:51], v2 offset:2048\nv_mfma_f32_16x16x32_bf16 v[36:39], a[16:19], v[68:71], v[36:39]\n" \
    "ds_read_b128 v[52:55], v2 offset:10240\nv_mfma_f32_16x16x32_bf16 v[28:31], a[20:23], v[64:67], v[28:31]\nbuffer_load_dwordx4 v1, s[64:67], s69 offen offset:1024 sc1 lds\n" \
    "s_add_u32 s69, s54, -256\nv_mfma_f32_16x16x32_bf16 v[40:43], a[20:23], v[68:71], v[40:43]\nbuffer_load_dwordx4 v1, s[64:67], s69 offen offset:2048 sc1 lds\n" \
    "v_mfma_f32_16x16x32_bf16 v[32:35], v[132:135], v[64:67], v[32:35]\nbuffer_store_dwordx4 v[16:19], v8, s[64:67], s74 offen sc1\n" \
    "v_mfma_f32_16x16x32_bf16 v[44:47], v[132:135], v[68:71], v[44:47]\ns_waitcnt lgkmcnt(2)\nv_mfma_f32_16x16x32_bf16 v[24:27], a[24:27], v[72:75], v[24:27]\n" \
    "ds_read_b128 v[56:59], v3 offset:2048\nv_mfma_f32_16x16x32_bf16 v[36:39], a[24:27], v[76:79], v[36:39]\nds_read_b128 v[60:63], v3 offset:10240\n" \
    "v_mfma_f32_16x16x32_bf16 v[28:31], a[28:31], v[72:75], v[28:31]\ns_add_u32 s69, s54, -1152\nbuffer_load_dwordx4 v1, s[64:67], s69 offen offset:3072 sc1 lds\n" \
    "v_mfma_f32_16x16x32_bf16 v[40:43], a[28:31], v[76:79], v[40:43]\nv_mfma_f32_16x16x32_bf16 v[32:35], v[136:139], v[72:75], v[32:35]\n" \
    "v_mfma_f32_16x16x32_bf16 v[44:47], v[136:139], v[76:79], v[44:47]\ns_waitcnt lgkmcnt(2)\nv_mfma_f32_16x16x32_bf16 v[24:27], a[32:35], v[48:51], v[24:27]\n" \
    "ds_read_b128 v[64:67], v2 offset:3072\nv_mfma_f32_16x16x32_bf16 v[36:39], a[32:35], v[52:55], v[36:39]\nds_read_b128 v[68:71], v2 offset:11264\n" \
    "v_mfma_f32_16x16x32_bf16 v[28:31], a[36:39], v[48:51], v[28:31]\ns_and_b32 s37, s50, s16\ns_lshl_b32 s37, s37, 19\ns_add_u32 s37, s37, s53\n" \
    "v_mfma_f32_16x16x32_bf16 v[40:43], a[36:39], v[52:55], v[40:43]\ns_add_u32 s71, s48, s37\ns_lshr_b32 s71, s71, 8\ns_xnor_b32 s71, s71, s49\n" \
    "v_mfma_f32_16x16x32_bf16 v[32:35], v[140:143], v[48:51], v[32:35]\ns_and_b32 s71, s71, 0x2000\ns_add_u32 s71, s71, s37\n" \
    "v_mfma_f32_16x16x32_bf16 v[44:47], v[140:143], v[52:55], v[44:47]\ns_waitcnt lgkmcnt(2)\nv_mfma_f32_16x16x32_bf16 v[24:27], a[40:43], v[56:59], v[24:27]\n" \
    "ds_read_b128 v[72:75], v3 offset:3072\nv_mfma_f32_16x16x32_bf16 v[36:39], a[40:43], v[60:63], v[36:39]\nds_read_b128 v[76:79], v3 offset:11264\ns_add_u32 s75, s74, 4096\n" \
    "v_mfma_f32_16x16x32_bf16 v[28:31], a[44:47], v[56:59], v[28:31]\nbuffer_store_dwordx4 v[16:19], v8, s[64:67], s75 offen sc1\n" \
    "v_mfma_f32_16x16x32_bf16 v[40:43], a[44:47], v[60:63], v[40:43]\nv_mfma_f32_16x16x32_bf16 v[32:35], v[144:147], v[56:59], v[32:35]\n" \
    "v_mfma_f32_16x16x32_bf16 v[44:47], v[144:147], v[60:63], v[44:47]\ns_waitcnt vmcnt(17) lgkmcnt(0)\ns_barrier\n" \
    "v_mfma_f32_16x16x32_bf16 v[24:27], a[48:51], v[64:67], v[24:27]\nds_read_b128 v[48:51], v2 offset:16384\nv_mfma_f32_16x16x32_bf16 v[36:39], a[48:51], v[68:71], v[36:39]\n" \
    "ds_read_b128 v[52:55], v2 offset:24576\nv_mfma_f32_16x16x32_bf16 v[28:31], a[52:55], v[64:67], v[28:31]\ns_add_u32 s39, s50, 1\ns_add_u32 s97, s39, 1\n" \
    "s_min_u32 s97, s97, 63\nv_mfma_f32_16x16x32_bf16 v[40:43], a[52:55], v[68:71], v[40:43]\ns_lshr_b32 s98, s39, s27\ns_add_u32 s98, s98, s28\ns_min_u32 s98, s98, s29\n" \
    "v_mfma_f32_16x16x32_bf16 v[32:35], v[148:151], v[64:67], v[32:35]\ns_sub_u32 s98, s39, s98\ns_sub_u32 s99, s39, s17\n" \
    "v_mfma_f32_16x16x32_bf16 v[44:47], v[148:151], v[68:71], v[44:47]\ns_cmp_gt_i32 s51, s97\ns_cselect_b32 s96, s98, 0x7fffffff\ns_sub_u32 s39, s39, 3\n" \
    "v_mfma_f32_16x16x32_bf16 v[24:27], a[56:59], v[72:75], v[24:27]\nds_read_b128 v[56:59], v3 offset:16384\nv_mfma_f32_16x16x32_bf16 v[36:39], a[56:59], v[76:79], v[36:39]\n" \
    "ds_read_b128 v[60:63], v3 offset:24576\nv_mfma_f32_16x16x32_bf16 v[28:31], a[60:63], v[72:75], v[28:31]\ns_cmp_lt_u32 s39, 61\ns_cselect_b32 s96, s96, 0x7fffffff\n" \
    "v_mfma_f32_16x16x32_bf16 v[40:43], a[60:63], v[76:79], v[40:43]\nv_mfma_f32_16x16x32_bf16 v[32:35], v[152:155], v[72:75], v[32:35]\ns_add_u32 s75, s74, 16384\n" \
    "v_mfma_f32_16x16x32_bf16 v[44:47], v[152:155], v[76:79], v[44:47]\ns_waitcnt lgkmcnt(2)\nv_mfma_f32_16x16x32_bf16 v[24:27], a[64:67], v[48:51], v[24:27]\n" \
    "ds_read_b128 v[64:67], v2 offset:17408\nv_mfma_f32_16x16x32_bf16 v[36:39], a[64:67], v[52:55], v[36:39]\nds_read_b128 v[68:71], v2 offset:25600\n" \
    "v_mfma_f32_16x16x32_bf16 v[28:31], a[68:71], v[48:51], v[28:31]\ns_add_u32 m0, s52, 0\ns_add_u32 s69, s70, 0\n" \
    "v_mfma_f32_16x16x32_bf16 v[40:43], a[68:71], v[52:55], v[40:43]\nbuffer_load_dwordx4 v1, s[64:67], s69 offen offset:0 sc1 lds\n" \
    "v_mfma_f32_16x16x32_bf16 v[32:35], v[156:159], v[48:51], v[32:35]\nbuffer_store_dwordx4 v[16:19], v8, s[64:67], s75 offen sc1\n" \
    "v_mfma_f32_16x16x32_bf16 v[44:47], v[156:159], v[52:55], v[44:47]\ns_waitcnt lgkmcnt(2)\nv_mfma_f32_16x16x32_bf16 v[24:27], a[72:75], v[56:59], v[24:27]\n" \
    "ds_read_b128 v[72:75], v3 offset:17408\nv_mfma_f32_16x16x32_bf16 v[36:39], a[72:75], v[60:63], v[36:39]\nds_read_b128 v[76:79], v3 offset:25600\n" \
    "v_mfma_f32_16x16x32_bf16 v[28:31], a[76:79], v[56:59], v[28:31]\ns_add_u32 s69, s70, -896\nbuffer_load_dwordx4 v1, s[64:67], s69 offen offset:1024 sc1 lds\n" \
    "v_mfma_f32_16x16x32_bf16 v[40:43], a[76:79], v[60:63], v[40:43]\nv_mfma_f32_16x16x32_bf16 v[32:35], v[160:163], v[56:59], v[32:35]\n" \
    "v_mfma_f32_16x16x32_bf16 v[44:47], v[160:163], v[60:63], v[44:47]\ns_waitcnt lgkmcnt(2)\nv_mfma_f32_16x16x32_bf16 v[24:27], a[80:83], v[64:67], v[24:27]\n" \
    "ds_read_b128 v[48:51], v2 offset:18432\nv_mfma_f32_16x16x32_bf16 v[36:39], a[80:83], v[68:71], v[36:39]\nds_read_b128 v[52:55], v2 offset:26624\ns_add_u32 s75, s74, 20480\n" \
    "v_mfma_f32_16x16x32_bf16 v[28:31], a[84:87], v[64:67], v[28:31]\ns_add_u32 s69, s70, -1792\nbuffer_load_dwordx4 v1, s[64:67], s69 offen offset:2048 sc1 lds\n" \
    "v_mfma_f32_16x16x32_bf16 v[40:43], a[84:87], v[68:71], v[40:43]\nv_xor_b32 v6, v15, v6\nv_mfma_f32_16x16x32_bf16 v[32:35], v[164:167], v[64:67], v[32:35]\n" \
    "buffer_store_dwordx4 v[16:19], v8, s[64:67], s75 offen sc1\nv_mfma_f32_16x16x32_bf16 v[44:47], v[164:167], v[68:71], v[44:47]\ns_waitcnt lgkmcnt(2)\n" \
    "s_sub_u32 s74, s54, s19\ns_xor_b32 s74, s74, s57\nv_mfma_f32_16x16x32_bf16 v[24:27], a[88:91], v[72:75], v[24:27]\nds_read_b128 v[56:59], v3 offset:18432\n" \
    "v_mfma_f32_16x16x32_bf16 v[36:39], a[88:91], v[76:79], v[36:39]\nds_read_b128 v[60:63], v3 offset:26624\nv_mfma_f32_16x16x32_bf16 v[28:31], a[92:95], v[72:75], v[28:31]\n" \
    "s_add_u32 s69, s70, -2688\nbuffer_load_dwordx4 v1, s[64:67], s69 offen offset:3072 sc1 lds\nv_mfma_f32_16x16x32_bf16 v[40:43], a[92:95], v[76:79], v[40:43]\n" \
    "v_mfma_f32_16x16x32_bf16 v[32:35], v[168:171], v[72:75], v[32:35]\nv_mfma_f32_16x16x32_bf16 v[44:47], v[168:171], v[76:79], v[44:47]\ns_waitcnt lgkmcnt(2)\n" \
    "v_mfma_f32_16x16x32_bf16 v[24:27], a[96:99], v[48:51], v[24:27]\nds_read_b128 v[64:67], v2 offset:19456\nv_mfma_f32_16x16x32_bf16 v[36:39], a[96:99], v[52:55], v[36:39]\n" \
    "ds_read_b128 v[68:71], v2 offset:27648\nv_mfma_f32_16x16x32_bf16 v[28:31], a[100:103], v[48:51], v[28:31]\nbuffer_load_dword v20, v6, s[64:67], 0 offen sc1\n" \
    "v_mfma_f32_16x16x32_bf16 v[40:43], a[100:103], v[52:55], v[40:43]\nv_mfma_f32_16x16x32_bf16 v[32:35], v[172:175], v[48:51], v[32:35]\n" \
    "buffer_store_dwordx4 v[16:19], v8, s[64:67], s74 offen sc1\nv_mfma_f32_16x16x32_bf16 v[44:47], v[172:175], v[52:55], v[44:47]\ns_waitcnt lgkmcnt(2)\n" \
    "v_mfma_f32_16x16x32_bf16 v[24:27], a[104:107], v[56:59], v[24:27]\nds_read_b128 v[72:75], v3 offset:19456\n" \
    "v_mfma_f32_16x16x32_bf16 v[36:39], a[104:107], v[60:63], v[36:39]\nds_read_b128 v[76:79], v3 offset:27648\n" \
    "v_mfma_f32_16x16x32_bf16 v[28:31], a[108:111], v[56:59], v[28:31]\nv_mfma_f32_16x16x32_bf16 v[40:43], a[108:111], v[60:63], v[40:43]\n" \
    "v_mfma_f32_16x16x32_bf16 v[32:35], v[176:179], v[56:59], v[32:35]\nv_mfma_f32_16x16x32_bf16 v[44:47], v[176:179], v[60:63], v[44:47]\ns_waitcnt vmcnt(14) lgkmcnt(0)\n" \
    "s_barrier\nv_mfma_f32_16x16x32_bf16 v[24:27], a[112:115], v[64:67], v[24:27]\nds_read_b128 v[48:51], v2 offset:32768\nv_mov_b32 v123, s50\n" \
    "v_mfma_f32_16x16x32_bf16 v[36:39], a[112:115], v[68:71], v[36:39]\nds_read_b128 v[52:55], v2 offset:40960\n" \
    "v_mfma_f32_16x16x32_bf16 v[28:31], a[116:119], v[64:67], v[28:31]\nbuffer_store_dword v123, v5, s[64:67], 0 offen sc1\n" \
    "v_mfma_f32_16x16x32_bf16 v[40:43], a[116:119], v[68:71], v[40:43]\nv_mfma_f32_16x16x32_bf16 v[32:35], v[180:183], v[64:67], v[32:35]\n" \
    "v_mfma_f32_16x16x32_bf16 v[44:47], v[180:183], v[68:71], v[44:47]\nv_mfma_f32_16x16x32_bf16 v[24:27], a[120:123], v[72:75], v[24:27]\n" \
    "ds_read_b128 v[56:59], v3 offset:32768\nv_mfma_f32_16x16x32_bf16 v[36:39], a[120:123], v[76:79], v[36:39]\nds_read_b128 v[60:63], v3 offset:40960\n" \
    "s_add_u32 s75, s74, 4096\nv_mfma_f32_16x16x32_bf16 v[28:31], a[124:127], v[72:75], v[28:31]\nbuffer_store_dwordx4 v[16:19], v8, s[64:67], s75 offen sc1\n" \
    "v_mfma_f32_16x16x32_bf16 v[40:43], a[124:127], v[76:79], v[40:43]\nv_mfma_f32_16x16x32_bf16 v[32:35], v[184:187], v[72:75], v[32:35]\n" \
    "v_mfma_f32_16x16x32_bf16 v[44:47], v[184:187], v[76:79], v[44:47]\ns_waitcnt lgkmcnt(2)\nv_mfma_f32_16x16x32_bf16 v[24:27], a[128:131], v[48:51], v[24:27]\n" \
    "ds_read_b128 v[64:67], v2 offset:33792\nv_mfma_f32_16x16x32_bf16 v[36:39], a[128:131], v[52:55], v[36:39]\nds_read_b128 v[68:71], v2 offset:41984\n" \
    "v_mfma_f32_16x16x32_bf16 v[28:31], a[132:135], v[48:51], v[28:31]\ns_add_u32 m0, s52, 16384\ns_add_u32 s69, s70, 512\n" \
    "v_mfma_f32_16x16x32_bf16 v[40:43], a[132:135], v[52:55], v[40:43]\nbuffer_load_dwordx4 v1, s[64:67], s69 offen offset:0 sc1 lds\n" \
    "v_mfma_f32_16x16x32_bf16 v[32:35], v[188:191], v[48:51], v[32:35]\nv_mfma_f32_16x16x32_bf16 v[44:47], v[188:191], v[52:55], v[44:47]\ns_waitcnt lgkmcnt(2)\n" \
    "v_mfma_f32_16x16x32_bf16 v[24:27], a[136:139], v[56:59], v[24:27]\nds_read_b128 v[72:75], v3 offset:33792\n" \
    "v_mfma_f32_16x16x32_bf16 v[36:39], a[136:139], v[60:63], v[36:39]\nds_read_b128 v[76:79], v3 offset:41984\n" \
    "v_mfma_f32_16x16x32_bf16 v[28:31], a[140:143], v[56:59], v[28:31]\ns_add_u32 s69, s70, -384\nbuffer_load_dwordx4 v1, s[64:67], s69 offen offset:1024 sc1 lds\n" \
    "v_mfma_f32_16x16x32_bf16 v[40:43], a[140:143], v[60:63], v[40:43]\nv_mfma_f32_16x16x32_bf16 v[32:35], v[192:195], v[56:59], v[32:35]\ns_add_u32 s75, s74, 16384\n" \
    "v_mfma_f32_16x16x32_bf16 v[44:47], v[192:195], v[60:63], v[44:47]\ns_waitcnt lgkmcnt(2)\nv_mfma_f32_16x16x32_bf16 v[24:27], a[144:147], v[64:67], v[24:27]\n" \
    "ds_read_b128 v[48:51], v2 offset:34816\nv_mfma_f32_16x16x32_bf16 v[36:39], a[144:147], v[68:71], v[36:39]\nds_read_b128 v[52:55], v2 offset:43008\n" \
    "v_mfma_f32_16x16x32_bf16 v[28:31], a[148:151], v[64:67], v[28:31]\ns_add_u32 s69, s70, -1280\nbuffer_load_dwordx4 v1, s[64:67], s69 offen offset:2048 sc1 lds\n" \
    "v_mfma_f32_16x16x32_bf16 v[40:43], a[148:151], v[68:71], v[40:43]\nbuffer_store_dwordx4 v[16:19], v8, s[64:67], s75 offen sc1\n" \
    "v_mfma_f32_16x16x32_bf16 v[32:35], v[196:199], v[64:67], v[32:35]\nv_mfma_f32_16x16x32_bf16 v[44:47], v[196:199], v[68:71], v[44:47]\ns_waitcnt lgkmcnt(2)\n" \
    "v_mfma_f32_16x16x32_bf16 v[24:27], a[152:155], v[72:75], v[24:27]\nds_read_b128 v[56:59], v3 offset:34816\n" \
    "v_mfma_f32_16x16x32_bf16 v[36:39], a[152:155], v[76:79], v[36:39]\nds_read_b128 v[60:63], v3 offset:43008\n" \
    "v_mfma_f32_16x16x32_bf16 v[28:31], a[156:159], v[72:75], v[28:31]\ns_add_u32 s69, s70, -2176\nbuffer_load_dwordx4 v1, s[64:67], s69 offen offset:3072 sc1 lds\n" \
    "v_mfma_f32_16x16x32_bf16 v[40:43], a[156:159], v[76:79], v[40:43]\nv_mfma_f32_16x16x32_bf16 v[32:35], v[200:203], v[72:75], v[32:35]\n" \
    "v_mfma_f32_16x16x32_bf16 v[44:47], v[200:203], v[76:79], v[44:47]\ns_waitcnt lgkmcnt(2)\nv_mfma_f32_16x16x32_bf16 v[24:27], a[160:163], v[48:51], v[24:27]\n" \
    "ds_read_b128 v[64:67], v2 offset:35840\nv_mfma_f32_16x16x32_bf16 v[36:39], a[160:163], v[52:55], v[36:39]\nds_read_b128 v[68:71], v2 offset:44032\n" \
    "s_add_u32 s75, s74, 20480\nv_mfma_f32_16x16x32_bf16 v[28:31], a[164:167], v[48:51], v[28:31]\ns_waitcnt vmcnt(8)\nds_write_b32 v7, v20\n" \
    "v_mfma_f32_16x16x32_bf16 v[40:43], a[164:167], v[52:55], v[40:43]\nbuffer_store_dwordx4 v[16:19], v8, s[64:67], s75 offen sc1\n" \
    "v_mfma_f32_16x16x32_bf16 v[32:35], v[204:207], v[48:51], v[32:35]\ns_and_b32 s76, s50, s16\ns_lshl_b32 s76, s76, 19\ns_add_u32 s76, s76, s20\n" \
    "v_mfma_f32_16x16x32_bf16 v[44:47], v[204:207], v[52:55], v[44:47]\ns_waitcnt lgkmcnt(3)\ns_add_u32 s74, s48, s76\ns_lshr_b32 s74, s74, 8\n" \
    "v_mfma_f32_16x16x32_bf16 v[24:27], a[168:171], v[56:59], v[24:27]\nds_read_b128 v[72:75], v3 offset:35840\ns_xor_b32 s74, s74, s49\n" \
    "v_mfma_f32_16x16x32_bf16 v[36:39], a[168:171], v[60:63], v[36:39]\nds_read_b128 v[76:79], v3 offset:44032\ns_and_b32 s74, s74, 0x2000\n" \
    "v_mfma_f32_16x16x32_bf16 v[28:31], a[172:175], v[56:59], v[28:31]\ns_add_u32 s74, s74, s76\ns_xor_b32 s74, s74, s58\n" \
    "v_mfma_f32_16x16x32_bf16 v[40:43], a[172:175], v[60:63], v[40:43]\nv_mfma_f32_16x16x32_bf16 v[32:35], v[208:211], v[56:59], v[32:35]\n" \
    "v_mfma_f32_16x16x32_bf16 v[44:47], v[208:211], v[60:63], v[44:47]\ns_waitcnt lgkmcnt(0)\ns_barrier\nv_mfma_f32_16x16x32_bf16 v[24:27], a[176:179], v[64:67], v[24:27]\n" \
    "ds_read_b128 v[48:51], v2 offset:49152\nv_mfma_f32_16x16x32_bf16 v[36:39], a[176:179], v[68:71], v[36:39]\nds_read_b128 v[52:55], v2 offset:57344\n" \
    "v_mfma_f32_16x16x32_bf16 v[28:31], a[180:183], v[64:67], v[28:31]\nds_read_b96 v[108:110], v14\nv_mfma_f32_16x16x32_bf16 v[40:43], a[180:183], v[68:71], v[40:43]\n" \
    "v_mfma_f32_16x16x32_bf16 v[32:35], v[212:215], v[64:67], v[32:35]\nbuffer_store_dwordx4 v[16:19], v8, s[64:67], s74 offen sc1\n" \
    "v_mfma_f32_16x16x32_bf16 v[44:47], v[212:215], v[68:71], v[44:47]\nv_mfma_f32_16x16x32_bf16 v[24:27], a[184:187], v[72:75], v[24:27]\n" \
    "ds_read_b128 v[56:59], v3 offset:49152\nv_mfma_f32_16x16x32_bf16 v[36:39], a[184:187], v[76:79], v[36:39]\nds_read_b128 v[60:63], v3 offset:57344\n" \
    "v_mfma_f32_16x16x32_bf16 v[28:31], a[188:191], v[72:75], v[28:31]\ns_waitcnt lgkmcnt(2)\nv_readfirstlane_b32 s72, v108\nv_readfirstlane_b32 s73, v109\n" \
    "v_mfma_f32_16x16x32_bf16 v[40:43], a[188:191], v[76:79], v[40:43]\nv_mfma_f32_16x16x32_bf16 v[32:35], v[216:219], v[72:75], v[32:35]\ns_and_b32 s72, s72, 127\n" \
    "s_cmp_gt_i32 s72, s96\ns_cselect_b32 s41, s73, 0x80000000\nv_mfma_f32_16x16x32_bf16 v[44:47], v[216:219], v[76:79], v[44:47]\n" \
    "v_mfma_f32_16x16x32_bf16 v[24:27], a[192:195], v[48:51], v[24:27]\nds_read_b128 v[64:67], v2 offset:50176\n" \
    "v_mfma_f32_16x16x32_bf16 v[36:39], a[192:195], v[52:55], v[36:39]\nds_read_b128 v[68:71], v2 offset:58368\n" \
    "v_mfma_f32_16x16x32_bf16 v[28:31], a[196:199], v[48:51], v[28:31]\ns_add_u32 m0, s52, 32768\ns_add_u32 s69, s70, 1024\n" \
    "v_mfma_f32_16x16x32_bf16 v[40:43], a[196:199], v[52:55], v[40:43]\nbuffer_load_dwordx4 v1, s[64:67], s69 offen offset:0 sc1 lds\n" \
    "v_mfma_f32_16x16x32_bf16 v[32:35], v[220:223], v[48:51], v[32:35]\nv_mfma_f32_16x16x32_bf16 v[44:47], v[220:223], v[52:55], v[44:47]\ns_waitcnt lgkmcnt(2)\n" \
    "v_mfma_f32_16x16x32_bf16 v[24:27], a[200:203], v[56:59], v[24:27]\nds_read_b128 v[72:75], v3 offset:50176\n" \
    "v_mfma_f32_16x16x32_bf16 v[36:39], a[200:203], v[60:63], v[36:39]\nds_read_b128 v[76:79], v3 offset:58368\ns_add_u32 s75, s74, 4096\n" \
    "v_mfma_f32_16x16x32_bf16 v[28:31], a[204:207], v[56:59], v[28:31]\ns_add_u32 s69, s70, 128\nbuffer_load_dwordx4 v1, s[64:67], s69 offen offset:1024 sc1 lds\n" \
    "v_mfma_f32_16x16x32_bf16 v[40:43], a[204:207], v[60:63], v[40:43]\nbuffer_store_dwordx4 v[16:19], v8, s[64:67], s75 offen sc1\n" \
    "v_mfma_f32_16x16x32_bf16 v[32:35], v[224:227], v[56:59], v[32:35]\ns_add_u32 s76, s50, 1\ns_and_b32 s76, s76, s16\ns_lshl_b32 s76, s76, 18\n" \
    "v_mfma_f32_16x16x32_bf16 v[44:47], v[224:227], v[60:63], v[44:47]\ns_waitcnt lgkmcnt(2)\ns_add_u32 s76, s76, s59\ns_add_u32 s77, s48, s76\n" \
    "v_mfma_f32_16x16x32_bf16 v[24:27], a[208:211], v[64:67], v[24:27]\nds_read_b128 v[48:51], v2 offset:51200\ns_lshr_b32 s77, s77, 8\n" \
    "v_mfma_f32_16x16x32_bf16 v[36:39], a[208:211], v[68:71], v[36:39]\nds_read_b128 v[52:55], v2 offset:59392\ns_xor_b32 s77, s77, s49\n" \
    "v_mfma_f32_16x16x32_bf16 v[28:31], a[212:215], v[64:67], v[28:31]\ns_add_u32 s69, s70, -768\nbuffer_load_dwordx4 v1, s[64:67], s69 offen offset:2048 sc1 lds\n" \
    "v_mfma_f32_16x16x32_bf16 v[40:43], a[212:215], v[68:71], v[40:43]\ns_and_b32 s77, s77, 0x2000\ns_add_u32 s77, s77, s76\ns_add_u32 s77, s77, s60\n" \
    "v_mfma_f32_16x16x32_bf16 v[32:35], v[228:231], v[64:67], v[32:35]\ns_cmp_lt_u32 s50, 63\ns_cselect_b32 s74, s77, s74\n" \
    "v_mfma_f32_16x16x32_bf16 v[44:47], v[228:231], v[68:71], v[44:47]\ns_waitcnt lgkmcnt(2)\nv_mfma_f32_16x16x32_bf16 v[24:27], a[216:219], v[72:75], v[24:27]\n" \
    "ds_read_b128 v[56:59], v3 offset:51200\nv_mfma_f32_16x16x32_bf16 v[36:39], a[216:219], v[76:79], v[36:39]\nds_read_b128 v[60:63], v3 offset:59392\n" \
    "v_mfma_f32_16x16x32_bf16 v[28:31], a[220:223], v[72:75], v[28:31]\ns_add_u32 s69, s70, -1664\nbuffer_load_dwordx4 v1, s[64:67], s69 offen offset:3072 sc1 lds\n" \
    "v_mfma_f32_16x16x32_bf16 v[40:43], a[220:223], v[76:79], v[40:43]\nv_mfma_f32_16x16x32_bf16 v[32:35], v[232:235], v[72:75], v[32:35]\n" \
    "buffer_store_dwordx4 v[16:19], v8, s[64:67], s74 offen sc1\nv_mfma_f32_16x16x32_bf16 v[44:47], v[232:235], v[76:79], v[44:47]\ns_waitcnt lgkmcnt(2)\n" \
    "v_mfma_f32_16x16x32_bf16 v[24:27], a[224:227], v[48:51], v[24:27]\nds_read_b128 v[64:67], v2 offset:52224\n" \
    "v_mfma_f32_16x16x32_bf16 v[36:39], a[224:227], v[52:55], v[36:39]\nds_read_b128 v[68:71], v2 offset:60416\n" \
    "v_mfma_f32_16x16x32_bf16 v[28:31], a[228:231], v[48:51], v[28:31]\nds_read_b128 v[72:75], v3 offset:52224\n" \
    "v_mfma_f32_16x16x32_bf16 v[40:43], a[228:231], v[52:55], v[40:43]\nds_read_b128 v[76:79], v3 offset:60416\n" \
    "v_mfma_f32_16x16x32_bf16 v[32:35], v[236:239], v[48:51], v[32:35]\nv_mfma_f32_16x16x32_bf16 v[44:47], v[236:239], v[52:55], v[44:47]\ns_waitcnt lgkmcnt(4)\n" \
    "v_mfma_f32_16x16x32_bf16 v[32:35], v[240:243], v[56:59], v[32:35]\ns_xor_b32 s75, s71, 0x2000\ns_add_u32 s76, s75, 0x1000\n" \
    "v_mfma_f32_16x16x32_bf16 v[44:47], v[240:243], v[60:63], v[44:47]\ns_waitcnt lgkmcnt(2)\nv_mfma_f32_16x16x32_bf16 v[32:35], v[244:247], v[64:67], v[32:35]\n" \
    "s_waitcnt lgkmcnt(0)\ns_barrier\nv_mfma_f32_16x16x32_bf16 v[44:47], v[244:247], v[68:71], v[44:47]\nds_read_b128 v[48:51], v2 offset:0\n" \
    "v_mfma_f32_16x16x32_bf16 v[32:35], v[248:251], v[72:75], v[32:35]\nds_read_b128 v[52:55], v2 offset:8192\n" \
    "v_mfma_f32_16x16x32_bf16 v[44:47], v[248:251], v[76:79], v[44:47]\ns_mov_b32 s54, s70\ns_add_u32 s50, s50, 1\n" \
    "v_mfma_f32_16x16x32_bf16 v[24:27], a[232:235], v[56:59], v[24:27]\ns_nop 1\nv_mfma_f32_16x16x32_bf16 v[24:27], a[240:243], v[64:67], v[24:27]\ns_nop 0\n" \
    "v_cvt_pk_bf16_f32 v80, v32, v33\nv_cvt_pk_bf16_f32 v81, v34, v35\nv_mfma_f32_16x16x32_bf16 v[24:27], a[248:251], v[72:75], v[24:27]\nv_cvt_pk_bf16_f32 v82, v44, v45\n" \
    "v_cvt_pk_bf16_f32 v83, v46, v47\nv_mfma_f32_16x16x32_bf16 v[28:31], a[236:239], v[56:59], v[28:31]\nv_permlane16_swap_b32 v80, v82\n" \
    "v_mfma_f32_16x16x32_bf16 v[28:31], a[244:247], v[64:67], v[28:31]\nds_read_b128 v[56:59], v3 offset:0\nv_mfma_f32_16x16x32_bf16 v[28:31], a[252:255], v[72:75], v[28:31]\n" \
    "v_permlane16_swap_b32 v81, v83\nv_mfma_f32_16x16x32_bf16 v[36:39], a[232:235], v[60:63], v[36:39]\nbuffer_store_dwordx4 v[80:83], v4, s[64:67], s71 offen sc1\n" \
    "v_mfma_f32_16x16x32_bf16 v[36:39], a[240:243], v[68:71], v[36:39]\nv_cvt_pk_bf16_f32 v84, v24, v25\nv_cvt_pk_bf16_f32 v85, v26, v27\n" \
    "v_mfma_f32_16x16x32_bf16 v[36:39], a[248:251], v[76:79], v[36:39]\ns_nop 0\nv_cvt_pk_bf16_f32 v86, v28, v29\nv_cvt_pk_bf16_f32 v87, v30, v31\n" \
    "v_mfma_f32_16x16x32_bf16 v[40:43], a[236:239], v[60:63], v[40:43]\nbuffer_store_dwordx4 v[84:87], v8, s[64:67], s75 offen sc1\n" \
    "v_mfma_f32_16x16x32_bf16 v[40:43], a[244:247], v[68:71], v[40:43]\nds_read_b128 v[60:63], v3 offset:8192\n" \
    "v_mfma_f32_16x16x32_bf16 v[40:43], a[252:255], v[76:79], v[40:43]\ns_cmp_gt_i32 s41, s99\nv_cvt_pk_bf16_f32 v88, v36, v37\nv_cvt_pk_bf16_f32 v89, v38, v39\ns_nop 4\n" \
    "v_cvt_pk_bf16_f32 v90, v40, v41\nv_cvt_pk_bf16_f32 v91, v42, v43\nbuffer_store_dwordx4 v[88:91], v8, s[64:67], s76 offen sc1\ns_cbranch_scc1 .Lprvgo_%=\n.Lprvslow_%=:\n" \
    "s_waitcnt vmcnt(0) lgkmcnt(0)\ns_cmp_eq_u32 s50, 64\ns_cbranch_scc1 .Lprvdone_%=\ns_barrier\nv_mov_b32 v96, s50\nbuffer_store_dword v96, v5, s[64:67], 0 offen sc1\n" \
    "s_cmp_gt_i32 s51, s97\ns_cbranch_scc0 .Lprvpoll_%=\ns_cmp_gt_i32 s72, s98\ns_cbranch_scc0 .Lprvpoll_%=\ns_cmp_gt_i32 s73, s99\ns_cbranch_scc0 .Lprvpoll_%=\n" \
    "s_branch .Lprvgo_%=\n.Lprvpoll_%=:\ns_mov_b32 s68, 0\ns_cmp_lg_u32 s55, 0\ns_cbranch_scc1 .Lprvjoin_%=\n.Lprvpl2_%=:\ns_cmp_gt_i32 s51, s97\ns_cbranch_scc1 .Lprvpl3_%=\n" \
    "v_lshlrev_b32 v96, 3, v0\nbuffer_load_dword v96, v96, s[64:67], s36 offen sc1\ns_waitcnt vmcnt(0)\nv_cmp_gt_u32 vcc, 32, v96\ns_nop 4\ns_ff1_i32_b64 s51, vcc\n" \
    "s_min_u32 s51, s51, 64\n.Lprvpl3_%=:\nbuffer_load_dword v96, v6, s[64:67], 0 offen sc1\ns_waitcnt vmcnt(0)\nv_readlane_b32 s73, v96, 1\n.Lprvpl4_%=:\n" \
    "v_readlane_b32 s72, v96, 0\ns_and_b32 s72, s72, 127\ns_cmp_gt_i32 s51, s97\ns_cbranch_scc0 .Lprvok_%=\ns_cmp_gt_i32 s72, s98\ns_cbranch_scc0 .Lprvok_%=\n" \
    "s_cmp_gt_i32 s73, s99\ns_cbranch_scc0 .Lprvok_%=\nv_mov_b32 v96, s51\nds_write_b32 v7, v96\n.Lprvjoin_%=:\ns_waitcnt lgkmcnt(0)\ns_barrier\nds_read_b32 v96, v14\n" \
    "s_waitcnt lgkmcnt(0)\nv_readfirstlane_b32 s51, v96\ns_branch .Lprvgo_%=\n.Lprvok_%=:\ns_sleep 5\ns_add_u32 s68, s68, 1\ns_and_b32 s43, s68, 63\ns_cbranch_scc1 .Lprvpl2_%=\n" \
    "s_cmp_gt_u32 s68, 0x200000\ns_cbranch_scc1 .Lprvabort_%=\nbuffer_load_dword v97, off, s[64:67], 0 sc1\ns_waitcnt vmcnt(0)\nv_readfirstlane_b32 s43, v97\n" \
    "s_cmp_eq_u32 s43, 1\ns_cbranch_scc0 .Lprvpl2_%=\ns_endpgm\n.Lprvabort_%=:\nv_mov_b32 v96, 1\nbuffer_store_dword v96, off, s[64:67], 0 sc1\ns_endpgm\n.Lprvdone_%=:\n"
#define ROLE_R \
    "v_mov_b32 v17, 0\ns_add_u32 s70, s50, 2\ns_min_i32 s70, s70, 63\ns_add_u32 s71, s50, -2\ns_mov_b32 s68, 0\ns_branch .Lr9_%=\n.Lr8_%=:\n" \
    "buffer_load_dword v17, v8, s[64:67], 0 offen sc1\ns_waitcnt vmcnt(0)\n.Lr9_%=:\nv_cmp_lt_i32 vcc, s70, v17\ns_nop 3\ns_and_b32 s69, vcc_lo, 0xff\ns_cmp_eq_u32 s69, 0xff\n" \
    "s_cbranch_scc0 .Lr10_%=\ns_mov_b32 s68, 0\ns_add_u32 s69, s50, 2\ns_and_b32 s69, s69, s41\ns_lshl_b32 s69, s69, 19\ns_add_u32 s69, s69, s35\ns_add_u32 s84, s48, s69\n" \
    "s_lshr_b32 s84, s84, 8\ns_xor_b32 s84, s84, s49\ns_and_b32 s84, s84, 0x2000\ns_add_u32 s84, s84, s69\n.Lr11_%=:\ns_add_u32 s85, s84, 0\n" \
    "buffer_load_dwordx4 v[104:107], v0, s[64:67], s85 offen offset:0 sc1\nbuffer_load_dwordx4 v[108:111], v0, s[64:67], s85 offen offset:1024 sc1\n" \
    "buffer_load_dwordx4 v[112:115], v0, s[64:67], s85 offen offset:2048 sc1\nbuffer_load_dwordx4 v[116:119], v0, s[64:67], s85 offen offset:3072 sc1\n" \
    "buffer_load_dwordx4 v[72:75], v1, s[64:67], s85 offen offset:0 sc1\nbuffer_load_dwordx4 v[76:79], v1, s[64:67], s85 offen offset:1024 sc1\n" \
    "buffer_load_dwordx4 v[80:83], v1, s[64:67], s85 offen offset:2048 sc1\nbuffer_load_dwordx4 v[84:87], v1, s[64:67], s85 offen offset:3072 sc1\ns_add_u32 s85, s84, 16384\n" \
    "buffer_load_dwordx4 v[88:91], v0, s[64:67], s85 offen offset:0 sc1\nbuffer_load_dwordx4 v[92:95], v0, s[64:67], s85 offen offset:1024 sc1\n" \
    "buffer_load_dwordx4 v[96:99], v0, s[64:67], s85 offen offset:2048 sc1\n.Lr12_%=:\nv_mov_b32 v22, 0\ns_waitcnt vmcnt(10)\n" \
    "v_mfma_f32_16x16x32_bf16 v[24:27], v[104:107], a[0:3], 0\nbuffer_load_dwordx4 v[100:103], v0, s[64:67], s85 offen offset:3072 sc1\n" \
    "v_mfma_f32_16x16x32_bf16 v[28:31], v[104:107], a[16:19], 0\ns_waitcnt vmcnt(10)\nv_mfma_f32_16x16x32_bf16 v[24:27], v[108:111], a[4:7], v[24:27]\n" \
    "buffer_load_dwordx4 v[104:107], v1, s[64:67], s85 offen offset:0 sc1\nv_mfma_f32_16x16x32_bf16 v[28:31], v[108:111], a[20:23], v[28:31]\ns_waitcnt vmcnt(10)\n" \
    "v_mfma_f32_16x16x32_bf16 v[24:27], v[112:115], a[8:11], v[24:27]\nbuffer_load_dwordx4 v[108:111], v1, s[64:67], s85 offen offset:1024 sc1\n" \
    "v_mfma_f32_16x16x32_bf16 v[28:31], v[112:115], a[24:27], v[28:31]\ns_waitcnt vmcnt(10)\nv_mfma_f32_16x16x32_bf16 v[24:27], v[116:119], a[12:15], v[24:27]\n" \
    "buffer_load_dwordx4 v[112:115], v1, s[64:67], s85 offen offset:2048 sc1\nv_mfma_f32_16x16x32_bf16 v[28:31], v[116:119], a[28:31], v[28:31]\ns_waitcnt vmcnt(10)\n" \
    "v_mfma_f32_16x16x32_bf16 v[32:35], v[72:75], a[0:3], 0\nbuffer_load_dwordx4 v[116:119], v1, s[64:67], s85 offen offset:3072 sc1\n" \
    "v_mfma_f32_16x16x32_bf16 v[36:39], v[72:75], a[16:19], 0\ns_waitcnt vmcnt(10)\nv_mfma_f32_16x16x32_bf16 v[32:35], v[76:79], a[4:7], v[32:35]\ns_add_u32 s85, s84, 32768\n" \
    "buffer_load_dwordx4 v[72:75], v0, s[64:67], s85 offen offset:0 sc1\nv_mfma_f32_16x16x32_bf16 v[36:39], v[76:79], a[20:23], v[36:39]\nv_cvt_pk_bf16_f32 v120, v24, v25\n" \
    "v_cvt_pk_bf16_f32 v122, v26, v27\ns_waitcnt vmcnt(10)\nv_mfma_f32_16x16x32_bf16 v[32:35], v[80:83], a[8:11], v[32:35]\n" \
    "buffer_load_dwordx4 v[76:79], v0, s[64:67], s85 offen offset:1024 sc1\nv_mfma_f32_16x16x32_bf16 v[36:39], v[80:83], a[24:27], v[36:39]\nv_cvt_pk_bf16_f32 v152, v28, v29\n" \
    "v_cvt_pk_bf16_f32 v154, v30, v31\ns_waitcnt vmcnt(10)\nv_mfma_f32_16x16x32_bf16 v[32:35], v[84:87], a[12:15], v[32:35]\n" \
    "buffer_load_dwordx4 v[80:83], v0, s[64:67], s85 offen offset:2048 sc1\nv_mfma_f32_16x16x32_bf16 v[36:39], v[84:87], a[28:31], v[36:39]\ns_waitcnt vmcnt(10)\n" \
    "v_mfma_f32_16x16x32_bf16 v[24:27], v[88:91], a[32:35], 0\nbuffer_load_dwordx4 v[84:87], v0, s[64:67], s85 offen offset:3072 sc1\n" \
    "v_mfma_f32_16x16x32_bf16 v[28:31], v[88:91], a[48:51], 0\ns_waitcnt vmcnt(10)\nv_mfma_f32_16x16x32_bf16 v[24:27], v[92:95], a[36:39], v[24:27]\n" \
    "buffer_load_dwordx4 v[88:91], v1, s[64:67], s85 offen offset:0 sc1\nv_mfma_f32_16x16x32_bf16 v[28:31], v[92:95], a[52:55], v[28:31]\nv_cvt_pk_bf16_f32 v121, v32, v33\n" \
    "v_cvt_pk_bf16_f32 v123, v34, v35\ns_waitcnt vmcnt(10)\nv_mfma_f32_16x16x32_bf16 v[24:27], v[96:99], a[40:43], v[24:27]\n" \
    "buffer_load_dwordx4 v[92:95], v1, s[64:67], s85 offen offset:1024 sc1\nv_mfma_f32_16x16x32_bf16 v[28:31], v[96:99], a[56:59], v[28:31]\nv_cvt_pk_bf16_f32 v153, v36, v37\n" \
    "v_cvt_pk_bf16_f32 v155, v38, v39\ns_waitcnt vmcnt(10)\nv_mfma_f32_16x16x32_bf16 v[24:27], v[100:103], a[44:47], v[24:27]\n" \
    "buffer_load_dwordx4 v[96:99], v1, s[64:67], s85 offen offset:2048 sc1\nv_mfma_f32_16x16x32_bf16 v[28:31], v[100:103], a[60:63], v[28:31]\ns_waitcnt vmcnt(10)\n" \
    "v_mfma_f32_16x16x32_bf16 v[32:35], v[104:107], a[32:35], 0\nbuffer_load_dwordx4 v[100:103], v1, s[64:67], s85 offen offset:3072 sc1\n" \
    "v_mfma_f32_16x16x32_bf16 v[36:39], v[104:107], a[48:51], 0\ns_waitcnt vmcnt(10)\nv_mfma_f32_16x16x32_bf16 v[32:35], v[108:111], a[36:39], v[32:35]\n" \
    "s_add_u32 s85, s84, 49152\nbuffer_load_dwordx4 v[104:107], v0, s[64:67], s85 offen offset:0 sc1\nv_mfma_f32_16x16x32_bf16 v[36:39], v[108:111], a[52:55], v[36:39]\n" \
    "v_cvt_pk_bf16_f32 v124, v24, v25\nv_cvt_pk_bf16_f32 v126, v26, v27\ns_waitcnt vmcnt(10)\nv_mfma_f32_16x16x32_bf16 v[32:35], v[112:115], a[40:43], v[32:35]\n" \
    "buffer_load_dwordx4 v[108:111], v0, s[64:67], s85 offen offset:1024 sc1\nv_mfma_f32_16x16x32_bf16 v[36:39], v[112:115], a[56:59], v[36:39]\n" \
    "v_cvt_pk_bf16_f32 v156, v28, v29\nv_cvt_pk_bf16_f32 v158, v30, v31\ns_waitcnt vmcnt(10)\nv_mfma_f32_16x16x32_bf16 v[32:35], v[116:119], a[44:47], v[32:35]\n" \
    "buffer_load_dwordx4 v[112:115], v0, s[64:67], s85 offen offset:2048 sc1\nv_mfma_f32_16x16x32_bf16 v[36:39], v[116:119], a[60:63], v[36:39]\ns_waitcnt vmcnt(10)\n" \
    "v_mfma_f32_16x16x32_bf16 v[24:27], v[72:75], a[64:67], 0\nbuffer_load_dwordx4 v[116:119], v0, s[64:67], s85 offen offset:3072 sc1\n" \
    "v_mfma_f32_16x16x32_bf16 v[28:31], v[72:75], a[80:83], 0\ns_waitcnt vmcnt(10)\nv_mfma_f32_16x16x32_bf16 v[24:27], v[76:79], a[68:71], v[24:27]\n" \
    "buffer_load_dwordx4 v[72:75], v1, s[64:67], s85 offen offset:0 sc1\nv_mfma_f32_16x16x32_bf16 v[28:31], v[76:79], a[84:87], v[28:31]\nv_cvt_pk_bf16_f32 v125, v32, v33\n" \
    "v_cvt_pk_bf16_f32 v127, v34, v35\ns_waitcnt vmcnt(10)\nv_mfma_f32_16x16x32_bf16 v[24:27], v[80:83], a[72:75], v[24:27]\n" \
    "buffer_load_dwordx4 v[76:79], v1, s[64:67], s85 offen offset:1024 sc1\nv_mfma_f32_16x16x32_bf16 v[28:31], v[80:83], a[88:91], v[28:31]\nv_cvt_pk_bf16_f32 v157, v36, v37\n" \
    "v_cvt_pk_bf16_f32 v159, v38, v39\ns_waitcnt vmcnt(10)\nv_mfma_f32_16x16x32_bf16 v[24:27], v[84:87], a[76:79], v[24:27]\n" \
    "buffer_load_dwordx4 v[80:83], v1, s[64:67], s85 offen offset:2048 sc1\nv_mfma_f32_16x16x32_bf16 v[28:31], v[84:87], a[92:95], v[28:31]\ns_waitcnt vmcnt(10)\n" \
    "v_mfma_f32_16x16x32_bf16 v[32:35], v[88:91], a[64:67], 0\nbuffer_load_dwordx4 v[84:87], v1, s[64:67], s85 offen offset:3072 sc1\n" \
    "v_mfma_f32_16x16x32_bf16 v[36:39], v[88:91], a[80:83], 0\ns_waitcnt vmcnt(10)\nv_mfma_f32_16x16x32_bf16 v[32:35], v[92:95], a[68:71], v[32:35]\ns_add_u32 s85, s84, 65536\n" \
    "buffer_load_dwordx4 v[88:91], v0, s[64:67], s85 offen offset:0 sc1\nv_mfma_f32_16x16x32_bf16 v[36:39], v[92:95], a[84:87], v[36:39]\nv_cvt_pk_bf16_f32 v128, v24, v25\n" \
    "v_cvt_pk_bf16_f32 v130, v26, v27\ns_waitcnt vmcnt(10)\nv_mfma_f32_16x16x32_bf16 v[32:35], v[96:99], a[72:75], v[32:35]\n" \
    "buffer_load_dwordx4 v[92:95], v0, s[64:67], s85 offen offset:1024 sc1\nv_mfma_f32_16x16x32_bf16 v[36:39], v[96:99], a[88:91], v[36:39]\nv_cvt_pk_bf16_f32 v160, v28, v29\n" \
    "v_cvt_pk_bf16_f32 v162, v30, v31\ns_waitcnt vmcnt(10)\nv_mfma_f32_16x16x32_bf16 v[32:35], v[100:103], a[76:79], v[32:35]\n" \
    "buffer_load_dwordx4 v[96:99], v0, s[64:67], s85 offen offset:2048 sc1\nv_mfma_f32_16x16x32_bf16 v[36:39], v[100:103], a[92:95], v[36:39]\ns_waitcnt vmcnt(10)\n" \
    "v_mfma_f32_16x16x32_bf16 v[24:27], v[104:107], a[96:99], 0\nbuffer_load_dwordx4 v[100:103], v0, s[64:67], s85 offen offset:3072 sc1\n" \
    "v_mfma_f32_16x16x32_bf16 v[28:31], v[104:107], a[112:115], 0\ns_waitcnt vmcnt(10)\nv_mfma_f32_16x16x32_bf16 v[24:27], v[108:111], a[100:103], v[24:27]\n" \
    "buffer_load_dwordx4 v[104:107], v1, s[64:67], s85 offen offset:0 sc1\nv_mfma_f32_16x16x32_bf16 v[28:31], v[108:111], a[116:119], v[28:31]\n" \
    "v_cvt_pk_bf16_f32 v129, v32, v33\nv_cvt_pk_bf16_f32 v131, v34, v35\ns_waitcnt vmcnt(10)\nv_mfma_f32_16x16x32_bf16 v[24:27], v[112:115], a[104:107], v[24:27]\n" \
    "buffer_load_dwordx4 v[108:111], v1, s[64:67], s85 offen offset:1024 sc1\nv_mfma_f32_16x16x32_bf16 v[28:31], v[112:115], a[120:123], v[28:31]\n" \
    "v_cvt_pk_bf16_f32 v161, v36, v37\nv_cvt_pk_bf16_f32 v163, v38, v39\ns_waitcnt vmcnt(10)\nv_mfma_f32_16x16x32_bf16 v[24:27], v[116:119], a[108:111], v[24:27]\n" \
    "buffer_load_dwordx4 v[112:115], v1, s[64:67], s85 offen offset:2048 sc1\nv_mfma_f32_16x16x32_bf16 v[28:31], v[116:119], a[124:127], v[28:31]\ns_waitcnt vmcnt(10)\n" \
    "v_mfma_f32_16x16x32_bf16 v[32:35], v[72:75], a[96:99], 0\nbuffer_load_dwordx4 v[116:119], v1, s[64:67], s85 offen offset:3072 sc1\n" \
    "v_mfma_f32_16x16x32_bf16 v[36:39], v[72:75], a[112:115], 0\ns_waitcnt vmcnt(10)\nv_mfma_f32_16x16x32_bf16 v[32:35], v[76:79], a[100:103], v[32:35]\n" \
    "s_add_u32 s85, s84, 81920\nbuffer_load_dwordx4 v[72:75], v0, s[64:67], s85 offen offset:0 sc1\nv_mfma_f32_16x16x32_bf16 v[36:39], v[76:79], a[116:119], v[36:39]\n" \
    "v_cvt_pk_bf16_f32 v132, v24, v25\nv_cvt_pk_bf16_f32 v134, v26, v27\ns_waitcnt vmcnt(10)\nv_mfma_f32_16x16x32_bf16 v[32:35], v[80:83], a[104:107], v[32:35]\n" \
    "buffer_load_dwordx4 v[76:79], v0, s[64:67], s85 offen offset:1024 sc1\nv_mfma_f32_16x16x32_bf16 v[36:39], v[80:83], a[120:123], v[36:39]\nv_cvt_pk_bf16_f32 v164, v28, v29\n" \
    "v_cvt_pk_bf16_f32 v166, v30, v31\ns_waitcnt vmcnt(10)\nv_mfma_f32_16x16x32_bf16 v[32:35], v[84:87], a[108:111], v[32:35]\n" \
    "buffer_load_dwordx4 v[80:83], v0, s[64:67], s85 offen offset:2048 sc1\nv_mfma_f32_16x16x32_bf16 v[36:39], v[84:87], a[124:127], v[36:39]\ns_waitcnt vmcnt(10)\n" \
    "v_mfma_f32_16x16x32_bf16 v[24:27], v[88:91], a[128:131], 0\nbuffer_load_dwordx4 v[84:87], v0, s[64:67], s85 offen offset:3072 sc1\n" \
    "v_mfma_f32_16x16x32_bf16 v[28:31], v[88:91], a[144:147], 0\ns_waitcnt vmcnt(10)\nv_mfma_f32_16x16x32_bf16 v[24:27], v[92:95], a[132:135], v[24:27]\n" \
    "buffer_load_dwordx4 v[88:91], v1, s[64:67], s85 offen offset:0 sc1\nv_mfma_f32_16x16x32_bf16 v[28:31], v[92:95], a[148:151], v[28:31]\nv_cvt_pk_bf16_f32 v133, v32, v33\n" \
    "v_cvt_pk_bf16_f32 v135, v34, v35\ns_waitcnt vmcnt(10)\nv_mfma_f32_16x16x32_bf16 v[24:27], v[96:99], a[136:139], v[24:27]\n" \
    "buffer_load_dwordx4 v[92:95], v1, s[64:67], s85 offen offset:1024 sc1\nv_mfma_f32_16x16x32_bf16 v[28:31], v[96:99], a[152:155], v[28:31]\nv_cvt_pk_bf16_f32 v165, v36, v37\n" \
    "v_cvt_pk_bf16_f32 v167, v38, v39\ns_waitcnt vmcnt(10)\nv_mfma_f32_16x16x32_bf16 v[24:27], v[100:103], a[140:143], v[24:27]\n" \
    "buffer_load_dwordx4 v[96:99], v1, s[64:67], s85 offen offset:2048 sc1\nv_mfma_f32_16x16x32_bf16 v[28:31], v[100:103], a[156:159], v[28:31]\ns_waitcnt vmcnt(10)\n" \
    "v_mfma_f32_16x16x32_bf16 v[32:35], v[104:107], a[128:131], 0\nbuffer_load_dwordx4 v[100:103], v1, s[64:67], s85 offen offset:3072 sc1\n" \
    "v_mfma_f32_16x16x32_bf16 v[36:39], v[104:107], a[144:147], 0\ns_waitcnt vmcnt(10)\nv_mfma_f32_16x16x32_bf16 v[32:35], v[108:111], a[132:135], v[32:35]\n" \
    "s_add_u32 s85, s84, 98304\nbuffer_load_dwordx4 v[104:107], v0, s[64:67], s85 offen offset:0 sc1\nv_mfma_f32_16x16x32_bf16 v[36:39], v[108:111], a[148:151], v[36:39]\n" \
    "v_cvt_pk_bf16_f32 v136, v24, v25\nv_cvt_pk_bf16_f32 v138, v26, v27\ns_waitcnt vmcnt(10)\nv_mfma_f32_16x16x32_bf16 v[32:35], v[112:115], a[136:139], v[32:35]\n" \
    "buffer_load_dwordx4 v[108:111], v0, s[64:67], s85 offen offset:1024 sc1\nv_mfma_f32_16x16x32_bf16 v[36:39], v[112:115], a[152:155], v[36:39]\n" \
    "v_cvt_pk_bf16_f32 v168, v28, v29\nv_cvt_pk_bf16_f32 v170, v30, v31\ns_waitcnt vmcnt(10)\nv_mfma_f32_16x16x32_bf16 v[32:35], v[116:119], a[140:143], v[32:35]\n" \
    "buffer_load_dwordx4 v[112:115], v0, s[64:67], s85 offen offset:2048 sc1\nv_mfma_f32_16x16x32_bf16 v[36:39], v[116:119], a[156:159], v[36:39]\ns_waitcnt vmcnt(10)\n" \
    "v_mfma_f32_16x16x32_bf16 v[24:27], v[72:75], a[160:163], 0\nbuffer_load_dwordx4 v[116:119], v0, s[64:67], s85 offen offset:3072 sc1\n" \
    "v_mfma_f32_16x16x32_bf16 v[28:31], v[72:75], a[176:179], 0\ns_waitcnt vmcnt(10)\nv_mfma_f32_16x16x32_bf16 v[24:27], v[76:79], a[164:167], v[24:27]\n" \
    "buffer_load_dwordx4 v[72:75], v1, s[64:67], s85 offen offset:0 sc1\nv_mfma_f32_16x16x32_bf16 v[28:31], v[76:79], a[180:183], v[28:31]\nv_cvt_pk_bf16_f32 v137, v32, v33\n" \
    "v_cvt_pk_bf16_f32 v139, v34, v35\ns_waitcnt vmcnt(10)\nv_mfma_f32_16x16x32_bf16 v[24:27], v[80:83], a[168:171], v[24:27]\n" \
    "buffer_load_dwordx4 v[76:79], v1, s[64:67], s85 offen offset:1024 sc1\nv_mfma_f32_16x16x32_bf16 v[28:31], v[80:83], a[184:187], v[28:31]\nv_cvt_pk_bf16_f32 v169, v36, v37\n" \
    "v_cvt_pk_bf16_f32 v171, v38, v39\ns_waitcnt vmcnt(10)\nv_mfma_f32_16x16x32_bf16 v[24:27], v[84:87], a[172:175], v[24:27]\n" \
    "buffer_load_dwordx4 v[80:83], v1, s[64:67], s85 offen offset:2048 sc1\nv_mfma_f32_16x16x32_bf16 v[28:31], v[84:87], a[188:191], v[28:31]\ns_waitcnt vmcnt(10)\n" \
    "v_mfma_f32_16x16x32_bf16 v[32:35], v[88:91], a[160:163], 0\nbuffer_load_dwordx4 v[84:87], v1, s[64:67], s85 offen offset:3072 sc1\n" \
    "v_mfma_f32_16x16x32_bf16 v[36:39], v[88:91], a[176:179], 0\ns_waitcnt vmcnt(10)\nv_mfma_f32_16x16x32_bf16 v[32:35], v[92:95], a[164:167], v[32:35]\n" \
    "s_add_u32 s85, s84, 114688\nbuffer_load_dwordx4 v[88:91], v0, s[64:67], s85 offen offset:0 sc1\nv_mfma_f32_16x16x32_bf16 v[36:39], v[92:95], a[180:183], v[36:39]\n" \
    "v_cvt_pk_bf16_f32 v140, v24, v25\nv_cvt_pk_bf16_f32 v142, v26, v27\ns_waitcnt vmcnt(10)\nv_mfma_f32_16x16x32_bf16 v[32:35], v[96:99], a[168:171], v[32:35]\n" \
    "buffer_load_dwordx4 v[92:95], v0, s[64:67], s85 offen offset:1024 sc1\nv_mfma_f32_16x16x32_bf16 v[36:39], v[96:99], a[184:187], v[36:39]\nv_cvt_pk_bf16_f32 v172, v28, v29\n" \
    "v_cvt_pk_bf16_f32 v174, v30, v31\ns_waitcnt vmcnt(10)\nv_mfma_f32_16x16x32_bf16 v[32:35], v[100:103], a[172:175], v[32:35]\n" \
    "buffer_load_dwordx4 v[96:99], v0, s[64:67], s85 offen offset:2048 sc1\nv_mfma_f32_16x16x32_bf16 v[36:39], v[100:103], a[188:191], v[36:39]\ns_waitcnt vmcnt(10)\n" \
    "v_mfma_f32_16x16x32_bf16 v[24:27], v[104:107], a[192:195], 0\nbuffer_load_dwordx4 v[100:103], v0, s[64:67], s85 offen offset:3072 sc1\n" \
    "v_mfma_f32_16x16x32_bf16 v[28:31], v[104:107], a[208:211], 0\ns_waitcnt vmcnt(10)\nv_mfma_f32_16x16x32_bf16 v[24:27], v[108:111], a[196:199], v[24:27]\n" \
    "buffer_load_dwordx4 v[104:107], v1, s[64:67], s85 offen offset:0 sc1\nv_mfma_f32_16x16x32_bf16 v[28:31], v[108:111], a[212:215], v[28:31]\n" \
    "v_cvt_pk_bf16_f32 v141, v32, v33\nv_cvt_pk_bf16_f32 v143, v34, v35\ns_waitcnt vmcnt(10)\nv_mfma_f32_16x16x32_bf16 v[24:27], v[112:115], a[200:203], v[24:27]\n" \
    "buffer_load_dwordx4 v[108:111], v1, s[64:67], s85 offen offset:1024 sc1\nv_mfma_f32_16x16x32_bf16 v[28:31], v[112:115], a[216:219], v[28:31]\n" \
    "v_cvt_pk_bf16_f32 v173, v36, v37\nv_cvt_pk_bf16_f32 v175, v38, v39\ns_waitcnt vmcnt(10)\nv_mfma_f32_16x16x32_bf16 v[24:27], v[116:119], a[204:207], v[24:27]\n" \
    "buffer_load_dwordx4 v[112:115], v1, s[64:67], s85 offen offset:2048 sc1\nv_mfma_f32_16x16x32_bf16 v[28:31], v[116:119], a[220:223], v[28:31]\ns_waitcnt vmcnt(10)\n" \
    "v_mfma_f32_16x16x32_bf16 v[32:35], v[72:75], a[192:195], 0\nbuffer_load_dwordx4 v[116:119], v1, s[64:67], s85 offen offset:3072 sc1\n" \
    "v_mfma_f32_16x16x32_bf16 v[36:39], v[72:75], a[208:211], 0\ns_waitcnt vmcnt(10)\nv_mfma_f32_16x16x32_bf16 v[32:35], v[76:79], a[196:199], v[32:35]\n" \
    "v_mfma_f32_16x16x32_bf16 v[36:39], v[76:79], a[212:215], v[36:39]\nv_cvt_pk_bf16_f32 v144, v24, v25\nv_cvt_pk_bf16_f32 v146, v26, v27\ns_waitcnt vmcnt(9)\n" \
    "v_mfma_f32_16x16x32_bf16 v[32:35], v[80:83], a[200:203], v[32:35]\nv_mfma_f32_16x16x32_bf16 v[36:39], v[80:83], a[216:219], v[36:39]\nv_cvt_pk_bf16_f32 v176, v28, v29\n" \
    "v_cvt_pk_bf16_f32 v178, v30, v31\ns_waitcnt vmcnt(8)\nv_mfma_f32_16x16x32_bf16 v[32:35], v[84:87], a[204:207], v[32:35]\n" \
    "v_mfma_f32_16x16x32_bf16 v[36:39], v[84:87], a[220:223], v[36:39]\ns_waitcnt vmcnt(7)\nv_mfma_f32_16x16x32_bf16 v[24:27], v[88:91], a[224:227], 0\n" \
    "v_mfma_f32_16x16x32_bf16 v[28:31], v[88:91], a[240:243], 0\ns_waitcnt vmcnt(6)\nv_mfma_f32_16x16x32_bf16 v[24:27], v[92:95], a[228:231], v[24:27]\n" \
    "v_mfma_f32_16x16x32_bf16 v[28:31], v[92:95], a[244:247], v[28:31]\ns_nop 0\nv_cvt_pk_bf16_f32 v145, v32, v33\nv_cvt_pk_bf16_f32 v147, v34, v35\ns_waitcnt vmcnt(5)\n" \
    "v_mfma_f32_16x16x32_bf16 v[24:27], v[96:99], a[232:235], v[24:27]\nv_mfma_f32_16x16x32_bf16 v[28:31], v[96:99], a[248:251], v[28:31]\nv_cvt_pk_bf16_f32 v177, v36, v37\n" \
    "v_cvt_pk_bf16_f32 v179, v38, v39\ns_waitcnt vmcnt(4)\nv_mfma_f32_16x16x32_bf16 v[24:27], v[100:103], a[236:239], v[24:27]\n" \
    "v_mfma_f32_16x16x32_bf16 v[28:31], v[100:103], a[252:255], v[28:31]\ns_waitcnt vmcnt(3)\nv_mfma_f32_16x16x32_bf16 v[32:35], v[104:107], a[224:227], 0\n" \
    "v_mfma_f32_16x16x32_bf16 v[36:39], v[104:107], a[240:243], 0\ns_waitcnt vmcnt(2)\nv_mfma_f32_16x16x32_bf16 v[32:35], v[108:111], a[228:231], v[32:35]\n" \
    "v_mfma_f32_16x16x32_bf16 v[36:39], v[108:111], a[244:247], v[36:39]\ns_nop 0\nv_cvt_pk_bf16_f32 v148, v24, v25\nv_cvt_pk_bf16_f32 v150, v26, v27\ns_waitcnt vmcnt(1)\n" \
    "v_mfma_f32_16x16x32_bf16 v[32:35], v[112:115], a[232:235], v[32:35]\nv_mfma_f32_16x16x32_bf16 v[36:39], v[112:115], a[248:251], v[36:39]\nv_cvt_pk_bf16_f32 v180, v28, v29\n" \
    "v_cvt_pk_bf16_f32 v182, v30, v31\ns_waitcnt vmcnt(0)\nv_mfma_f32_16x16x32_bf16 v[32:35], v[116:119], a[236:239], v[32:35]\n" \
    "v_mfma_f32_16x16x32_bf16 v[36:39], v[116:119], a[252:255], v[36:39]\ns_nop 6\nv_cvt_pk_bf16_f32 v149, v32, v33\nv_cvt_pk_bf16_f32 v151, v34, v35\n" \
    "v_cvt_pk_bf16_f32 v181, v36, v37\nv_cvt_pk_bf16_f32 v183, v38, v39\ns_add_u32 s50, s50, 2\ns_cmp_gt_i32 s50, s42\ns_cselect_b32 s69, s45, 0\ns_sub_u32 s51, s51, s69\n" \
    "s_memrealtime s[72:73]\ns_waitcnt lgkmcnt(0)\ns_mov_b32 s51, s72\ns_add_u32 s69, s50, 0\ns_and_b32 s69, s69, s41\ns_lshl_b32 s69, s69, 20\ns_add_u32 s69, s69, s32\n" \
    "s_add_u32 s74, s48, s69\ns_lshr_b32 s74, s74, 8\ns_xor_b32 s74, s74, s49\ns_and_b32 s74, s74, 0x2000\ns_add_u32 s74, s74, s69\ns_add_u32 s69, s50, 0\n" \
    "s_and_b32 s69, s69, s41\ns_lshl_b32 s69, s69, 19\ns_add_u32 s69, s69, s33\ns_add_u32 s87, s48, s69\ns_lshr_b32 s87, s87, 8\ns_xor_b32 s87, s87, s49\n" \
    "s_and_b32 s87, s87, 0x2000\ns_add_u32 s87, s87, s69\ns_add_u32 s69, s50, 1\ns_and_b32 s69, s69, s41\ns_lshl_b32 s69, s69, 18\ns_add_u32 s69, s69, s34\n" \
    "s_add_u32 s88, s48, s69\ns_lshr_b32 s88, s88, 8\ns_xor_b32 s88, s88, s49\ns_and_b32 s88, s88, 0x2000\ns_add_u32 s88, s88, s69\ns_xor_b32 s89, s88, 0x1000\n" \
    "s_mov_b32 s68, 0\ns_add_u32 s78, s87, s38\n.Lr18_%=:\ns_sub_u32 s70, s51, 52\n.Lr27_%=:\ns_sleep 1\ns_memrealtime s[72:73]\ns_waitcnt lgkmcnt(0)\ns_sub_u32 s72, s72, s70\n" \
    "s_cmp_lt_i32 s72, 0\ns_cbranch_scc1 .Lr27_%=\nbuffer_load_dword v20, off, s[64:67], s78 sc1\nbuffer_load_dword v17, v8, s[64:67], 0 offen sc1\ns_waitcnt vmcnt(1)\n" \
    "v_readfirstlane_b32 s70, v20\ns_memrealtime s[72:73]\ns_cmp_eq_u32 s70, -1\ns_cbranch_scc1 .Lr19_%=\ns_lshr_b32 s71, s70, 8\ns_waitcnt lgkmcnt(0)\ns_sub_u32 s71, s72, s71\n" \
    "s_and_b32 s71, s71, 0xffffff\ns_sub_u32 s51, s72, s71\ns_add_u32 s51, s51, 176\ns_and_b32 s70, s70, 128\ns_lshl_b32 s52, s70, 5\ns_xor_b32 s87, s87, s52\ns_mov_b32 s68, 0\n" \
    "s_sub_u32 s70, s51, s54\n.Lr28_%=:\ns_memrealtime s[72:73]\ns_waitcnt lgkmcnt(0)\ns_sub_u32 s72, s72, s70\ns_cmp_lt_i32 s72, 0\ns_cbranch_scc1 .Lr28_%=\n" \
    "buffer_load_dwordx4 v[40:43], v0, s[64:67], s74 offen offset:0 sc1\nbuffer_load_dwordx4 v[44:47], v0, s[64:67], s74 offen offset:1024 sc1\ns_add_u32 s69, s74, 0x4000\n" \
    "buffer_load_dwordx4 v[48:51], v0, s[64:67], s69 offen offset:0 sc1\nbuffer_load_dwordx4 v[52:55], v0, s[64:67], s69 offen offset:1024 sc1\n.Lr29_%=:\n" \
    "s_memrealtime s[72:73]\ns_waitcnt lgkmcnt(0)\ns_sub_u32 s72, s72, s51\ns_cmp_lt_i32 s72, -3\ns_cbranch_scc1 .Lr29_%=\ns_cmp_ge_i32 s72, 0\ns_cbranch_scc1 .Lr30_%=\n" \
    "s_nop 2\ns_cmp_ge_i32 s72, -1\ns_cbranch_scc1 .Lr30_%=\ns_nop 2\ns_cmp_ge_i32 s72, -2\ns_cbranch_scc1 .Lr30_%=\ns_nop 2\n.Lr30_%=:\n" \
    "buffer_load_dwordx2 v[116:117], v5, s[64:67], s87 offen offset:0 sc1\ns_nop 5\nbuffer_load_dwordx2 v[118:119], v5, s[64:67], s87 offen offset:128 sc1\ns_nop 5\n" \
    "buffer_load_dwordx4 v[100:103], v0, s[64:67], s87 offen offset:0 sc1\ns_nop 10\nbuffer_load_dwordx4 v[104:107], v0, s[64:67], s87 offen offset:1024 sc1\ns_nop 10\n" \
    "buffer_load_dwordx4 v[108:111], v0, s[64:67], s87 offen offset:2048 sc1\ns_nop 10\nbuffer_load_dwordx4 v[112:115], v0, s[64:67], s87 offen offset:3072 sc1\n.Lr22_%=:\n" \
    "v_mov_b64 v[24:25], 0\nv_mov_b64 v[26:27], 0\nv_mov_b64 v[28:29], 0\nv_mov_b64 v[30:31], 0\ns_waitcnt vmcnt(6)\nv_max3_u32 v22, v40, v41, v42\n" \
    "v_max3_u32 v22, v22, v43, v44\nv_max3_u32 v22, v22, v45, v46\nv_max3_u32 v22, v22, v47, v48\nv_max3_u32 v22, v22, v49, v50\nv_max3_u32 v22, v22, v51, v52\n" \
    "v_max3_u32 v22, v22, v53, v54\nv_max_u32 v22, v22, v55\ns_waitcnt vmcnt(4)\nv_smfmac_f32_16x16x64_bf16 v[24:27], v[116:119], v[40:47], v4\nv_max3_u32 v22, v22, v116, v117\n" \
    "v_smfmac_f32_16x16x64_bf16 v[28:31], v[116:119], v[48:55], v4\nv_max3_u32 v22, v22, v118, v119\ns_waitcnt vmcnt(3)\n" \
    "v_smfmac_f32_16x16x64_bf16 v[24:27], v[100:103], v[120:127], v4\nv_max3_u32 v22, v22, v100, v101\nv_smfmac_f32_16x16x64_bf16 v[28:31], v[100:103], v[152:159], v4\n" \
    "v_max3_u32 v22, v22, v102, v103\ns_waitcnt vmcnt(2)\nv_smfmac_f32_16x16x64_bf16 v[24:27], v[104:107], v[128:135], v4\nv_max3_u32 v22, v22, v104, v105\n" \
    "v_smfmac_f32_16x16x64_bf16 v[28:31], v[104:107], v[160:167], v4\nv_max3_u32 v22, v22, v106, v107\ns_waitcnt vmcnt(1)\n" \
    "v_smfmac_f32_16x16x64_bf16 v[24:27], v[108:111], v[136:143], v4\nv_max3_u32 v22, v22, v108, v109\nv_smfmac_f32_16x16x64_bf16 v[28:31], v[108:111], v[168:175], v4\n" \
    "v_max3_u32 v22, v22, v110, v111\ns_waitcnt vmcnt(0)\nv_smfmac_f32_16x16x64_bf16 v[24:27], v[112:115], v[144:151], v4\nv_max3_u32 v22, v22, v112, v113\n" \
    "v_smfmac_f32_16x16x64_bf16 v[28:31], v[112:115], v[176:183], v4\nv_max3_u32 v22, v22, v114, v115\nv_cmp_eq_u32 vcc, -1, v22\ns_nop 5\nv_permlane32_swap_b32 v24, v28\n" \
    "v_permlane32_swap_b32 v25, v29\nv_permlane32_swap_b32 v26, v30\nv_permlane32_swap_b32 v27, v31\nv_pk_add_f32 v[24:25], v[24:25], v[28:29]\n" \
    "v_pk_add_f32 v[26:27], v[26:27], v[30:31]\nds_write2_b32 v6, v24, v25 offset0:0 offset1:36\nds_write2_b32 v6, v26, v27 offset0:72 offset1:108\ns_cbranch_vccnz .Lr23_%=\n" \
    "s_waitcnt lgkmcnt(0)\ns_barrier\nds_read_b128 v[24:27], v7 offset:0\nds_read_b128 v[28:31], v7 offset:1152\nds_read_b128 v[32:35], v7 offset:2304\n" \
    "ds_read_b128 v[36:39], v7 offset:3456\ns_waitcnt lgkmcnt(2)\nv_pk_add_f32 v[24:25], v[24:25], v[28:29]\nv_pk_add_f32 v[26:27], v[26:27], v[30:31]\ns_waitcnt lgkmcnt(0)\n" \
    "v_pk_add_f32 v[32:33], v[32:33], v[36:37]\nv_pk_add_f32 v[34:35], v[34:35], v[38:39]\nv_pk_add_f32 v[24:25], v[24:25], v[32:33]\nv_pk_add_f32 v[26:27], v[26:27], v[34:35]\n" \
    "v_cvt_pk_bf16_f32 v248, v24, v25\nv_cvt_pk_bf16_f32 v249, v26, v27\ns_nop 0\nv_permlane32_swap_b32 v248, v250\nv_permlane32_swap_b32 v249, v251\ns_mov_b32 exec_hi, 0\n" \
    "buffer_store_dwordx4 v[248:251], v0, s[64:67], s88 offen sc1\nbuffer_store_dwordx4 v[248:251], v0, s[64:67], s89 offen sc1\ns_mov_b32 exec_hi, -1\ns_add_u32 s51, s51, s55\n" \
    "s_add_u32 s51, s51, s55\ns_add_u32 s70, s50, 2\ns_min_i32 s70, s70, 63\ns_add_u32 s71, s50, -2\ns_mov_b32 s68, 0\ns_branch .Lr35_%=\n.Lr34_%=:\n" \
    "buffer_load_dword v17, v8, s[64:67], 0 offen sc1\ns_waitcnt vmcnt(0)\n.Lr35_%=:\nv_cmp_lt_i32 vcc, s70, v17\ns_nop 3\ns_and_b32 s69, vcc_lo, 0xff\ns_cmp_eq_u32 s69, 0xff\n" \
    "s_cbranch_scc0 .Lr36_%=\ns_mov_b64 exec, 0xff\nv_mov_b32 v8, s39\ns_mov_b64 exec, -1\ns_mov_b32 s68, 0\ns_add_u32 s69, s50, 2\ns_and_b32 s69, s69, s41\n" \
    "s_lshl_b32 s69, s69, 19\ns_add_u32 s69, s69, s35\ns_add_u32 s84, s48, s69\ns_lshr_b32 s84, s84, 8\ns_xor_b32 s84, s84, s49\ns_and_b32 s84, s84, 0x2000\n" \
    "s_add_u32 s84, s84, s69\n.Lr37_%=:\ns_add_u32 s85, s84, 0\nbuffer_load_dwordx4 v[104:107], v0, s[64:67], s85 offen offset:0 sc1\n" \
    "buffer_load_dwordx4 v[108:111], v0, s[64:67], s85 offen offset:1024 sc1\nbuffer_load_dwordx4 v[112:115], v0, s[64:67], s85 offen offset:2048 sc1\n" \
    "buffer_load_dwordx4 v[116:119], v0, s[64:67], s85 offen offset:3072 sc1\nbuffer_load_dwordx4 v[72:75], v1, s[64:67], s85 offen offset:0 sc1\n" \
    "buffer_load_dwordx4 v[76:79], v1, s[64:67], s85 offen offset:1024 sc1\nbuffer_load_dwordx4 v[80:83], v1, s[64:67], s85 offen offset:2048 sc1\n" \
    "buffer_load_dwordx4 v[84:87], v1, s[64:67], s85 offen offset:3072 sc1\ns_add_u32 s85, s84, 16384\nbuffer_load_dwordx4 v[88:91], v0, s[64:67], s85 offen offset:0 sc1\n" \
    "buffer_load_dwordx4 v[92:95], v0, s[64:67], s85 offen offset:1024 sc1\nbuffer_load_dwordx4 v[96:99], v0, s[64:67], s85 offen offset:2048 sc1\n.Lr38_%=:\nv_mov_b32 v22, 0\n" \
    "s_waitcnt vmcnt(10)\nv_mfma_f32_16x16x32_bf16 v[24:27], v[104:107], a[0:3], 0\nbuffer_load_dwordx4 v[100:103], v0, s[64:67], s85 offen offset:3072 sc1\n" \
    "v_mfma_f32_16x16x32_bf16 v[28:31], v[104:107], a[16:19], 0\ns_add_u32 s69, s50, 2\ns_and_b32 s69, s69, s41\ns_lshl_b32 s69, s69, 20\ns_add_u32 s69, s69, s32\n" \
    "s_add_u32 s74, s48, s69\ns_lshr_b32 s74, s74, 8\ns_xor_b32 s74, s74, s49\ns_waitcnt vmcnt(10)\nv_mfma_f32_16x16x32_bf16 v[24:27], v[108:111], a[4:7], v[24:27]\n" \
    "buffer_load_dwordx4 v[104:107], v1, s[64:67], s85 offen offset:0 sc1\nv_mfma_f32_16x16x32_bf16 v[28:31], v[108:111], a[20:23], v[28:31]\ns_and_b32 s74, s74, 0x2000\n" \
    "s_add_u32 s74, s74, s69\ns_add_u32 s69, s50, 2\ns_and_b32 s69, s69, s41\ns_lshl_b32 s69, s69, 19\ns_add_u32 s69, s69, s33\ns_add_u32 s87, s48, s69\ns_waitcnt vmcnt(10)\n" \
    "v_mfma_f32_16x16x32_bf16 v[24:27], v[112:115], a[8:11], v[24:27]\nbuffer_load_dwordx4 v[108:111], v1, s[64:67], s85 offen offset:1024 sc1\n" \
    "v_mfma_f32_16x16x32_bf16 v[28:31], v[112:115], a[24:27], v[28:31]\ns_lshr_b32 s87, s87, 8\ns_xor_b32 s87, s87, s49\ns_and_b32 s87, s87, 0x2000\ns_add_u32 s87, s87, s69\n" \
    "s_add_u32 s69, s50, 3\ns_and_b32 s69, s69, s41\ns_lshl_b32 s69, s69, 18\ns_waitcnt vmcnt(10)\nv_mfma_f32_16x16x32_bf16 v[24:27], v[116:119], a[12:15], v[24:27]\n" \
    "buffer_load_dwordx4 v[112:115], v1, s[64:67], s85 offen offset:2048 sc1\nv_mfma_f32_16x16x32_bf16 v[28:31], v[116:119], a[28:31], v[28:31]\ns_add_u32 s69, s69, s34\n" \
    "s_add_u32 s88, s48, s69\ns_lshr_b32 s88, s88, 8\ns_xor_b32 s88, s88, s49\ns_and_b32 s88, s88, 0x2000\ns_add_u32 s88, s88, s69\ns_xor_b32 s89, s88, 0x1000\n" \
    "s_waitcnt vmcnt(10)\nv_mfma_f32_16x16x32_bf16 v[32:35], v[72:75], a[0:3], 0\nbuffer_load_dwordx4 v[116:119], v1, s[64:67], s85 offen offset:3072 sc1\n" \
    "v_mfma_f32_16x16x32_bf16 v[36:39], v[72:75], a[16:19], 0\ns_add_u32 s69, s50, 0\ns_lshl_b32 s69, s69, 20\ns_add_u32 s69, s69, s36\ns_add_u32 s75, s48, s69\n" \
    "s_lshr_b32 s75, s75, 8\ns_xor_b32 s75, s75, s49\ns_and_b32 s75, s75, 0x2000\ns_waitcnt vmcnt(10)\nv_mfma_f32_16x16x32_bf16 v[32:35], v[76:79], a[4:7], v[32:35]\n" \
    "s_add_u32 s85, s84, 32768\nbuffer_load_dwordx4 v[72:75], v0, s[64:67], s85 offen offset:0 sc1\nv_mfma_f32_16x16x32_bf16 v[36:39], v[76:79], a[20:23], v[36:39]\n" \
    "v_cvt_pk_bf16_f32 v184, v24, v25\nv_cvt_pk_bf16_f32 v186, v26, v27\ns_add_u32 s75, s75, s69\ns_add_u32 s76, s75, 0x4000\ns_add_u32 s77, s75, s37\n" \
    "s_xor_b32 s79, s75, 0x2000\ns_add_u32 s82, s50, 0\ns_waitcnt vmcnt(10)\nv_mfma_f32_16x16x32_bf16 v[32:35], v[80:83], a[8:11], v[32:35]\n" \
    "buffer_load_dwordx4 v[76:79], v0, s[64:67], s85 offen offset:1024 sc1\nv_mfma_f32_16x16x32_bf16 v[36:39], v[80:83], a[24:27], v[36:39]\nv_cvt_pk_bf16_f32 v216, v28, v29\n" \
    "v_cvt_pk_bf16_f32 v218, v30, v31\ns_lshl_b32 s80, s82, 16\ns_lshl_b32 s82, s82, 2\ns_add_u32 s82, s82, s40\ns_waitcnt vmcnt(10)\n" \
    "v_mfma_f32_16x16x32_bf16 v[32:35], v[84:87], a[12:15], v[32:35]\nbuffer_load_dwordx4 v[80:83], v0, s[64:67], s85 offen offset:2048 sc1\n" \
    "v_mfma_f32_16x16x32_bf16 v[36:39], v[84:87], a[28:31], v[36:39]\ns_waitcnt vmcnt(10)\nv_mfma_f32_16x16x32_bf16 v[24:27], v[88:91], a[32:35], 0\n" \
    "buffer_load_dwordx4 v[84:87], v0, s[64:67], s85 offen offset:3072 sc1\nv_mfma_f32_16x16x32_bf16 v[28:31], v[88:91], a[48:51], 0\ns_waitcnt vmcnt(10)\n" \
    "v_mfma_f32_16x16x32_bf16 v[24:27], v[92:95], a[36:39], v[24:27]\nbuffer_load_dwordx4 v[88:91], v1, s[64:67], s85 offen offset:0 sc1\n" \
    "v_mfma_f32_16x16x32_bf16 v[28:31], v[92:95], a[52:55], v[28:31]\nv_cvt_pk_bf16_f32 v185, v32, v33\nv_cvt_pk_bf16_f32 v187, v34, v35\ns_waitcnt vmcnt(10)\n" \
    "v_mfma_f32_16x16x32_bf16 v[24:27], v[96:99], a[40:43], v[24:27]\nbuffer_load_dwordx4 v[92:95], v1, s[64:67], s85 offen offset:1024 sc1\n" \
    "v_mfma_f32_16x16x32_bf16 v[28:31], v[96:99], a[56:59], v[28:31]\nv_cvt_pk_bf16_f32 v217, v36, v37\nv_cvt_pk_bf16_f32 v219, v38, v39\ns_waitcnt vmcnt(10)\n" \
    "v_mfma_f32_16x16x32_bf16 v[24:27], v[100:103], a[44:47], v[24:27]\nbuffer_load_dwordx4 v[96:99], v1, s[64:67], s85 offen offset:2048 sc1\n" \
    "v_mfma_f32_16x16x32_bf16 v[28:31], v[100:103], a[60:63], v[28:31]\ns_waitcnt vmcnt(10)\nv_mfma_f32_16x16x32_bf16 v[32:35], v[104:107], a[32:35], 0\n" \
    "buffer_load_dwordx4 v[100:103], v1, s[64:67], s85 offen offset:3072 sc1\nv_mfma_f32_16x16x32_bf16 v[36:39], v[104:107], a[48:51], 0\ns_waitcnt vmcnt(10)\n" \
    "v_mfma_f32_16x16x32_bf16 v[32:35], v[108:111], a[36:39], v[32:35]\ns_add_u32 s85, s84, 49152\nbuffer_load_dwordx4 v[104:107], v0, s[64:67], s85 offen offset:0 sc1\n" \
    "v_mfma_f32_16x16x32_bf16 v[36:39], v[108:111], a[52:55], v[36:39]\nv_cvt_pk_bf16_f32 v188, v24, v25\nv_cvt_pk_bf16_f32 v190, v26, v27\ns_waitcnt vmcnt(10)\n" \
    "v_mfma_f32_16x16x32_bf16 v[32:35], v[112:115], a[40:43], v[32:35]\nbuffer_load_dwordx4 v[108:111], v0, s[64:67], s85 offen offset:1024 sc1\n" \
    "v_mfma_f32_16x16x32_bf16 v[36:39], v[112:115], a[56:59], v[36:39]\nv_cvt_pk_bf16_f32 v220, v28, v29\nv_cvt_pk_bf16_f32 v222, v30, v31\ns_waitcnt vmcnt(10)\n" \
    "v_mfma_f32_16x16x32_bf16 v[32:35], v[116:119], a[44:47], v[32:35]\nbuffer_load_dwordx4 v[112:115], v0, s[64:67], s85 offen offset:2048 sc1\n" \
    "v_mfma_f32_16x16x32_bf16 v[36:39], v[116:119], a[60:63], v[36:39]\ns_waitcnt vmcnt(10)\nv_mfma_f32_16x16x32_bf16 v[24:27], v[72:75], a[64:67], 0\n" \
    "buffer_load_dwordx4 v[116:119], v0, s[64:67], s85 offen offset:3072 sc1\nv_mfma_f32_16x16x32_bf16 v[28:31], v[72:75], a[80:83], 0\ns_waitcnt vmcnt(10)\n" \
    "v_mfma_f32_16x16x32_bf16 v[24:27], v[76:79], a[68:71], v[24:27]\nbuffer_load_dwordx4 v[72:75], v1, s[64:67], s85 offen offset:0 sc1\n" \
    "v_mfma_f32_16x16x32_bf16 v[28:31], v[76:79], a[84:87], v[28:31]\nv_cvt_pk_bf16_f32 v189, v32, v33\nv_cvt_pk_bf16_f32 v191, v34, v35\ns_waitcnt vmcnt(10)\n" \
    "v_mfma_f32_16x16x32_bf16 v[24:27], v[80:83], a[72:75], v[24:27]\nbuffer_load_dwordx4 v[76:79], v1, s[64:67], s85 offen offset:1024 sc1\n" \
    "v_mfma_f32_16x16x32_bf16 v[28:31], v[80:83], a[88:91], v[28:31]\nv_cvt_pk_bf16_f32 v221, v36, v37\nv_cvt_pk_bf16_f32 v223, v38, v39\ns_waitcnt vmcnt(10)\n" \
    "v_mfma_f32_16x16x32_bf16 v[24:27], v[84:87], a[76:79], v[24:27]\nbuffer_load_dwordx4 v[80:83], v1, s[64:67], s85 offen offset:2048 sc1\n" \
    "v_mfma_f32_16x16x32_bf16 v[28:31], v[84:87], a[92:95], v[28:31]\ns_waitcnt vmcnt(10)\nv_mfma_f32_16x16x32_bf16 v[32:35], v[88:91], a[64:67], 0\n" \
    "buffer_load_dwordx4 v[84:87], v1, s[64:67], s85 offen offset:3072 sc1\nv_mfma_f32_16x16x32_bf16 v[36:39], v[88:91], a[80:83], 0\ns_waitcnt vmcnt(10)\n" \
    "v_mfma_f32_16x16x32_bf16 v[32:35], v[92:95], a[68:71], v[32:35]\ns_add_u32 s85, s84, 65536\nbuffer_load_dwordx4 v[88:91], v0, s[64:67], s85 offen offset:0 sc1\n" \
    "v_mfma_f32_16x16x32_bf16 v[36:39], v[92:95], a[84:87], v[36:39]\nv_cvt_pk_bf16_f32 v192, v24, v25\nv_cvt_pk_bf16_f32 v194, v26, v27\ns_waitcnt vmcnt(10)\n" \
    "v_mfma_f32_16x16x32_bf16 v[32:35], v[96:99], a[72:75], v[32:35]\nbuffer_load_dwordx4 v[92:95], v0, s[64:67], s85 offen offset:1024 sc1\n" \
    "v_mfma_f32_16x16x32_bf16 v[36:39], v[96:99], a[88:91], v[36:39]\nv_cvt_pk_bf16_f32 v224, v28, v29\nv_cvt_pk_bf16_f32 v226, v30, v31\ns_waitcnt vmcnt(10)\n" \
    "v_mfma_f32_16x16x32_bf16 v[32:35], v[100:103], a[76:79], v[32:35]\nbuffer_load_dwordx4 v[96:99], v0, s[64:67], s85 offen offset:2048 sc1\n" \
    "v_mfma_f32_16x16x32_bf16 v[36:39], v[100:103], a[92:95], v[36:39]\ns_waitcnt vmcnt(10)\nv_mfma_f32_16x16x32_bf16 v[24:27], v[104:107], a[96:99], 0\n" \
    "buffer_load_dwordx4 v[100:103], v0, s[64:67], s85 offen offset:3072 sc1\nv_mfma_f32_16x16x32_bf16 v[28:31], v[104:107], a[112:115], 0\ns_waitcnt vmcnt(10)\n" \
    "v_mfma_f32_16x16x32_bf16 v[24:27], v[108:111], a[100:103], v[24:27]\nbuffer_load_dwordx4 v[104:107], v1, s[64:67], s85 offen offset:0 sc1\n" \
    "v_mfma_f32_16x16x32_bf16 v[28:31], v[108:111], a[116:119], v[28:31]\nv_cvt_pk_bf16_f32 v193, v32, v33\nv_cvt_pk_bf16_f32 v195, v34, v35\ns_waitcnt vmcnt(10)\n" \
    "v_mfma_f32_16x16x32_bf16 v[24:27], v[112:115], a[104:107], v[24:27]\nbuffer_load_dwordx4 v[108:111], v1, s[64:67], s85 offen offset:1024 sc1\n" \
    "v_mfma_f32_16x16x32_bf16 v[28:31], v[112:115], a[120:123], v[28:31]\nv_cvt_pk_bf16_f32 v225, v36, v37\nv_cvt_pk_bf16_f32 v227, v38, v39\ns_waitcnt vmcnt(10)\n" \
    "v_mfma_f32_16x16x32_bf16 v[24:27], v[116:119], a[108:111], v[24:27]\nbuffer_load_dwordx4 v[112:115], v1, s[64:67], s85 offen offset:2048 sc1\n" \
    "v_mfma_f32_16x16x32_bf16 v[28:31], v[116:119], a[124:127], v[28:31]\ns_waitcnt vmcnt(10)\nv_mfma_f32_16x16x32_bf16 v[32:35], v[72:75], a[96:99], 0\n" \
    "buffer_load_dwordx4 v[116:119], v1, s[64:67], s85 offen offset:3072 sc1\nv_mfma_f32_16x16x32_bf16 v[36:39], v[72:75], a[112:115], 0\ns_waitcnt vmcnt(10)\n" \
    "v_mfma_f32_16x16x32_bf16 v[32:35], v[76:79], a[100:103], v[32:35]\ns_add_u32 s85, s84, 81920\nbuffer_load_dwordx4 v[72:75], v0, s[64:67], s85 offen offset:0 sc1\n" \
    "v_mfma_f32_16x16x32_bf16 v[36:39], v[76:79], a[116:119], v[36:39]\nv_cvt_pk_bf16_f32 v196, v24, v25\nv_cvt_pk_bf16_f32 v198, v26, v27\ns_waitcnt vmcnt(10)\n" \
    "v_mfma_f32_16x16x32_bf16 v[32:35], v[80:83], a[104:107], v[32:35]\nbuffer_load_dwordx4 v[76:79], v0, s[64:67], s85 offen offset:1024 sc1\n" \
    "v_mfma_f32_16x16x32_bf16 v[36:39], v[80:83], a[120:123], v[36:39]\nv_cvt_pk_bf16_f32 v228, v28, v29\nv_cvt_pk_bf16_f32 v230, v30, v31\ns_waitcnt vmcnt(10)\n" \
    "v_mfma_f32_16x16x32_bf16 v[32:35], v[84:87], a[108:111], v[32:35]\nbuffer_load_dwordx4 v[80:83], v0, s[64:67], s85 offen offset:2048 sc1\n" \
    "v_mfma_f32_16x16x32_bf16 v[36:39], v[84:87], a[124:127], v[36:39]\ns_waitcnt vmcnt(10)\nv_mfma_f32_16x16x32_bf16 v[24:27], v[88:91], a[128:131], 0\n" \
    "buffer_load_dwordx4 v[84:87], v0, s[64:67], s85 offen offset:3072 sc1\nv_mfma_f32_16x16x32_bf16 v[28:31], v[88:91], a[144:147], 0\ns_waitcnt vmcnt(10)\n" \
    "v_mfma_f32_16x16x32_bf16 v[24:27], v[92:95], a[132:135], v[24:27]\nbuffer_load_dwordx4 v[88:91], v1, s[64:67], s85 offen offset:0 sc1\n" \
    "v_mfma_f32_16x16x32_bf16 v[28:31], v[92:95], a[148:151], v[28:31]\nv_cvt_pk_bf16_f32 v197, v32, v33\nv_cvt_pk_bf16_f32 v199, v34, v35\ns_waitcnt vmcnt(10)\n" \
    "v_mfma_f32_16x16x32_bf16 v[24:27], v[96:99], a[136:139], v[24:27]\nbuffer_load_dwordx4 v[92:95], v1, s[64:67], s85 offen offset:1024 sc1\n" \
    "v_mfma_f32_16x16x32_bf16 v[28:31], v[96:99], a[152:155], v[28:31]\nv_cvt_pk_bf16_f32 v229, v36, v37\nv_cvt_pk_bf16_f32 v231, v38, v39\ns_waitcnt vmcnt(10)\n" \
    "v_mfma_f32_16x16x32_bf16 v[24:27], v[100:103], a[140:143], v[24:27]\nbuffer_load_dwordx4 v[96:99], v1, s[64:67], s85 offen offset:2048 sc1\n" \
    "v_mfma_f32_16x16x32_bf16 v[28:31], v[100:103], a[156:159], v[28:31]\ns_waitcnt vmcnt(10)\nv_mfma_f32_16x16x32_bf16 v[32:35], v[104:107], a[128:131], 0\n" \
    "buffer_load_dwordx4 v[100:103], v1, s[64:67], s85 offen offset:3072 sc1\nv_mfma_f32_16x16x32_bf16 v[36:39], v[104:107], a[144:147], 0\ns_waitcnt vmcnt(10)\n" \
    "v_mfma_f32_16x16x32_bf16 v[32:35], v[108:111], a[132:135], v[32:35]\ns_add_u32 s85, s84, 98304\nbuffer_load_dwordx4 v[104:107], v0, s[64:67], s85 offen offset:0 sc1\n" \
    "v_mfma_f32_16x16x32_bf16 v[36:39], v[108:111], a[148:151], v[36:39]\nv_cvt_pk_bf16_f32 v200, v24, v25\nv_cvt_pk_bf16_f32 v202, v26, v27\ns_waitcnt vmcnt(10)\n" \
    "v_mfma_f32_16x16x32_bf16 v[32:35], v[112:115], a[136:139], v[32:35]\nbuffer_load_dwordx4 v[108:111], v0, s[64:67], s85 offen offset:1024 sc1\n" \
    "v_mfma_f32_16x16x32_bf16 v[36:39], v[112:115], a[152:155], v[36:39]\nv_cvt_pk_bf16_f32 v232, v28, v29\nv_cvt_pk_bf16_f32 v234, v30, v31\ns_waitcnt vmcnt(10)\n" \
    "v_mfma_f32_16x16x32_bf16 v[32:35], v[116:119], a[140:143], v[32:35]\nbuffer_load_dwordx4 v[112:115], v0, s[64:67], s85 offen offset:2048 sc1\n" \
    "v_mfma_f32_16x16x32_bf16 v[36:39], v[116:119], a[156:159], v[36:39]\ns_waitcnt vmcnt(10)\nv_mfma_f32_16x16x32_bf16 v[24:27], v[72:75], a[160:163], 0\n" \
    "buffer_load_dwordx4 v[116:119], v0, s[64:67], s85 offen offset:3072 sc1\nv_mfma_f32_16x16x32_bf16 v[28:31], v[72:75], a[176:179], 0\n" \
    "buffer_load_dword v17, v8, s[64:67], 0 offen sc1\ns_waitcnt vmcnt(11)\nv_mfma_f32_16x16x32_bf16 v[24:27], v[76:79], a[164:167], v[24:27]\n" \
    "buffer_load_dwordx4 v[72:75], v1, s[64:67], s85 offen offset:0 sc1\nv_mfma_f32_16x16x32_bf16 v[28:31], v[76:79], a[180:183], v[28:31]\nv_cvt_pk_bf16_f32 v201, v32, v33\n" \
    "v_cvt_pk_bf16_f32 v203, v34, v35\ns_waitcnt vmcnt(11)\nv_mfma_f32_16x16x32_bf16 v[24:27], v[80:83], a[168:171], v[24:27]\n" \
    "buffer_load_dwordx4 v[76:79], v1, s[64:67], s85 offen offset:1024 sc1\nv_mfma_f32_16x16x32_bf16 v[28:31], v[80:83], a[184:187], v[28:31]\nv_cvt_pk_bf16_f32 v233, v36, v37\n" \
    "v_cvt_pk_bf16_f32 v235, v38, v39\ns_waitcnt vmcnt(11)\nv_mfma_f32_16x16x32_bf16 v[24:27], v[84:87], a[172:175], v[24:27]\n" \
    "buffer_load_dwordx4 v[80:83], v1, s[64:67], s85 offen offset:2048 sc1\nv_mfma_f32_16x16x32_bf16 v[28:31], v[84:87], a[188:191], v[28:31]\ns_waitcnt vmcnt(11)\n" \
    "v_mfma_f32_16x16x32_bf16 v[32:35], v[88:91], a[160:163], 0\nbuffer_load_dwordx4 v[84:87], v1, s[64:67], s85 offen offset:3072 sc1\n" \
    "v_mfma_f32_16x16x32_bf16 v[36:39], v[88:91], a[176:179], 0\ns_waitcnt vmcnt(11)\nv_mfma_f32_16x16x32_bf16 v[32:35], v[92:95], a[164:167], v[32:35]\n" \
    "s_add_u32 s85, s84, 114688\nbuffer_load_dwordx4 v[88:91], v0, s[64:67], s85 offen offset:0 sc1\nv_mfma_f32_16x16x32_bf16 v[36:39], v[92:95], a[180:183], v[36:39]\n" \
    "v_cvt_pk_bf16_f32 v204, v24, v25\nv_cvt_pk_bf16_f32 v206, v26, v27\ns_waitcnt vmcnt(11)\nv_mfma_f32_16x16x32_bf16 v[32:35], v[96:99], a[168:171], v[32:35]\n" \
    "buffer_load_dwordx4 v[92:95], v0, s[64:67], s85 offen offset:1024 sc1\nv_mfma_f32_16x16x32_bf16 v[36:39], v[96:99], a[184:187], v[36:39]\nv_cvt_pk_bf16_f32 v236, v28, v29\n" \
    "v_cvt_pk_bf16_f32 v238, v30, v31\ns_add_u32 s69, s50, 4\ns_and_b32 s69, s69, s41\ns_lshl_b32 s69, s69, 19\ns_add_u32 s69, s69, s35\ns_add_u32 s84, s48, s69\n" \
    "s_waitcnt vmcnt(11)\nv_mfma_f32_16x16x32_bf16 v[32:35], v[100:103], a[172:175], v[32:35]\nbuffer_load_dwordx4 v[96:99], v0, s[64:67], s85 offen offset:2048 sc1\n" \
    "v_mfma_f32_16x16x32_bf16 v[36:39], v[100:103], a[188:191], v[36:39]\ns_lshr_b32 s84, s84, 8\ns_xor_b32 s84, s84, s49\ns_and_b32 s84, s84, 0x2000\ns_add_u32 s84, s84, s69\n" \
    "s_waitcnt vmcnt(11)\nv_mfma_f32_16x16x32_bf16 v[24:27], v[104:107], a[192:195], 0\nbuffer_load_dwordx4 v[100:103], v0, s[64:67], s85 offen offset:3072 sc1\n" \
    "v_mfma_f32_16x16x32_bf16 v[28:31], v[104:107], a[208:211], 0\ns_waitcnt vmcnt(11)\nv_mfma_f32_16x16x32_bf16 v[24:27], v[108:111], a[196:199], v[24:27]\n" \
    "buffer_load_dwordx4 v[104:107], v1, s[64:67], s85 offen offset:0 sc1\nv_mfma_f32_16x16x32_bf16 v[28:31], v[108:111], a[212:215], v[28:31]\n" \
    "v_cvt_pk_bf16_f32 v205, v32, v33\nv_cvt_pk_bf16_f32 v207, v34, v35\ns_waitcnt vmcnt(11)\nv_mfma_f32_16x16x32_bf16 v[24:27], v[112:115], a[200:203], v[24:27]\n" \
    "buffer_load_dwordx4 v[108:111], v1, s[64:67], s85 offen offset:1024 sc1\nv_mfma_f32_16x16x32_bf16 v[28:31], v[112:115], a[216:219], v[28:31]\n" \
    "v_cvt_pk_bf16_f32 v237, v36, v37\nv_cvt_pk_bf16_f32 v239, v38, v39\ns_waitcnt vmcnt(11)\nv_mfma_f32_16x16x32_bf16 v[24:27], v[116:119], a[204:207], v[24:27]\n" \
    "buffer_load_dwordx4 v[112:115], v1, s[64:67], s85 offen offset:2048 sc1\nv_mfma_f32_16x16x32_bf16 v[28:31], v[116:119], a[220:223], v[28:31]\ns_waitcnt vmcnt(10)\n" \
    "v_mfma_f32_16x16x32_bf16 v[32:35], v[72:75], a[192:195], 0\nbuffer_load_dwordx4 v[116:119], v1, s[64:67], s85 offen offset:3072 sc1\n" \
    "v_mfma_f32_16x16x32_bf16 v[36:39], v[72:75], a[208:211], 0\ns_mov_b32 s86, 0\nv_mov_b32 v18, 0x7f000000\ns_add_u32 s70, s50, 4\ns_min_i32 s70, s70, 63\n" \
    "s_add_u32 s71, s50, 0\nv_readlane_b32 s69, v17, 9\ns_cmp_gt_i32 s69, s71\ns_cbranch_scc0 .Lr44_%=\ns_mov_b32 s86, 1\nv_mov_b32 v18, v0\n.Lr44_%=:\n" \
    "v_add_u32 v19, 0x1000, v18\ns_waitcnt vmcnt(10)\nv_mfma_f32_16x16x32_bf16 v[32:35], v[76:79], a[196:199], v[32:35]\n" \
    "v_mfma_f32_16x16x32_bf16 v[36:39], v[76:79], a[212:215], v[36:39]\nv_cvt_pk_bf16_f32 v208, v24, v25\nv_cvt_pk_bf16_f32 v210, v26, v27\n" \
    "buffer_load_dwordx4 v[72:75], v18, s[64:67], s75 offen offset:0 sc1\ns_waitcnt vmcnt(10)\nv_mfma_f32_16x16x32_bf16 v[32:35], v[80:83], a[200:203], v[32:35]\n" \
    "v_mfma_f32_16x16x32_bf16 v[36:39], v[80:83], a[216:219], v[36:39]\nv_cvt_pk_bf16_f32 v240, v28, v29\nv_cvt_pk_bf16_f32 v242, v30, v31\n" \
    "buffer_load_dwordx4 v[76:79], v18, s[64:67], s75 offen offset:1024 sc1\ns_waitcnt vmcnt(10)\nv_mfma_f32_16x16x32_bf16 v[32:35], v[84:87], a[204:207], v[32:35]\n" \
    "v_mfma_f32_16x16x32_bf16 v[36:39], v[84:87], a[220:223], v[36:39]\nbuffer_load_dwordx4 v[80:83], v18, s[64:67], s75 offen offset:2048 sc1\ns_waitcnt vmcnt(10)\n" \
    "v_mfma_f32_16x16x32_bf16 v[24:27], v[88:91], a[224:227], 0\nv_mfma_f32_16x16x32_bf16 v[28:31], v[88:91], a[240:243], 0\n" \
    "buffer_load_dwordx4 v[84:87], v18, s[64:67], s75 offen offset:3072 sc1\ns_waitcnt vmcnt(10)\nv_mfma_f32_16x16x32_bf16 v[24:27], v[92:95], a[228:231], v[24:27]\n" \
    "v_mfma_f32_16x16x32_bf16 v[28:31], v[92:95], a[244:247], v[28:31]\nv_cvt_pk_bf16_f32 v209, v32, v33\nv_cvt_pk_bf16_f32 v211, v34, v35\n" \
    "buffer_load_dwordx4 v[88:91], v19, s[64:67], s75 offen offset:0 sc1\ns_waitcnt vmcnt(10)\nv_mfma_f32_16x16x32_bf16 v[24:27], v[96:99], a[232:235], v[24:27]\n" \
    "v_mfma_f32_16x16x32_bf16 v[28:31], v[96:99], a[248:251], v[28:31]\nv_cvt_pk_bf16_f32 v241, v36, v37\nv_cvt_pk_bf16_f32 v243, v38, v39\n" \
    "buffer_load_dwordx4 v[92:95], v19, s[64:67], s75 offen offset:1024 sc1\ns_waitcnt vmcnt(10)\nv_mfma_f32_16x16x32_bf16 v[24:27], v[100:103], a[236:239], v[24:27]\n" \
    "v_mfma_f32_16x16x32_bf16 v[28:31], v[100:103], a[252:255], v[28:31]\nbuffer_load_dwordx4 v[96:99], v19, s[64:67], s75 offen offset:2048 sc1\n" \
    "buffer_load_dwordx2 v[252:253], v11, s[60:63], s80 offen\ns_waitcnt vmcnt(11)\nv_mfma_f32_16x16x32_bf16 v[32:35], v[104:107], a[224:227], 0\n" \
    "v_mfma_f32_16x16x32_bf16 v[36:39], v[104:107], a[240:243], 0\ns_waitcnt vmcnt(10)\nv_mfma_f32_16x16x32_bf16 v[32:35], v[108:111], a[228:231], v[32:35]\n" \
    "v_mfma_f32_16x16x32_bf16 v[36:39], v[108:111], a[244:247], v[36:39]\nv_cvt_pk_bf16_f32 v212, v24, v25\nv_cvt_pk_bf16_f32 v214, v26, v27\ns_waitcnt vmcnt(9)\n" \
    "v_mfma_f32_16x16x32_bf16 v[32:35], v[112:115], a[232:235], v[32:35]\nv_mfma_f32_16x16x32_bf16 v[36:39], v[112:115], a[248:251], v[36:39]\nv_cvt_pk_bf16_f32 v244, v28, v29\n" \
    "v_cvt_pk_bf16_f32 v246, v30, v31\ns_waitcnt vmcnt(8)\nv_mfma_f32_16x16x32_bf16 v[32:35], v[116:119], a[236:239], v[32:35]\n" \
    "v_mfma_f32_16x16x32_bf16 v[36:39], v[116:119], a[252:255], v[36:39]\ns_nop 6\nv_cvt_pk_bf16_f32 v213, v32, v33\nv_cvt_pk_bf16_f32 v215, v34, v35\n" \
    "v_cvt_pk_bf16_f32 v245, v36, v37\nv_cvt_pk_bf16_f32 v247, v38, v39\ns_add_u32 s50, s50, 2\ns_cmp_gt_i32 s50, s42\ns_cselect_b32 s69, s45, 0\ns_sub_u32 s51, s51, s69\n" \
    ".Lr3_%=:\ns_mov_b32 s68, 0\ns_add_u32 s78, s87, s38\n.Lr47_%=:\ns_sub_u32 s70, s51, 52\n.Lr56_%=:\ns_sleep 1\ns_memrealtime s[72:73]\ns_waitcnt lgkmcnt(0)\n" \
    "s_sub_u32 s72, s72, s70\ns_cmp_lt_i32 s72, 0\ns_cbranch_scc1 .Lr56_%=\nbuffer_load_dword v20, off, s[64:67], s78 sc1\nbuffer_load_dword v17, v8, s[64:67], 0 offen sc1\n" \
    "s_waitcnt vmcnt(1)\nv_readfirstlane_b32 s70, v20\ns_memrealtime s[72:73]\ns_cmp_eq_u32 s70, -1\ns_cbranch_scc1 .Lr48_%=\ns_lshr_b32 s71, s70, 8\ns_waitcnt lgkmcnt(0)\n" \
    "s_sub_u32 s71, s72, s71\ns_and_b32 s71, s71, 0xffffff\ns_sub_u32 s51, s72, s71\ns_add_u32 s51, s51, 176\ns_and_b32 s70, s70, 128\ns_lshl_b32 s52, s70, 5\n" \
    "s_xor_b32 s87, s87, s52\ns_mov_b32 s68, 0\ns_sub_u32 s70, s51, s54\n.Lr57_%=:\ns_memrealtime s[72:73]\ns_waitcnt lgkmcnt(0)\ns_sub_u32 s72, s72, s70\ns_cmp_lt_i32 s72, 0\n" \
    "s_cbranch_scc1 .Lr57_%=\nbuffer_load_dwordx4 v[56:59], v0, s[64:67], s74 offen offset:0 sc1\nbuffer_load_dwordx4 v[60:63], v0, s[64:67], s74 offen offset:1024 sc1\n" \
    "s_add_u32 s69, s74, 0x4000\nbuffer_load_dwordx4 v[64:67], v0, s[64:67], s69 offen offset:0 sc1\nbuffer_load_dwordx4 v[68:71], v0, s[64:67], s69 offen offset:1024 sc1\n" \
    ".Lr58_%=:\ns_memrealtime s[72:73]\ns_waitcnt lgkmcnt(0)\ns_sub_u32 s72, s72, s51\ns_cmp_lt_i32 s72, -3\ns_cbranch_scc1 .Lr58_%=\ns_cmp_ge_i32 s72, 0\n" \
    "s_cbranch_scc1 .Lr59_%=\ns_nop 2\ns_cmp_ge_i32 s72, -1\ns_cbranch_scc1 .Lr59_%=\ns_nop 2\ns_cmp_ge_i32 s72, -2\ns_cbranch_scc1 .Lr59_%=\ns_nop 2\n.Lr59_%=:\n" \
    "buffer_load_dwordx2 v[116:117], v5, s[64:67], s87 offen offset:0 sc1\ns_nop 5\nbuffer_load_dwordx2 v[118:119], v5, s[64:67], s87 offen offset:128 sc1\ns_nop 5\n" \
    "buffer_load_dwordx4 v[100:103], v0, s[64:67], s87 offen offset:0 sc1\ns_nop 10\nbuffer_load_dwordx4 v[104:107], v0, s[64:67], s87 offen offset:1024 sc1\ns_nop 10\n" \
    "buffer_load_dwordx4 v[108:111], v0, s[64:67], s87 offen offset:2048 sc1\ns_nop 10\nbuffer_load_dwordx4 v[112:115], v0, s[64:67], s87 offen offset:3072 sc1\n.Lr51_%=:\n" \
    "v_mov_b64 v[24:25], 0\nv_mov_b64 v[26:27], 0\nv_mov_b64 v[28:29], 0\nv_mov_b64 v[30:31], 0\ns_waitcnt vmcnt(6)\nv_max3_u32 v22, v56, v57, v58\n" \
    "v_max3_u32 v22, v22, v59, v60\nv_max3_u32 v22, v22, v61, v62\nv_max3_u32 v22, v22, v63, v64\nv_max3_u32 v22, v22, v65, v66\nv_max3_u32 v22, v22, v67, v68\n" \
    "v_max3_u32 v22, v22, v69, v70\nv_max_u32 v22, v22, v71\ns_waitcnt vmcnt(4)\nv_smfmac_f32_16x16x64_bf16 v[24:27], v[116:119], v[56:63], v4\nv_max3_u32 v22, v22, v116, v117\n" \
    "v_smfmac_f32_16x16x64_bf16 v[28:31], v[116:119], v[64:71], v4\nv_max3_u32 v22, v22, v118, v119\ns_waitcnt vmcnt(3)\n" \
    "v_smfmac_f32_16x16x64_bf16 v[24:27], v[100:103], v[184:191], v4\nv_max3_u32 v22, v22, v100, v101\nv_smfmac_f32_16x16x64_bf16 v[28:31], v[100:103], v[216:223], v4\n" \
    "v_max3_u32 v22, v22, v102, v103\nbuffer_load_dwordx4 v[100:103], v19, s[64:67], s75 offen offset:3072 sc1\ns_waitcnt vmcnt(3)\n" \
    "v_smfmac_f32_16x16x64_bf16 v[24:27], v[104:107], v[192:199], v4\nv_max3_u32 v22, v22, v104, v105\nv_smfmac_f32_16x16x64_bf16 v[28:31], v[104:107], v[224:231], v4\n" \
    "v_max3_u32 v22, v22, v106, v107\nbuffer_load_dwordx4 v[104:107], v18, s[64:67], s76 offen offset:0 sc1\ns_waitcnt vmcnt(3)\n" \
    "v_smfmac_f32_16x16x64_bf16 v[24:27], v[108:111], v[200:207], v4\nv_max3_u32 v22, v22, v108, v109\nv_smfmac_f32_16x16x64_bf16 v[28:31], v[108:111], v[232:239], v4\n" \
    "v_max3_u32 v22, v22, v110, v111\nbuffer_load_dwordx4 v[108:111], v18, s[64:67], s76 offen offset:1024 sc1\ns_waitcnt vmcnt(3)\n" \
    "v_smfmac_f32_16x16x64_bf16 v[24:27], v[112:115], v[208:215], v4\nv_max3_u32 v22, v22, v112, v113\nv_smfmac_f32_16x16x64_bf16 v[28:31], v[112:115], v[240:247], v4\n" \
    "v_max3_u32 v22, v22, v114, v115\nv_cmp_eq_u32 vcc, -1, v22\ns_nop 5\nv_permlane32_swap_b32 v24, v28\nv_permlane32_swap_b32 v25, v29\nv_permlane32_swap_b32 v26, v30\n" \
    "v_permlane32_swap_b32 v27, v31\nv_pk_add_f32 v[24:25], v[24:25], v[28:29]\nv_pk_add_f32 v[26:27], v[26:27], v[30:31]\nds_write2_b32 v6, v24, v25 offset0:0 offset1:36\n" \
    "ds_write2_b32 v6, v26, v27 offset0:72 offset1:108\ns_cbranch_vccnz .Lr52_%=\nbuffer_load_dwordx4 v[112:115], v18, s[64:67], s76 offen offset:2048 sc1\n" \
    "s_waitcnt lgkmcnt(0)\ns_barrier\nds_read_b128 v[24:27], v7 offset:0\nds_read_b128 v[28:31], v7 offset:1152\nds_read_b128 v[32:35], v7 offset:2304\n" \
    "ds_read_b128 v[36:39], v7 offset:3456\ns_waitcnt lgkmcnt(2)\nv_pk_add_f32 v[24:25], v[24:25], v[28:29]\nv_pk_add_f32 v[26:27], v[26:27], v[30:31]\ns_waitcnt lgkmcnt(0)\n" \
    "v_pk_add_f32 v[32:33], v[32:33], v[36:37]\nv_pk_add_f32 v[34:35], v[34:35], v[38:39]\nv_pk_add_f32 v[24:25], v[24:25], v[32:33]\nv_pk_add_f32 v[26:27], v[26:27], v[34:35]\n" \
    "v_cvt_pk_bf16_f32 v248, v24, v25\nv_cvt_pk_bf16_f32 v249, v26, v27\ns_nop 0\nv_permlane32_swap_b32 v248, v250\nv_permlane32_swap_b32 v249, v251\ns_mov_b32 exec_hi, 0\n" \
    "buffer_store_dwordx4 v[248:251], v0, s[64:67], s88 offen sc1\nbuffer_store_dwordx4 v[248:251], v0, s[64:67], s89 offen sc1\ns_mov_b32 exec_hi, -1\ns_add_u32 s51, s51, s55\n" \
    "s_add_u32 s51, s51, s55\ns_mov_b32 s68, 0\ns_add_u32 s70, s50, 2\ns_min_i32 s70, s70, 63\ns_add_u32 s71, s50, -2\ns_mov_b32 s68, 0\ns_branch .Lr71_%=\n.Lr70_%=:\n" \
    "buffer_load_dword v17, v8, s[64:67], 0 offen sc1\ns_waitcnt vmcnt(0)\n.Lr71_%=:\nv_cmp_lt_i32 vcc, s70, v17\ns_nop 3\ns_and_b32 s69, vcc_lo, 0xff\ns_cmp_eq_u32 s69, 0xff\n" \
    "s_cbranch_scc0 .Lr72_%=\ns_mov_b32 s68, 0\ns_cmp_eq_u32 s86, 1\ns_cbranch_scc1 .Lr64_%=\ns_add_u32 s70, s50, 2\ns_min_i32 s70, s70, 63\ns_add_u32 s71, s50, -2\n" \
    "s_mov_b32 s68, 0\ns_branch .Lr74_%=\n.Lr73_%=:\nbuffer_load_dword v17, v8, s[64:67], 0 offen sc1\ns_waitcnt vmcnt(0)\n.Lr74_%=:\nv_readlane_b32 s69, v17, 9\n" \
    "s_cmp_gt_i32 s69, s71\ns_cbranch_scc0 .Lr75_%=\ns_mov_b32 s68, 0\n.Lr63_%=:\nbuffer_load_dwordx2 v[252:253], v11, s[60:63], s80 offen\n" \
    "buffer_load_dwordx4 v[72:75], v0, s[64:67], s75 offen offset:0 sc1\nbuffer_load_dwordx4 v[76:79], v0, s[64:67], s75 offen offset:1024 sc1\n" \
    "buffer_load_dwordx4 v[80:83], v0, s[64:67], s75 offen offset:2048 sc1\nbuffer_load_dwordx4 v[84:87], v0, s[64:67], s75 offen offset:3072 sc1\n" \
    "buffer_load_dwordx4 v[88:91], v1, s[64:67], s75 offen offset:0 sc1\nbuffer_load_dwordx4 v[92:95], v1, s[64:67], s75 offen offset:1024 sc1\n" \
    "buffer_load_dwordx4 v[96:99], v1, s[64:67], s75 offen offset:2048 sc1\nbuffer_load_dwordx4 v[100:103], v1, s[64:67], s75 offen offset:3072 sc1\n" \
    "buffer_load_dwordx4 v[104:107], v0, s[64:67], s76 offen offset:0 sc1\nbuffer_load_dwordx4 v[108:111], v0, s[64:67], s76 offen offset:1024 sc1\n" \
    "buffer_load_dwordx4 v[112:115], v0, s[64:67], s76 offen offset:2048 sc1\n.Lr64_%=:\nv_mov_b32 v22, 0\ns_waitcnt vmcnt(10)\n" \
    "v_mfma_f32_16x16x32_bf16 v[24:27], v[120:123], v[72:75], 0\nbuffer_load_dwordx4 v[116:119], v0, s[64:67], s76 offen offset:3072 sc1\n" \
    "v_mfma_f32_16x16x32_bf16 v[28:31], v[152:155], v[72:75], 0\nv_max3_u32 v22, v22, v72, v73\nv_max3_u32 v22, v22, v74, v75\ns_waitcnt vmcnt(10)\n" \
    "v_mfma_f32_16x16x32_bf16 v[32:35], v[120:123], v[76:79], 0\nbuffer_load_dwordx4 v[72:75], v1, s[64:67], s76 offen offset:0 sc1\n" \
    "v_mfma_f32_16x16x32_bf16 v[36:39], v[152:155], v[76:79], 0\nv_max3_u32 v22, v22, v76, v77\nv_max3_u32 v22, v22, v78, v79\ns_waitcnt vmcnt(10)\n" \
    "v_mfma_f32_16x16x32_bf16 v[24:27], v[124:127], v[80:83], v[24:27]\nbuffer_load_dwordx4 v[76:79], v1, s[64:67], s76 offen offset:1024 sc1\n" \
    "v_mfma_f32_16x16x32_bf16 v[28:31], v[156:159], v[80:83], v[28:31]\nv_max3_u32 v22, v22, v80, v81\nv_max3_u32 v22, v22, v82, v83\ns_waitcnt vmcnt(10)\n" \
    "v_mfma_f32_16x16x32_bf16 v[32:35], v[124:127], v[84:87], v[32:35]\nbuffer_load_dwordx4 v[80:83], v1, s[64:67], s76 offen offset:2048 sc1\n" \
    "v_mfma_f32_16x16x32_bf16 v[36:39], v[156:159], v[84:87], v[36:39]\nv_max3_u32 v22, v22, v84, v85\nv_max3_u32 v22, v22, v86, v87\ns_waitcnt vmcnt(10)\n" \
    "v_mfma_f32_16x16x32_bf16 v[24:27], v[128:131], v[88:91], v[24:27]\nbuffer_load_dwordx4 v[84:87], v1, s[64:67], s76 offen offset:3072 sc1\n" \
    "v_mfma_f32_16x16x32_bf16 v[28:31], v[160:163], v[88:91], v[28:31]\nv_max3_u32 v22, v22, v88, v89\nv_max3_u32 v22, v22, v90, v91\ns_waitcnt vmcnt(10)\n" \
    "v_mfma_f32_16x16x32_bf16 v[32:35], v[128:131], v[92:95], v[32:35]\nbuffer_load_dwordx4 v[88:91], v0, s[64:67], s77 offen offset:0 sc1\n" \
    "v_mfma_f32_16x16x32_bf16 v[36:39], v[160:163], v[92:95], v[36:39]\nv_max3_u32 v22, v22, v92, v93\nv_max3_u32 v22, v22, v94, v95\ns_waitcnt vmcnt(10)\n" \
    "v_mfma_f32_16x16x32_bf16 v[24:27], v[132:135], v[96:99], v[24:27]\nbuffer_load_dwordx4 v[92:95], v0, s[64:67], s77 offen offset:1024 sc1\n" \
    "v_mfma_f32_16x16x32_bf16 v[28:31], v[164:167], v[96:99], v[28:31]\nv_max3_u32 v22, v22, v96, v97\nv_max3_u32 v22, v22, v98, v99\ns_waitcnt vmcnt(10)\n" \
    "v_mfma_f32_16x16x32_bf16 v[32:35], v[132:135], v[100:103], v[32:35]\nbuffer_load_dwordx4 v[96:99], v0, s[64:67], s77 offen offset:2048 sc1\n" \
    "v_mfma_f32_16x16x32_bf16 v[36:39], v[164:167], v[100:103], v[36:39]\nv_max3_u32 v22, v22, v100, v101\nv_max3_u32 v22, v22, v102, v103\ns_waitcnt vmcnt(10)\n" \
    "v_mfma_f32_16x16x32_bf16 v[24:27], v[136:139], v[104:107], v[24:27]\nbuffer_load_dwordx4 v[100:103], v0, s[64:67], s77 offen offset:3072 sc1\n" \
    "v_mfma_f32_16x16x32_bf16 v[28:31], v[168:171], v[104:107], v[28:31]\nv_max3_u32 v22, v22, v104, v105\nv_max3_u32 v22, v22, v106, v107\ns_waitcnt vmcnt(10)\n" \
    "v_mfma_f32_16x16x32_bf16 v[32:35], v[136:139], v[108:111], v[32:35]\ns_add_u32 s85, s84, 0\nbuffer_load_dwordx4 v[104:107], v0, s[64:67], s85 offen offset:0 sc1\n" \
    "v_mfma_f32_16x16x32_bf16 v[36:39], v[168:171], v[108:111], v[36:39]\nv_max3_u32 v22, v22, v108, v109\nv_max3_u32 v22, v22, v110, v111\ns_waitcnt vmcnt(10)\n" \
    "v_mfma_f32_16x16x32_bf16 v[24:27], v[140:143], v[112:115], v[24:27]\nbuffer_load_dwordx4 v[108:111], v0, s[64:67], s85 offen offset:1024 sc1\n" \
    "v_mfma_f32_16x16x32_bf16 v[28:31], v[172:175], v[112:115], v[28:31]\nv_max3_u32 v22, v22, v112, v113\nv_max3_u32 v22, v22, v114, v115\ns_waitcnt vmcnt(10)\n" \
    "v_mfma_f32_16x16x32_bf16 v[32:35], v[140:143], v[116:119], v[32:35]\nbuffer_load_dwordx4 v[112:115], v0, s[64:67], s85 offen offset:2048 sc1\n" \
    "v_mfma_f32_16x16x32_bf16 v[36:39], v[172:175], v[116:119], v[36:39]\nv_max3_u32 v22, v22, v116, v117\nv_max3_u32 v22, v22, v118, v119\ns_waitcnt vmcnt(10)\n" \
    "v_mfma_f32_16x16x32_bf16 v[24:27], v[144:147], v[72:75], v[24:27]\nbuffer_load_dwordx4 v[116:119], v0, s[64:67], s85 offen offset:3072 sc1\n" \
    "v_mfma_f32_16x16x32_bf16 v[28:31], v[176:179], v[72:75], v[28:31]\nv_max3_u32 v22, v22, v72, v73\nv_max3_u32 v22, v22, v74, v75\ns_waitcnt vmcnt(10)\n" \
    "v_mfma_f32_16x16x32_bf16 v[32:35], v[144:147], v[76:79], v[32:35]\nbuffer_load_dwordx4 v[72:75], v1, s[64:67], s85 offen offset:0 sc1\n" \
    "v_mfma_f32_16x16x32_bf16 v[36:39], v[176:179], v[76:79], v[36:39]\nv_max3_u32 v22, v22, v76, v77\nv_max3_u32 v22, v22, v78, v79\ns_waitcnt vmcnt(10)\n" \
    "v_mfma_f32_16x16x32_bf16 v[24:27], v[148:151], v[80:83], v[24:27]\nbuffer_load_dwordx4 v[76:79], v1, s[64:67], s85 offen offset:1024 sc1\n" \
    "v_mfma_f32_16x16x32_bf16 v[28:31], v[180:183], v[80:83], v[28:31]\nv_max3_u32 v22, v22, v80, v81\nv_max3_u32 v22, v22, v82, v83\ns_waitcnt vmcnt(10)\n" \
    "v_mfma_f32_16x16x32_bf16 v[32:35], v[148:151], v[84:87], v[32:35]\nbuffer_load_dwordx4 v[80:83], v1, s[64:67], s85 offen offset:2048 sc1\n" \
    "v_mfma_f32_16x16x32_bf16 v[36:39], v[180:183], v[84:87], v[36:39]\nv_max3_u32 v22, v22, v84, v85\nv_max3_u32 v22, v22, v86, v87\ns_waitcnt vmcnt(10)\n" \
    "v_mfma_f32_16x16x32_bf16 v[24:27], v[40:43], v[88:91], v[24:27]\nbuffer_load_dwordx4 v[84:87], v1, s[64:67], s85 offen offset:3072 sc1\n" \
    "v_mfma_f32_16x16x32_bf16 v[28:31], v[48:51], v[88:91], v[28:31]\nv_max3_u32 v22, v22, v88, v89\nv_max3_u32 v22, v22, v90, v91\ns_waitcnt vmcnt(10)\n" \
    "v_mfma_f32_16x16x32_bf16 v[32:35], v[40:43], v[92:95], v[32:35]\ns_add_u32 s85, s84, 16384\nbuffer_load_dwordx4 v[88:91], v0, s[64:67], s85 offen offset:0 sc1\n" \
    "v_mfma_f32_16x16x32_bf16 v[36:39], v[48:51], v[92:95], v[36:39]\nv_max3_u32 v22, v22, v92, v93\nv_max3_u32 v22, v22, v94, v95\ns_waitcnt vmcnt(10)\n" \
    "v_mfma_f32_16x16x32_bf16 v[24:27], v[44:47], v[96:99], v[24:27]\nbuffer_load_dwordx4 v[92:95], v0, s[64:67], s85 offen offset:1024 sc1\n" \
    "v_mfma_f32_16x16x32_bf16 v[28:31], v[52:55], v[96:99], v[28:31]\nv_max3_u32 v22, v22, v96, v97\nv_max3_u32 v22, v22, v98, v99\ns_waitcnt vmcnt(10)\n" \
    "v_mfma_f32_16x16x32_bf16 v[32:35], v[44:47], v[100:103], v[32:35]\nbuffer_load_dwordx4 v[96:99], v0, s[64:67], s85 offen offset:2048 sc1\n" \
    "v_mfma_f32_16x16x32_bf16 v[36:39], v[52:55], v[100:103], v[36:39]\nv_max3_u32 v22, v22, v100, v101\nv_max3_u32 v22, v22, v102, v103\nv_cmp_eq_u32 vcc, -1, v22\n" \
    "s_cbranch_vccnz .Lr65_%=\nds_write_b128 v9, v[24:27] offset:0\nds_write_b128 v9, v[28:31] offset:64\ns_waitcnt vmcnt(10)\n" \
    "v_mfma_f32_16x16x32_bf16 v[24:27], v[104:107], a[0:3], 0\nbuffer_load_dwordx4 v[100:103], v0, s[64:67], s85 offen offset:3072 sc1\n" \
    "v_mfma_f32_16x16x32_bf16 v[28:31], v[104:107], a[16:19], 0\nds_write_b128 v9, v[32:35] offset:2304\nds_write_b128 v9, v[36:39] offset:2368\ns_waitcnt vmcnt(10)\n" \
    "v_mfma_f32_16x16x32_bf16 v[24:27], v[108:111], a[4:7], v[24:27]\nbuffer_load_dwordx4 v[104:107], v1, s[64:67], s85 offen offset:0 sc1\n" \
    "v_mfma_f32_16x16x32_bf16 v[28:31], v[108:111], a[20:23], v[28:31]\nds_read_b128 v[18:21], v16\ns_waitcnt vmcnt(10)\n" \
    "v_mfma_f32_16x16x32_bf16 v[24:27], v[112:115], a[8:11], v[24:27]\nbuffer_load_dwordx4 v[108:111], v1, s[64:67], s85 offen offset:1024 sc1\n" \
    "v_mfma_f32_16x16x32_bf16 v[28:31], v[112:115], a[24:27], v[28:31]\ns_waitcnt lgkmcnt(0)\ns_barrier\nds_read_b128 v[40:43], v10 offset:0\n" \
    "ds_read_b128 v[44:47], v10 offset:4608\ns_waitcnt vmcnt(10)\nv_mfma_f32_16x16x32_bf16 v[24:27], v[116:119], a[12:15], v[24:27]\n" \
    "buffer_load_dwordx4 v[112:115], v1, s[64:67], s85 offen offset:2048 sc1\nv_mfma_f32_16x16x32_bf16 v[28:31], v[116:119], a[28:31], v[28:31]\n" \
    "ds_read_b128 v[48:51], v10 offset:9216\nds_read_b128 v[52:55], v10 offset:13824\nv_lshlrev_b32 v248, 16, v252\nv_and_b32 v249, 0xffff0000, v252\n" \
    "v_lshlrev_b32 v250, 16, v253\ns_waitcnt vmcnt(10)\nv_mfma_f32_16x16x32_bf16 v[32:35], v[72:75], a[0:3], 0\n" \
    "buffer_load_dwordx4 v[116:119], v1, s[64:67], s85 offen offset:3072 sc1\nv_mfma_f32_16x16x32_bf16 v[36:39], v[72:75], a[16:19], 0\nv_and_b32 v251, 0xffff0000, v253\n" \
    "s_waitcnt vmcnt(10)\nv_mfma_f32_16x16x32_bf16 v[32:35], v[76:79], a[4:7], v[32:35]\ns_add_u32 s85, s84, 32768\n" \
    "buffer_load_dwordx4 v[72:75], v0, s[64:67], s85 offen offset:0 sc1\nv_mfma_f32_16x16x32_bf16 v[36:39], v[76:79], a[20:23], v[36:39]\nv_cvt_pk_bf16_f32 v120, v24, v25\n" \
    "v_cvt_pk_bf16_f32 v122, v26, v27\ns_waitcnt lgkmcnt(0)\nv_add_f32 v40, v40, v44\nv_add_f32 v41, v41, v45\nv_add_f32 v42, v42, v46\nv_add_f32 v43, v43, v47\n" \
    "s_waitcnt vmcnt(10)\nv_mfma_f32_16x16x32_bf16 v[32:35], v[80:83], a[8:11], v[32:35]\nbuffer_load_dwordx4 v[76:79], v0, s[64:67], s85 offen offset:1024 sc1\n" \
    "v_mfma_f32_16x16x32_bf16 v[36:39], v[80:83], a[24:27], v[36:39]\nv_cvt_pk_bf16_f32 v152, v28, v29\nv_cvt_pk_bf16_f32 v154, v30, v31\nv_add_f32 v48, v48, v52\n" \
    "v_add_f32 v49, v49, v53\nv_add_f32 v50, v50, v54\nv_add_f32 v51, v51, v55\nv_add_f32 v40, v40, v48\ns_waitcnt vmcnt(10)\n" \
    "v_mfma_f32_16x16x32_bf16 v[32:35], v[84:87], a[12:15], v[32:35]\nbuffer_load_dwordx4 v[80:83], v0, s[64:67], s85 offen offset:2048 sc1\n" \
    "v_mfma_f32_16x16x32_bf16 v[36:39], v[84:87], a[28:31], v[36:39]\nv_add_f32 v41, v41, v49\nv_add_f32 v42, v42, v50\nv_add_f32 v43, v43, v51\nv_fma_f32 v248, v40, v18, v248\n" \
    "v_fma_f32 v249, v41, v19, v249\nv_fma_f32 v250, v42, v20, v250\nv_fma_f32 v251, v43, v21, v251\ns_waitcnt vmcnt(10)\n" \
    "v_mfma_f32_16x16x32_bf16 v[24:27], v[88:91], a[32:35], 0\nbuffer_load_dwordx4 v[84:87], v0, s[64:67], s85 offen offset:3072 sc1\n" \
    "v_mfma_f32_16x16x32_bf16 v[28:31], v[88:91], a[48:51], 0\nv_cvt_pk_bf16_f32 v40, v248, v249\nv_cvt_pk_bf16_f32 v41, v250, v251\nv_lshlrev_b32 v248, 16, v40\n" \
    "v_and_b32 v249, 0xffff0000, v40\nv_lshlrev_b32 v250, 16, v41\nv_and_b32 v251, 0xffff0000, v41\nv_add_f32 v18, v248, v249\ns_waitcnt vmcnt(10)\n" \
    "v_mfma_f32_16x16x32_bf16 v[24:27], v[92:95], a[36:39], v[24:27]\nbuffer_load_dwordx4 v[88:91], v1, s[64:67], s85 offen offset:0 sc1\n" \
    "v_mfma_f32_16x16x32_bf16 v[28:31], v[92:95], a[52:55], v[28:31]\nv_cvt_pk_bf16_f32 v121, v32, v33\nv_cvt_pk_bf16_f32 v123, v34, v35\nv_add_f32 v19, v250, v251\n" \
    "v_min_u32 v40, -2, v40\nv_min_u32 v41, -2, v41\nv_add_f32 v18, v18, v19\nv_permlane32_swap_b32 v40, v42\ns_waitcnt vmcnt(10)\n" \
    "v_mfma_f32_16x16x32_bf16 v[24:27], v[96:99], a[40:43], v[24:27]\nbuffer_load_dwordx4 v[92:95], v1, s[64:67], s85 offen offset:1024 sc1\n" \
    "v_mfma_f32_16x16x32_bf16 v[28:31], v[96:99], a[56:59], v[28:31]\nv_cvt_pk_bf16_f32 v153, v36, v37\nv_cvt_pk_bf16_f32 v155, v38, v39\nv_permlane32_swap_b32 v41, v43\n" \
    "buffer_store_dwordx4 v[40:43], v12, s[64:67], s79 offen sc1\ns_waitcnt vmcnt(11)\nv_mfma_f32_16x16x32_bf16 v[24:27], v[100:103], a[44:47], v[24:27]\n" \
    "buffer_load_dwordx4 v[96:99], v1, s[64:67], s85 offen offset:2048 sc1\nv_mfma_f32_16x16x32_bf16 v[28:31], v[100:103], a[60:63], v[28:31]\n" \
    "v_add_f32_dpp v19, v18, v18 row_ror:8 row_mask:0xf bank_mask:0xf\nv_add_f32_dpp v20, v18, v18 row_ror:8 row_mask:0xf bank_mask:0xf\ns_nop 1\n" \
    "v_permlane16_swap_b32 v19, v20\nv_add_f32 v18, v19, v20\nv_add_f32 v19, v19, v20\ns_nop 1\nv_permlane32_swap_b32 v18, v19\ns_waitcnt vmcnt(11)\n" \
    "v_mfma_f32_16x16x32_bf16 v[32:35], v[104:107], a[32:35], 0\nbuffer_load_dwordx4 v[100:103], v1, s[64:67], s85 offen offset:3072 sc1\n" \
    "v_mfma_f32_16x16x32_bf16 v[36:39], v[104:107], a[48:51], 0\nv_add_f32 v18, v18, v19\nv_mul_f32 v18, 0x3d000000, v18\nv_sub_f32 v44, v248, v18\nv_sub_f32 v45, v249, v18\n" \
    "v_sub_f32 v46, v250, v18\nv_sub_f32 v47, v251, v18\nv_mul_f32 v19, v44, v44\ns_waitcnt vmcnt(11)\nv_mfma_f32_16x16x32_bf16 v[32:35], v[108:111], a[36:39], v[32:35]\n" \
    "s_add_u32 s85, s84, 49152\nbuffer_load_dwordx4 v[104:107], v0, s[64:67], s85 offen offset:0 sc1\nv_mfma_f32_16x16x32_bf16 v[36:39], v[108:111], a[52:55], v[36:39]\n" \
    "v_cvt_pk_bf16_f32 v124, v24, v25\nv_cvt_pk_bf16_f32 v126, v26, v27\nv_fmac_f32 v19, v45, v45\nv_fmac_f32 v19, v46, v46\nv_fmac_f32 v19, v47, v47\nv_min_u32 v18, -2, v18\n" \
    "s_nop 0\nv_add_f32_dpp v20, v19, v19 row_ror:8 row_mask:0xf bank_mask:0xf\ns_waitcnt vmcnt(11)\nv_mfma_f32_16x16x32_bf16 v[32:35], v[112:115], a[40:43], v[32:35]\n" \
    "buffer_load_dwordx4 v[108:111], v0, s[64:67], s85 offen offset:1024 sc1\nv_mfma_f32_16x16x32_bf16 v[36:39], v[112:115], a[56:59], v[36:39]\n" \
    "v_cvt_pk_bf16_f32 v156, v28, v29\nv_cvt_pk_bf16_f32 v158, v30, v31\nv_add_f32_dpp v21, v19, v19 row_ror:8 row_mask:0xf bank_mask:0xf\ns_nop 1\n" \
    "v_permlane16_swap_b32 v20, v21\nv_add_f32 v19, v20, v21\nv_add_f32 v20, v20, v21\ns_waitcnt vmcnt(11)\nv_mfma_f32_16x16x32_bf16 v[32:35], v[116:119], a[44:47], v[32:35]\n" \
    "buffer_load_dwordx4 v[112:115], v0, s[64:67], s85 offen offset:2048 sc1\nv_mfma_f32_16x16x32_bf16 v[36:39], v[116:119], a[60:63], v[36:39]\nv_permlane32_swap_b32 v19, v20\n" \
    "v_add_f32 v19, v19, v20\nv_min_u32 v19, -2, v19\nbuffer_store_dwordx2 v[18:19], v13, s[64:67], s79 offen sc1\ns_waitcnt vmcnt(12)\n" \
    "v_mfma_f32_16x16x32_bf16 v[24:27], v[72:75], a[64:67], 0\nbuffer_load_dwordx4 v[116:119], v0, s[64:67], s85 offen offset:3072 sc1\n" \
    "v_mfma_f32_16x16x32_bf16 v[28:31], v[72:75], a[80:83], 0\ns_add_u32 s69, s50, -1\nv_mov_b32 v22, s69\nbuffer_store_dword v22, v14, s[64:67], 0 offen sc1\n" \
    "s_add_u32 s69, s50, 2\ns_and_b32 s69, s69, s41\ns_waitcnt vmcnt(13)\nv_mfma_f32_16x16x32_bf16 v[24:27], v[76:79], a[68:71], v[24:27]\n" \
    "buffer_load_dwordx4 v[72:75], v1, s[64:67], s85 offen offset:0 sc1\nv_mfma_f32_16x16x32_bf16 v[28:31], v[76:79], a[84:87], v[28:31]\nv_cvt_pk_bf16_f32 v125, v32, v33\n" \
    "v_cvt_pk_bf16_f32 v127, v34, v35\ns_lshl_b32 s69, s69, 20\ns_add_u32 s69, s69, s32\ns_add_u32 s74, s48, s69\ns_lshr_b32 s74, s74, 8\ns_xor_b32 s74, s74, s49\n" \
    "s_waitcnt vmcnt(13)\nv_mfma_f32_16x16x32_bf16 v[24:27], v[80:83], a[72:75], v[24:27]\nbuffer_load_dwordx4 v[76:79], v1, s[64:67], s85 offen offset:1024 sc1\n" \
    "v_mfma_f32_16x16x32_bf16 v[28:31], v[80:83], a[88:91], v[28:31]\nv_cvt_pk_bf16_f32 v157, v36, v37\nv_cvt_pk_bf16_f32 v159, v38, v39\ns_and_b32 s74, s74, 0x2000\n" \
    "s_add_u32 s74, s74, s69\ns_add_u32 s69, s50, 2\ns_and_b32 s69, s69, s41\ns_lshl_b32 s69, s69, 19\ns_waitcnt vmcnt(13)\n" \
    "v_mfma_f32_16x16x32_bf16 v[24:27], v[84:87], a[76:79], v[24:27]\nbuffer_load_dwordx4 v[80:83], v1, s[64:67], s85 offen offset:2048 sc1\n" \
    "v_mfma_f32_16x16x32_bf16 v[28:31], v[84:87], a[92:95], v[28:31]\ns_add_u32 s69, s69, s33\ns_add_u32 s87, s48, s69\ns_lshr_b32 s87, s87, 8\ns_xor_b32 s87, s87, s49\n" \
    "s_and_b32 s87, s87, 0x2000\ns_add_u32 s87, s87, s69\ns_add_u32 s69, s50, 3\ns_waitcnt vmcnt(13)\nv_mfma_f32_16x16x32_bf16 v[32:35], v[88:91], a[64:67], 0\n" \
    "buffer_load_dwordx4 v[84:87], v1, s[64:67], s85 offen offset:3072 sc1\nv_mfma_f32_16x16x32_bf16 v[36:39], v[88:91], a[80:83], 0\ns_and_b32 s69, s69, s41\n" \
    "s_lshl_b32 s69, s69, 18\ns_add_u32 s69, s69, s34\ns_add_u32 s88, s48, s69\ns_lshr_b32 s88, s88, 8\ns_xor_b32 s88, s88, s49\ns_and_b32 s88, s88, 0x2000\n" \
    "s_waitcnt vmcnt(13)\nv_mfma_f32_16x16x32_bf16 v[32:35], v[92:95], a[68:71], v[32:35]\ns_add_u32 s85, s84, 65536\n" \
    "buffer_load_dwordx4 v[88:91], v0, s[64:67], s85 offen offset:0 sc1\nv_mfma_f32_16x16x32_bf16 v[36:39], v[92:95], a[84:87], v[36:39]\nv_cvt_pk_bf16_f32 v128, v24, v25\n" \
    "v_cvt_pk_bf16_f32 v130, v26, v27\ns_add_u32 s88, s88, s69\ns_xor_b32 s89, s88, 0x1000\ns_add_u32 s69, s50, 0\ns_lshl_b32 s69, s69, 20\ns_add_u32 s69, s69, s36\n" \
    "s_waitcnt vmcnt(12)\nv_mfma_f32_16x16x32_bf16 v[32:35], v[96:99], a[72:75], v[32:35]\nbuffer_load_dwordx4 v[92:95], v0, s[64:67], s85 offen offset:1024 sc1\n" \
    "v_mfma_f32_16x16x32_bf16 v[36:39], v[96:99], a[88:91], v[36:39]\nv_cvt_pk_bf16_f32 v160, v28, v29\nv_cvt_pk_bf16_f32 v162, v30, v31\ns_add_u32 s75, s48, s69\n" \
    "s_lshr_b32 s75, s75, 8\ns_xor_b32 s75, s75, s49\ns_and_b32 s75, s75, 0x2000\ns_add_u32 s75, s75, s69\ns_waitcnt vmcnt(12)\n" \
    "v_mfma_f32_16x16x32_bf16 v[32:35], v[100:103], a[76:79], v[32:35]\nbuffer_load_dwordx4 v[96:99], v0, s[64:67], s85 offen offset:2048 sc1\n" \
    "v_mfma_f32_16x16x32_bf16 v[36:39], v[100:103], a[92:95], v[36:39]\ns_add_u32 s76, s75, 0x4000\ns_add_u32 s77, s75, s37\ns_xor_b32 s79, s75, 0x2000\ns_add_u32 s82, s50, 0\n" \
    "s_lshl_b32 s80, s82, 16\ns_lshl_b32 s82, s82, 2\ns_add_u32 s82, s82, s40\ns_waitcnt vmcnt(12)\nv_mfma_f32_16x16x32_bf16 v[24:27], v[104:107], a[96:99], 0\n" \
    "buffer_load_dwordx4 v[100:103], v0, s[64:67], s85 offen offset:3072 sc1\nv_mfma_f32_16x16x32_bf16 v[28:31], v[104:107], a[112:115], 0\ns_waitcnt vmcnt(12)\n" \
    "v_mfma_f32_16x16x32_bf16 v[24:27], v[108:111], a[100:103], v[24:27]\nbuffer_load_dwordx4 v[104:107], v1, s[64:67], s85 offen offset:0 sc1\n" \
    "v_mfma_f32_16x16x32_bf16 v[28:31], v[108:111], a[116:119], v[28:31]\nv_cvt_pk_bf16_f32 v129, v32, v33\nv_cvt_pk_bf16_f32 v131, v34, v35\ns_waitcnt vmcnt(12)\n" \
    "v_mfma_f32_16x16x32_bf16 v[24:27], v[112:115], a[104:107], v[24:27]\nbuffer_load_dwordx4 v[108:111], v1, s[64:67], s85 offen offset:1024 sc1\n" \
    "v_mfma_f32_16x16x32_bf16 v[28:31], v[112:115], a[120:123], v[28:31]\nv_cvt_pk_bf16_f32 v161, v36, v37\nv_cvt_pk_bf16_f32 v163, v38, v39\ns_waitcnt vmcnt(11)\n" \
    "v_mfma_f32_16x16x32_bf16 v[24:27], v[116:119], a[108:111], v[24:27]\nbuffer_load_dwordx4 v[112:115], v1, s[64:67], s85 offen offset:2048 sc1\n" \
    "v_mfma_f32_16x16x32_bf16 v[28:31], v[116:119], a[124:127], v[28:31]\ns_waitcnt vmcnt(10)\nv_mfma_f32_16x16x32_bf16 v[32:35], v[72:75], a[96:99], 0\n" \
    "buffer_load_dwordx4 v[116:119], v1, s[64:67], s85 offen offset:3072 sc1\nv_mfma_f32_16x16x32_bf16 v[36:39], v[72:75], a[112:115], 0\ns_waitcnt vmcnt(10)\n" \
    "v_mfma_f32_16x16x32_bf16 v[32:35], v[76:79], a[100:103], v[32:35]\ns_add_u32 s85, s84, 81920\nbuffer_load_dwordx4 v[72:75], v0, s[64:67], s85 offen offset:0 sc1\n" \
    "v_mfma_f32_16x16x32_bf16 v[36:39], v[76:79], a[116:119], v[36:39]\nv_cvt_pk_bf16_f32 v132, v24, v25\nv_cvt_pk_bf16_f32 v134, v26, v27\ns_waitcnt vmcnt(10)\n" \
    "v_mfma_f32_16x16x32_bf16 v[32:35], v[80:83], a[104:107], v[32:35]\nbuffer_load_dwordx4 v[76:79], v0, s[64:67], s85 offen offset:1024 sc1\n" \
    "v_mfma_f32_16x16x32_bf16 v[36:39], v[80:83], a[120:123], v[36:39]\nv_cvt_pk_bf16_f32 v164, v28, v29\nv_cvt_pk_bf16_f32 v166, v30, v31\ns_waitcnt vmcnt(10)\n" \
    "v_mfma_f32_16x16x32_bf16 v[32:35], v[84:87], a[108:111], v[32:35]\nbuffer_load_dwordx4 v[80:83], v0, s[64:67], s85 offen offset:2048 sc1\n" \
    "v_mfma_f32_16x16x32_bf16 v[36:39], v[84:87], a[124:127], v[36:39]\ns_waitcnt vmcnt(10)\nv_mfma_f32_16x16x32_bf16 v[24:27], v[88:91], a[128:131], 0\n" \
    "buffer_load_dwordx4 v[84:87], v0, s[64:67], s85 offen offset:3072 sc1\nv_mfma_f32_16x16x32_bf16 v[28:31], v[88:91], a[144:147], 0\ns_waitcnt vmcnt(10)\n" \
    "v_mfma_f32_16x16x32_bf16 v[24:27], v[92:95], a[132:135], v[24:27]\nbuffer_load_dwordx4 v[88:91], v1, s[64:67], s85 offen offset:0 sc1\n" \
    "v_mfma_f32_16x16x32_bf16 v[28:31], v[92:95], a[148:151], v[28:31]\nv_cvt_pk_bf16_f32 v133, v32, v33\nv_cvt_pk_bf16_f32 v135, v34, v35\ns_waitcnt vmcnt(10)\n" \
    "v_mfma_f32_16x16x32_bf16 v[24:27], v[96:99], a[136:139], v[24:27]\nbuffer_load_dwordx4 v[92:95], v1, s[64:67], s85 offen offset:1024 sc1\n" \
    "v_mfma_f32_16x16x32_bf16 v[28:31], v[96:99], a[152:155], v[28:31]\nv_cvt_pk_bf16_f32 v165, v36, v37\nv_cvt_pk_bf16_f32 v167, v38, v39\ns_waitcnt vmcnt(10)\n" \
    "v_mfma_f32_16x16x32_bf16 v[24:27], v[100:103], a[140:143], v[24:27]\nbuffer_load_dwordx4 v[96:99], v1, s[64:67], s85 offen offset:2048 sc1\n" \
    "v_mfma_f32_16x16x32_bf16 v[28:31], v[100:103], a[156:159], v[28:31]\ns_waitcnt vmcnt(10)\nv_mfma_f32_16x16x32_bf16 v[32:35], v[104:107], a[128:131], 0\n" \
    "buffer_load_dwordx4 v[100:103], v1, s[64:67], s85 offen offset:3072 sc1\nv_mfma_f32_16x16x32_bf16 v[36:39], v[104:107], a[144:147], 0\ns_waitcnt vmcnt(10)\n" \
    "v_mfma_f32_16x16x32_bf16 v[32:35], v[108:111], a[132:135], v[32:35]\ns_add_u32 s85, s84, 98304\nbuffer_load_dwordx4 v[104:107], v0, s[64:67], s85 offen offset:0 sc1\n" \
    "v_mfma_f32_16x16x32_bf16 v[36:39], v[108:111], a[148:151], v[36:39]\nv_cvt_pk_bf16_f32 v136, v24, v25\nv_cvt_pk_bf16_f32 v138, v26, v27\ns_waitcnt vmcnt(10)\n" \
    "v_mfma_f32_16x16x32_bf16 v[32:35], v[112:115], a[136:139], v[32:35]\nbuffer_load_dwordx4 v[108:111], v0, s[64:67], s85 offen offset:1024 sc1\n" \
    "v_mfma_f32_16x16x32_bf16 v[36:39], v[112:115], a[152:155], v[36:39]\nv_cvt_pk_bf16_f32 v168, v28, v29\nv_cvt_pk_bf16_f32 v170, v30, v31\ns_waitcnt vmcnt(10)\n" \
    "v_mfma_f32_16x16x32_bf16 v[32:35], v[116:119], a[140:143], v[32:35]\nbuffer_load_dwordx4 v[112:115], v0, s[64:67], s85 offen offset:2048 sc1\n" \
    "v_mfma_f32_16x16x32_bf16 v[36:39], v[116:119], a[156:159], v[36:39]\ns_waitcnt vmcnt(10)\nv_mfma_f32_16x16x32_bf16 v[24:27], v[72:75], a[160:163], 0\n" \
    "buffer_load_dwordx4 v[116:119], v0, s[64:67], s85 offen offset:3072 sc1\nv_mfma_f32_16x16x32_bf16 v[28:31], v[72:75], a[176:179], 0\n" \
    "buffer_load_dword v17, v8, s[64:67], 0 offen sc1\ns_waitcnt vmcnt(11)\nv_mfma_f32_16x16x32_bf16 v[24:27], v[76:79], a[164:167], v[24:27]\n" \
    "buffer_load_dwordx4 v[72:75], v1, s[64:67], s85 offen offset:0 sc1\nv_mfma_f32_16x16x32_bf16 v[28:31], v[76:79], a[180:183], v[28:31]\nv_cvt_pk_bf16_f32 v137, v32, v33\n" \
    "v_cvt_pk_bf16_f32 v139, v34, v35\ns_waitcnt vmcnt(11)\nv_mfma_f32_16x16x32_bf16 v[24:27], v[80:83], a[168:171], v[24:27]\n" \
    "buffer_load_dwordx4 v[76:79], v1, s[64:67], s85 offen offset:1024 sc1\nv_mfma_f32_16x16x32_bf16 v[28:31], v[80:83], a[184:187], v[28:31]\nv_cvt_pk_bf16_f32 v169, v36, v37\n" \
    "v_cvt_pk_bf16_f32 v171, v38, v39\ns_waitcnt vmcnt(11)\nv_mfma_f32_16x16x32_bf16 v[24:27], v[84:87], a[172:175], v[24:27]\n" \
    "buffer_load_dwordx4 v[80:83], v1, s[64:67], s85 offen offset:2048 sc1\nv_mfma_f32_16x16x32_bf16 v[28:31], v[84:87], a[188:191], v[28:31]\ns_waitcnt vmcnt(11)\n" \
    "v_mfma_f32_16x16x32_bf16 v[32:35], v[88:91], a[160:163], 0\nbuffer_load_dwordx4 v[84:87], v1, s[64:67], s85 offen offset:3072 sc1\n" \
    "v_mfma_f32_16x16x32_bf16 v[36:39], v[88:91], a[176:179], 0\ns_waitcnt vmcnt(11)\nv_mfma_f32_16x16x32_bf16 v[32:35], v[92:95], a[164:167], v[32:35]\n" \
    "s_add_u32 s85, s84, 114688\nbuffer_load_dwordx4 v[88:91], v0, s[64:67], s85 offen offset:0 sc1\nv_mfma_f32_16x16x32_bf16 v[36:39], v[92:95], a[180:183], v[36:39]\n" \
    "v_cvt_pk_bf16_f32 v140, v24, v25\nv_cvt_pk_bf16_f32 v142, v26, v27\ns_waitcnt vmcnt(11)\nv_mfma_f32_16x16x32_bf16 v[32:35], v[96:99], a[168:171], v[32:35]\n" \
    "buffer_load_dwordx4 v[92:95], v0, s[64:67], s85 offen offset:1024 sc1\nv_mfma_f32_16x16x32_bf16 v[36:39], v[96:99], a[184:187], v[36:39]\nv_cvt_pk_bf16_f32 v172, v28, v29\n" \
    "v_cvt_pk_bf16_f32 v174, v30, v31\ns_add_u32 s69, s50, 4\ns_and_b32 s69, s69, s41\ns_lshl_b32 s69, s69, 19\ns_add_u32 s69, s69, s35\ns_add_u32 s84, s48, s69\n" \
    "s_waitcnt vmcnt(11)\nv_mfma_f32_16x16x32_bf16 v[32:35], v[100:103], a[172:175], v[32:35]\nbuffer_load_dwordx4 v[96:99], v0, s[64:67], s85 offen offset:2048 sc1\n" \
    "v_mfma_f32_16x16x32_bf16 v[36:39], v[100:103], a[188:191], v[36:39]\ns_lshr_b32 s84, s84, 8\ns_xor_b32 s84, s84, s49\ns_and_b32 s84, s84, 0x2000\ns_add_u32 s84, s84, s69\n" \
    "s_waitcnt vmcnt(11)\nv_mfma_f32_16x16x32_bf16 v[24:27], v[104:107], a[192:195], 0\nbuffer_load_dwordx4 v[100:103], v0, s[64:67], s85 offen offset:3072 sc1\n" \
    "v_mfma_f32_16x16x32_bf16 v[28:31], v[104:107], a[208:211], 0\ns_waitcnt vmcnt(11)\nv_mfma_f32_16x16x32_bf16 v[24:27], v[108:111], a[196:199], v[24:27]\n" \
    "buffer_load_dwordx4 v[104:107], v1, s[64:67], s85 offen offset:0 sc1\nv_mfma_f32_16x16x32_bf16 v[28:31], v[108:111], a[212:215], v[28:31]\n" \
    "v_cvt_pk_bf16_f32 v141, v32, v33\nv_cvt_pk_bf16_f32 v143, v34, v35\ns_waitcnt vmcnt(11)\nv_mfma_f32_16x16x32_bf16 v[24:27], v[112:115], a[200:203], v[24:27]\n" \
    "buffer_load_dwordx4 v[108:111], v1, s[64:67], s85 offen offset:1024 sc1\nv_mfma_f32_16x16x32_bf16 v[28:31], v[112:115], a[216:219], v[28:31]\n" \
    "v_cvt_pk_bf16_f32 v173, v36, v37\nv_cvt_pk_bf16_f32 v175, v38, v39\ns_waitcnt vmcnt(11)\nv_mfma_f32_16x16x32_bf16 v[24:27], v[116:119], a[204:207], v[24:27]\n" \
    "buffer_load_dwordx4 v[112:115], v1, s[64:67], s85 offen offset:2048 sc1\nv_mfma_f32_16x16x32_bf16 v[28:31], v[116:119], a[220:223], v[28:31]\ns_waitcnt vmcnt(10)\n" \
    "v_mfma_f32_16x16x32_bf16 v[32:35], v[72:75], a[192:195], 0\nbuffer_load_dwordx4 v[116:119], v1, s[64:67], s85 offen offset:3072 sc1\n" \
    "v_mfma_f32_16x16x32_bf16 v[36:39], v[72:75], a[208:211], 0\ns_mov_b32 s86, 0\nv_mov_b32 v18, 0x7f000000\ns_add_u32 s70, s50, 4\ns_min_i32 s70, s70, 63\n" \
    "s_add_u32 s71, s50, 0\nv_readlane_b32 s69, v17, 10\ns_cmp_ge_i32 s69, s71\ns_cbranch_scc0 .Lr76_%=\ns_mov_b32 s86, 1\nv_mov_b32 v18, v0\n.Lr76_%=:\n" \
    "v_add_u32 v19, 0x1000, v18\ns_waitcnt vmcnt(10)\nv_mfma_f32_16x16x32_bf16 v[32:35], v[76:79], a[196:199], v[32:35]\n" \
    "v_mfma_f32_16x16x32_bf16 v[36:39], v[76:79], a[212:215], v[36:39]\nv_cvt_pk_bf16_f32 v144, v24, v25\nv_cvt_pk_bf16_f32 v146, v26, v27\n" \
    "buffer_load_dwordx4 v[72:75], v18, s[64:67], s75 offen offset:0 sc1\ns_waitcnt vmcnt(10)\nv_mfma_f32_16x16x32_bf16 v[32:35], v[80:83], a[200:203], v[32:35]\n" \
    "v_mfma_f32_16x16x32_bf16 v[36:39], v[80:83], a[216:219], v[36:39]\nv_cvt_pk_bf16_f32 v176, v28, v29\nv_cvt_pk_bf16_f32 v178, v30, v31\n" \
    "buffer_load_dwordx4 v[76:79], v18, s[64:67], s75 offen offset:1024 sc1\ns_waitcnt vmcnt(10)\nv_mfma_f32_16x16x32_bf16 v[32:35], v[84:87], a[204:207], v[32:35]\n" \
    "v_mfma_f32_16x16x32_bf16 v[36:39], v[84:87], a[220:223], v[36:39]\nbuffer_load_dwordx4 v[80:83], v18, s[64:67], s75 offen offset:2048 sc1\ns_waitcnt vmcnt(10)\n" \
    "v_mfma_f32_16x16x32_bf16 v[24:27], v[88:91], a[224:227], 0\nv_mfma_f32_16x16x32_bf16 v[28:31], v[88:91], a[240:243], 0\n" \
    "buffer_load_dwordx4 v[84:87], v18, s[64:67], s75 offen offset:3072 sc1\ns_waitcnt vmcnt(10)\nv_mfma_f32_16x16x32_bf16 v[24:27], v[92:95], a[228:231], v[24:27]\n" \
    "v_mfma_f32_16x16x32_bf16 v[28:31], v[92:95], a[244:247], v[28:31]\nv_cvt_pk_bf16_f32 v145, v32, v33\nv_cvt_pk_bf16_f32 v147, v34, v35\n" \
    "buffer_load_dwordx4 v[88:91], v19, s[64:67], s75 offen offset:0 sc1\ns_waitcnt vmcnt(10)\nv_mfma_f32_16x16x32_bf16 v[24:27], v[96:99], a[232:235], v[24:27]\n" \
    "v_mfma_f32_16x16x32_bf16 v[28:31], v[96:99], a[248:251], v[28:31]\nv_cvt_pk_bf16_f32 v177, v36, v37\nv_cvt_pk_bf16_f32 v179, v38, v39\n" \
    "buffer_load_dwordx4 v[92:95], v19, s[64:67], s75 offen offset:1024 sc1\ns_waitcnt vmcnt(10)\nv_mfma_f32_16x16x32_bf16 v[24:27], v[100:103], a[236:239], v[24:27]\n" \
    "v_mfma_f32_16x16x32_bf16 v[28:31], v[100:103], a[252:255], v[28:31]\nbuffer_load_dwordx4 v[96:99], v19, s[64:67], s75 offen offset:2048 sc1\n" \
    "buffer_load_dwordx2 v[252:253], v11, s[60:63], s80 offen\ns_waitcnt vmcnt(11)\nv_mfma_f32_16x16x32_bf16 v[32:35], v[104:107], a[224:227], 0\n" \
    "v_mfma_f32_16x16x32_bf16 v[36:39], v[104:107], a[240:243], 0\ns_waitcnt vmcnt(10)\nv_mfma_f32_16x16x32_bf16 v[32:35], v[108:111], a[228:231], v[32:35]\n" \
    "v_mfma_f32_16x16x32_bf16 v[36:39], v[108:111], a[244:247], v[36:39]\nv_cvt_pk_bf16_f32 v148, v24, v25\nv_cvt_pk_bf16_f32 v150, v26, v27\ns_waitcnt vmcnt(9)\n" \
    "v_mfma_f32_16x16x32_bf16 v[32:35], v[112:115], a[232:235], v[32:35]\nv_mfma_f32_16x16x32_bf16 v[36:39], v[112:115], a[248:251], v[36:39]\nv_cvt_pk_bf16_f32 v180, v28, v29\n" \
    "v_cvt_pk_bf16_f32 v182, v30, v31\ns_waitcnt vmcnt(8)\nv_mfma_f32_16x16x32_bf16 v[32:35], v[116:119], a[236:239], v[32:35]\n" \
    "v_mfma_f32_16x16x32_bf16 v[36:39], v[116:119], a[252:255], v[36:39]\ns_nop 6\nv_cvt_pk_bf16_f32 v149, v32, v33\nv_cvt_pk_bf16_f32 v151, v34, v35\n" \
    "v_cvt_pk_bf16_f32 v181, v36, v37\nv_cvt_pk_bf16_f32 v183, v38, v39\ns_add_u32 s50, s50, 2\ns_cmp_gt_i32 s50, s42\ns_cselect_b32 s69, s45, 0\ns_sub_u32 s51, s51, s69\n" \
    "s_mov_b32 s68, 0\ns_add_u32 s78, s87, s38\n.Lr79_%=:\ns_sub_u32 s70, s51, 52\n.Lr88_%=:\ns_sleep 1\ns_memrealtime s[72:73]\ns_waitcnt lgkmcnt(0)\ns_sub_u32 s72, s72, s70\n" \
    "s_cmp_lt_i32 s72, 0\ns_cbranch_scc1 .Lr88_%=\nbuffer_load_dword v20, off, s[64:67], s78 sc1\nbuffer_load_dword v17, v8, s[64:67], 0 offen sc1\ns_waitcnt vmcnt(1)\n" \
    "v_readfirstlane_b32 s70, v20\ns_memrealtime s[72:73]\ns_cmp_eq_u32 s70, -1\ns_cbranch_scc1 .Lr80_%=\ns_lshr_b32 s71, s70, 8\ns_waitcnt lgkmcnt(0)\ns_sub_u32 s71, s72, s71\n" \
    "s_and_b32 s71, s71, 0xffffff\ns_sub_u32 s51, s72, s71\ns_add_u32 s51, s51, 176\ns_and_b32 s70, s70, 128\ns_lshl_b32 s52, s70, 5\ns_xor_b32 s87, s87, s52\ns_mov_b32 s68, 0\n" \
    "s_sub_u32 s70, s51, s54\n.Lr89_%=:\ns_memrealtime s[72:73]\ns_waitcnt lgkmcnt(0)\ns_sub_u32 s72, s72, s70\ns_cmp_lt_i32 s72, 0\ns_cbranch_scc1 .Lr89_%=\n" \
    "buffer_load_dwordx4 v[40:43], v0, s[64:67], s74 offen offset:0 sc1\nbuffer_load_dwordx4 v[44:47], v0, s[64:67], s74 offen offset:1024 sc1\ns_add_u32 s69, s74, 0x4000\n" \
    "buffer_load_dwordx4 v[48:51], v0, s[64:67], s69 offen offset:0 sc1\nbuffer_load_dwordx4 v[52:55], v0, s[64:67], s69 offen offset:1024 sc1\n.Lr90_%=:\n" \
    "s_memrealtime s[72:73]\ns_waitcnt lgkmcnt(0)\ns_sub_u32 s72, s72, s51\ns_cmp_lt_i32 s72, -3\ns_cbranch_scc1 .Lr90_%=\ns_cmp_ge_i32 s72, 0\ns_cbranch_scc1 .Lr91_%=\n" \
    "s_nop 2\ns_cmp_ge_i32 s72, -1\ns_cbranch_scc1 .Lr91_%=\ns_nop 2\ns_cmp_ge_i32 s72, -2\ns_cbranch_scc1 .Lr91_%=\ns_nop 2\n.Lr91_%=:\n" \
    "buffer_load_dwordx2 v[116:117], v5, s[64:67], s87 offen offset:0 sc1\ns_nop 5\nbuffer_load_dwordx2 v[118:119], v5, s[64:67], s87 offen offset:128 sc1\ns_nop 5\n" \
    "buffer_load_dwordx4 v[100:103], v0, s[64:67], s87 offen offset:0 sc1\ns_nop 10\nbuffer_load_dwordx4 v[104:107], v0, s[64:67], s87 offen offset:1024 sc1\ns_nop 10\n" \
    "buffer_load_dwordx4 v[108:111], v0, s[64:67], s87 offen offset:2048 sc1\ns_nop 10\nbuffer_load_dwordx4 v[112:115], v0, s[64:67], s87 offen offset:3072 sc1\n.Lr83_%=:\n" \
    "v_mov_b64 v[24:25], 0\nv_mov_b64 v[26:27], 0\nv_mov_b64 v[28:29], 0\nv_mov_b64 v[30:31], 0\ns_waitcnt vmcnt(6)\nv_max3_u32 v22, v40, v41, v42\n" \
    "v_max3_u32 v22, v22, v43, v44\nv_max3_u32 v22, v22, v45, v46\nv_max3_u32 v22, v22, v47, v48\nv_max3_u32 v22, v22, v49, v50\nv_max3_u32 v22, v22, v51, v52\n" \
    "v_max3_u32 v22, v22, v53, v54\nv_max_u32 v22, v22, v55\ns_waitcnt vmcnt(4)\nv_smfmac_f32_16x16x64_bf16 v[24:27], v[116:119], v[40:47], v4\nv_max3_u32 v22, v22, v116, v117\n" \
    "v_smfmac_f32_16x16x64_bf16 v[28:31], v[116:119], v[48:55], v4\nv_max3_u32 v22, v22, v118, v119\ns_waitcnt vmcnt(3)\n" \
    "v_smfmac_f32_16x16x64_bf16 v[24:27], v[100:103], v[120:127], v4\nv_max3_u32 v22, v22, v100, v101\nv_smfmac_f32_16x16x64_bf16 v[28:31], v[100:103], v[152:159], v4\n" \
    "v_max3_u32 v22, v22, v102, v103\nbuffer_load_dwordx4 v[100:103], v19, s[64:67], s75 offen offset:3072 sc1\ns_waitcnt vmcnt(3)\n" \
    "v_smfmac_f32_16x16x64_bf16 v[24:27], v[104:107], v[128:135], v4\nv_max3_u32 v22, v22, v104, v105\nv_smfmac_f32_16x16x64_bf16 v[28:31], v[104:107], v[160:167], v4\n" \
    "v_max3_u32 v22, v22, v106, v107\nbuffer_load_dwordx4 v[104:107], v18, s[64:67], s76 offen offset:0 sc1\ns_waitcnt vmcnt(3)\n" \
    "v_smfmac_f32_16x16x64_bf16 v[24:27], v[108:111], v[136:143], v4\nv_max3_u32 v22, v22, v108, v109\nv_smfmac_f32_16x16x64_bf16 v[28:31], v[108:111], v[168:175], v4\n" \
    "v_max3_u32 v22, v22, v110, v111\nbuffer_load_dwordx4 v[108:111], v18, s[64:67], s76 offen offset:1024 sc1\ns_waitcnt vmcnt(3)\n" \
    "v_smfmac_f32_16x16x64_bf16 v[24:27], v[112:115], v[144:151], v4\nv_max3_u32 v22, v22, v112, v113\nv_smfmac_f32_16x16x64_bf16 v[28:31], v[112:115], v[176:183], v4\n" \
    "v_max3_u32 v22, v22, v114, v115\nv_cmp_eq_u32 vcc, -1, v22\ns_nop 5\nv_permlane32_swap_b32 v24, v28\nv_permlane32_swap_b32 v25, v29\nv_permlane32_swap_b32 v26, v30\n" \
    "v_permlane32_swap_b32 v27, v31\nv_pk_add_f32 v[24:25], v[24:25], v[28:29]\nv_pk_add_f32 v[26:27], v[26:27], v[30:31]\nds_write2_b32 v6, v24, v25 offset0:0 offset1:36\n" \
    "ds_write2_b32 v6, v26, v27 offset0:72 offset1:108\ns_cbranch_vccnz .Lr84_%=\nbuffer_load_dwordx4 v[112:115], v18, s[64:67], s76 offen offset:2048 sc1\n" \
    "s_waitcnt lgkmcnt(0)\ns_barrier\nds_read_b128 v[24:27], v7 offset:0\nds_read_b128 v[28:31], v7 offset:1152\nds_read_b128 v[32:35], v7 offset:2304\n" \
    "ds_read_b128 v[36:39], v7 offset:3456\ns_waitcnt lgkmcnt(2)\nv_pk_add_f32 v[24:25], v[24:25], v[28:29]\nv_pk_add_f32 v[26:27], v[26:27], v[30:31]\ns_waitcnt lgkmcnt(0)\n" \
    "v_pk_add_f32 v[32:33], v[32:33], v[36:37]\nv_pk_add_f32 v[34:35], v[34:35], v[38:39]\nv_pk_add_f32 v[24:25], v[24:25], v[32:33]\nv_pk_add_f32 v[26:27], v[26:27], v[34:35]\n" \
    "v_cvt_pk_bf16_f32 v248, v24, v25\nv_cvt_pk_bf16_f32 v249, v26, v27\ns_nop 0\nv_permlane32_swap_b32 v248, v250\nv_permlane32_swap_b32 v249, v251\ns_mov_b32 exec_hi, 0\n" \
    "buffer_store_dwordx4 v[248:251], v0, s[64:67], s88 offen sc1\nbuffer_store_dwordx4 v[248:251], v0, s[64:67], s89 offen sc1\ns_mov_b32 exec_hi, -1\ns_add_u32 s51, s51, s55\n" \
    "s_add_u32 s51, s51, s55\ns_mov_b32 s68, 0\ns_add_u32 s70, s50, 2\ns_min_i32 s70, s70, 63\ns_add_u32 s71, s50, -2\ns_mov_b32 s68, 0\ns_branch .Lr103_%=\n.Lr102_%=:\n" \
    "buffer_load_dword v17, v8, s[64:67], 0 offen sc1\ns_waitcnt vmcnt(0)\n.Lr103_%=:\nv_cmp_lt_i32 vcc, s70, v17\ns_nop 3\ns_and_b32 s69, vcc_lo, 0xff\ns_cmp_eq_u32 s69, 0xff\n" \
    "s_cbranch_scc0 .Lr104_%=\ns_mov_b32 s68, 0\ns_cmp_eq_u32 s86, 1\ns_cbranch_scc1 .Lr96_%=\ns_add_u32 s70, s50, 2\ns_min_i32 s70, s70, 63\ns_add_u32 s71, s50, -2\n" \
    "s_mov_b32 s68, 0\ns_branch .Lr106_%=\n.Lr105_%=:\nbuffer_load_dword v17, v8, s[64:67], 0 offen sc1\ns_waitcnt vmcnt(0)\n.Lr106_%=:\nv_readlane_b32 s69, v17, 9\n" \
    "s_cmp_gt_i32 s69, s71\ns_cbranch_scc0 .Lr107_%=\ns_mov_b32 s68, 0\n.Lr95_%=:\nbuffer_load_dwordx2 v[252:253], v11, s[60:63], s80 offen\n" \
    "buffer_load_dwordx4 v[72:75], v0, s[64:67], s75 offen offset:0 sc1\nbuffer_load_dwordx4 v[76:79], v0, s[64:67], s75 offen offset:1024 sc1\n" \
    "buffer_load_dwordx4 v[80:83], v0, s[64:67], s75 offen offset:2048 sc1\nbuffer_load_dwordx4 v[84:87], v0, s[64:67], s75 offen offset:3072 sc1\n" \
    "buffer_load_dwordx4 v[88:91], v1, s[64:67], s75 offen offset:0 sc1\nbuffer_load_dwordx4 v[92:95], v1, s[64:67], s75 offen offset:1024 sc1\n" \
    "buffer_load_dwordx4 v[96:99], v1, s[64:67], s75 offen offset:2048 sc1\nbuffer_load_dwordx4 v[100:103], v1, s[64:67], s75 offen offset:3072 sc1\n" \
    "buffer_load_dwordx4 v[104:107], v0, s[64:67], s76 offen offset:0 sc1\nbuffer_load_dwordx4 v[108:111], v0, s[64:67], s76 offen offset:1024 sc1\n" \
    "buffer_load_dwordx4 v[112:115], v0, s[64:67], s76 offen offset:2048 sc1\n.Lr96_%=:\nv_mov_b32 v22, 0\ns_waitcnt vmcnt(10)\n" \
    "v_mfma_f32_16x16x32_bf16 v[24:27], v[184:187], v[72:75], 0\nbuffer_load_dwordx4 v[116:119], v0, s[64:67], s76 offen offset:3072 sc1\n" \
    "v_mfma_f32_16x16x32_bf16 v[28:31], v[216:219], v[72:75], 0\nv_max3_u32 v22, v22, v72, v73\nv_max3_u32 v22, v22, v74, v75\ns_waitcnt vmcnt(10)\n" \
    "v_mfma_f32_16x16x32_bf16 v[32:35], v[184:187], v[76:79], 0\nbuffer_load_dwordx4 v[72:75], v1, s[64:67], s76 offen offset:0 sc1\n" \
    "v_mfma_f32_16x16x32_bf16 v[36:39], v[216:219], v[76:79], 0\nv_max3_u32 v22, v22, v76, v77\nv_max3_u32 v22, v22, v78, v79\ns_waitcnt vmcnt(10)\n" \
    "v_mfma_f32_16x16x32_bf16 v[24:27], v[188:191], v[80:83], v[24:27]\nbuffer_load_dwordx4 v[76:79], v1, s[64:67], s76 offen offset:1024 sc1\n" \
    "v_mfma_f32_16x16x32_bf16 v[28:31], v[220:223], v[80:83], v[28:31]\nv_max3_u32 v22, v22, v80, v81\nv_max3_u32 v22, v22, v82, v83\ns_waitcnt vmcnt(10)\n" \
    "v_mfma_f32_16x16x32_bf16 v[32:35], v[188:191], v[84:87], v[32:35]\nbuffer_load_dwordx4 v[80:83], v1, s[64:67], s76 offen offset:2048 sc1\n" \
    "v_mfma_f32_16x16x32_bf16 v[36:39], v[220:223], v[84:87], v[36:39]\nv_max3_u32 v22, v22, v84, v85\nv_max3_u32 v22, v22, v86, v87\ns_waitcnt vmcnt(10)\n" \
    "v_mfma_f32_16x16x32_bf16 v[24:27], v[192:195], v[88:91], v[24:27]\nbuffer_load_dwordx4 v[84:87], v1, s[64:67], s76 offen offset:3072 sc1\n" \
    "v_mfma_f32_16x16x32_bf16 v[28:31], v[224:227], v[88:91], v[28:31]\nv_max3_u32 v22, v22, v88, v89\nv_max3_u32 v22, v22, v90, v91\ns_waitcnt vmcnt(10)\n" \
    "v_mfma_f32_16x16x32_bf16 v[32:35], v[192:195], v[92:95], v[32:35]\nbuffer_load_dwordx4 v[88:91], v0, s[64:67], s77 offen offset:0 sc1\n" \
    "v_mfma_f32_16x16x32_bf16 v[36:39], v[224:227], v[92:95], v[36:39]\nv_max3_u32 v22, v22, v92, v93\nv_max3_u32 v22, v22, v94, v95\ns_waitcnt vmcnt(10)\n" \
    "v_mfma_f32_16x16x32_bf16 v[24:27], v[196:199], v[96:99], v[24:27]\nbuffer_load_dwordx4 v[92:95], v0, s[64:67], s77 offen offset:1024 sc1\n" \
    "v_mfma_f32_16x16x32_bf16 v[28:31], v[228:231], v[96:99], v[28:31]\nv_max3_u32 v22, v22, v96, v97\nv_max3_u32 v22, v22, v98, v99\ns_waitcnt vmcnt(10)\n" \
    "v_mfma_f32_16x16x32_bf16 v[32:35], v[196:199], v[100:103], v[32:35]\nbuffer_load_dwordx4 v[96:99], v0, s[64:67], s77 offen offset:2048 sc1\n" \
    "v_mfma_f32_16x16x32_bf16 v[36:39], v[228:231], v[100:103], v[36:39]\nv_max3_u32 v22, v22, v100, v101\nv_max3_u32 v22, v22, v102, v103\ns_waitcnt vmcnt(10)\n" \
    "v_mfma_f32_16x16x32_bf16 v[24:27], v[200:203], v[104:107], v[24:27]\nbuffer_load_dwordx4 v[100:103], v0, s[64:67], s77 offen offset:3072 sc1\n" \
    "v_mfma_f32_16x16x32_bf16 v[28:31], v[232:235], v[104:107], v[28:31]\nv_max3_u32 v22, v22, v104, v105\nv_max3_u32 v22, v22, v106, v107\ns_waitcnt vmcnt(10)\n" \
    "v_mfma_f32_16x16x32_bf16 v[32:35], v[200:203], v[108:111], v[32:35]\ns_add_u32 s85, s84, 0\nbuffer_load_dwordx4 v[104:107], v0, s[64:67], s85 offen offset:0 sc1\n" \
    "v_mfma_f32_16x16x32_bf16 v[36:39], v[232:235], v[108:111], v[36:39]\nv_max3_u32 v22, v22, v108, v109\nv_max3_u32 v22, v22, v110, v111\ns_waitcnt vmcnt(10)\n" \
    "v_mfma_f32_16x16x32_bf16 v[24:27], v[204:207], v[112:115], v[24:27]\nbuffer_load_dwordx4 v[108:111], v0, s[64:67], s85 offen offset:1024 sc1\n" \
    "v_mfma_f32_16x16x32_bf16 v[28:31], v[236:239], v[112:115], v[28:31]\nv_max3_u32 v22, v22, v112, v113\nv_max3_u32 v22, v22, v114, v115\ns_waitcnt vmcnt(10)\n" \
    "v_mfma_f32_16x16x32_bf16 v[32:35], v[204:207], v[116:119], v[32:35]\nbuffer_load_dwordx4 v[112:115], v0, s[64:67], s85 offen offset:2048 sc1\n" \
    "v_mfma_f32_16x16x32_bf16 v[36:39], v[236:239], v[116:119], v[36:39]\nv_max3_u32 v22, v22, v116, v117\nv_max3_u32 v22, v22, v118, v119\ns_waitcnt vmcnt(10)\n" \
    "v_mfma_f32_16x16x32_bf16 v[24:27], v[208:211], v[72:75], v[24:27]\nbuffer_load_dwordx4 v[116:119], v0, s[64:67], s85 offen offset:3072 sc1\n" \
    "v_mfma_f32_16x16x32_bf16 v[28:31], v[240:243], v[72:75], v[28:31]\nv_max3_u32 v22, v22, v72, v73\nv_max3_u32 v22, v22, v74, v75\ns_waitcnt vmcnt(10)\n" \
    "v_mfma_f32_16x16x32_bf16 v[32:35], v[208:211], v[76:79], v[32:35]\nbuffer_load_dwordx4 v[72:75], v1, s[64:67], s85 offen offset:0 sc1\n" \
    "v_mfma_f32_16x16x32_bf16 v[36:39], v[240:243], v[76:79], v[36:39]\nv_max3_u32 v22, v22, v76, v77\nv_max3_u32 v22, v22, v78, v79\ns_waitcnt vmcnt(10)\n" \
    "v_mfma_f32_16x16x32_bf16 v[24:27], v[212:215], v[80:83], v[24:27]\nbuffer_load_dwordx4 v[76:79], v1, s[64:67], s85 offen offset:1024 sc1\n" \
    "v_mfma_f32_16x16x32_bf16 v[28:31], v[244:247], v[80:83], v[28:31]\nv_max3_u32 v22, v22, v80, v81\nv_max3_u32 v22, v22, v82, v83\ns_waitcnt vmcnt(10)\n" \
    "v_mfma_f32_16x16x32_bf16 v[32:35], v[212:215], v[84:87], v[32:35]\nbuffer_load_dwordx4 v[80:83], v1, s[64:67], s85 offen offset:2048 sc1\n" \
    "v_mfma_f32_16x16x32_bf16 v[36:39], v[244:247], v[84:87], v[36:39]\nv_max3_u32 v22, v22, v84, v85\nv_max3_u32 v22, v22, v86, v87\ns_waitcnt vmcnt(10)\n" \
    "v_mfma_f32_16x16x32_bf16 v[24:27], v[56:59], v[88:91], v[24:27]\nbuffer_load_dwordx4 v[84:87], v1, s[64:67], s85 offen offset:3072 sc1\n" \
    "v_mfma_f32_16x16x32_bf16 v[28:31], v[64:67], v[88:91], v[28:31]\nv_max3_u32 v22, v22, v88, v89\nv_max3_u32 v22, v22, v90, v91\ns_waitcnt vmcnt(10)\n" \
    "v_mfma_f32_16x16x32_bf16 v[32:35], v[56:59], v[92:95], v[32:35]\ns_add_u32 s85, s84, 16384\nbuffer_load_dwordx4 v[88:91], v0, s[64:67], s85 offen offset:0 sc1\n" \
    "v_mfma_f32_16x16x32_bf16 v[36:39], v[64:67], v[92:95], v[36:39]\nv_max3_u32 v22, v22, v92, v93\nv_max3_u32 v22, v22, v94, v95\ns_waitcnt vmcnt(10)\n" \
    "v_mfma_f32_16x16x32_bf16 v[24:27], v[60:63], v[96:99], v[24:27]\nbuffer_load_dwordx4 v[92:95], v0, s[64:67], s85 offen offset:1024 sc1\n" \
    "v_mfma_f32_16x16x32_bf16 v[28:31], v[68:71], v[96:99], v[28:31]\nv_max3_u32 v22, v22, v96, v97\nv_max3_u32 v22, v22, v98, v99\ns_waitcnt vmcnt(10)\n" \
    "v_mfma_f32_16x16x32_bf16 v[32:35], v[60:63], v[100:103], v[32:35]\nbuffer_load_dwordx4 v[96:99], v0, s[64:67], s85 offen offset:2048 sc1\n" \
    "v_mfma_f32_16x16x32_bf16 v[36:39], v[68:71], v[100:103], v[36:39]\nv_max3_u32 v22, v22, v100, v101\nv_max3_u32 v22, v22, v102, v103\nv_cmp_eq_u32 vcc, -1, v22\n" \
    "s_cbranch_vccnz .Lr97_%=\nds_write_b128 v9, v[24:27] offset:0\nds_write_b128 v9, v[28:31] offset:64\ns_waitcnt vmcnt(10)\n" \
    "v_mfma_f32_16x16x32_bf16 v[24:27], v[104:107], a[0:3], 0\nbuffer_load_dwordx4 v[100:103], v0, s[64:67], s85 offen offset:3072 sc1\n" \
    "v_mfma_f32_16x16x32_bf16 v[28:31], v[104:107], a[16:19], 0\nds_write_b128 v9, v[32:35] offset:2304\nds_write_b128 v9, v[36:39] offset:2368\ns_waitcnt vmcnt(10)\n" \
    "v_mfma_f32_16x16x32_bf16 v[24:27], v[108:111], a[4:7], v[24:27]\nbuffer_load_dwordx4 v[104:107], v1, s[64:67], s85 offen offset:0 sc1\n" \
    "v_mfma_f32_16x16x32_bf16 v[28:31], v[108:111], a[20:23], v[28:31]\nds_read_b128 v[18:21], v16\ns_waitcnt vmcnt(10)\n" \
    "v_mfma_f32_16x16x32_bf16 v[24:27], v[112:115], a[8:11], v[24:27]\nbuffer_load_dwordx4 v[108:111], v1, s[64:67], s85 offen offset:1024 sc1\n" \
    "v_mfma_f32_16x16x32_bf16 v[28:31], v[112:115], a[24:27], v[28:31]\ns_waitcnt lgkmcnt(0)\ns_barrier\nds_read_b128 v[56:59], v10 offset:0\n" \
    "ds_read_b128 v[60:63], v10 offset:4608\ns_waitcnt vmcnt(10)\nv_mfma_f32_16x16x32_bf16 v[24:27], v[116:119], a[12:15], v[24:27]\n" \
    "buffer_load_dwordx4 v[112:115], v1, s[64:67], s85 offen offset:2048 sc1\nv_mfma_f32_16x16x32_bf16 v[28:31], v[116:119], a[28:31], v[28:31]\n" \
    "ds_read_b128 v[64:67], v10 offset:9216\nds_read_b128 v[68:71], v10 offset:13824\nv_lshlrev_b32 v248, 16, v252\nv_and_b32 v249, 0xffff0000, v252\n" \
    "v_lshlrev_b32 v250, 16, v253\ns_waitcnt vmcnt(10)\nv_mfma_f32_16x16x32_bf16 v[32:35], v[72:75], a[0:3], 0\n" \
    "buffer_load_dwordx4 v[116:119], v1, s[64:67], s85 offen offset:3072 sc1\nv_mfma_f32_16x16x32_bf16 v[36:39], v[72:75], a[16:19], 0\nv_and_b32 v251, 0xffff0000, v253\n" \
    "s_waitcnt vmcnt(10)\nv_mfma_f32_16x16x32_bf16 v[32:35], v[76:79], a[4:7], v[32:35]\ns_add_u32 s85, s84, 32768\n" \
    "buffer_load_dwordx4 v[72:75], v0, s[64:67], s85 offen offset:0 sc1\nv_mfma_f32_16x16x32_bf16 v[36:39], v[76:79], a[20:23], v[36:39]\nv_cvt_pk_bf16_f32 v184, v24, v25\n" \
    "v_cvt_pk_bf16_f32 v186, v26, v27\ns_waitcnt lgkmcnt(0)\nv_add_f32 v56, v56, v60\nv_add_f32 v57, v57, v61\nv_add_f32 v58, v58, v62\nv_add_f32 v59, v59, v63\n" \
    "s_waitcnt vmcnt(10)\nv_mfma_f32_16x16x32_bf16 v[32:35], v[80:83], a[8:11], v[32:35]\nbuffer_load_dwordx4 v[76:79], v0, s[64:67], s85 offen offset:1024 sc1\n" \
    "v_mfma_f32_16x16x32_bf16 v[36:39], v[80:83], a[24:27], v[36:39]\nv_cvt_pk_bf16_f32 v216, v28, v29\nv_cvt_pk_bf16_f32 v218, v30, v31\nv_add_f32 v64, v64, v68\n" \
    "v_add_f32 v65, v65, v69\nv_add_f32 v66, v66, v70\nv_add_f32 v67, v67, v71\nv_add_f32 v56, v56, v64\ns_waitcnt vmcnt(10)\n" \
    "v_mfma_f32_16x16x32_bf16 v[32:35], v[84:87], a[12:15], v[32:35]\nbuffer_load_dwordx4 v[80:83], v0, s[64:67], s85 offen offset:2048 sc1\n" \
    "v_mfma_f32_16x16x32_bf16 v[36:39], v[84:87], a[28:31], v[36:39]\nv_add_f32 v57, v57, v65\nv_add_f32 v58, v58, v66\nv_add_f32 v59, v59, v67\nv_fma_f32 v248, v56, v18, v248\n" \
    "v_fma_f32 v249, v57, v19, v249\nv_fma_f32 v250, v58, v20, v250\nv_fma_f32 v251, v59, v21, v251\ns_waitcnt vmcnt(10)\n" \
    "v_mfma_f32_16x16x32_bf16 v[24:27], v[88:91], a[32:35], 0\nbuffer_load_dwordx4 v[84:87], v0, s[64:67], s85 offen offset:3072 sc1\n" \
    "v_mfma_f32_16x16x32_bf16 v[28:31], v[88:91], a[48:51], 0\nv_cvt_pk_bf16_f32 v56, v248, v249\nv_cvt_pk_bf16_f32 v57, v250, v251\nv_lshlrev_b32 v248, 16, v56\n" \
    "v_and_b32 v249, 0xffff0000, v56\nv_lshlrev_b32 v250, 16, v57\nv_and_b32 v251, 0xffff0000, v57\nv_add_f32 v18, v248, v249\ns_waitcnt vmcnt(10)\n" \
    "v_mfma_f32_16x16x32_bf16 v[24:27], v[92:95], a[36:39], v[24:27]\nbuffer_load_dwordx4 v[88:91], v1, s[64:67], s85 offen offset:0 sc1\n" \
    "v_mfma_f32_16x16x32_bf16 v[28:31], v[92:95], a[52:55], v[28:31]\nv_cvt_pk_bf16_f32 v185, v32, v33\nv_cvt_pk_bf16_f32 v187, v34, v35\nv_add_f32 v19, v250, v251\n" \
    "v_min_u32 v56, -2, v56\nv_min_u32 v57, -2, v57\nv_add_f32 v18, v18, v19\nv_permlane32_swap_b32 v56, v58\ns_waitcnt vmcnt(10)\n" \
    "v_mfma_f32_16x16x32_bf16 v[24:27], v[96:99], a[40:43], v[24:27]\nbuffer_load_dwordx4 v[92:95], v1, s[64:67], s85 offen offset:1024 sc1\n" \
    "v_mfma_f32_16x16x32_bf16 v[28:31], v[96:99], a[56:59], v[28:31]\nv_cvt_pk_bf16_f32 v217, v36, v37\nv_cvt_pk_bf16_f32 v219, v38, v39\nv_permlane32_swap_b32 v57, v59\n" \
    "buffer_store_dwordx4 v[56:59], v12, s[64:67], s79 offen sc1\ns_waitcnt vmcnt(11)\nv_mfma_f32_16x16x32_bf16 v[24:27], v[100:103], a[44:47], v[24:27]\n" \
    "buffer_load_dwordx4 v[96:99], v1, s[64:67], s85 offen offset:2048 sc1\nv_mfma_f32_16x16x32_bf16 v[28:31], v[100:103], a[60:63], v[28:31]\n" \
    "v_add_f32_dpp v19, v18, v18 row_ror:8 row_mask:0xf bank_mask:0xf\nv_add_f32_dpp v20, v18, v18 row_ror:8 row_mask:0xf bank_mask:0xf\ns_nop 1\n" \
    "v_permlane16_swap_b32 v19, v20\nv_add_f32 v18, v19, v20\nv_add_f32 v19, v19, v20\ns_nop 1\nv_permlane32_swap_b32 v18, v19\ns_waitcnt vmcnt(11)\n" \
    "v_mfma_f32_16x16x32_bf16 v[32:35], v[104:107], a[32:35], 0\nbuffer_load_dwordx4 v[100:103], v1, s[64:67], s85 offen offset:3072 sc1\n" \
    "v_mfma_f32_16x16x32_bf16 v[36:39], v[104:107], a[48:51], 0\nv_add_f32 v18, v18, v19\nv_mul_f32 v18, 0x3d000000, v18\nv_sub_f32 v60, v248, v18\nv_sub_f32 v61, v249, v18\n" \
    "v_sub_f32 v62, v250, v18\nv_sub_f32 v63, v251, v18\nv_mul_f32 v19, v60, v60\ns_waitcnt vmcnt(11)\nv_mfma_f32_16x16x32_bf16 v[32:35], v[108:111], a[36:39], v[32:35]\n" \
    "s_add_u32 s85, s84, 49152\nbuffer_load_dwordx4 v[104:107], v0, s[64:67], s85 offen offset:0 sc1\nv_mfma_f32_16x16x32_bf16 v[36:39], v[108:111], a[52:55], v[36:39]\n" \
    "v_cvt_pk_bf16_f32 v188, v24, v25\nv_cvt_pk_bf16_f32 v190, v26, v27\nv_fmac_f32 v19, v61, v61\nv_fmac_f32 v19, v62, v62\nv_fmac_f32 v19, v63, v63\nv_min_u32 v18, -2, v18\n" \
    "s_nop 0\nv_add_f32_dpp v20, v19, v19 row_ror:8 row_mask:0xf bank_mask:0xf\ns_waitcnt vmcnt(11)\nv_mfma_f32_16x16x32_bf16 v[32:35], v[112:115], a[40:43], v[32:35]\n" \
    "buffer_load_dwordx4 v[108:111], v0, s[64:67], s85 offen offset:1024 sc1\nv_mfma_f32_16x16x32_bf16 v[36:39], v[112:115], a[56:59], v[36:39]\n" \
    "v_cvt_pk_bf16_f32 v220, v28, v29\nv_cvt_pk_bf16_f32 v222, v30, v31\nv_add_f32_dpp v21, v19, v19 row_ror:8 row_mask:0xf bank_mask:0xf\ns_nop 1\n" \
    "v_permlane16_swap_b32 v20, v21\nv_add_f32 v19, v20, v21\nv_add_f32 v20, v20, v21\ns_waitcnt vmcnt(11)\nv_mfma_f32_16x16x32_bf16 v[32:35], v[116:119], a[44:47], v[32:35]\n" \
    "buffer_load_dwordx4 v[112:115], v0, s[64:67], s85 offen offset:2048 sc1\nv_mfma_f32_16x16x32_bf16 v[36:39], v[116:119], a[60:63], v[36:39]\nv_permlane32_swap_b32 v19, v20\n" \
    "v_add_f32 v19, v19, v20\nv_min_u32 v19, -2, v19\nbuffer_store_dwordx2 v[18:19], v13, s[64:67], s79 offen sc1\ns_waitcnt vmcnt(12)\n" \
    "v_mfma_f32_16x16x32_bf16 v[24:27], v[72:75], a[64:67], 0\nbuffer_load_dwordx4 v[116:119], v0, s[64:67], s85 offen offset:3072 sc1\n" \
    "v_mfma_f32_16x16x32_bf16 v[28:31], v[72:75], a[80:83], 0\ns_add_u32 s69, s50, -1\nv_mov_b32 v22, s69\nbuffer_store_dword v22, v14, s[64:67], 0 offen sc1\n" \
    "s_add_u32 s69, s50, 2\ns_and_b32 s69, s69, s41\ns_waitcnt vmcnt(13)\nv_mfma_f32_16x16x32_bf16 v[24:27], v[76:79], a[68:71], v[24:27]\n" \
    "buffer_load_dwordx4 v[72:75], v1, s[64:67], s85 offen offset:0 sc1\nv_mfma_f32_16x16x32_bf16 v[28:31], v[76:79], a[84:87], v[28:31]\nv_cvt_pk_bf16_f32 v189, v32, v33\n" \
    "v_cvt_pk_bf16_f32 v191, v34, v35\ns_lshl_b32 s69, s69, 20\ns_add_u32 s69, s69, s32\ns_add_u32 s74, s48, s69\ns_lshr_b32 s74, s74, 8\ns_xor_b32 s74, s74, s49\n" \
    "s_waitcnt vmcnt(13)\nv_mfma_f32_16x16x32_bf16 v[24:27], v[80:83], a[72:75], v[24:27]\nbuffer_load_dwordx4 v[76:79], v1, s[64:67], s85 offen offset:1024 sc1\n" \
    "v_mfma_f32_16x16x32_bf16 v[28:31], v[80:83], a[88:91], v[28:31]\nv_cvt_pk_bf16_f32 v221, v36, v37\nv_cvt_pk_bf16_f32 v223, v38, v39\ns_and_b32 s74, s74, 0x2000\n" \
    "s_add_u32 s74, s74, s69\ns_add_u32 s69, s50, 2\ns_and_b32 s69, s69, s41\ns_lshl_b32 s69, s69, 19\ns_waitcnt vmcnt(13)\n" \
    "v_mfma_f32_16x16x32_bf16 v[24:27], v[84:87], a[76:79], v[24:27]\nbuffer_load_dwordx4 v[80:83], v1, s[64:67], s85 offen offset:2048 sc1\n" \
    "v_mfma_f32_16x16x32_bf16 v[28:31], v[84:87], a[92:95], v[28:31]\ns_add_u32 s69, s69, s33\ns_add_u32 s87, s48, s69\ns_lshr_b32 s87, s87, 8\ns_xor_b32 s87, s87, s49\n" \
    "s_and_b32 s87, s87, 0x2000\ns_add_u32 s87, s87, s69\ns_add_u32 s69, s50, 3\ns_waitcnt vmcnt(13)\nv_mfma_f32_16x16x32_bf16 v[32:35], v[88:91], a[64:67], 0\n" \
    "buffer_load_dwordx4 v[84:87], v1, s[64:67], s85 offen offset:3072 sc1\nv_mfma_f32_16x16x32_bf16 v[36:39], v[88:91], a[80:83], 0\ns_and_b32 s69, s69, s41\n" \
    "s_lshl_b32 s69, s69, 18\ns_add_u32 s69, s69, s34\ns_add_u32 s88, s48, s69\ns_lshr_b32 s88, s88, 8\ns_xor_b32 s88, s88, s49\ns_and_b32 s88, s88, 0x2000\n" \
    "s_waitcnt vmcnt(13)\nv_mfma_f32_16x16x32_bf16 v[32:35], v[92:95], a[68:71], v[32:35]\ns_add_u32 s85, s84, 65536\n" \
    "buffer_load_dwordx4 v[88:91], v0, s[64:67], s85 offen offset:0 sc1\nv_mfma_f32_16x16x32_bf16 v[36:39], v[92:95], a[84:87], v[36:39]\nv_cvt_pk_bf16_f32 v192, v24, v25\n" \
    "v_cvt_pk_bf16_f32 v194, v26, v27\ns_add_u32 s88, s88, s69\ns_xor_b32 s89, s88, 0x1000\ns_add_u32 s69, s50, 0\ns_lshl_b32 s69, s69, 20\ns_add_u32 s69, s69, s36\n" \
    "s_waitcnt vmcnt(12)\nv_mfma_f32_16x16x32_bf16 v[32:35], v[96:99], a[72:75], v[32:35]\nbuffer_load_dwordx4 v[92:95], v0, s[64:67], s85 offen offset:1024 sc1\n" \
    "v_mfma_f32_16x16x32_bf16 v[36:39], v[96:99], a[88:91], v[36:39]\nv_cvt_pk_bf16_f32 v224, v28, v29\nv_cvt_pk_bf16_f32 v226, v30, v31\ns_add_u32 s75, s48, s69\n" \
    "s_lshr_b32 s75, s75, 8\ns_xor_b32 s75, s75, s49\ns_and_b32 s75, s75, 0x2000\ns_add_u32 s75, s75, s69\ns_waitcnt vmcnt(12)\n" \
    "v_mfma_f32_16x16x32_bf16 v[32:35], v[100:103], a[76:79], v[32:35]\nbuffer_load_dwordx4 v[96:99], v0, s[64:67], s85 offen offset:2048 sc1\n" \
    "v_mfma_f32_16x16x32_bf16 v[36:39], v[100:103], a[92:95], v[36:39]\ns_add_u32 s76, s75, 0x4000\ns_add_u32 s77, s75, s37\ns_xor_b32 s79, s75, 0x2000\ns_add_u32 s82, s50, 0\n" \
    "s_lshl_b32 s80, s82, 16\ns_lshl_b32 s82, s82, 2\ns_add_u32 s82, s82, s40\ns_waitcnt vmcnt(12)\nv_mfma_f32_16x16x32_bf16 v[24:27], v[104:107], a[96:99], 0\n" \
    "buffer_load_dwordx4 v[100:103], v0, s[64:67], s85 offen offset:3072 sc1\nv_mfma_f32_16x16x32_bf16 v[28:31], v[104:107], a[112:115], 0\ns_waitcnt vmcnt(12)\n" \
    "v_mfma_f32_16x16x32_bf16 v[24:27], v[108:111], a[100:103], v[24:27]\nbuffer_load_dwordx4 v[104:107], v1, s[64:67], s85 offen offset:0 sc1\n" \
    "v_mfma_f32_16x16x32_bf16 v[28:31], v[108:111], a[116:119], v[28:31]\nv_cvt_pk_bf16_f32 v193, v32, v33\nv_cvt_pk_bf16_f32 v195, v34, v35\ns_waitcnt vmcnt(12)\n" \
    "v_mfma_f32_16x16x32_bf16 v[24:27], v[112:115], a[104:107], v[24:27]\nbuffer_load_dwordx4 v[108:111], v1, s[64:67], s85 offen offset:1024 sc1\n" \
    "v_mfma_f32_16x16x32_bf16 v[28:31], v[112:115], a[120:123], v[28:31]\nv_cvt_pk_bf16_f32 v225, v36, v37\nv_cvt_pk_bf16_f32 v227, v38, v39\ns_waitcnt vmcnt(11)\n" \
    "v_mfma_f32_16x16x32_bf16 v[24:27], v[116:119], a[108:111], v[24:27]\nbuffer_load_dwordx4 v[112:115], v1, s[64:67], s85 offen offset:2048 sc1\n" \
    "v_mfma_f32_16x16x32_bf16 v[28:31], v[116:119], a[124:127], v[28:31]\ns_waitcnt vmcnt(10)\nv_mfma_f32_16x16x32_bf16 v[32:35], v[72:75], a[96:99], 0\n" \
    "buffer_load_dwordx4 v[116:119], v1, s[64:67], s85 offen offset:3072 sc1\nv_mfma_f32_16x16x32_bf16 v[36:39], v[72:75], a[112:115], 0\ns_waitcnt vmcnt(10)\n" \
    "v_mfma_f32_16x16x32_bf16 v[32:35], v[76:79], a[100:103], v[32:35]\ns_add_u32 s85, s84, 81920\nbuffer_load_dwordx4 v[72:75], v0, s[64:67], s85 offen offset:0 sc1\n" \
    "v_mfma_f32_16x16x32_bf16 v[36:39], v[76:79], a[116:119], v[36:39]\nv_cvt_pk_bf16_f32 v196, v24, v25\nv_cvt_pk_bf16_f32 v198, v26, v27\ns_waitcnt vmcnt(10)\n" \
    "v_mfma_f32_16x16x32_bf16 v[32:35], v[80:83], a[104:107], v[32:35]\nbuffer_load_dwordx4 v[76:79], v0, s[64:67], s85 offen offset:1024 sc1\n" \
    "v_mfma_f32_16x16x32_bf16 v[36:39], v[80:83], a[120:123], v[36:39]\nv_cvt_pk_bf16_f32 v228, v28, v29\nv_cvt_pk_bf16_f32 v230, v30, v31\ns_waitcnt vmcnt(10)\n" \
    "v_mfma_f32_16x16x32_bf16 v[32:35], v[84:87], a[108:111], v[32:35]\nbuffer_load_dwordx4 v[80:83], v0, s[64:67], s85 offen offset:2048 sc1\n" \
    "v_mfma_f32_16x16x32_bf16 v[36:39], v[84:87], a[124:127], v[36:39]\ns_waitcnt vmcnt(10)\nv_mfma_f32_16x16x32_bf16 v[24:27], v[88:91], a[128:131], 0\n" \
    "buffer_load_dwordx4 v[84:87], v0, s[64:67], s85 offen offset:3072 sc1\nv_mfma_f32_16x16x32_bf16 v[28:31], v[88:91], a[144:147], 0\ns_waitcnt vmcnt(10)\n" \
    "v_mfma_f32_16x16x32_bf16 v[24:27], v[92:95], a[132:135], v[24:27]\nbuffer_load_dwordx4 v[88:91], v1, s[64:67], s85 offen offset:0 sc1\n" \
    "v_mfma_f32_16x16x32_bf16 v[28:31], v[92:95], a[148:151], v[28:31]\nv_cvt_pk_bf16_f32 v197, v32, v33\nv_cvt_pk_bf16_f32 v199, v34, v35\ns_waitcnt vmcnt(10)\n" \
    "v_mfma_f32_16x16x32_bf16 v[24:27], v[96:99], a[136:139], v[24:27]\nbuffer_load_dwordx4 v[92:95], v1, s[64:67], s85 offen offset:1024 sc1\n" \
    "v_mfma_f32_16x16x32_bf16 v[28:31], v[96:99], a[152:155], v[28:31]\nv_cvt_pk_bf16_f32 v229, v36, v37\nv_cvt_pk_bf16_f32 v231, v38, v39\ns_waitcnt vmcnt(10)\n" \
    "v_mfma_f32_16x16x32_bf16 v[24:27], v[100:103], a[140:143], v[24:27]\nbuffer_load_dwordx4 v[96:99], v1, s[64:67], s85 offen offset:2048 sc1\n" \
    "v_mfma_f32_16x16x32_bf16 v[28:31], v[100:103], a[156:159], v[28:31]\ns_waitcnt vmcnt(10)\nv_mfma_f32_16x16x32_bf16 v[32:35], v[104:107], a[128:131], 0\n" \
    "buffer_load_dwordx4 v[100:103], v1, s[64:67], s85 offen offset:3072 sc1\nv_mfma_f32_16x16x32_bf16 v[36:39], v[104:107], a[144:147], 0\ns_waitcnt vmcnt(10)\n" \
    "v_mfma_f32_16x16x32_bf16 v[32:35], v[108:111], a[132:135], v[32:35]\ns_add_u32 s85, s84, 98304\nbuffer_load_dwordx4 v[104:107], v0, s[64:67], s85 offen offset:0 sc1\n" \
    "v_mfma_f32_16x16x32_bf16 v[36:39], v[108:111], a[148:151], v[36:39]\nv_cvt_pk_bf16_f32 v200, v24, v25\nv_cvt_pk_bf16_f32 v202, v26, v27\ns_waitcnt vmcnt(10)\n" \
    "v_mfma_f32_16x16x32_bf16 v[32:35], v[112:115], a[136:139], v[32:35]\nbuffer_load_dwordx4 v[108:111], v0, s[64:67], s85 offen offset:1024 sc1\n" \
    "v_mfma_f32_16x16x32_bf16 v[36:39], v[112:115], a[152:155], v[36:39]\nv_cvt_pk_bf16_f32 v232, v28, v29\nv_cvt_pk_bf16_f32 v234, v30, v31\ns_waitcnt vmcnt(10)\n" \
    "v_mfma_f32_16x16x32_bf16 v[32:35], v[116:119], a[140:143], v[32:35]\nbuffer_load_dwordx4 v[112:115], v0, s[64:67], s85 offen offset:2048 sc1\n" \
    "v_mfma_f32_16x16x32_bf16 v[36:39], v[116:119], a[156:159], v[36:39]\ns_waitcnt vmcnt(10)\nv_mfma_f32_16x16x32_bf16 v[24:27], v[72:75], a[160:163], 0\n" \
    "buffer_load_dwordx4 v[116:119], v0, s[64:67], s85 offen offset:3072 sc1\nv_mfma_f32_16x16x32_bf16 v[28:31], v[72:75], a[176:179], 0\n" \
    "buffer_load_dword v17, v8, s[64:67], 0 offen sc1\ns_waitcnt vmcnt(11)\nv_mfma_f32_16x16x32_bf16 v[24:27], v[76:79], a[164:167], v[24:27]\n" \
    "buffer_load_dwordx4 v[72:75], v1, s[64:67], s85 offen offset:0 sc1\nv_mfma_f32_16x16x32_bf16 v[28:31], v[76:79], a[180:183], v[28:31]\nv_cvt_pk_bf16_f32 v201, v32, v33\n" \
    "v_cvt_pk_bf16_f32 v203, v34, v35\ns_waitcnt vmcnt(11)\nv_mfma_f32_16x16x32_bf16 v[24:27], v[80:83], a[168:171], v[24:27]\n" \
    "buffer_load_dwordx4 v[76:79], v1, s[64:67], s85 offen offset:1024 sc1\nv_mfma_f32_16x16x32_bf16 v[28:31], v[80:83], a[184:187], v[28:31]\nv_cvt_pk_bf16_f32 v233, v36, v37\n" \
    "v_cvt_pk_bf16_f32 v235, v38, v39\ns_waitcnt vmcnt(11)\nv_mfma_f32_16x16x32_bf16 v[24:27], v[84:87], a[172:175], v[24:27]\n" \
    "buffer_load_dwordx4 v[80:83], v1, s[64:67], s85 offen offset:2048 sc1\nv_mfma_f32_16x16x32_bf16 v[28:31], v[84:87], a[188:191], v[28:31]\ns_waitcnt vmcnt(11)\n" \
    "v_mfma_f32_16x16x32_bf16 v[32:35], v[88:91], a[160:163], 0\nbuffer_load_dwordx4 v[84:87], v1, s[64:67], s85 offen offset:3072 sc1\n" \
    "v_mfma_f32_16x16x32_bf16 v[36:39], v[88:91], a[176:179], 0\ns_waitcnt vmcnt(11)\nv_mfma_f32_16x16x32_bf16 v[32:35], v[92:95], a[164:167], v[32:35]\n" \
    "s_add_u32 s85, s84, 114688\nbuffer_load_dwordx4 v[88:91], v0, s[64:67], s85 offen offset:0 sc1\nv_mfma_f32_16x16x32_bf16 v[36:39], v[92:95], a[180:183], v[36:39]\n" \
    "v_cvt_pk_bf16_f32 v204, v24, v25\nv_cvt_pk_bf16_f32 v206, v26, v27\ns_waitcnt vmcnt(11)\nv_mfma_f32_16x16x32_bf16 v[32:35], v[96:99], a[168:171], v[32:35]\n" \
    "buffer_load_dwordx4 v[92:95], v0, s[64:67], s85 offen offset:1024 sc1\nv_mfma_f32_16x16x32_bf16 v[36:39], v[96:99], a[184:187], v[36:39]\nv_cvt_pk_bf16_f32 v236, v28, v29\n" \
    "v_cvt_pk_bf16_f32 v238, v30, v31\ns_add_u32 s69, s50, 4\ns_and_b32 s69, s69, s41\ns_lshl_b32 s69, s69, 19\ns_add_u32 s69, s69, s35\ns_add_u32 s84, s48, s69\n" \
    "s_waitcnt vmcnt(11)\nv_mfma_f32_16x16x32_bf16 v[32:35], v[100:103], a[172:175], v[32:35]\nbuffer_load_dwordx4 v[96:99], v0, s[64:67], s85 offen offset:2048 sc1\n" \
    "v_mfma_f32_16x16x32_bf16 v[36:39], v[100:103], a[188:191], v[36:39]\ns_lshr_b32 s84, s84, 8\ns_xor_b32 s84, s84, s49\ns_and_b32 s84, s84, 0x2000\ns_add_u32 s84, s84, s69\n" \
    "s_waitcnt vmcnt(11)\nv_mfma_f32_16x16x32_bf16 v[24:27], v[104:107], a[192:195], 0\nbuffer_load_dwordx4 v[100:103], v0, s[64:67], s85 offen offset:3072 sc1\n" \
    "v_mfma_f32_16x16x32_bf16 v[28:31], v[104:107], a[208:211], 0\ns_waitcnt vmcnt(11)\nv_mfma_f32_16x16x32_bf16 v[24:27], v[108:111], a[196:199], v[24:27]\n" \
    "buffer_load_dwordx4 v[104:107], v1, s[64:67], s85 offen offset:0 sc1\nv_mfma_f32_16x16x32_bf16 v[28:31], v[108:111], a[212:215], v[28:31]\n" \
    "v_cvt_pk_bf16_f32 v205, v32, v33\nv_cvt_pk_bf16_f32 v207, v34, v35\ns_waitcnt vmcnt(11)\nv_mfma_f32_16x16x32_bf16 v[24:27], v[112:115], a[200:203], v[24:27]\n" \
    "buffer_load_dwordx4 v[108:111], v1, s[64:67], s85 offen offset:1024 sc1\nv_mfma_f32_16x16x32_bf16 v[28:31], v[112:115], a[216:219], v[28:31]\n" \
    "v_cvt_pk_bf16_f32 v237, v36, v37\nv_cvt_pk_bf16_f32 v239, v38, v39\ns_waitcnt vmcnt(11)\nv_mfma_f32_16x16x32_bf16 v[24:27], v[116:119], a[204:207], v[24:27]\n" \
    "buffer_load_dwordx4 v[112:115], v1, s[64:67], s85 offen offset:2048 sc1\nv_mfma_f32_16x16x32_bf16 v[28:31], v[116:119], a[220:223], v[28:31]\ns_waitcnt vmcnt(10)\n" \
    "v_mfma_f32_16x16x32_bf16 v[32:35], v[72:75], a[192:195], 0\nbuffer_load_dwordx4 v[116:119], v1, s[64:67], s85 offen offset:3072 sc1\n" \
    "v_mfma_f32_16x16x32_bf16 v[36:39], v[72:75], a[208:211], 0\ns_mov_b32 s86, 0\nv_mov_b32 v18, 0x7f000000\ns_add_u32 s70, s50, 4\ns_min_i32 s70, s70, 63\n" \
    "s_add_u32 s71, s50, 0\nv_readlane_b32 s69, v17, 10\ns_cmp_ge_i32 s69, s71\ns_cbranch_scc0 .Lr108_%=\ns_mov_b32 s86, 1\nv_mov_b32 v18, v0\n.Lr108_%=:\n" \
    "v_add_u32 v19, 0x1000, v18\ns_waitcnt vmcnt(10)\nv_mfma_f32_16x16x32_bf16 v[32:35], v[76:79], a[196:199], v[32:35]\n" \
    "v_mfma_f32_16x16x32_bf16 v[36:39], v[76:79], a[212:215], v[36:39]\nv_cvt_pk_bf16_f32 v208, v24, v25\nv_cvt_pk_bf16_f32 v210, v26, v27\n" \
    "buffer_load_dwordx4 v[72:75], v18, s[64:67], s75 offen offset:0 sc1\ns_waitcnt vmcnt(10)\nv_mfma_f32_16x16x32_bf16 v[32:35], v[80:83], a[200:203], v[32:35]\n" \
    "v_mfma_f32_16x16x32_bf16 v[36:39], v[80:83], a[216:219], v[36:39]\nv_cvt_pk_bf16_f32 v240, v28, v29\nv_cvt_pk_bf16_f32 v242, v30, v31\n" \
    "buffer_load_dwordx4 v[76:79], v18, s[64:67], s75 offen offset:1024 sc1\ns_waitcnt vmcnt(10)\nv_mfma_f32_16x16x32_bf16 v[32:35], v[84:87], a[204:207], v[32:35]\n" \
    "v_mfma_f32_16x16x32_bf16 v[36:39], v[84:87], a[220:223], v[36:39]\nbuffer_load_dwordx4 v[80:83], v18, s[64:67], s75 offen offset:2048 sc1\ns_waitcnt vmcnt(10)\n" \
    "v_mfma_f32_16x16x32_bf16 v[24:27], v[88:91], a[224:227], 0\nv_mfma_f32_16x16x32_bf16 v[28:31], v[88:91], a[240:243], 0\n" \
    "buffer_load_dwordx4 v[84:87], v18, s[64:67], s75 offen offset:3072 sc1\ns_waitcnt vmcnt(10)\nv_mfma_f32_16x16x32_bf16 v[24:27], v[92:95], a[228:231], v[24:27]\n" \
    "v_mfma_f32_16x16x32_bf16 v[28:31], v[92:95], a[244:247], v[28:31]\nv_cvt_pk_bf16_f32 v209, v32, v33\nv_cvt_pk_bf16_f32 v211, v34, v35\n" \
    "buffer_load_dwordx4 v[88:91], v19, s[64:67], s75 offen offset:0 sc1\ns_waitcnt vmcnt(10)\nv_mfma_f32_16x16x32_bf16 v[24:27], v[96:99], a[232:235], v[24:27]\n" \
    "v_mfma_f32_16x16x32_bf16 v[28:31], v[96:99], a[248:251], v[28:31]\nv_cvt_pk_bf16_f32 v241, v36, v37\nv_cvt_pk_bf16_f32 v243, v38, v39\n" \
    "buffer_load_dwordx4 v[92:95], v19, s[64:67], s75 offen offset:1024 sc1\ns_waitcnt vmcnt(10)\nv_mfma_f32_16x16x32_bf16 v[24:27], v[100:103], a[236:239], v[24:27]\n" \
    "v_mfma_f32_16x16x32_bf16 v[28:31], v[100:103], a[252:255], v[28:31]\nbuffer_load_dwordx4 v[96:99], v19, s[64:67], s75 offen offset:2048 sc1\n" \
    "buffer_load_dwordx2 v[252:253], v11, s[60:63], s80 offen\ns_waitcnt vmcnt(11)\nv_mfma_f32_16x16x32_bf16 v[32:35], v[104:107], a[224:227], 0\n" \
    "v_mfma_f32_16x16x32_bf16 v[36:39], v[104:107], a[240:243], 0\ns_waitcnt vmcnt(10)\nv_mfma_f32_16x16x32_bf16 v[32:35], v[108:111], a[228:231], v[32:35]\n" \
    "v_mfma_f32_16x16x32_bf16 v[36:39], v[108:111], a[244:247], v[36:39]\nv_cvt_pk_bf16_f32 v212, v24, v25\nv_cvt_pk_bf16_f32 v214, v26, v27\ns_waitcnt vmcnt(9)\n" \
    "v_mfma_f32_16x16x32_bf16 v[32:35], v[112:115], a[232:235], v[32:35]\nv_mfma_f32_16x16x32_bf16 v[36:39], v[112:115], a[248:251], v[36:39]\nv_cvt_pk_bf16_f32 v244, v28, v29\n" \
    "v_cvt_pk_bf16_f32 v246, v30, v31\ns_waitcnt vmcnt(8)\nv_mfma_f32_16x16x32_bf16 v[32:35], v[116:119], a[236:239], v[32:35]\n" \
    "v_mfma_f32_16x16x32_bf16 v[36:39], v[116:119], a[252:255], v[36:39]\ns_nop 6\nv_cvt_pk_bf16_f32 v213, v32, v33\nv_cvt_pk_bf16_f32 v215, v34, v35\n" \
    "v_cvt_pk_bf16_f32 v245, v36, v37\nv_cvt_pk_bf16_f32 v247, v38, v39\ns_add_u32 s50, s50, 2\ns_cmp_gt_i32 s50, s42\ns_cselect_b32 s69, s45, 0\ns_sub_u32 s51, s51, s69\n" \
    "s_cmp_lt_u32 s50, 62\ns_cbranch_scc1 .Lr3_%=\ns_cmp_eq_u32 s50, 63\ns_cbranch_scc1 .Lr4_%=\ns_mov_b32 s68, 0\ns_add_u32 s78, s87, s38\n.Lr111_%=:\ns_sub_u32 s70, s51, 52\n" \
    ".Lr120_%=:\ns_sleep 1\ns_memrealtime s[72:73]\ns_waitcnt lgkmcnt(0)\ns_sub_u32 s72, s72, s70\ns_cmp_lt_i32 s72, 0\ns_cbranch_scc1 .Lr120_%=\n" \
    "buffer_load_dword v20, off, s[64:67], s78 sc1\nbuffer_load_dword v17, v8, s[64:67], 0 offen sc1\ns_waitcnt vmcnt(1)\nv_readfirstlane_b32 s70, v20\ns_memrealtime s[72:73]\n" \
    "s_cmp_eq_u32 s70, -1\ns_cbranch_scc1 .Lr112_%=\ns_lshr_b32 s71, s70, 8\ns_waitcnt lgkmcnt(0)\ns_sub_u32 s71, s72, s71\ns_and_b32 s71, s71, 0xffffff\n" \
    "s_sub_u32 s51, s72, s71\ns_add_u32 s51, s51, 176\ns_and_b32 s70, s70, 128\ns_lshl_b32 s52, s70, 5\ns_xor_b32 s87, s87, s52\ns_mov_b32 s68, 0\ns_sub_u32 s70, s51, s54\n" \
    ".Lr121_%=:\ns_memrealtime s[72:73]\ns_waitcnt lgkmcnt(0)\ns_sub_u32 s72, s72, s70\ns_cmp_lt_i32 s72, 0\ns_cbranch_scc1 .Lr121_%=\n" \
    "buffer_load_dwordx4 v[56:59], v0, s[64:67], s74 offen offset:0 sc1\nbuffer_load_dwordx4 v[60:63], v0, s[64:67], s74 offen offset:1024 sc1\ns_add_u32 s69, s74, 0x4000\n" \
    "buffer_load_dwordx4 v[64:67], v0, s[64:67], s69 offen offset:0 sc1\nbuffer_load_dwordx4 v[68:71], v0, s[64:67], s69 offen offset:1024 sc1\n.Lr122_%=:\n" \
    "s_memrealtime s[72:73]\ns_waitcnt lgkmcnt(0)\ns_sub_u32 s72, s72, s51\ns_cmp_lt_i32 s72, -3\ns_cbranch_scc1 .Lr122_%=\ns_cmp_ge_i32 s72, 0\ns_cbranch_scc1 .Lr123_%=\n" \
    "s_nop 2\ns_cmp_ge_i32 s72, -1\ns_cbranch_scc1 .Lr123_%=\ns_nop 2\ns_cmp_ge_i32 s72, -2\ns_cbranch_scc1 .Lr123_%=\ns_nop 2\n.Lr123_%=:\n" \
    "buffer_load_dwordx2 v[116:117], v5, s[64:67], s87 offen offset:0 sc1\ns_nop 5\nbuffer_load_dwordx2 v[118:119], v5, s[64:67], s87 offen offset:128 sc1\ns_nop 5\n" \
    "buffer_load_dwordx4 v[100:103], v0, s[64:67], s87 offen offset:0 sc1\ns_nop 10\nbuffer_load_dwordx4 v[104:107], v0, s[64:67], s87 offen offset:1024 sc1\ns_nop 10\n" \
    "buffer_load_dwordx4 v[108:111], v0, s[64:67], s87 offen offset:2048 sc1\ns_nop 10\nbuffer_load_dwordx4 v[112:115], v0, s[64:67], s87 offen offset:3072 sc1\n.Lr115_%=:\n" \
    "v_mov_b64 v[24:25], 0\nv_mov_b64 v[26:27], 0\nv_mov_b64 v[28:29], 0\nv_mov_b64 v[30:31], 0\ns_waitcnt vmcnt(6)\nv_max3_u32 v22, v56, v57, v58\n" \
    "v_max3_u32 v22, v22, v59, v60\nv_max3_u32 v22, v22, v61, v62\nv_max3_u32 v22, v22, v63, v64\nv_max3_u32 v22, v22, v65, v66\nv_max3_u32 v22, v22, v67, v68\n" \
    "v_max3_u32 v22, v22, v69, v70\nv_max_u32 v22, v22, v71\ns_waitcnt vmcnt(4)\nv_smfmac_f32_16x16x64_bf16 v[24:27], v[116:119], v[56:63], v4\nv_max3_u32 v22, v22, v116, v117\n" \
    "v_smfmac_f32_16x16x64_bf16 v[28:31], v[116:119], v[64:71], v4\nv_max3_u32 v22, v22, v118, v119\ns_waitcnt vmcnt(3)\n" \
    "v_smfmac_f32_16x16x64_bf16 v[24:27], v[100:103], v[184:191], v4\nv_max3_u32 v22, v22, v100, v101\nv_smfmac_f32_16x16x64_bf16 v[28:31], v[100:103], v[216:223], v4\n" \
    "v_max3_u32 v22, v22, v102, v103\ns_waitcnt vmcnt(2)\nv_smfmac_f32_16x16x64_bf16 v[24:27], v[104:107], v[192:199], v4\nv_max3_u32 v22, v22, v104, v105\n" \
    "v_smfmac_f32_16x16x64_bf16 v[28:31], v[104:107], v[224:231], v4\nv_max3_u32 v22, v22, v106, v107\ns_waitcnt vmcnt(1)\n" \
    "v_smfmac_f32_16x16x64_bf16 v[24:27], v[108:111], v[200:207], v4\nv_max3_u32 v22, v22, v108, v109\nv_smfmac_f32_16x16x64_bf16 v[28:31], v[108:111], v[232:239], v4\n" \
    "v_max3_u32 v22, v22, v110, v111\ns_waitcnt vmcnt(0)\nv_smfmac_f32_16x16x64_bf16 v[24:27], v[112:115], v[208:215], v4\nv_max3_u32 v22, v22, v112, v113\n" \
    "v_smfmac_f32_16x16x64_bf16 v[28:31], v[112:115], v[240:247], v4\nv_max3_u32 v22, v22, v114, v115\nv_cmp_eq_u32 vcc, -1, v22\ns_nop 5\nv_permlane32_swap_b32 v24, v28\n" \
    "v_permlane32_swap_b32 v25, v29\nv_permlane32_swap_b32 v26, v30\nv_permlane32_swap_b32 v27, v31\nv_pk_add_f32 v[24:25], v[24:25], v[28:29]\n" \
    "v_pk_add_f32 v[26:27], v[26:27], v[30:31]\nds_write2_b32 v6, v24, v25 offset0:0 offset1:36\nds_write2_b32 v6, v26, v27 offset0:72 offset1:108\ns_cbranch_vccnz .Lr116_%=\n" \
    "s_waitcnt lgkmcnt(0)\ns_barrier\nds_read_b128 v[24:27], v7 offset:0\nds_read_b128 v[28:31], v7 offset:1152\nds_read_b128 v[32:35], v7 offset:2304\n" \
    "ds_read_b128 v[36:39], v7 offset:3456\ns_waitcnt lgkmcnt(2)\nv_pk_add_f32 v[24:25], v[24:25], v[28:29]\nv_pk_add_f32 v[26:27], v[26:27], v[30:31]\ns_waitcnt lgkmcnt(0)\n" \
    "v_pk_add_f32 v[32:33], v[32:33], v[36:37]\nv_pk_add_f32 v[34:35], v[34:35], v[38:39]\nv_pk_add_f32 v[24:25], v[24:25], v[32:33]\nv_pk_add_f32 v[26:27], v[26:27], v[34:35]\n" \
    "v_cvt_pk_bf16_f32 v248, v24, v25\nv_cvt_pk_bf16_f32 v249, v26, v27\ns_nop 0\nv_permlane32_swap_b32 v248, v250\nv_permlane32_swap_b32 v249, v251\ns_mov_b32 exec_hi, 0\n" \
    "buffer_store_dwordx4 v[248:251], v0, s[64:67], s88 offen sc1\nbuffer_store_dwordx4 v[248:251], v0, s[64:67], s89 offen sc1\ns_mov_b32 exec_hi, -1\ns_add_u32 s51, s51, s55\n" \
    "s_add_u32 s51, s51, s55\n.Lr4_%=:\ns_mov_b32 s68, 0\ns_cmp_eq_u32 s86, 1\ns_cbranch_scc1 .Lr128_%=\ns_add_u32 s70, s50, 2\ns_min_i32 s70, s70, 63\ns_add_u32 s71, s50, -2\n" \
    "s_mov_b32 s68, 0\ns_branch .Lr135_%=\n.Lr134_%=:\nbuffer_load_dword v17, v8, s[64:67], 0 offen sc1\ns_waitcnt vmcnt(0)\n.Lr135_%=:\nv_readlane_b32 s69, v17, 9\n" \
    "s_cmp_gt_i32 s69, s71\ns_cbranch_scc0 .Lr136_%=\ns_mov_b32 s68, 0\n.Lr127_%=:\nbuffer_load_dwordx2 v[252:253], v11, s[60:63], s80 offen\n" \
    "buffer_load_dwordx4 v[72:75], v0, s[64:67], s75 offen offset:0 sc1\nbuffer_load_dwordx4 v[76:79], v0, s[64:67], s75 offen offset:1024 sc1\n" \
    "buffer_load_dwordx4 v[80:83], v0, s[64:67], s75 offen offset:2048 sc1\nbuffer_load_dwordx4 v[84:87], v0, s[64:67], s75 offen offset:3072 sc1\n" \
    "buffer_load_dwordx4 v[88:91], v1, s[64:67], s75 offen offset:0 sc1\nbuffer_load_dwordx4 v[92:95], v1, s[64:67], s75 offen offset:1024 sc1\n" \
    "buffer_load_dwordx4 v[96:99], v1, s[64:67], s75 offen offset:2048 sc1\n.Lr128_%=:\nbuffer_load_dwordx4 v[100:103], v1, s[64:67], s75 offen offset:3072 sc1\n" \
    "buffer_load_dwordx4 v[104:107], v0, s[64:67], s76 offen offset:0 sc1\nbuffer_load_dwordx4 v[108:111], v0, s[64:67], s76 offen offset:1024 sc1\n" \
    "buffer_load_dwordx4 v[112:115], v0, s[64:67], s76 offen offset:2048 sc1\nv_mov_b32 v22, 0\ns_waitcnt vmcnt(10)\nv_mfma_f32_16x16x32_bf16 v[24:27], v[120:123], v[72:75], 0\n" \
    "buffer_load_dwordx4 v[116:119], v0, s[64:67], s76 offen offset:3072 sc1\nv_mfma_f32_16x16x32_bf16 v[28:31], v[152:155], v[72:75], 0\nv_max3_u32 v22, v22, v72, v73\n" \
    "v_max3_u32 v22, v22, v74, v75\ns_waitcnt vmcnt(10)\nv_mfma_f32_16x16x32_bf16 v[32:35], v[120:123], v[76:79], 0\n" \
    "buffer_load_dwordx4 v[72:75], v1, s[64:67], s76 offen offset:0 sc1\nv_mfma_f32_16x16x32_bf16 v[36:39], v[152:155], v[76:79], 0\nv_max3_u32 v22, v22, v76, v77\n" \
    "v_max3_u32 v22, v22, v78, v79\ns_waitcnt vmcnt(10)\nv_mfma_f32_16x16x32_bf16 v[24:27], v[124:127], v[80:83], v[24:27]\n" \
    "buffer_load_dwordx4 v[76:79], v1, s[64:67], s76 offen offset:1024 sc1\nv_mfma_f32_16x16x32_bf16 v[28:31], v[156:159], v[80:83], v[28:31]\nv_max3_u32 v22, v22, v80, v81\n" \
    "v_max3_u32 v22, v22, v82, v83\ns_waitcnt vmcnt(10)\nv_mfma_f32_16x16x32_bf16 v[32:35], v[124:127], v[84:87], v[32:35]\n" \
    "buffer_load_dwordx4 v[80:83], v1, s[64:67], s76 offen offset:2048 sc1\nv_mfma_f32_16x16x32_bf16 v[36:39], v[156:159], v[84:87], v[36:39]\nv_max3_u32 v22, v22, v84, v85\n" \
    "v_max3_u32 v22, v22, v86, v87\ns_waitcnt vmcnt(10)\nv_mfma_f32_16x16x32_bf16 v[24:27], v[128:131], v[88:91], v[24:27]\n" \
    "buffer_load_dwordx4 v[84:87], v1, s[64:67], s76 offen offset:3072 sc1\nv_mfma_f32_16x16x32_bf16 v[28:31], v[160:163], v[88:91], v[28:31]\nv_max3_u32 v22, v22, v88, v89\n" \
    "v_max3_u32 v22, v22, v90, v91\ns_waitcnt vmcnt(10)\nv_mfma_f32_16x16x32_bf16 v[32:35], v[128:131], v[92:95], v[32:35]\n" \
    "buffer_load_dwordx4 v[88:91], v0, s[64:67], s77 offen offset:0 sc1\nv_mfma_f32_16x16x32_bf16 v[36:39], v[160:163], v[92:95], v[36:39]\nv_max3_u32 v22, v22, v92, v93\n" \
    "v_max3_u32 v22, v22, v94, v95\ns_waitcnt vmcnt(10)\nv_mfma_f32_16x16x32_bf16 v[24:27], v[132:135], v[96:99], v[24:27]\n" \
    "buffer_load_dwordx4 v[92:95], v0, s[64:67], s77 offen offset:1024 sc1\nv_mfma_f32_16x16x32_bf16 v[28:31], v[164:167], v[96:99], v[28:31]\nv_max3_u32 v22, v22, v96, v97\n" \
    "v_max3_u32 v22, v22, v98, v99\ns_waitcnt vmcnt(10)\nv_mfma_f32_16x16x32_bf16 v[32:35], v[132:135], v[100:103], v[32:35]\n" \
    "buffer_load_dwordx4 v[96:99], v0, s[64:67], s77 offen offset:2048 sc1\nv_mfma_f32_16x16x32_bf16 v[36:39], v[164:167], v[100:103], v[36:39]\n" \
    "v_max3_u32 v22, v22, v100, v101\nv_max3_u32 v22, v22, v102, v103\ns_waitcnt vmcnt(10)\nv_mfma_f32_16x16x32_bf16 v[24:27], v[136:139], v[104:107], v[24:27]\n" \
    "buffer_load_dwordx4 v[100:103], v0, s[64:67], s77 offen offset:3072 sc1\nv_mfma_f32_16x16x32_bf16 v[28:31], v[168:171], v[104:107], v[28:31]\n" \
    "v_max3_u32 v22, v22, v104, v105\nv_max3_u32 v22, v22, v106, v107\ns_waitcnt vmcnt(10)\nv_mfma_f32_16x16x32_bf16 v[32:35], v[136:139], v[108:111], v[32:35]\n" \
    "v_mfma_f32_16x16x32_bf16 v[36:39], v[168:171], v[108:111], v[36:39]\nv_max3_u32 v22, v22, v108, v109\nv_max3_u32 v22, v22, v110, v111\nv_mov_b32 v17, 1\n" \
    "s_cmp_eq_u32 s56, 3\ns_cselect_b64 exec, 1, 0\nbuffer_atomic_add v17, off, s[64:67], s82 sc0\ns_mov_b64 exec, -1\ns_waitcnt vmcnt(9)\n" \
    "v_mfma_f32_16x16x32_bf16 v[24:27], v[140:143], v[112:115], v[24:27]\nv_mfma_f32_16x16x32_bf16 v[28:31], v[172:175], v[112:115], v[28:31]\nv_max3_u32 v22, v22, v112, v113\n" \
    "v_max3_u32 v22, v22, v114, v115\ns_waitcnt vmcnt(8)\nv_mfma_f32_16x16x32_bf16 v[32:35], v[140:143], v[116:119], v[32:35]\n" \
    "v_mfma_f32_16x16x32_bf16 v[36:39], v[172:175], v[116:119], v[36:39]\nv_max3_u32 v22, v22, v116, v117\nv_max3_u32 v22, v22, v118, v119\ns_waitcnt vmcnt(7)\n" \
    "v_mfma_f32_16x16x32_bf16 v[24:27], v[144:147], v[72:75], v[24:27]\nv_mfma_f32_16x16x32_bf16 v[28:31], v[176:179], v[72:75], v[28:31]\nv_max3_u32 v22, v22, v72, v73\n" \
    "v_max3_u32 v22, v22, v74, v75\ns_waitcnt vmcnt(6)\nv_mfma_f32_16x16x32_bf16 v[32:35], v[144:147], v[76:79], v[32:35]\n" \
    "v_mfma_f32_16x16x32_bf16 v[36:39], v[176:179], v[76:79], v[36:39]\nv_max3_u32 v22, v22, v76, v77\nv_max3_u32 v22, v22, v78, v79\ns_waitcnt vmcnt(5)\n" \
    "v_mfma_f32_16x16x32_bf16 v[24:27], v[148:151], v[80:83], v[24:27]\nv_mfma_f32_16x16x32_bf16 v[28:31], v[180:183], v[80:83], v[28:31]\nv_max3_u32 v22, v22, v80, v81\n" \
    "v_max3_u32 v22, v22, v82, v83\ns_waitcnt vmcnt(4)\nv_mfma_f32_16x16x32_bf16 v[32:35], v[148:151], v[84:87], v[32:35]\n" \
    "v_mfma_f32_16x16x32_bf16 v[36:39], v[180:183], v[84:87], v[36:39]\nv_max3_u32 v22, v22, v84, v85\nv_max3_u32 v22, v22, v86, v87\ns_waitcnt vmcnt(3)\n" \
    "v_mfma_f32_16x16x32_bf16 v[24:27], v[40:43], v[88:91], v[24:27]\nv_mfma_f32_16x16x32_bf16 v[28:31], v[48:51], v[88:91], v[28:31]\nv_max3_u32 v22, v22, v88, v89\n" \
    "v_max3_u32 v22, v22, v90, v91\ns_waitcnt vmcnt(2)\nv_mfma_f32_16x16x32_bf16 v[32:35], v[40:43], v[92:95], v[32:35]\n" \
    "v_mfma_f32_16x16x32_bf16 v[36:39], v[48:51], v[92:95], v[36:39]\nv_max3_u32 v22, v22, v92, v93\nv_max3_u32 v22, v22, v94, v95\ns_waitcnt vmcnt(1)\n" \
    "v_mfma_f32_16x16x32_bf16 v[24:27], v[44:47], v[96:99], v[24:27]\nv_mfma_f32_16x16x32_bf16 v[28:31], v[52:55], v[96:99], v[28:31]\nv_max3_u32 v22, v22, v96, v97\n" \
    "v_max3_u32 v22, v22, v98, v99\ns_waitcnt vmcnt(0)\nv_mfma_f32_16x16x32_bf16 v[32:35], v[44:47], v[100:103], v[32:35]\n" \
    "v_mfma_f32_16x16x32_bf16 v[36:39], v[52:55], v[100:103], v[36:39]\nv_max3_u32 v22, v22, v100, v101\nv_max3_u32 v22, v22, v102, v103\nv_cmp_eq_u32 vcc, -1, v22\n" \
    "s_cbranch_vccnz .Lr129_%=\nds_write_b128 v9, v[24:27] offset:0\nds_write_b128 v9, v[28:31] offset:64\ns_nop 0\nds_write_b128 v9, v[32:35] offset:2304\n" \
    "ds_write_b128 v9, v[36:39] offset:2368\nds_read_b128 v[18:21], v16\ns_waitcnt lgkmcnt(0)\ns_barrier\nds_read_b128 v[40:43], v10 offset:0\n" \
    "ds_read_b128 v[44:47], v10 offset:4608\nds_read_b128 v[48:51], v10 offset:9216\nds_read_b128 v[52:55], v10 offset:13824\nv_lshlrev_b32 v248, 16, v252\n" \
    "v_and_b32 v249, 0xffff0000, v252\nv_lshlrev_b32 v250, 16, v253\nv_and_b32 v251, 0xffff0000, v253\ns_waitcnt lgkmcnt(0)\nv_add_f32 v40, v40, v44\nv_add_f32 v41, v41, v45\n" \
    "v_add_f32 v42, v42, v46\nv_add_f32 v43, v43, v47\nv_add_f32 v48, v48, v52\nv_add_f32 v49, v49, v53\nv_add_f32 v50, v50, v54\nv_add_f32 v51, v51, v55\n" \
    "v_add_f32 v40, v40, v48\nv_add_f32 v41, v41, v49\nv_add_f32 v42, v42, v50\nv_add_f32 v43, v43, v51\nv_fma_f32 v248, v40, v18, v248\nv_fma_f32 v249, v41, v19, v249\n" \
    "v_fma_f32 v250, v42, v20, v250\nv_fma_f32 v251, v43, v21, v251\nv_cvt_pk_bf16_f32 v40, v248, v249\ns_waitcnt vmcnt(0)\nv_readfirstlane_b32 s69, v17\ns_cmp_ge_u32 s69, 31\n" \
    "s_cbranch_scc1 .Lr132_%=\n.Lr133_%=:\nv_cvt_pk_bf16_f32 v41, v250, v251\nv_lshlrev_b32 v248, 16, v40\nv_and_b32 v249, 0xffff0000, v40\nv_lshlrev_b32 v250, 16, v41\n" \
    "v_and_b32 v251, 0xffff0000, v41\nv_add_f32 v18, v248, v249\nv_add_f32 v19, v250, v251\nv_min_u32 v40, -2, v40\nv_min_u32 v41, -2, v41\nv_add_f32 v18, v18, v19\n" \
    "v_permlane32_swap_b32 v40, v42\nv_permlane32_swap_b32 v41, v43\nbuffer_store_dwordx4 v[40:43], v12, s[64:67], s79 offen sc1\n" \
    "v_add_f32_dpp v19, v18, v18 row_ror:8 row_mask:0xf bank_mask:0xf\nv_add_f32_dpp v20, v18, v18 row_ror:8 row_mask:0xf bank_mask:0xf\ns_nop 1\n" \
    "v_permlane16_swap_b32 v19, v20\nv_add_f32 v18, v19, v20\nv_add_f32 v19, v19, v20\ns_nop 1\nv_permlane32_swap_b32 v18, v19\nv_add_f32 v18, v18, v19\n" \
    "v_mul_f32 v18, 0x3d000000, v18\nv_sub_f32 v44, v248, v18\nv_sub_f32 v45, v249, v18\nv_sub_f32 v46, v250, v18\nv_sub_f32 v47, v251, v18\nv_mul_f32 v19, v44, v44\n" \
    "v_fmac_f32 v19, v45, v45\nv_fmac_f32 v19, v46, v46\nv_fmac_f32 v19, v47, v47\nv_min_u32 v18, -2, v18\ns_nop 0\n" \
    "v_add_f32_dpp v20, v19, v19 row_ror:8 row_mask:0xf bank_mask:0xf\nv_add_f32_dpp v21, v19, v19 row_ror:8 row_mask:0xf bank_mask:0xf\ns_nop 1\n" \
    "v_permlane16_swap_b32 v20, v21\nv_add_f32 v19, v20, v21\nv_add_f32 v20, v20, v21\ns_nop 1\nv_permlane32_swap_b32 v19, v20\nv_add_f32 v19, v19, v20\nv_min_u32 v19, -2, v19\n" \
    "buffer_store_dwordx2 v[18:19], v13, s[64:67], s79 offen sc1\ns_barrier\ns_add_u32 s69, s50, 0\ns_and_b32 s69, s69, s41\ns_lshl_b32 s69, s69, 20\ns_add_u32 s69, s69, s32\n" \
    "s_add_u32 s74, s48, s69\ns_lshr_b32 s74, s74, 8\ns_xor_b32 s74, s74, s49\ns_and_b32 s74, s74, 0x2000\ns_add_u32 s74, s74, s69\ns_add_u32 s69, s50, 0\n" \
    "s_and_b32 s69, s69, s41\ns_lshl_b32 s69, s69, 19\ns_add_u32 s69, s69, s33\ns_add_u32 s87, s48, s69\ns_lshr_b32 s87, s87, 8\ns_xor_b32 s87, s87, s49\n" \
    "s_and_b32 s87, s87, 0x2000\ns_add_u32 s87, s87, s69\ns_add_u32 s69, s50, 1\ns_and_b32 s69, s69, s41\ns_lshl_b32 s69, s69, 18\ns_add_u32 s69, s69, s34\n" \
    "s_add_u32 s88, s48, s69\ns_lshr_b32 s88, s88, 8\ns_xor_b32 s88, s88, s49\ns_and_b32 s88, s88, 0x2000\ns_add_u32 s88, s88, s69\ns_xor_b32 s89, s88, 0x1000\n" \
    "s_add_u32 s78, s87, s38\ns_mov_b32 s68, 0\n.Lr5_%=:\nbuffer_load_dword v20, off, s[64:67], s78 sc1\ns_waitcnt vmcnt(0)\nv_readfirstlane_b32 s70, v20\n" \
    "s_memrealtime s[72:73]\ns_cmp_eq_u32 s70, -1\ns_cbranch_scc1 .Lr6_%=\ns_lshr_b32 s71, s70, 8\ns_waitcnt lgkmcnt(0)\ns_sub_u32 s71, s72, s71\ns_and_b32 s71, s71, 0xffffff\n" \
    "s_sub_u32 s70, s72, s71\ns_cmp_eq_u32 s50, 63\ns_cselect_b32 s71, s44, s43\ns_add_u32 s70, s70, s71\n.Lr137_%=:\ns_sleep 1\ns_memrealtime s[72:73]\ns_waitcnt lgkmcnt(0)\n" \
    "s_sub_u32 s72, s72, s70\ns_cmp_lt_i32 s72, 0\ns_cbranch_scc1 .Lr137_%=\ns_mov_b32 s68, 0\ns_add_u32 s69, s50, 0\ns_lshl_b32 s69, s69, 20\ns_add_u32 s69, s69, s36\n" \
    "s_add_u32 s75, s48, s69\ns_lshr_b32 s75, s75, 8\ns_xor_b32 s75, s75, s49\ns_and_b32 s75, s75, 0x2000\ns_add_u32 s75, s75, s69\ns_add_u32 s76, s75, 0x4000\n" \
    "s_add_u32 s77, s75, s37\ns_xor_b32 s79, s75, 0x2000\ns_add_u32 s82, s50, 0\ns_lshl_b32 s80, s82, 16\ns_lshl_b32 s82, s82, 2\ns_add_u32 s82, s82, s40\n" \
    "s_add_u32 s69, s50, 0\ns_and_b32 s69, s69, s41\ns_lshl_b32 s69, s69, 20\ns_add_u32 s69, s69, s32\ns_add_u32 s74, s48, s69\ns_lshr_b32 s74, s74, 8\ns_xor_b32 s74, s74, s49\n" \
    "s_and_b32 s74, s74, 0x2000\ns_add_u32 s74, s74, s69\n.Lr138_%=:\nbuffer_load_dwordx4 v[56:59], v0, s[64:67], s74 offen offset:0 sc1\n" \
    "buffer_load_dwordx4 v[60:63], v0, s[64:67], s74 offen offset:1024 sc1\ns_add_u32 s69, s74, 0x4000\nbuffer_load_dwordx4 v[64:67], v0, s[64:67], s69 offen offset:0 sc1\n" \
    "buffer_load_dwordx4 v[68:71], v0, s[64:67], s69 offen offset:1024 sc1\nbuffer_load_dwordx2 v[252:253], v11, s[60:63], s80 offen\n" \
    "buffer_load_dwordx4 v[72:75], v0, s[64:67], s75 offen offset:0 sc1\nbuffer_load_dwordx4 v[76:79], v0, s[64:67], s75 offen offset:1024 sc1\n" \
    "buffer_load_dwordx4 v[80:83], v0, s[64:67], s75 offen offset:2048 sc1\nbuffer_load_dwordx4 v[84:87], v0, s[64:67], s75 offen offset:3072 sc1\n" \
    "buffer_load_dwordx4 v[88:91], v1, s[64:67], s75 offen offset:0 sc1\nbuffer_load_dwordx4 v[92:95], v1, s[64:67], s75 offen offset:1024 sc1\n" \
    "buffer_load_dwordx4 v[96:99], v1, s[64:67], s75 offen offset:2048 sc1\nbuffer_load_dwordx4 v[100:103], v1, s[64:67], s75 offen offset:3072 sc1\n" \
    "buffer_load_dwordx4 v[104:107], v0, s[64:67], s76 offen offset:0 sc1\nbuffer_load_dwordx4 v[108:111], v0, s[64:67], s76 offen offset:1024 sc1\n" \
    "buffer_load_dwordx4 v[112:115], v0, s[64:67], s76 offen offset:2048 sc1\n.Lr139_%=:\nv_mov_b32 v22, 0\ns_waitcnt vmcnt(10)\n" \
    "v_mfma_f32_16x16x32_bf16 v[24:27], v[184:187], v[72:75], 0\nbuffer_load_dwordx4 v[116:119], v0, s[64:67], s76 offen offset:3072 sc1\n" \
    "v_mfma_f32_16x16x32_bf16 v[28:31], v[216:219], v[72:75], 0\nv_max3_u32 v22, v22, v72, v73\nv_max3_u32 v22, v22, v74, v75\ns_waitcnt vmcnt(10)\n" \
    "v_mfma_f32_16x16x32_bf16 v[32:35], v[184:187], v[76:79], 0\nbuffer_load_dwordx4 v[72:75], v1, s[64:67], s76 offen offset:0 sc1\n" \
    "v_mfma_f32_16x16x32_bf16 v[36:39], v[216:219], v[76:79], 0\nv_max3_u32 v22, v22, v76, v77\nv_max3_u32 v22, v22, v78, v79\ns_waitcnt vmcnt(10)\n" \
    "v_mfma_f32_16x16x32_bf16 v[24:27], v[188:191], v[80:83], v[24:27]\nbuffer_load_dwordx4 v[76:79], v1, s[64:67], s76 offen offset:1024 sc1\n" \
    "v_mfma_f32_16x16x32_bf16 v[28:31], v[220:223], v[80:83], v[28:31]\nv_max3_u32 v22, v22, v80, v81\nv_max3_u32 v22, v22, v82, v83\ns_waitcnt vmcnt(10)\n" \
    "v_mfma_f32_16x16x32_bf16 v[32:35], v[188:191], v[84:87], v[32:35]\nbuffer_load_dwordx4 v[80:83], v1, s[64:67], s76 offen offset:2048 sc1\n" \
    "v_mfma_f32_16x16x32_bf16 v[36:39], v[220:223], v[84:87], v[36:39]\nv_max3_u32 v22, v22, v84, v85\nv_max3_u32 v22, v22, v86, v87\ns_waitcnt vmcnt(10)\n" \
    "v_mfma_f32_16x16x32_bf16 v[24:27], v[192:195], v[88:91], v[24:27]\nbuffer_load_dwordx4 v[84:87], v1, s[64:67], s76 offen offset:3072 sc1\n" \
    "v_mfma_f32_16x16x32_bf16 v[28:31], v[224:227], v[88:91], v[28:31]\nv_max3_u32 v22, v22, v88, v89\nv_max3_u32 v22, v22, v90, v91\ns_waitcnt vmcnt(10)\n" \
    "v_mfma_f32_16x16x32_bf16 v[32:35], v[192:195], v[92:95], v[32:35]\nbuffer_load_dwordx4 v[88:91], v0, s[64:67], s77 offen offset:0 sc1\n" \
    "v_mfma_f32_16x16x32_bf16 v[36:39], v[224:227], v[92:95], v[36:39]\nv_max3_u32 v22, v22, v92, v93\nv_max3_u32 v22, v22, v94, v95\ns_waitcnt vmcnt(10)\n" \
    "v_mfma_f32_16x16x32_bf16 v[24:27], v[196:199], v[96:99], v[24:27]\nbuffer_load_dwordx4 v[92:95], v0, s[64:67], s77 offen offset:1024 sc1\n" \
    "v_mfma_f32_16x16x32_bf16 v[28:31], v[228:231], v[96:99], v[28:31]\nv_max3_u32 v22, v22, v96, v97\nv_max3_u32 v22, v22, v98, v99\ns_waitcnt vmcnt(10)\n" \
    "v_mfma_f32_16x16x32_bf16 v[32:35], v[196:199], v[100:103], v[32:35]\nbuffer_load_dwordx4 v[96:99], v0, s[64:67], s77 offen offset:2048 sc1\n" \
    "v_mfma_f32_16x16x32_bf16 v[36:39], v[228:231], v[100:103], v[36:39]\nv_max3_u32 v22, v22, v100, v101\nv_max3_u32 v22, v22, v102, v103\ns_waitcnt vmcnt(10)\n" \
    "v_mfma_f32_16x16x32_bf16 v[24:27], v[200:203], v[104:107], v[24:27]\nbuffer_load_dwordx4 v[100:103], v0, s[64:67], s77 offen offset:3072 sc1\n" \
    "v_mfma_f32_16x16x32_bf16 v[28:31], v[232:235], v[104:107], v[28:31]\nv_max3_u32 v22, v22, v104, v105\nv_max3_u32 v22, v22, v106, v107\ns_waitcnt vmcnt(10)\n" \
    "v_mfma_f32_16x16x32_bf16 v[32:35], v[200:203], v[108:111], v[32:35]\nv_mfma_f32_16x16x32_bf16 v[36:39], v[232:235], v[108:111], v[36:39]\nv_max3_u32 v22, v22, v108, v109\n" \
    "v_max3_u32 v22, v22, v110, v111\nv_mov_b32 v17, 1\ns_cmp_eq_u32 s56, 3\ns_cselect_b64 exec, 1, 0\nbuffer_atomic_add v17, off, s[64:67], s82 sc0\ns_mov_b64 exec, -1\n" \
    "s_waitcnt vmcnt(9)\nv_mfma_f32_16x16x32_bf16 v[24:27], v[204:207], v[112:115], v[24:27]\nv_mfma_f32_16x16x32_bf16 v[28:31], v[236:239], v[112:115], v[28:31]\n" \
    "v_max3_u32 v22, v22, v112, v113\nv_max3_u32 v22, v22, v114, v115\ns_waitcnt vmcnt(8)\nv_mfma_f32_16x16x32_bf16 v[32:35], v[204:207], v[116:119], v[32:35]\n" \
    "v_mfma_f32_16x16x32_bf16 v[36:39], v[236:239], v[116:119], v[36:39]\nv_max3_u32 v22, v22, v116, v117\nv_max3_u32 v22, v22, v118, v119\ns_waitcnt vmcnt(7)\n" \
    "v_mfma_f32_16x16x32_bf16 v[24:27], v[208:211], v[72:75], v[24:27]\nv_mfma_f32_16x16x32_bf16 v[28:31], v[240:243], v[72:75], v[28:31]\nv_max3_u32 v22, v22, v72, v73\n" \
    "v_max3_u32 v22, v22, v74, v75\ns_waitcnt vmcnt(6)\nv_mfma_f32_16x16x32_bf16 v[32:35], v[208:211], v[76:79], v[32:35]\n" \
    "v_mfma_f32_16x16x32_bf16 v[36:39], v[240:243], v[76:79], v[36:39]\nv_max3_u32 v22, v22, v76, v77\nv_max3_u32 v22, v22, v78, v79\ns_waitcnt vmcnt(5)\n" \
    "v_mfma_f32_16x16x32_bf16 v[24:27], v[212:215], v[80:83], v[24:27]\nv_mfma_f32_16x16x32_bf16 v[28:31], v[244:247], v[80:83], v[28:31]\nv_max3_u32 v22, v22, v80, v81\n" \
    "v_max3_u32 v22, v22, v82, v83\ns_waitcnt vmcnt(4)\nv_mfma_f32_16x16x32_bf16 v[32:35], v[212:215], v[84:87], v[32:35]\n" \
    "v_mfma_f32_16x16x32_bf16 v[36:39], v[244:247], v[84:87], v[36:39]\nv_max3_u32 v22, v22, v84, v85\nv_max3_u32 v22, v22, v86, v87\nv_max3_u32 v22, v22, v56, v57\n" \
    "v_max3_u32 v22, v22, v58, v59\nv_max3_u32 v22, v22, v60, v61\nv_max3_u32 v22, v22, v62, v63\nv_max3_u32 v22, v22, v64, v65\nv_max3_u32 v22, v22, v66, v67\n" \
    "v_max3_u32 v22, v22, v68, v69\nv_max3_u32 v22, v22, v70, v71\ns_waitcnt vmcnt(3)\nv_mfma_f32_16x16x32_bf16 v[24:27], v[56:59], v[88:91], v[24:27]\n" \
    "v_mfma_f32_16x16x32_bf16 v[28:31], v[64:67], v[88:91], v[28:31]\nv_max3_u32 v22, v22, v88, v89\nv_max3_u32 v22, v22, v90, v91\ns_waitcnt vmcnt(2)\n" \
    "v_mfma_f32_16x16x32_bf16 v[32:35], v[56:59], v[92:95], v[32:35]\nv_mfma_f32_16x16x32_bf16 v[36:39], v[64:67], v[92:95], v[36:39]\nv_max3_u32 v22, v22, v92, v93\n" \
    "v_max3_u32 v22, v22, v94, v95\ns_waitcnt vmcnt(1)\nv_mfma_f32_16x16x32_bf16 v[24:27], v[60:63], v[96:99], v[24:27]\n" \
    "v_mfma_f32_16x16x32_bf16 v[28:31], v[68:71], v[96:99], v[28:31]\nv_max3_u32 v22, v22, v96, v97\nv_max3_u32 v22, v22, v98, v99\ns_waitcnt vmcnt(0)\n" \
    "v_mfma_f32_16x16x32_bf16 v[32:35], v[60:63], v[100:103], v[32:35]\nv_mfma_f32_16x16x32_bf16 v[36:39], v[68:71], v[100:103], v[36:39]\nv_max3_u32 v22, v22, v100, v101\n" \
    "v_max3_u32 v22, v22, v102, v103\nv_cmp_eq_u32 vcc, -1, v22\ns_cbranch_vccnz .Lr140_%=\nds_write_b128 v9, v[24:27] offset:0\nds_write_b128 v9, v[28:31] offset:64\ns_nop 0\n" \
    "ds_write_b128 v9, v[32:35] offset:2304\nds_write_b128 v9, v[36:39] offset:2368\nds_read_b128 v[18:21], v16\ns_waitcnt lgkmcnt(0)\ns_barrier\n" \
    "ds_read_b128 v[56:59], v10 offset:0\nds_read_b128 v[60:63], v10 offset:4608\nds_read_b128 v[64:67], v10 offset:9216\nds_read_b128 v[68:71], v10 offset:13824\n" \
    "v_lshlrev_b32 v248, 16, v252\nv_and_b32 v249, 0xffff0000, v252\nv_lshlrev_b32 v250, 16, v253\nv_and_b32 v251, 0xffff0000, v253\ns_waitcnt lgkmcnt(0)\n" \
    "v_add_f32 v56, v56, v60\nv_add_f32 v57, v57, v61\nv_add_f32 v58, v58, v62\nv_add_f32 v59, v59, v63\nv_add_f32 v64, v64, v68\nv_add_f32 v65, v65, v69\n" \
    "v_add_f32 v66, v66, v70\nv_add_f32 v67, v67, v71\nv_add_f32 v56, v56, v64\nv_add_f32 v57, v57, v65\nv_add_f32 v58, v58, v66\nv_add_f32 v59, v59, v67\n" \
    "v_fma_f32 v248, v56, v18, v248\nv_fma_f32 v249, v57, v19, v249\nv_fma_f32 v250, v58, v20, v250\nv_fma_f32 v251, v59, v21, v251\nv_cvt_pk_bf16_f32 v56, v248, v249\n" \
    "s_waitcnt vmcnt(0)\nv_readfirstlane_b32 s69, v17\ns_cmp_ge_u32 s69, 31\ns_cbranch_scc1 .Lr143_%=\n.Lr144_%=:\nv_cvt_pk_bf16_f32 v57, v250, v251\n" \
    "v_lshlrev_b32 v248, 16, v56\nv_and_b32 v249, 0xffff0000, v56\nv_lshlrev_b32 v250, 16, v57\nv_and_b32 v251, 0xffff0000, v57\nv_add_f32 v18, v248, v249\n" \
    "v_add_f32 v19, v250, v251\nv_min_u32 v56, -2, v56\nv_min_u32 v57, -2, v57\nv_add_f32 v18, v18, v19\nv_permlane32_swap_b32 v56, v58\nv_permlane32_swap_b32 v57, v59\n" \
    "buffer_store_dwordx4 v[56:59], v12, s[64:67], s79 offen sc1\nv_add_f32_dpp v19, v18, v18 row_ror:8 row_mask:0xf bank_mask:0xf\n" \
    "v_add_f32_dpp v20, v18, v18 row_ror:8 row_mask:0xf bank_mask:0xf\ns_nop 1\nv_permlane16_swap_b32 v19, v20\nv_add_f32 v18, v19, v20\nv_add_f32 v19, v19, v20\ns_nop 1\n" \
    "v_permlane32_swap_b32 v18, v19\nv_add_f32 v18, v18, v19\nv_mul_f32 v18, 0x3d000000, v18\nv_sub_f32 v60, v248, v18\nv_sub_f32 v61, v249, v18\nv_sub_f32 v62, v250, v18\n" \
    "v_sub_f32 v63, v251, v18\nv_mul_f32 v19, v60, v60\nv_fmac_f32 v19, v61, v61\nv_fmac_f32 v19, v62, v62\nv_fmac_f32 v19, v63, v63\nv_min_u32 v18, -2, v18\ns_nop 0\n" \
    "v_add_f32_dpp v20, v19, v19 row_ror:8 row_mask:0xf bank_mask:0xf\nv_add_f32_dpp v21, v19, v19 row_ror:8 row_mask:0xf bank_mask:0xf\ns_nop 1\n" \
    "v_permlane16_swap_b32 v20, v21\nv_add_f32 v19, v20, v21\nv_add_f32 v20, v20, v21\ns_nop 1\nv_permlane32_swap_b32 v19, v20\nv_add_f32 v19, v19, v20\nv_min_u32 v19, -2, v19\n" \
    "buffer_store_dwordx2 v[18:19], v13, s[64:67], s79 offen sc1\ns_barrier\ns_branch .Lr2_%=\n.Lr10_%=:\ns_sleep 3\ns_add_u32 s68, s68, 1\ns_and_b32 s69, s68, 63\n" \
    "s_cbranch_scc1 .Lr8_%=\ns_cmp_gt_u32 s68, 0x200000\ns_cbranch_scc1 .Lr1_%=\nbuffer_load_dword v21, off, s[64:67], 0 sc1\ns_waitcnt vmcnt(0)\nv_readfirstlane_b32 s69, v21\n" \
    "s_cmp_eq_u32 s69, 1\ns_cbranch_scc0 .Lr8_%=\ns_endpgm\n.Lr19_%=:\ns_waitcnt lgkmcnt(0)\n.Lr20_%=:\ns_add_u32 s51, s51, 20\ns_sub_u32 s70, s51, s72\ns_cmp_lt_i32 s70, 52\n" \
    "s_cbranch_scc1 .Lr20_%=\ns_add_u32 s68, s68, 1\ns_and_b32 s69, s68, 63\ns_cbranch_scc1 .Lr18_%=\ns_cmp_gt_u32 s68, 0x200000\ns_cbranch_scc1 .Lr1_%=\n" \
    "buffer_load_dword v21, off, s[64:67], 0 sc1\ns_waitcnt vmcnt(0)\nv_readfirstlane_b32 s69, v21\ns_cmp_eq_u32 s69, 1\ns_cbranch_scc0 .Lr18_%=\ns_endpgm\n.Lr23_%=:\n" \
    "v_max3_u32 v22, v40, v41, v42\nv_max3_u32 v22, v22, v43, v44\nv_max3_u32 v22, v22, v45, v46\nv_max3_u32 v22, v22, v47, v48\nv_max3_u32 v22, v22, v49, v50\n" \
    "v_max3_u32 v22, v22, v51, v52\nv_max3_u32 v22, v22, v53, v54\nv_max_u32 v22, v22, v55\nv_cmp_eq_u32 vcc, -1, v22\ns_add_u32 s51, s51, s53\ns_xor_b32 s87, s87, 0x1000\n" \
    "s_add_u32 s68, s68, 1\ns_and_b32 s69, s68, 63\ns_cbranch_scc1 .Lr24_%=\ns_cmp_gt_u32 s68, 0x200000\ns_cbranch_scc1 .Lr1_%=\nbuffer_load_dword v21, off, s[64:67], 0 sc1\n" \
    "s_waitcnt vmcnt(0)\nv_readfirstlane_b32 s69, v21\ns_cmp_eq_u32 s69, 1\ns_cbranch_scc0 .Lr24_%=\ns_endpgm\n.Lr24_%=:\ns_cbranch_vccz .Lr25_%=\ns_sub_u32 s70, s51, s54\n" \
    ".Lr31_%=:\ns_memrealtime s[72:73]\ns_waitcnt lgkmcnt(0)\ns_sub_u32 s72, s72, s70\ns_cmp_lt_i32 s72, 0\ns_cbranch_scc1 .Lr31_%=\n" \
    "buffer_load_dwordx4 v[40:43], v0, s[64:67], s74 offen offset:0 sc1\nbuffer_load_dwordx4 v[44:47], v0, s[64:67], s74 offen offset:1024 sc1\ns_add_u32 s69, s74, 0x4000\n" \
    "buffer_load_dwordx4 v[48:51], v0, s[64:67], s69 offen offset:0 sc1\nbuffer_load_dwordx4 v[52:55], v0, s[64:67], s69 offen offset:1024 sc1\n.Lr25_%=:\n.Lr32_%=:\n" \
    "s_memrealtime s[72:73]\ns_waitcnt lgkmcnt(0)\ns_sub_u32 s72, s72, s51\ns_cmp_lt_i32 s72, -3\ns_cbranch_scc1 .Lr32_%=\ns_cmp_ge_i32 s72, 0\ns_cbranch_scc1 .Lr33_%=\n" \
    "s_nop 2\ns_cmp_ge_i32 s72, -1\ns_cbranch_scc1 .Lr33_%=\ns_nop 2\ns_cmp_ge_i32 s72, -2\ns_cbranch_scc1 .Lr33_%=\ns_nop 2\n.Lr33_%=:\n" \
    "buffer_load_dwordx2 v[116:117], v5, s[64:67], s87 offen offset:0 sc1\ns_nop 5\nbuffer_load_dwordx2 v[118:119], v5, s[64:67], s87 offen offset:128 sc1\ns_nop 5\n" \
    "buffer_load_dwordx4 v[100:103], v0, s[64:67], s87 offen offset:0 sc1\ns_nop 10\nbuffer_load_dwordx4 v[104:107], v0, s[64:67], s87 offen offset:1024 sc1\ns_nop 10\n" \
    "buffer_load_dwordx4 v[108:111], v0, s[64:67], s87 offen offset:2048 sc1\ns_nop 10\nbuffer_load_dwordx4 v[112:115], v0, s[64:67], s87 offen offset:3072 sc1\n" \
    "s_branch .Lr22_%=\n.Lr36_%=:\ns_sleep 3\ns_add_u32 s68, s68, 1\ns_and_b32 s69, s68, 63\ns_cbranch_scc1 .Lr34_%=\ns_cmp_gt_u32 s68, 0x200000\ns_cbranch_scc1 .Lr1_%=\n" \
    "buffer_load_dword v21, off, s[64:67], 0 sc1\ns_waitcnt vmcnt(0)\nv_readfirstlane_b32 s69, v21\ns_cmp_eq_u32 s69, 1\ns_cbranch_scc0 .Lr34_%=\ns_endpgm\n.Lr48_%=:\n" \
    "s_waitcnt lgkmcnt(0)\n.Lr49_%=:\ns_add_u32 s51, s51, s53\ns_sub_u32 s70, s51, s72\ns_cmp_lt_i32 s70, 52\ns_cbranch_scc1 .Lr49_%=\ns_add_u32 s68, s68, 1\n" \
    "s_and_b32 s69, s68, 63\ns_cbranch_scc1 .Lr47_%=\ns_cmp_gt_u32 s68, 0x200000\ns_cbranch_scc1 .Lr1_%=\nbuffer_load_dword v21, off, s[64:67], 0 sc1\ns_waitcnt vmcnt(0)\n" \
    "v_readfirstlane_b32 s69, v21\ns_cmp_eq_u32 s69, 1\ns_cbranch_scc0 .Lr47_%=\ns_endpgm\n.Lr52_%=:\nv_max3_u32 v22, v56, v57, v58\nv_max3_u32 v22, v22, v59, v60\n" \
    "v_max3_u32 v22, v22, v61, v62\nv_max3_u32 v22, v22, v63, v64\nv_max3_u32 v22, v22, v65, v66\nv_max3_u32 v22, v22, v67, v68\nv_max3_u32 v22, v22, v69, v70\n" \
    "v_max_u32 v22, v22, v71\nv_cmp_eq_u32 vcc, -1, v22\ns_add_u32 s51, s51, s53\ns_xor_b32 s87, s87, 0x1000\ns_add_u32 s68, s68, 1\ns_and_b32 s69, s68, 63\n" \
    "s_cbranch_scc1 .Lr53_%=\ns_cmp_gt_u32 s68, 0x200000\ns_cbranch_scc1 .Lr1_%=\nbuffer_load_dword v21, off, s[64:67], 0 sc1\ns_waitcnt vmcnt(0)\nv_readfirstlane_b32 s69, v21\n" \
    "s_cmp_eq_u32 s69, 1\ns_cbranch_scc0 .Lr53_%=\ns_endpgm\n.Lr53_%=:\ns_cbranch_vccz .Lr54_%=\ns_sub_u32 s70, s51, s54\n.Lr60_%=:\ns_memrealtime s[72:73]\n" \
    "s_waitcnt lgkmcnt(0)\ns_sub_u32 s72, s72, s70\ns_cmp_lt_i32 s72, 0\ns_cbranch_scc1 .Lr60_%=\nbuffer_load_dwordx4 v[56:59], v0, s[64:67], s74 offen offset:0 sc1\n" \
    "buffer_load_dwordx4 v[60:63], v0, s[64:67], s74 offen offset:1024 sc1\ns_add_u32 s69, s74, 0x4000\nbuffer_load_dwordx4 v[64:67], v0, s[64:67], s69 offen offset:0 sc1\n" \
    "buffer_load_dwordx4 v[68:71], v0, s[64:67], s69 offen offset:1024 sc1\n.Lr54_%=:\n.Lr61_%=:\ns_memrealtime s[72:73]\ns_waitcnt lgkmcnt(0)\ns_sub_u32 s72, s72, s51\n" \
    "s_cmp_lt_i32 s72, -3\ns_cbranch_scc1 .Lr61_%=\ns_cmp_ge_i32 s72, 0\ns_cbranch_scc1 .Lr62_%=\ns_nop 2\ns_cmp_ge_i32 s72, -1\ns_cbranch_scc1 .Lr62_%=\ns_nop 2\n" \
    "s_cmp_ge_i32 s72, -2\ns_cbranch_scc1 .Lr62_%=\ns_nop 2\n.Lr62_%=:\nbuffer_load_dwordx2 v[116:117], v5, s[64:67], s87 offen offset:0 sc1\ns_nop 5\n" \
    "buffer_load_dwordx2 v[118:119], v5, s[64:67], s87 offen offset:128 sc1\ns_nop 5\nbuffer_load_dwordx4 v[100:103], v0, s[64:67], s87 offen offset:0 sc1\ns_nop 10\n" \
    "buffer_load_dwordx4 v[104:107], v0, s[64:67], s87 offen offset:1024 sc1\ns_nop 10\nbuffer_load_dwordx4 v[108:111], v0, s[64:67], s87 offen offset:2048 sc1\ns_nop 10\n" \
    "buffer_load_dwordx4 v[112:115], v0, s[64:67], s87 offen offset:3072 sc1\ns_branch .Lr51_%=\n.Lr72_%=:\ns_sleep 3\ns_add_u32 s68, s68, 1\ns_and_b32 s69, s68, 63\n" \
    "s_cbranch_scc1 .Lr70_%=\ns_cmp_gt_u32 s68, 0x200000\ns_cbranch_scc1 .Lr1_%=\nbuffer_load_dword v21, off, s[64:67], 0 sc1\ns_waitcnt vmcnt(0)\nv_readfirstlane_b32 s69, v21\n" \
    "s_cmp_eq_u32 s69, 1\ns_cbranch_scc0 .Lr70_%=\ns_endpgm\n.Lr75_%=:\ns_sleep 3\ns_add_u32 s68, s68, 1\ns_and_b32 s69, s68, 63\ns_cbranch_scc1 .Lr73_%=\n" \
    "s_cmp_gt_u32 s68, 0x200000\ns_cbranch_scc1 .Lr1_%=\nbuffer_load_dword v21, off, s[64:67], 0 sc1\ns_waitcnt vmcnt(0)\nv_readfirstlane_b32 s69, v21\ns_cmp_eq_u32 s69, 1\n" \
    "s_cbranch_scc0 .Lr73_%=\ns_endpgm\n.Lr65_%=:\ns_waitcnt vmcnt(0)\ns_sleep 4\ns_add_u32 s68, s68, 1\ns_and_b32 s69, s68, 63\ns_cbranch_scc1 .Lr63_%=\n" \
    "s_cmp_gt_u32 s68, 0x200000\ns_cbranch_scc1 .Lr1_%=\nbuffer_load_dword v21, off, s[64:67], 0 sc1\ns_waitcnt vmcnt(0)\nv_readfirstlane_b32 s69, v21\ns_cmp_eq_u32 s69, 1\n" \
    "s_cbranch_scc0 .Lr63_%=\ns_endpgm\n.Lr80_%=:\ns_waitcnt lgkmcnt(0)\n.Lr81_%=:\ns_add_u32 s51, s51, s53\ns_sub_u32 s70, s51, s72\ns_cmp_lt_i32 s70, 52\n" \
    "s_cbranch_scc1 .Lr81_%=\ns_add_u32 s68, s68, 1\ns_and_b32 s69, s68, 63\ns_cbranch_scc1 .Lr79_%=\ns_cmp_gt_u32 s68, 0x200000\ns_cbranch_scc1 .Lr1_%=\n" \
    "buffer_load_dword v21, off, s[64:67], 0 sc1\ns_waitcnt vmcnt(0)\nv_readfirstlane_b32 s69, v21\ns_cmp_eq_u32 s69, 1\ns_cbranch_scc0 .Lr79_%=\ns_endpgm\n.Lr84_%=:\n" \
    "v_max3_u32 v22, v40, v41, v42\nv_max3_u32 v22, v22, v43, v44\nv_max3_u32 v22, v22, v45, v46\nv_max3_u32 v22, v22, v47, v48\nv_max3_u32 v22, v22, v49, v50\n" \
    "v_max3_u32 v22, v22, v51, v52\nv_max3_u32 v22, v22, v53, v54\nv_max_u32 v22, v22, v55\nv_cmp_eq_u32 vcc, -1, v22\ns_add_u32 s51, s51, s53\ns_xor_b32 s87, s87, 0x1000\n" \
    "s_add_u32 s68, s68, 1\ns_and_b32 s69, s68, 63\ns_cbranch_scc1 .Lr85_%=\ns_cmp_gt_u32 s68, 0x200000\ns_cbranch_scc1 .Lr1_%=\nbuffer_load_dword v21, off, s[64:67], 0 sc1\n" \
    "s_waitcnt vmcnt(0)\nv_readfirstlane_b32 s69, v21\ns_cmp_eq_u32 s69, 1\ns_cbranch_scc0 .Lr85_%=\ns_endpgm\n.Lr85_%=:\ns_cbranch_vccz .Lr86_%=\ns_sub_u32 s70, s51, s54\n" \
    ".Lr92_%=:\ns_memrealtime s[72:73]\ns_waitcnt lgkmcnt(0)\ns_sub_u32 s72, s72, s70\ns_cmp_lt_i32 s72, 0\ns_cbranch_scc1 .Lr92_%=\n" \
    "buffer_load_dwordx4 v[40:43], v0, s[64:67], s74 offen offset:0 sc1\nbuffer_load_dwordx4 v[44:47], v0, s[64:67], s74 offen offset:1024 sc1\ns_add_u32 s69, s74, 0x4000\n" \
    "buffer_load_dwordx4 v[48:51], v0, s[64:67], s69 offen offset:0 sc1\nbuffer_load_dwordx4 v[52:55], v0, s[64:67], s69 offen offset:1024 sc1\n.Lr86_%=:\n.Lr93_%=:\n" \
    "s_memrealtime s[72:73]\ns_waitcnt lgkmcnt(0)\ns_sub_u32 s72, s72, s51\ns_cmp_lt_i32 s72, -3\ns_cbranch_scc1 .Lr93_%=\ns_cmp_ge_i32 s72, 0\ns_cbranch_scc1 .Lr94_%=\n" \
    "s_nop 2\ns_cmp_ge_i32 s72, -1\ns_cbranch_scc1 .Lr94_%=\ns_nop 2\ns_cmp_ge_i32 s72, -2\ns_cbranch_scc1 .Lr94_%=\ns_nop 2\n.Lr94_%=:\n" \
    "buffer_load_dwordx2 v[116:117], v5, s[64:67], s87 offen offset:0 sc1\ns_nop 5\nbuffer_load_dwordx2 v[118:119], v5, s[64:67], s87 offen offset:128 sc1\ns_nop 5\n" \
    "buffer_load_dwordx4 v[100:103], v0, s[64:67], s87 offen offset:0 sc1\ns_nop 10\nbuffer_load_dwordx4 v[104:107], v0, s[64:67], s87 offen offset:1024 sc1\ns_nop 10\n" \
    "buffer_load_dwordx4 v[108:111], v0, s[64:67], s87 offen offset:2048 sc1\ns_nop 10\nbuffer_load_dwordx4 v[112:115], v0, s[64:67], s87 offen offset:3072 sc1\n" \
    "s_branch .Lr83_%=\n.Lr104_%=:\ns_sleep 3\ns_add_u32 s68, s68, 1\ns_and_b32 s69, s68, 63\ns_cbranch_scc1 .Lr102_%=\ns_cmp_gt_u32 s68, 0x200000\ns_cbranch_scc1 .Lr1_%=\n" \
    "buffer_load_dword v21, off, s[64:67], 0 sc1\ns_waitcnt vmcnt(0)\nv_readfirstlane_b32 s69, v21\ns_cmp_eq_u32 s69, 1\ns_cbranch_scc0 .Lr102_%=\ns_endpgm\n.Lr107_%=:\n" \
    "s_sleep 3\ns_add_u32 s68, s68, 1\ns_and_b32 s69, s68, 63\ns_cbranch_scc1 .Lr105_%=\ns_cmp_gt_u32 s68, 0x200000\ns_cbranch_scc1 .Lr1_%=\n" \
    "buffer_load_dword v21, off, s[64:67], 0 sc1\ns_waitcnt vmcnt(0)\nv_readfirstlane_b32 s69, v21\ns_cmp_eq_u32 s69, 1\ns_cbranch_scc0 .Lr105_%=\ns_endpgm\n.Lr97_%=:\n" \
    "s_waitcnt vmcnt(0)\ns_sleep 4\ns_add_u32 s68, s68, 1\ns_and_b32 s69, s68, 63\ns_cbranch_scc1 .Lr95_%=\ns_cmp_gt_u32 s68, 0x200000\ns_cbranch_scc1 .Lr1_%=\n" \
    "buffer_load_dword v21, off, s[64:67], 0 sc1\ns_waitcnt vmcnt(0)\nv_readfirstlane_b32 s69, v21\ns_cmp_eq_u32 s69, 1\ns_cbranch_scc0 .Lr95_%=\ns_endpgm\n.Lr112_%=:\n" \
    "s_waitcnt lgkmcnt(0)\n.Lr113_%=:\ns_add_u32 s51, s51, s53\ns_sub_u32 s70, s51, s72\ns_cmp_lt_i32 s70, 52\ns_cbranch_scc1 .Lr113_%=\ns_add_u32 s68, s68, 1\n" \
    "s_and_b32 s69, s68, 63\ns_cbranch_scc1 .Lr111_%=\ns_cmp_gt_u32 s68, 0x200000\ns_cbranch_scc1 .Lr1_%=\nbuffer_load_dword v21, off, s[64:67], 0 sc1\ns_waitcnt vmcnt(0)\n" \
    "v_readfirstlane_b32 s69, v21\ns_cmp_eq_u32 s69, 1\ns_cbranch_scc0 .Lr111_%=\ns_endpgm\n.Lr116_%=:\nv_max3_u32 v22, v56, v57, v58\nv_max3_u32 v22, v22, v59, v60\n" \
    "v_max3_u32 v22, v22, v61, v62\nv_max3_u32 v22, v22, v63, v64\nv_max3_u32 v22, v22, v65, v66\nv_max3_u32 v22, v22, v67, v68\nv_max3_u32 v22, v22, v69, v70\n" \
    "v_max_u32 v22, v22, v71\nv_cmp_eq_u32 vcc, -1, v22\ns_add_u32 s51, s51, s53\ns_xor_b32 s87, s87, 0x1000\ns_add_u32 s68, s68, 1\ns_and_b32 s69, s68, 63\n" \
    "s_cbranch_scc1 .Lr117_%=\ns_cmp_gt_u32 s68, 0x200000\ns_cbranch_scc1 .Lr1_%=\nbuffer_load_dword v21, off, s[64:67], 0 sc1\ns_waitcnt vmcnt(0)\n" \
    "v_readfirstlane_b32 s69, v21\ns_cmp_eq_u32 s69, 1\ns_cbranch_scc0 .Lr117_%=\ns_endpgm\n.Lr117_%=:\ns_cbranch_vccz .Lr118_%=\ns_sub_u32 s70, s51, s54\n.Lr124_%=:\n" \
    "s_memrealtime s[72:73]\ns_waitcnt lgkmcnt(0)\ns_sub_u32 s72, s72, s70\ns_cmp_lt_i32 s72, 0\ns_cbranch_scc1 .Lr124_%=\n" \
    "buffer_load_dwordx4 v[56:59], v0, s[64:67], s74 offen offset:0 sc1\nbuffer_load_dwordx4 v[60:63], v0, s[64:67], s74 offen offset:1024 sc1\ns_add_u32 s69, s74, 0x4000\n" \
    "buffer_load_dwordx4 v[64:67], v0, s[64:67], s69 offen offset:0 sc1\nbuffer_load_dwordx4 v[68:71], v0, s[64:67], s69 offen offset:1024 sc1\n.Lr118_%=:\n.Lr125_%=:\n" \
    "s_memrealtime s[72:73]\ns_waitcnt lgkmcnt(0)\ns_sub_u32 s72, s72, s51\ns_cmp_lt_i32 s72, -3\ns_cbranch_scc1 .Lr125_%=\ns_cmp_ge_i32 s72, 0\ns_cbranch_scc1 .Lr126_%=\n" \
    "s_nop 2\ns_cmp_ge_i32 s72, -1\ns_cbranch_scc1 .Lr126_%=\ns_nop 2\ns_cmp_ge_i32 s72, -2\ns_cbranch_scc1 .Lr126_%=\ns_nop 2\n.Lr126_%=:\n" \
    "buffer_load_dwordx2 v[116:117], v5, s[64:67], s87 offen offset:0 sc1\ns_nop 5\nbuffer_load_dwordx2 v[118:119], v5, s[64:67], s87 offen offset:128 sc1\ns_nop 5\n" \
    "buffer_load_dwordx4 v[100:103], v0, s[64:67], s87 offen offset:0 sc1\ns_nop 10\nbuffer_load_dwordx4 v[104:107], v0, s[64:67], s87 offen offset:1024 sc1\ns_nop 10\n" \
    "buffer_load_dwordx4 v[108:111], v0, s[64:67], s87 offen offset:2048 sc1\ns_nop 10\nbuffer_load_dwordx4 v[112:115], v0, s[64:67], s87 offen offset:3072 sc1\n" \
    "s_branch .Lr115_%=\n.Lr136_%=:\ns_sleep 3\ns_add_u32 s68, s68, 1\ns_and_b32 s69, s68, 63\ns_cbranch_scc1 .Lr134_%=\ns_cmp_gt_u32 s68, 0x200000\ns_cbranch_scc1 .Lr1_%=\n" \
    "buffer_load_dword v21, off, s[64:67], 0 sc1\ns_waitcnt vmcnt(0)\nv_readfirstlane_b32 s69, v21\ns_cmp_eq_u32 s69, 1\ns_cbranch_scc0 .Lr134_%=\ns_endpgm\n.Lr129_%=:\n" \
    "s_waitcnt vmcnt(0)\ns_sleep 4\ns_add_u32 s68, s68, 1\ns_and_b32 s69, s68, 63\ns_cbranch_scc1 .Lr127_%=\ns_cmp_gt_u32 s68, 0x200000\ns_cbranch_scc1 .Lr1_%=\n" \
    "buffer_load_dword v21, off, s[64:67], 0 sc1\ns_waitcnt vmcnt(0)\nv_readfirstlane_b32 s69, v21\ns_cmp_eq_u32 s69, 1\ns_cbranch_scc0 .Lr127_%=\ns_endpgm\n.Lr132_%=:\n" \
    "s_add_u32 s69, s50, -1\nv_mov_b32 v22, s69\nbuffer_atomic_umax v22, v15, s[64:67], 0 offen sc1\ns_branch .Lr133_%=\n.Lr140_%=:\ns_waitcnt vmcnt(0)\ns_sleep 4\n" \
    "s_add_u32 s68, s68, 1\ns_and_b32 s69, s68, 63\ns_cbranch_scc1 .Lr138_%=\ns_cmp_gt_u32 s68, 0x200000\ns_cbranch_scc1 .Lr1_%=\nbuffer_load_dword v21, off, s[64:67], 0 sc1\n" \
    "s_waitcnt vmcnt(0)\nv_readfirstlane_b32 s69, v21\ns_cmp_eq_u32 s69, 1\ns_cbranch_scc0 .Lr138_%=\ns_endpgm\n.Lr143_%=:\ns_add_u32 s69, s50, 1\nv_mov_b32 v22, s69\n" \
    "buffer_atomic_umax v22, v15, s[64:67], 0 offen sc1\ns_branch .Lr144_%=\n.Lr6_%=:\ns_sleep 3\ns_add_u32 s68, s68, 1\ns_and_b32 s69, s68, 63\ns_cbranch_scc1 .Lr5_%=\n" \
    "s_cmp_gt_u32 s68, 0x200000\ns_cbranch_scc1 .Lr1_%=\nbuffer_load_dword v21, off, s[64:67], 0 sc1\ns_waitcnt vmcnt(0)\nv_readfirstlane_b32 s69, v21\ns_cmp_eq_u32 s69, 1\n" \
    "s_cbranch_scc0 .Lr5_%=\ns_endpgm\n.Lr1_%=:\nv_mov_b32 v21, 1\nbuffer_store_dword v21, off, s[64:67], 0 sc1\ns_endpgm\n.Lr2_%=:\n"
#define ROLE_F1 \
    "s_mov_b32 s80, 0\ns_lshl_b32 s79, s54, 12\n.Lf7_%=:\ns_mov_b32 s68, 0\n.Lf6_%=:\ns_mov_b32 s71, s50\ns_and_b32 s69, s71, 1\ns_lshl_b32 s69, s69, 2\n" \
    "buffer_load_dword v9, v7, s[64:67], s69 offen sc1\ns_waitcnt vmcnt(0)\nv_readfirstlane_b32 s78, v9\ns_cmp_gt_i32 s78, s50\ns_cbranch_scc0 .Lf8_%=\n.Lf3_%=:\n.Lf13_%=:\n" \
    "s_add_u32 s72, s52, s51\nbuffer_load_dwordx2 v[104:105], v2, s[64:67], s53 offen offset:0 sc1\ns_add_u32 m0, s72, 0\ns_add_u32 s70, s53, s42\n" \
    "buffer_load_dwordx4 v0, s[64:67], s70 offen offset:0 sc1 lds\nbuffer_load_dwordx4 v0, s[64:67], s70 offen offset:1024 sc1 lds\n" \
    "buffer_load_dwordx4 v0, s[64:67], s70 offen offset:2048 sc1 lds\nbuffer_load_dwordx4 v0, s[64:67], s70 offen offset:3072 sc1 lds\n" \
    "buffer_load_dwordx2 v[106:107], v2, s[64:67], s53 offen offset:256 sc1\nbuffer_load_dwordx2 v[108:109], v2, s[64:67], s53 offen offset:512 sc1\n" \
    "buffer_load_dwordx2 v[110:111], v2, s[64:67], s53 offen offset:768 sc1\ns_add_u32 m0, s72, 16384\ns_add_u32 s70, s53, s43\n" \
    "buffer_load_dwordx4 v0, s[64:67], s70 offen offset:0 sc1 lds\nbuffer_load_dwordx4 v0, s[64:67], s70 offen offset:1024 sc1 lds\n" \
    "buffer_load_dwordx4 v0, s[64:67], s70 offen offset:2048 sc1 lds\nbuffer_load_dwordx4 v0, s[64:67], s70 offen offset:3072 sc1 lds\n.Lf14_%=:\ns_waitcnt vmcnt(11)\n" \
    "v_cmp_eq_u32 vcc, -1, v104\ns_cbranch_vccnz .Lf12_%=\nv_add_u32 v8, s51, v1\ns_waitcnt vmcnt(7)\ns_barrier\nds_read_b128 v[24:27], v8 offset:0\n" \
    "ds_read_b128 v[28:31], v8 offset:512\nds_read_b128 v[32:35], v8 offset:2048\nds_read_b128 v[36:39], v8 offset:2560\nds_read_b128 v[40:43], v8 offset:4096\n" \
    "ds_read_b128 v[44:47], v8 offset:4608\nds_read_b128 v[48:51], v8 offset:6144\nds_read_b128 v[52:55], v8 offset:6656\ns_waitcnt lgkmcnt(6)\n" \
    "v_mfma_f32_32x32x16_bf16 v[56:71], a[0:3], v[24:27], 0\nds_read_b128 v[24:27], v8 offset:8192\ns_add_u32 m0, s72, 32768\ns_add_u32 s70, s53, s44\n" \
    "buffer_load_dwordx4 v0, s[64:67], s70 offen offset:0 sc1 lds\nv_mfma_f32_32x32x16_bf16 v[56:71], a[4:7], v[28:31], v[56:71]\ns_waitcnt lgkmcnt(5)\n" \
    "ds_read_b128 v[28:31], v8 offset:8704\nv_mfma_f32_32x32x16_bf16 v[56:71], a[8:11], v[32:35], v[56:71]\nds_read_b128 v[32:35], v8 offset:10240\n" \
    "buffer_load_dwordx4 v0, s[64:67], s70 offen offset:1024 sc1 lds\ns_add_u32 s71, s50, 1\ns_cmp_ge_u32 s71, s56\ns_addc_u32 s71, s71, 0\n" \
    "v_mfma_f32_32x32x16_bf16 v[56:71], a[12:15], v[36:39], v[56:71]\ns_waitcnt lgkmcnt(5)\nds_read_b128 v[36:39], v8 offset:10752\ns_min_u32 s73, s71, 63\n" \
    "s_lshl_b32 s73, s73, 20\ns_add_u32 s73, s73, s40\ns_add_u32 s74, s48, s73\nv_mfma_f32_32x32x16_bf16 v[56:71], a[16:19], v[40:43], v[56:71]\n" \
    "ds_read_b128 v[40:43], v8 offset:12288\nbuffer_load_dwordx4 v0, s[64:67], s70 offen offset:2048 sc1 lds\ns_lshr_b32 s74, s74, 8\ns_xor_b32 s74, s74, s49\n" \
    "s_and_b32 s74, s74, 0x2000\nv_mfma_f32_32x32x16_bf16 v[56:71], a[20:23], v[44:47], v[56:71]\ns_waitcnt lgkmcnt(5)\nds_read_b128 v[44:47], v8 offset:12800\n" \
    "s_add_u32 s74, s74, s73\ns_xor_b32 s75, s51, 0x10000\ns_add_u32 s75, s75, s52\nv_mfma_f32_32x32x16_bf16 v[56:71], a[24:27], v[48:51], v[56:71]\n" \
    "ds_read_b128 v[48:51], v8 offset:14336\nbuffer_load_dwordx4 v0, s[64:67], s70 offen offset:3072 sc1 lds\nv_mfma_f32_32x32x16_bf16 v[56:71], a[28:31], v[52:55], v[56:71]\n" \
    "s_waitcnt lgkmcnt(5)\ns_waitcnt vmcnt(4)\ns_barrier\nds_read_b128 v[52:55], v8 offset:14848\nv_mfma_f32_32x32x16_bf16 v[56:71], a[32:35], v[24:27], v[56:71]\n" \
    "ds_read_b128 v[24:27], v8 offset:16384\ns_add_u32 m0, s72, 49152\ns_add_u32 s70, s53, s45\nv_mfma_f32_32x32x16_bf16 v[56:71], a[36:39], v[28:31], v[56:71]\n" \
    "s_waitcnt lgkmcnt(5)\nds_read_b128 v[28:31], v8 offset:16896\nbuffer_load_dwordx4 v0, s[64:67], s70 offen offset:0 sc1 lds\n" \
    "v_mfma_f32_32x32x16_bf16 v[56:71], a[40:43], v[32:35], v[56:71]\nds_read_b128 v[32:35], v8 offset:18432\nv_mfma_f32_32x32x16_bf16 v[56:71], a[44:47], v[36:39], v[56:71]\n" \
    "s_waitcnt lgkmcnt(5)\nds_read_b128 v[36:39], v8 offset:18944\nbuffer_load_dwordx4 v0, s[64:67], s70 offen offset:1024 sc1 lds\n" \
    "v_mfma_f32_32x32x16_bf16 v[56:71], a[48:51], v[40:43], v[56:71]\nds_read_b128 v[40:43], v8 offset:20480\ns_and_b32 s69, s71, 1\ns_lshl_b32 s69, s69, 2\n" \
    "buffer_load_dword v9, v7, s[64:67], s69 offen sc1\nv_mfma_f32_32x32x16_bf16 v[56:71], a[52:55], v[44:47], v[56:71]\ns_waitcnt lgkmcnt(5)\n" \
    "ds_read_b128 v[44:47], v8 offset:20992\nbuffer_load_dwordx4 v0, s[64:67], s70 offen offset:2048 sc1 lds\nv_mfma_f32_32x32x16_bf16 v[56:71], a[56:59], v[48:51], v[56:71]\n" \
    "ds_read_b128 v[48:51], v8 offset:22528\nv_mfma_f32_32x32x16_bf16 v[56:71], a[60:63], v[52:55], v[56:71]\ns_waitcnt lgkmcnt(5)\nds_read_b128 v[52:55], v8 offset:23040\n" \
    "buffer_load_dwordx4 v0, s[64:67], s70 offen offset:3072 sc1 lds\nv_mfma_f32_32x32x16_bf16 v[56:71], a[64:67], v[24:27], v[56:71]\nds_read_b128 v[24:27], v8 offset:24576\n" \
    "v_mov_b32 v10, 1\nv_mfma_f32_32x32x16_bf16 v[56:71], a[68:71], v[28:31], v[56:71]\ns_waitcnt lgkmcnt(5)\nds_read_b128 v[28:31], v8 offset:25088\ns_lshl_b32 s81, s50, 2\n" \
    "s_add_u32 s81, s81, s55\nv_mfma_f32_32x32x16_bf16 v[56:71], a[72:75], v[32:35], v[56:71]\nds_read_b128 v[32:35], v8 offset:26624\n" \
    "v_mfma_f32_32x32x16_bf16 v[56:71], a[76:79], v[36:39], v[56:71]\ns_waitcnt lgkmcnt(5)\nds_read_b128 v[36:39], v8 offset:27136\n" \
    "v_mfma_f32_32x32x16_bf16 v[56:71], a[80:83], v[40:43], v[56:71]\nds_read_b128 v[40:43], v8 offset:28672\nv_mfma_f32_32x32x16_bf16 v[56:71], a[84:87], v[44:47], v[56:71]\n" \
    "s_waitcnt lgkmcnt(5)\nds_read_b128 v[44:47], v8 offset:29184\nv_mfma_f32_32x32x16_bf16 v[56:71], a[88:91], v[48:51], v[56:71]\nds_read_b128 v[48:51], v8 offset:30720\n" \
    "v_max3_u32 v12, v104, v105, v106\nv_max3_u32 v12, v12, v107, v108\nv_max3_u32 v12, v12, v109, v110\nv_max_u32 v12, v12, v111\n" \
    "v_mfma_f32_32x32x16_bf16 v[56:71], a[92:95], v[52:55], v[56:71]\ns_waitcnt lgkmcnt(5)\ns_waitcnt vmcnt(5)\ns_barrier\nds_read_b128 v[52:55], v8 offset:31232\n" \
    "v_mfma_f32_32x32x16_bf16 v[56:71], a[96:99], v[24:27], v[56:71]\nds_read_b128 v[24:27], v8 offset:32768\nv_cmp_eq_u32 vcc, -1, v12\n" \
    "v_mfma_f32_32x32x16_bf16 v[56:71], a[100:103], v[28:31], v[56:71]\ns_waitcnt lgkmcnt(5)\nds_read_b128 v[28:31], v8 offset:33280\ns_cbranch_vccnz .Lf16_%=\n.Lf17_%=:\n" \
    "v_add_f32 v12, v104, v106\nv_add_f32 v13, v108, v110\nv_mfma_f32_32x32x16_bf16 v[56:71], a[104:107], v[32:35], v[56:71]\nds_read_b128 v[32:35], v8 offset:34816\n" \
    "s_waitcnt vmcnt(2)\nv_add_f32 v12, v12, v13\nv_add_f32 v105, v105, v107\nv_add_f32 v109, v109, v111\nv_readfirstlane_b32 s76, v9\n" \
    "v_mfma_f32_32x32x16_bf16 v[56:71], a[108:111], v[36:39], v[56:71]\ns_waitcnt lgkmcnt(5)\nds_read_b128 v[36:39], v8 offset:35328\ns_cmp_gt_i32 s76, s71\n" \
    "s_cselect_b32 s76, 0, 0x7f000000\nv_add_f32_dpp v13, v12, v12 row_ror:8 row_mask:0xf bank_mask:0xf\nv_add_f32_dpp v14, v12, v12 row_ror:8 row_mask:0xf bank_mask:0xf\n" \
    "v_mfma_f32_32x32x16_bf16 v[56:71], a[112:115], v[40:43], v[56:71]\nds_read_b128 v[40:43], v8 offset:36864\nv_permlane16_swap_b32 v13, v14\nv_add_f32 v12, v13, v14\n" \
    "v_add_f32 v13, v13, v14\nv_or_b32 v11, s76, v0\nv_mfma_f32_32x32x16_bf16 v[56:71], a[116:119], v[44:47], v[56:71]\ns_waitcnt lgkmcnt(5)\n" \
    "ds_read_b128 v[44:47], v8 offset:37376\nv_permlane32_swap_b32 v12, v13\nv_add_f32 v12, v12, v13\nv_mul_f32 v12, 0x3d000000, v12\n" \
    "v_mfma_f32_32x32x16_bf16 v[56:71], a[120:123], v[48:51], v[56:71]\nds_read_b128 v[48:51], v8 offset:38912\nv_sub_f32 v104, v104, v12\nv_sub_f32 v106, v106, v12\n" \
    "v_sub_f32 v108, v108, v12\nv_sub_f32 v110, v110, v12\nv_mul_f32 v13, v104, v104\nv_mfma_f32_32x32x16_bf16 v[56:71], a[124:127], v[52:55], v[56:71]\ns_waitcnt lgkmcnt(5)\n" \
    "ds_read_b128 v[52:55], v8 offset:39424\nv_fmac_f32 v13, v106, v106\nv_fmac_f32 v13, v108, v108\nv_fmac_f32 v13, v110, v110\nv_add_f32 v14, v105, v109\n" \
    "v_mfma_f32_32x32x16_bf16 v[56:71], a[128:131], v[24:27], v[56:71]\nds_read_b128 v[24:27], v8 offset:40960\ns_add_u32 m0, s75, 0\ns_add_u32 s70, s74, s42\n" \
    "buffer_load_dwordx4 v11, s[64:67], s70 offen offset:0 sc1 lds\nv_fmamk_f32 v13, v13, 0x42000000, v14\nv_mfma_f32_32x32x16_bf16 v[56:71], a[132:135], v[28:31], v[56:71]\n" \
    "s_waitcnt lgkmcnt(5)\nds_read_b128 v[28:31], v8 offset:41472\nbuffer_load_dwordx4 v11, s[64:67], s70 offen offset:1024 sc1 lds\n" \
    "v_add_f32_dpp v14, v13, v13 row_ror:8 row_mask:0xf bank_mask:0xf\nv_add_f32_dpp v15, v13, v13 row_ror:8 row_mask:0xf bank_mask:0xf\n" \
    "v_mfma_f32_32x32x16_bf16 v[56:71], a[136:139], v[32:35], v[56:71]\nds_read_b128 v[32:35], v8 offset:43008\nv_permlane16_swap_b32 v14, v15\nv_add_f32 v13, v14, v15\n" \
    "v_add_f32 v14, v14, v15\nv_mfma_f32_32x32x16_bf16 v[56:71], a[140:143], v[36:39], v[56:71]\ns_waitcnt lgkmcnt(5)\nds_read_b128 v[36:39], v8 offset:43520\n" \
    "v_permlane32_swap_b32 v13, v14\nv_add_f32 v13, v13, v14\nv_mov_b32 v14, 0x3727c5ac\nv_mfma_f32_32x32x16_bf16 v[56:71], a[144:147], v[40:43], v[56:71]\n" \
    "ds_read_b128 v[40:43], v8 offset:45056\nbuffer_load_dwordx4 v11, s[64:67], s70 offen offset:2048 sc1 lds\nv_fmamk_f32 v13, v13, 0x3a800000, v14\nv_rsq_f32 v13, v13\n" \
    "v_mfma_f32_32x32x16_bf16 v[56:71], a[148:151], v[44:47], v[56:71]\ns_waitcnt lgkmcnt(5)\nds_read_b128 v[44:47], v8 offset:45568\nds_write_b64 v3, v[12:13]\n" \
    "v_mfma_f32_32x32x16_bf16 v[56:71], a[152:155], v[48:51], v[56:71]\nds_read_b128 v[48:51], v8 offset:47104\n" \
    "buffer_load_dwordx4 v11, s[64:67], s70 offen offset:3072 sc1 lds\ns_add_u32 m0, s75, 16384\ns_add_u32 s70, s74, s43\n" \
    "v_mfma_f32_32x32x16_bf16 v[56:71], a[156:159], v[52:55], v[56:71]\ns_waitcnt lgkmcnt(6)\ns_waitcnt vmcnt(4) lgkmcnt(1)\ns_barrier\nds_read_b128 v[52:55], v8 offset:47616\n" \
    "v_mfma_f32_32x32x16_bf16 v[56:71], a[160:163], v[24:27], v[56:71]\nds_read_b128 v[24:27], v8 offset:49152\nbuffer_load_dwordx4 v11, s[64:67], s70 offen offset:0 sc1 lds\n" \
    "v_mfma_f32_32x32x16_bf16 v[56:71], a[164:167], v[28:31], v[56:71]\nds_read_b128 v[28:31], v8 offset:49664\ns_cmp_eq_u32 s54, 3\ns_cselect_b64 exec, 1, 0\n" \
    "buffer_atomic_add v10, off, s[64:67], s81 sc0\ns_mov_b64 exec, -1\nv_mfma_f32_32x32x16_bf16 v[56:71], a[168:171], v[32:35], v[56:71]\n" \
    "ds_read_b128 v[32:35], v8 offset:51200\nbuffer_load_dwordx4 v11, s[64:67], s70 offen offset:1024 sc1 lds\n" \
    "v_mfma_f32_32x32x16_bf16 v[56:71], a[172:175], v[36:39], v[56:71]\nds_read_b128 v[36:39], v8 offset:51712\n" \
    "buffer_load_dwordx4 v11, s[64:67], s70 offen offset:2048 sc1 lds\nv_mfma_f32_32x32x16_bf16 v[56:71], a[176:179], v[40:43], v[56:71]\n" \
    "ds_read_b128 v[40:43], v8 offset:53248\nds_read_b64 v[14:15], v4\nbuffer_load_dwordx4 v11, s[64:67], s70 offen offset:3072 sc1 lds\n" \
    "v_mfma_f32_32x32x16_bf16 v[56:71], a[180:183], v[44:47], v[56:71]\ns_waitcnt lgkmcnt(6)\nds_read_b128 v[44:47], v8 offset:53760\n" \
    "v_mfma_f32_32x32x16_bf16 v[56:71], a[184:187], v[48:51], v[56:71]\nds_read_b128 v[48:51], v8 offset:55296\ns_add_u32 m0, s75, 32768\ns_add_u32 s70, s74, s44\n" \
    "buffer_load_dwordx4 v11, s[64:67], s70 offen offset:0 sc1 lds\nv_mfma_f32_32x32x16_bf16 v[56:71], a[188:191], v[52:55], v[56:71]\ns_waitcnt lgkmcnt(6)\n" \
    "ds_read_b128 v[52:55], v8 offset:55808\ns_and_b32 s69, s71, 1\ns_lshl_b32 s69, s69, 2\nbuffer_load_dword v9, v7, s[64:67], s69 offen sc1\n" \
    "v_mfma_f32_32x32x16_bf16 v[56:71], a[192:195], v[24:27], v[56:71]\nds_read_b128 v[24:27], v8 offset:57344\n" \
    "buffer_load_dwordx4 v11, s[64:67], s70 offen offset:1024 sc1 lds\nv_mfma_f32_32x32x16_bf16 v[56:71], a[196:199], v[28:31], v[56:71]\ns_waitcnt lgkmcnt(6)\n" \
    "ds_read_b128 v[28:31], v8 offset:57856\nv_mfma_f32_32x32x16_bf16 v[56:71], a[200:203], v[32:35], v[56:71]\nds_read_b128 v[32:35], v8 offset:59392\n" \
    "buffer_load_dwordx4 v11, s[64:67], s70 offen offset:2048 sc1 lds\nv_mfma_f32_32x32x16_bf16 v[56:71], a[204:207], v[36:39], v[56:71]\ns_waitcnt lgkmcnt(5)\n" \
    "ds_read_b128 v[36:39], v8 offset:59904\nv_mfma_f32_32x32x16_bf16 v[56:71], a[208:211], v[40:43], v[56:71]\nds_read_b128 v[40:43], v8 offset:61440\n" \
    "buffer_load_dwordx4 v11, s[64:67], s70 offen offset:3072 sc1 lds\nv_mfma_f32_32x32x16_bf16 v[56:71], a[212:215], v[44:47], v[56:71]\ns_waitcnt lgkmcnt(5)\n" \
    "ds_read_b128 v[44:47], v8 offset:61952\nv_mfma_f32_32x32x16_bf16 v[56:71], a[216:219], v[48:51], v[56:71]\nds_read_b128 v[48:51], v8 offset:63488\n" \
    "s_add_u32 m0, s75, 49152\ns_add_u32 s70, s74, s45\nbuffer_load_dwordx4 v11, s[64:67], s70 offen offset:0 sc1 lds\n" \
    "v_mfma_f32_32x32x16_bf16 v[56:71], a[220:223], v[52:55], v[56:71]\ns_waitcnt lgkmcnt(5)\nds_read_b128 v[52:55], v8 offset:64000\n" \
    "v_mfma_f32_32x32x16_bf16 v[56:71], a[224:227], v[24:27], v[56:71]\nbuffer_load_dwordx4 v11, s[64:67], s70 offen offset:1024 sc1 lds\n" \
    "v_mfma_f32_32x32x16_bf16 v[56:71], a[228:231], v[28:31], v[56:71]\ns_waitcnt lgkmcnt(4)\nv_mfma_f32_32x32x16_bf16 v[56:71], a[232:235], v[32:35], v[56:71]\n" \
    "buffer_load_dwordx4 v11, s[64:67], s70 offen offset:2048 sc1 lds\nv_mfma_f32_32x32x16_bf16 v[56:71], a[236:239], v[36:39], v[56:71]\ns_waitcnt lgkmcnt(2)\n" \
    "v_mfma_f32_32x32x16_bf16 v[56:71], a[240:243], v[40:43], v[56:71]\nbuffer_load_dwordx4 v11, s[64:67], s70 offen offset:3072 sc1 lds\n" \
    "v_mfma_f32_32x32x16_bf16 v[56:71], a[244:247], v[44:47], v[56:71]\ns_waitcnt lgkmcnt(0)\ns_waitcnt vmcnt(9)\nv_readfirstlane_b32 s81, v10\n" \
    "v_mfma_f32_32x32x16_bf16 v[56:71], a[248:251], v[48:51], v[56:71]\nv_mfma_f32_32x32x16_bf16 v[56:71], a[252:255], v[52:55], v[56:71]\ns_waitcnt vmcnt(7)\n" \
    "v_readfirstlane_b32 s78, v9\ns_and_b32 s69, s71, 1\ns_lshl_b32 s69, s69, 2\ns_branch .Lf5_%=\n.Lf4_%=:\ns_add_u32 s72, s52, s51\nv_add_u32 v8, s51, v1\n" \
    "s_waitcnt vmcnt(16)\ns_barrier\nds_read_b128 v[24:27], v8 offset:0\nds_read_b128 v[28:31], v8 offset:512\nds_read_b128 v[32:35], v8 offset:2048\n" \
    "ds_read_b128 v[36:39], v8 offset:2560\nds_read_b128 v[40:43], v8 offset:4096\nds_read_b128 v[44:47], v8 offset:4608\nds_read_b128 v[48:51], v8 offset:6144\n" \
    "ds_read_b128 v[52:55], v8 offset:6656\ns_waitcnt lgkmcnt(6)\nv_mfma_f32_32x32x16_bf16 v[56:71], a[0:3], v[24:27], 0\nds_read_b128 v[24:27], v8 offset:8192\n" \
    "buffer_load_dwordx2 v[104:105], v2, s[64:67], s53 offen offset:0 sc1\nv_mfma_f32_32x32x16_bf16 v[56:71], a[4:7], v[28:31], v[56:71]\ns_waitcnt lgkmcnt(5)\n" \
    "ds_read_b128 v[28:31], v8 offset:8704\nbuffer_load_dwordx2 v[106:107], v2, s[64:67], s53 offen offset:256 sc1\n" \
    "v_mfma_f32_32x32x16_bf16 v[56:71], a[8:11], v[32:35], v[56:71]\nds_read_b128 v[32:35], v8 offset:10240\n" \
    "buffer_load_dwordx2 v[108:109], v2, s[64:67], s53 offen offset:512 sc1\ns_add_u32 s71, s50, 1\ns_cmp_ge_u32 s71, s56\ns_addc_u32 s71, s71, 0\n" \
    "v_mfma_f32_32x32x16_bf16 v[56:71], a[12:15], v[36:39], v[56:71]\ns_waitcnt lgkmcnt(5)\nds_read_b128 v[36:39], v8 offset:10752\n" \
    "buffer_load_dwordx2 v[110:111], v2, s[64:67], s53 offen offset:768 sc1\ns_min_u32 s73, s71, 63\ns_lshl_b32 s73, s73, 20\n" \
    "v_mfma_f32_32x32x16_bf16 v[56:71], a[16:19], v[40:43], v[56:71]\nds_read_b128 v[40:43], v8 offset:12288\ns_add_u32 s73, s73, s40\ns_add_u32 s74, s48, s73\n" \
    "s_lshr_b32 s74, s74, 8\ns_xor_b32 s74, s74, s49\ns_and_b32 s74, s74, 0x2000\nv_mfma_f32_32x32x16_bf16 v[56:71], a[20:23], v[44:47], v[56:71]\ns_waitcnt lgkmcnt(5)\n" \
    "ds_read_b128 v[44:47], v8 offset:12800\ns_add_u32 s74, s74, s73\ns_xor_b32 s75, s51, 0x10000\ns_add_u32 s75, s75, s52\n" \
    "v_mfma_f32_32x32x16_bf16 v[56:71], a[24:27], v[48:51], v[56:71]\nds_read_b128 v[48:51], v8 offset:14336\nv_mfma_f32_32x32x16_bf16 v[56:71], a[28:31], v[52:55], v[56:71]\n" \
    "s_waitcnt lgkmcnt(5)\ns_waitcnt vmcnt(16)\ns_barrier\nds_read_b128 v[52:55], v8 offset:14848\nv_mfma_f32_32x32x16_bf16 v[56:71], a[32:35], v[24:27], v[56:71]\n" \
    "ds_read_b128 v[24:27], v8 offset:16384\nv_mfma_f32_32x32x16_bf16 v[56:71], a[36:39], v[28:31], v[56:71]\ns_waitcnt lgkmcnt(5)\nds_read_b128 v[28:31], v8 offset:16896\n" \
    "v_mfma_f32_32x32x16_bf16 v[56:71], a[40:43], v[32:35], v[56:71]\nds_read_b128 v[32:35], v8 offset:18432\nv_mfma_f32_32x32x16_bf16 v[56:71], a[44:47], v[36:39], v[56:71]\n" \
    "s_waitcnt lgkmcnt(5)\nds_read_b128 v[36:39], v8 offset:18944\nv_mfma_f32_32x32x16_bf16 v[56:71], a[48:51], v[40:43], v[56:71]\nds_read_b128 v[40:43], v8 offset:20480\n" \
    "s_and_b32 s69, s71, 1\ns_lshl_b32 s69, s69, 2\nbuffer_load_dword v9, v7, s[64:67], s69 offen sc1\nv_mfma_f32_32x32x16_bf16 v[56:71], a[52:55], v[44:47], v[56:71]\n" \
    "s_waitcnt lgkmcnt(5)\nds_read_b128 v[44:47], v8 offset:20992\nv_mfma_f32_32x32x16_bf16 v[56:71], a[56:59], v[48:51], v[56:71]\nds_read_b128 v[48:51], v8 offset:22528\n" \
    "v_mfma_f32_32x32x16_bf16 v[56:71], a[60:63], v[52:55], v[56:71]\ns_waitcnt lgkmcnt(5)\nds_read_b128 v[52:55], v8 offset:23040\n" \
    "v_mfma_f32_32x32x16_bf16 v[56:71], a[64:67], v[24:27], v[56:71]\nds_read_b128 v[24:27], v8 offset:24576\nv_mov_b32 v10, 1\n" \
    "v_mfma_f32_32x32x16_bf16 v[56:71], a[68:71], v[28:31], v[56:71]\ns_waitcnt lgkmcnt(5)\nds_read_b128 v[28:31], v8 offset:25088\ns_lshl_b32 s81, s50, 2\n" \
    "s_add_u32 s81, s81, s55\nv_mfma_f32_32x32x16_bf16 v[56:71], a[72:75], v[32:35], v[56:71]\nds_read_b128 v[32:35], v8 offset:26624\n" \
    "v_mfma_f32_32x32x16_bf16 v[56:71], a[76:79], v[36:39], v[56:71]\ns_waitcnt lgkmcnt(5)\nds_read_b128 v[36:39], v8 offset:27136\n" \
    "v_mfma_f32_32x32x16_bf16 v[56:71], a[80:83], v[40:43], v[56:71]\nds_read_b128 v[40:43], v8 offset:28672\nv_mfma_f32_32x32x16_bf16 v[56:71], a[84:87], v[44:47], v[56:71]\n" \
    "s_waitcnt lgkmcnt(5)\nds_read_b128 v[44:47], v8 offset:29184\nv_mfma_f32_32x32x16_bf16 v[56:71], a[88:91], v[48:51], v[56:71]\nds_read_b128 v[48:51], v8 offset:30720\n" \
    "s_waitcnt vmcnt(1)\nv_max3_u32 v12, v104, v105, v106\nv_max3_u32 v12, v12, v107, v108\nv_max3_u32 v12, v12, v109, v110\nv_max_u32 v12, v12, v111\n" \
    "v_mfma_f32_32x32x16_bf16 v[56:71], a[92:95], v[52:55], v[56:71]\ns_waitcnt lgkmcnt(5)\ns_barrier\nds_read_b128 v[52:55], v8 offset:31232\n" \
    "v_mfma_f32_32x32x16_bf16 v[56:71], a[96:99], v[24:27], v[56:71]\nds_read_b128 v[24:27], v8 offset:32768\nv_cmp_eq_u32 vcc, -1, v12\n" \
    "v_mfma_f32_32x32x16_bf16 v[56:71], a[100:103], v[28:31], v[56:71]\ns_waitcnt lgkmcnt(5)\nds_read_b128 v[28:31], v8 offset:33280\ns_cbranch_vccnz .Lf19_%=\n.Lf20_%=:\n" \
    "v_add_f32 v12, v104, v106\nv_add_f32 v13, v108, v110\nv_mfma_f32_32x32x16_bf16 v[56:71], a[104:107], v[32:35], v[56:71]\nds_read_b128 v[32:35], v8 offset:34816\n" \
    "s_waitcnt vmcnt(0)\nv_add_f32 v12, v12, v13\nv_add_f32 v105, v105, v107\nv_add_f32 v109, v109, v111\nv_readfirstlane_b32 s76, v9\n" \
    "v_mfma_f32_32x32x16_bf16 v[56:71], a[108:111], v[36:39], v[56:71]\ns_waitcnt lgkmcnt(5)\nds_read_b128 v[36:39], v8 offset:35328\ns_cmp_gt_i32 s76, s71\n" \
    "s_cselect_b32 s76, 0, 0x7f000000\nv_add_f32_dpp v13, v12, v12 row_ror:8 row_mask:0xf bank_mask:0xf\nv_add_f32_dpp v14, v12, v12 row_ror:8 row_mask:0xf bank_mask:0xf\n" \
    "v_mfma_f32_32x32x16_bf16 v[56:71], a[112:115], v[40:43], v[56:71]\nds_read_b128 v[40:43], v8 offset:36864\nv_permlane16_swap_b32 v13, v14\nv_add_f32 v12, v13, v14\n" \
    "v_add_f32 v13, v13, v14\nv_or_b32 v11, s76, v0\nv_mfma_f32_32x32x16_bf16 v[56:71], a[116:119], v[44:47], v[56:71]\ns_waitcnt lgkmcnt(5)\n" \
    "ds_read_b128 v[44:47], v8 offset:37376\nv_permlane32_swap_b32 v12, v13\nv_add_f32 v12, v12, v13\nv_mul_f32 v12, 0x3d000000, v12\n" \
    "v_mfma_f32_32x32x16_bf16 v[56:71], a[120:123], v[48:51], v[56:71]\nds_read_b128 v[48:51], v8 offset:38912\nv_sub_f32 v104, v104, v12\nv_sub_f32 v106, v106, v12\n" \
    "v_sub_f32 v108, v108, v12\nv_sub_f32 v110, v110, v12\nv_mul_f32 v13, v104, v104\nv_mfma_f32_32x32x16_bf16 v[56:71], a[124:127], v[52:55], v[56:71]\ns_waitcnt lgkmcnt(5)\n" \
    "ds_read_b128 v[52:55], v8 offset:39424\nv_fmac_f32 v13, v106, v106\nv_fmac_f32 v13, v108, v108\nv_fmac_f32 v13, v110, v110\nv_add_f32 v14, v105, v109\n" \
    "v_mfma_f32_32x32x16_bf16 v[56:71], a[128:131], v[24:27], v[56:71]\nds_read_b128 v[24:27], v8 offset:40960\ns_add_u32 m0, s75, 0\ns_add_u32 s70, s74, s42\n" \
    "buffer_load_dwordx4 v11, s[64:67], s70 offen offset:0 sc1 lds\nv_fmamk_f32 v13, v13, 0x42000000, v14\nv_mfma_f32_32x32x16_bf16 v[56:71], a[132:135], v[28:31], v[56:71]\n" \
    "s_waitcnt lgkmcnt(5)\nds_read_b128 v[28:31], v8 offset:41472\nbuffer_load_dwordx4 v11, s[64:67], s70 offen offset:1024 sc1 lds\n" \
    "v_add_f32_dpp v14, v13, v13 row_ror:8 row_mask:0xf bank_mask:0xf\nv_add_f32_dpp v15, v13, v13 row_ror:8 row_mask:0xf bank_mask:0xf\n" \
    "v_mfma_f32_32x32x16_bf16 v[56:71], a[136:139], v[32:35], v[56:71]\nds_read_b128 v[32:35], v8 offset:43008\nv_permlane16_swap_b32 v14, v15\nv_add_f32 v13, v14, v15\n" \
    "v_add_f32 v14, v14, v15\nv_mfma_f32_32x32x16_bf16 v[56:71], a[140:143], v[36:39], v[56:71]\ns_waitcnt lgkmcnt(5)\nds_read_b128 v[36:39], v8 offset:43520\n" \
    "v_permlane32_swap_b32 v13, v14\nv_add_f32 v13, v13, v14\nv_mov_b32 v14, 0x3727c5ac\nv_mfma_f32_32x32x16_bf16 v[56:71], a[144:147], v[40:43], v[56:71]\n" \
    "ds_read_b128 v[40:43], v8 offset:45056\nbuffer_load_dwordx4 v11, s[64:67], s70 offen offset:2048 sc1 lds\nv_fmamk_f32 v13, v13, 0x3a800000, v14\nv_rsq_f32 v13, v13\n" \
    "v_mfma_f32_32x32x16_bf16 v[56:71], a[148:151], v[44:47], v[56:71]\ns_waitcnt lgkmcnt(5)\nds_read_b128 v[44:47], v8 offset:45568\nds_write_b64 v3, v[12:13]\n" \
    "v_mfma_f32_32x32x16_bf16 v[56:71], a[152:155], v[48:51], v[56:71]\nds_read_b128 v[48:51], v8 offset:47104\n" \
    "buffer_load_dwordx4 v11, s[64:67], s70 offen offset:3072 sc1 lds\ns_add_u32 m0, s75, 16384\ns_add_u32 s70, s74, s43\n" \
    "v_mfma_f32_32x32x16_bf16 v[56:71], a[156:159], v[52:55], v[56:71]\ns_waitcnt lgkmcnt(6)\ns_waitcnt lgkmcnt(1)\ns_barrier\nds_read_b128 v[52:55], v8 offset:47616\n" \
    "v_mfma_f32_32x32x16_bf16 v[56:71], a[160:163], v[24:27], v[56:71]\nds_read_b128 v[24:27], v8 offset:49152\nbuffer_load_dwordx4 v11, s[64:67], s70 offen offset:0 sc1 lds\n" \
    "v_mfma_f32_32x32x16_bf16 v[56:71], a[164:167], v[28:31], v[56:71]\nds_read_b128 v[28:31], v8 offset:49664\ns_cmp_eq_u32 s54, 3\ns_cselect_b64 exec, 1, 0\n" \
    "buffer_atomic_add v10, off, s[64:67], s81 sc0\ns_mov_b64 exec, -1\nv_mfma_f32_32x32x16_bf16 v[56:71], a[168:171], v[32:35], v[56:71]\n" \
    "ds_read_b128 v[32:35], v8 offset:51200\nbuffer_load_dwordx4 v11, s[64:67], s70 offen offset:1024 sc1 lds\n" \
    "v_mfma_f32_32x32x16_bf16 v[56:71], a[172:175], v[36:39], v[56:71]\nds_read_b128 v[36:39], v8 offset:51712\n" \
    "buffer_load_dwordx4 v11, s[64:67], s70 offen offset:2048 sc1 lds\nv_mfma_f32_32x32x16_bf16 v[56:71], a[176:179], v[40:43], v[56:71]\n" \
    "ds_read_b128 v[40:43], v8 offset:53248\nds_read_b64 v[14:15], v4\nbuffer_load_dwordx4 v11, s[64:67], s70 offen offset:3072 sc1 lds\n" \
    "v_mfma_f32_32x32x16_bf16 v[56:71], a[180:183], v[44:47], v[56:71]\ns_waitcnt lgkmcnt(6)\nds_read_b128 v[44:47], v8 offset:53760\n" \
    "v_mfma_f32_32x32x16_bf16 v[56:71], a[184:187], v[48:51], v[56:71]\nds_read_b128 v[48:51], v8 offset:55296\ns_add_u32 m0, s75, 32768\ns_add_u32 s70, s74, s44\n" \
    "buffer_load_dwordx4 v11, s[64:67], s70 offen offset:0 sc1 lds\nv_mfma_f32_32x32x16_bf16 v[56:71], a[188:191], v[52:55], v[56:71]\ns_waitcnt lgkmcnt(6)\n" \
    "ds_read_b128 v[52:55], v8 offset:55808\ns_and_b32 s69, s71, 1\ns_lshl_b32 s69, s69, 2\nbuffer_load_dword v9, v7, s[64:67], s69 offen sc1\n" \
    "v_mfma_f32_32x32x16_bf16 v[56:71], a[192:195], v[24:27], v[56:71]\nds_read_b128 v[24:27], v8 offset:57344\n" \
    "buffer_load_dwordx4 v11, s[64:67], s70 offen offset:1024 sc1 lds\nv_mfma_f32_32x32x16_bf16 v[56:71], a[196:199], v[28:31], v[56:71]\ns_waitcnt lgkmcnt(6)\n" \
    "ds_read_b128 v[28:31], v8 offset:57856\nv_mfma_f32_32x32x16_bf16 v[56:71], a[200:203], v[32:35], v[56:71]\nds_read_b128 v[32:35], v8 offset:59392\n" \
    "buffer_load_dwordx4 v11, s[64:67], s70 offen offset:2048 sc1 lds\nv_mfma_f32_32x32x16_bf16 v[56:71], a[204:207], v[36:39], v[56:71]\ns_waitcnt lgkmcnt(5)\n" \
    "ds_read_b128 v[36:39], v8 offset:59904\nv_mfma_f32_32x32x16_bf16 v[56:71], a[208:211], v[40:43], v[56:71]\nds_read_b128 v[40:43], v8 offset:61440\n" \
    "buffer_load_dwordx4 v11, s[64:67], s70 offen offset:3072 sc1 lds\nv_mfma_f32_32x32x16_bf16 v[56:71], a[212:215], v[44:47], v[56:71]\ns_waitcnt lgkmcnt(5)\n" \
    "ds_read_b128 v[44:47], v8 offset:61952\nv_mfma_f32_32x32x16_bf16 v[56:71], a[216:219], v[48:51], v[56:71]\nds_read_b128 v[48:51], v8 offset:63488\n" \
    "s_add_u32 m0, s75, 49152\ns_add_u32 s70, s74, s45\nbuffer_load_dwordx4 v11, s[64:67], s70 offen offset:0 sc1 lds\n" \
    "v_mfma_f32_32x32x16_bf16 v[56:71], a[220:223], v[52:55], v[56:71]\ns_waitcnt lgkmcnt(5)\nds_read_b128 v[52:55], v8 offset:64000\n" \
    "v_mfma_f32_32x32x16_bf16 v[56:71], a[224:227], v[24:27], v[56:71]\nbuffer_load_dwordx4 v11, s[64:67], s70 offen offset:1024 sc1 lds\n" \
    "v_mfma_f32_32x32x16_bf16 v[56:71], a[228:231], v[28:31], v[56:71]\ns_waitcnt lgkmcnt(4)\nv_mfma_f32_32x32x16_bf16 v[56:71], a[232:235], v[32:35], v[56:71]\n" \
    "buffer_load_dwordx4 v11, s[64:67], s70 offen offset:2048 sc1 lds\nv_mfma_f32_32x32x16_bf16 v[56:71], a[236:239], v[36:39], v[56:71]\ns_waitcnt lgkmcnt(2)\n" \
    "v_mfma_f32_32x32x16_bf16 v[56:71], a[240:243], v[40:43], v[56:71]\nbuffer_load_dwordx4 v11, s[64:67], s70 offen offset:3072 sc1 lds\n" \
    "v_mfma_f32_32x32x16_bf16 v[56:71], a[244:247], v[44:47], v[56:71]\ns_waitcnt lgkmcnt(0)\ns_waitcnt vmcnt(9)\nv_readfirstlane_b32 s81, v10\n" \
    "v_mfma_f32_32x32x16_bf16 v[56:71], a[248:251], v[48:51], v[56:71]\nv_mfma_f32_32x32x16_bf16 v[56:71], a[252:255], v[52:55], v[56:71]\ns_waitcnt vmcnt(7)\n" \
    "v_readfirstlane_b32 s78, v9\ns_and_b32 s69, s71, 1\ns_lshl_b32 s69, s69, 2\n.Lf5_%=:\nbuffer_load_dword v9, v7, s[64:67], s69 offen sc1\ns_nop 6\n" \
    "v_cmp_u_f32 vcc, v56, v56\ns_add_u32 s77, s53, s41\ns_cbranch_vccnz .Lf9_%=\n.Lf11_%=:\ns_cmp_gt_i32 s78, s71\ns_cselect_b32 s78, s76, 0\ns_cmp_lg_u32 s78, 0\n" \
    "s_cbranch_scc1 .Lf15_%=\nv_pk_fma_f32 v[56:57], v[72:73], v[14:15], v[56:57] op_sel_hi:[1,0,1] neg_lo:[1,0,0] neg_hi:[1,0,0]\n" \
    "v_pk_fma_f32 v[58:59], v[74:75], v[14:15], v[58:59] op_sel_hi:[1,0,1] neg_lo:[1,0,0] neg_hi:[1,0,0]\n" \
    "v_pk_fma_f32 v[60:61], v[76:77], v[14:15], v[60:61] op_sel_hi:[1,0,1] neg_lo:[1,0,0] neg_hi:[1,0,0]\n" \
    "v_pk_fma_f32 v[62:63], v[78:79], v[14:15], v[62:63] op_sel_hi:[1,0,1] neg_lo:[1,0,0] neg_hi:[1,0,0]\nv_pk_fma_f32 v[56:57], v[56:57], v[14:15], v[88:89] op_sel:[0,1,0]\n" \
    "v_pk_fma_f32 v[58:59], v[58:59], v[14:15], v[90:91] op_sel:[0,1,0]\nv_pk_fma_f32 v[60:61], v[60:61], v[14:15], v[92:93] op_sel:[0,1,0]\n" \
    "v_pk_fma_f32 v[62:63], v[62:63], v[14:15], v[94:95] op_sel:[0,1,0]\nv_pk_mul_f32 v[112:113], v[56:57], v[56:57]\nv_pk_mul_f32 v[114:115], v[58:59], v[58:59]\n" \
    "v_pk_mul_f32 v[116:117], v[60:61], v[60:61]\nv_pk_mul_f32 v[118:119], v[62:63], v[62:63]\n" \
    "v_pk_fma_f32 v[112:113], v[112:113], s[46:47], s[46:47] op_sel:[0,0,1] op_sel_hi:[1,0,1]\n" \
    "v_pk_fma_f32 v[114:115], v[114:115], s[46:47], s[46:47] op_sel:[0,0,1] op_sel_hi:[1,0,1]\n" \
    "v_pk_fma_f32 v[116:117], v[116:117], s[46:47], s[46:47] op_sel:[0,0,1] op_sel_hi:[1,0,1]\n" \
    "v_pk_fma_f32 v[118:119], v[118:119], s[46:47], s[46:47] op_sel:[0,0,1] op_sel_hi:[1,0,1]\nv_pk_mul_f32 v[112:113], v[112:113], v[56:57]\n" \
    "v_pk_mul_f32 v[114:115], v[114:115], v[58:59]\nv_pk_mul_f32 v[116:117], v[116:117], v[60:61]\nv_pk_mul_f32 v[118:119], v[118:119], v[62:63]\nv_exp_f32 v112, v112\n" \
    "v_exp_f32 v113, v113\nv_exp_f32 v114, v114\nv_exp_f32 v115, v115\nv_exp_f32 v116, v116\nv_exp_f32 v117, v117\nv_exp_f32 v118, v118\nv_exp_f32 v119, v119\n" \
    "v_pk_add_f32 v[112:113], v[112:113], 1.0 op_sel_hi:[1,0]\nv_pk_add_f32 v[114:115], v[114:115], 1.0 op_sel_hi:[1,0]\n" \
    "v_pk_add_f32 v[116:117], v[116:117], 1.0 op_sel_hi:[1,0]\nv_pk_add_f32 v[118:119], v[118:119], 1.0 op_sel_hi:[1,0]\nv_rcp_f32 v112, v112\nv_rcp_f32 v113, v113\n" \
    "v_rcp_f32 v114, v114\nv_rcp_f32 v115, v115\nv_rcp_f32 v116, v116\nv_rcp_f32 v117, v117\nv_rcp_f32 v118, v118\nv_rcp_f32 v119, v119\n" \
    "v_pk_mul_f32 v[56:57], v[56:57], v[112:113]\nv_pk_mul_f32 v[58:59], v[58:59], v[114:115]\nv_pk_mul_f32 v[60:61], v[60:61], v[116:117]\n" \
    "v_pk_mul_f32 v[62:63], v[62:63], v[118:119]\nv_cvt_pk_bf16_f32 v56, v56, v57\nv_cvt_pk_bf16_f32 v57, v58, v59\nv_cvt_pk_bf16_f32 v58, v60, v61\n" \
    "v_cvt_pk_bf16_f32 v59, v62, v63\nv_min_u32 v56, -2, v56\nv_min_u32 v57, -2, v57\nv_min_u32 v58, -2, v58\nv_min_u32 v59, -2, v59\ns_nop 0\nv_permlane32_swap_b32 v56, v58\n" \
    "v_permlane32_swap_b32 v57, v59\nbuffer_store_dwordx4 v[56:59], v5, s[64:67], s77 offen offset:0 sc1\n" \
    "v_pk_fma_f32 v[64:65], v[80:81], v[14:15], v[64:65] op_sel_hi:[1,0,1] neg_lo:[1,0,0] neg_hi:[1,0,0]\ns_cmp_ge_u32 s81, 13\ns_cbranch_scc1 .Lf22_%=\n.Lf23_%=:\n" \
    "v_pk_fma_f32 v[66:67], v[82:83], v[14:15], v[66:67] op_sel_hi:[1,0,1] neg_lo:[1,0,0] neg_hi:[1,0,0]\n" \
    "v_pk_fma_f32 v[68:69], v[84:85], v[14:15], v[68:69] op_sel_hi:[1,0,1] neg_lo:[1,0,0] neg_hi:[1,0,0]\n" \
    "v_pk_fma_f32 v[70:71], v[86:87], v[14:15], v[70:71] op_sel_hi:[1,0,1] neg_lo:[1,0,0] neg_hi:[1,0,0]\nv_pk_fma_f32 v[64:65], v[64:65], v[14:15], v[96:97] op_sel:[0,1,0]\n" \
    "v_pk_fma_f32 v[66:67], v[66:67], v[14:15], v[98:99] op_sel:[0,1,0]\nv_pk_fma_f32 v[68:69], v[68:69], v[14:15], v[100:101] op_sel:[0,1,0]\n" \
    "v_pk_fma_f32 v[70:71], v[70:71], v[14:15], v[102:103] op_sel:[0,1,0]\nv_pk_mul_f32 v[112:113], v[64:65], v[64:65]\nv_pk_mul_f32 v[114:115], v[66:67], v[66:67]\n" \
    "v_pk_mul_f32 v[116:117], v[68:69], v[68:69]\nv_pk_mul_f32 v[118:119], v[70:71], v[70:71]\n" \
    "v_pk_fma_f32 v[112:113], v[112:113], s[46:47], s[46:47] op_sel:[0,0,1] op_sel_hi:[1,0,1]\n" \
    "v_pk_fma_f32 v[114:115], v[114:115], s[46:47], s[46:47] op_sel:[0,0,1] op_sel_hi:[1,0,1]\n" \
    "v_pk_fma_f32 v[116:117], v[116:117], s[46:47], s[46:47] op_sel:[0,0,1] op_sel_hi:[1,0,1]\n" \
    "v_pk_fma_f32 v[118:119], v[118:119], s[46:47], s[46:47] op_sel:[0,0,1] op_sel_hi:[1,0,1]\nv_pk_mul_f32 v[112:113], v[112:113], v[64:65]\n" \
    "v_pk_mul_f32 v[114:115], v[114:115], v[66:67]\nv_pk_mul_f32 v[116:117], v[116:117], v[68:69]\nv_pk_mul_f32 v[118:119], v[118:119], v[70:71]\nv_exp_f32 v112, v112\n" \
    "v_exp_f32 v113, v113\nv_exp_f32 v114, v114\nv_exp_f32 v115, v115\nv_exp_f32 v116, v116\nv_exp_f32 v117, v117\nv_exp_f32 v118, v118\nv_exp_f32 v119, v119\n" \
    "v_pk_add_f32 v[112:113], v[112:113], 1.0 op_sel_hi:[1,0]\nv_pk_add_f32 v[114:115], v[114:115], 1.0 op_sel_hi:[1,0]\n" \
    "v_pk_add_f32 v[116:117], v[116:117], 1.0 op_sel_hi:[1,0]\nv_pk_add_f32 v[118:119], v[118:119], 1.0 op_sel_hi:[1,0]\nv_rcp_f32 v112, v112\nv_rcp_f32 v113, v113\n" \
    "v_rcp_f32 v114, v114\nv_rcp_f32 v115, v115\nv_rcp_f32 v116, v116\nv_rcp_f32 v117, v117\nv_rcp_f32 v118, v118\nv_rcp_f32 v119, v119\n" \
    "v_pk_mul_f32 v[64:65], v[64:65], v[112:113]\nv_pk_mul_f32 v[66:67], v[66:67], v[114:115]\nv_pk_mul_f32 v[68:69], v[68:69], v[116:117]\n" \
    "v_pk_mul_f32 v[70:71], v[70:71], v[118:119]\nv_cvt_pk_bf16_f32 v64, v64, v65\nv_cvt_pk_bf16_f32 v65, v66, v67\nv_cvt_pk_bf16_f32 v66, v68, v69\n" \
    "v_cvt_pk_bf16_f32 v67, v70, v71\nv_min_u32 v64, -2, v64\nv_min_u32 v65, -2, v65\nv_min_u32 v66, -2, v66\nv_min_u32 v67, -2, v67\ns_nop 0\nv_permlane32_swap_b32 v64, v66\n" \
    "v_permlane32_swap_b32 v65, v67\nbuffer_store_dwordx4 v[64:67], v5, s[64:67], s77 offen offset:512 sc1\ns_waitcnt vmcnt(2)\nv_readfirstlane_b32 s78, v9\ns_mov_b32 s50, s71\n" \
    "s_mov_b32 s53, s74\ns_xor_b32 s51, s51, 0x10000\ns_cmp_ge_u32 s50, 64\ns_cbranch_scc1 .Lf2_%=\ns_cmp_eq_u32 s76, 0\ns_cbranch_scc1 .Lf4_%=\ns_cmp_gt_i32 s78, s50\n" \
    "s_cbranch_scc1 .Lf3_%=\ns_branch .Lf7_%=\n.Lf8_%=:\ns_sleep 2\ns_add_u32 s68, s68, 1\ns_and_b32 s69, s68, 63\ns_cbranch_scc1 .Lf6_%=\ns_cmp_gt_u32 s68, 0x200000\n" \
    "s_cbranch_scc1 .Lf1_%=\nbuffer_load_dword v12, off, s[64:67], 0 sc1\ns_waitcnt vmcnt(0)\nv_readfirstlane_b32 s69, v12\ns_cmp_eq_u32 s69, 1\ns_cbranch_scc0 .Lf6_%=\n" \
    "s_endpgm\n.Lf15_%=:\nbuffer_load_dwordx2 v[104:105], v2, s[64:67], s74 offen offset:0 sc1\n" \
    "v_pk_fma_f32 v[56:57], v[72:73], v[14:15], v[56:57] op_sel_hi:[1,0,1] neg_lo:[1,0,0] neg_hi:[1,0,0]\n" \
    "v_pk_fma_f32 v[58:59], v[74:75], v[14:15], v[58:59] op_sel_hi:[1,0,1] neg_lo:[1,0,0] neg_hi:[1,0,0]\n" \
    "v_pk_fma_f32 v[60:61], v[76:77], v[14:15], v[60:61] op_sel_hi:[1,0,1] neg_lo:[1,0,0] neg_hi:[1,0,0]\n" \
    "v_pk_fma_f32 v[62:63], v[78:79], v[14:15], v[62:63] op_sel_hi:[1,0,1] neg_lo:[1,0,0] neg_hi:[1,0,0]\nv_pk_fma_f32 v[56:57], v[56:57], v[14:15], v[88:89] op_sel:[0,1,0]\n" \
    "v_pk_fma_f32 v[58:59], v[58:59], v[14:15], v[90:91] op_sel:[0,1,0]\nv_pk_fma_f32 v[60:61], v[60:61], v[14:15], v[92:93] op_sel:[0,1,0]\ns_add_u32 m0, s75, 0\n" \
    "s_add_u32 s70, s74, s42\nbuffer_load_dwordx4 v0, s[64:67], s70 offen offset:0 sc1 lds\nv_pk_fma_f32 v[62:63], v[62:63], v[14:15], v[94:95] op_sel:[0,1,0]\n" \
    "v_pk_mul_f32 v[112:113], v[56:57], v[56:57]\nv_pk_mul_f32 v[114:115], v[58:59], v[58:59]\nv_pk_mul_f32 v[116:117], v[60:61], v[60:61]\n" \
    "v_pk_mul_f32 v[118:119], v[62:63], v[62:63]\nv_pk_fma_f32 v[112:113], v[112:113], s[46:47], s[46:47] op_sel:[0,0,1] op_sel_hi:[1,0,1]\n" \
    "v_pk_fma_f32 v[114:115], v[114:115], s[46:47], s[46:47] op_sel:[0,0,1] op_sel_hi:[1,0,1]\nbuffer_load_dwordx4 v0, s[64:67], s70 offen offset:1024 sc1 lds\n" \
    "v_pk_fma_f32 v[116:117], v[116:117], s[46:47], s[46:47] op_sel:[0,0,1] op_sel_hi:[1,0,1]\n" \
    "v_pk_fma_f32 v[118:119], v[118:119], s[46:47], s[46:47] op_sel:[0,0,1] op_sel_hi:[1,0,1]\nv_pk_mul_f32 v[112:113], v[112:113], v[56:57]\n" \
    "v_pk_mul_f32 v[114:115], v[114:115], v[58:59]\nv_pk_mul_f32 v[116:117], v[116:117], v[60:61]\nv_pk_mul_f32 v[118:119], v[118:119], v[62:63]\nv_exp_f32 v112, v112\n" \
    "buffer_load_dwordx4 v0, s[64:67], s70 offen offset:2048 sc1 lds\nv_exp_f32 v113, v113\nv_exp_f32 v114, v114\nv_exp_f32 v115, v115\nv_exp_f32 v116, v116\n" \
    "v_exp_f32 v117, v117\nv_exp_f32 v118, v118\nv_exp_f32 v119, v119\nbuffer_load_dwordx4 v0, s[64:67], s70 offen offset:3072 sc1 lds\n" \
    "v_pk_add_f32 v[112:113], v[112:113], 1.0 op_sel_hi:[1,0]\nv_pk_add_f32 v[114:115], v[114:115], 1.0 op_sel_hi:[1,0]\n" \
    "v_pk_add_f32 v[116:117], v[116:117], 1.0 op_sel_hi:[1,0]\nv_pk_add_f32 v[118:119], v[118:119], 1.0 op_sel_hi:[1,0]\nv_rcp_f32 v112, v112\nv_rcp_f32 v113, v113\n" \
    "v_rcp_f32 v114, v114\nbuffer_load_dwordx2 v[106:107], v2, s[64:67], s74 offen offset:256 sc1\nv_rcp_f32 v115, v115\nv_rcp_f32 v116, v116\nv_rcp_f32 v117, v117\n" \
    "v_rcp_f32 v118, v118\nv_rcp_f32 v119, v119\nv_pk_mul_f32 v[56:57], v[56:57], v[112:113]\nv_pk_mul_f32 v[58:59], v[58:59], v[114:115]\n" \
    "buffer_load_dwordx2 v[108:109], v2, s[64:67], s74 offen offset:512 sc1\nv_pk_mul_f32 v[60:61], v[60:61], v[116:117]\nv_pk_mul_f32 v[62:63], v[62:63], v[118:119]\n" \
    "v_cvt_pk_bf16_f32 v56, v56, v57\nv_cvt_pk_bf16_f32 v57, v58, v59\nv_cvt_pk_bf16_f32 v58, v60, v61\nv_cvt_pk_bf16_f32 v59, v62, v63\nv_min_u32 v56, -2, v56\n" \
    "buffer_load_dwordx2 v[110:111], v2, s[64:67], s74 offen offset:768 sc1\nv_min_u32 v57, -2, v57\nv_min_u32 v58, -2, v58\nv_min_u32 v59, -2, v59\ns_nop 0\n" \
    "v_permlane32_swap_b32 v56, v58\nv_permlane32_swap_b32 v57, v59\nbuffer_store_dwordx4 v[56:59], v5, s[64:67], s77 offen offset:0 sc1\n" \
    "v_pk_fma_f32 v[64:65], v[80:81], v[14:15], v[64:65] op_sel_hi:[1,0,1] neg_lo:[1,0,0] neg_hi:[1,0,0]\ns_add_u32 m0, s75, 16384\ns_add_u32 s70, s74, s43\n" \
    "buffer_load_dwordx4 v0, s[64:67], s70 offen offset:0 sc1 lds\ns_cmp_ge_u32 s81, 13\ns_cbranch_scc1 .Lf24_%=\n.Lf25_%=:\n" \
    "v_pk_fma_f32 v[66:67], v[82:83], v[14:15], v[66:67] op_sel_hi:[1,0,1] neg_lo:[1,0,0] neg_hi:[1,0,0]\n" \
    "v_pk_fma_f32 v[68:69], v[84:85], v[14:15], v[68:69] op_sel_hi:[1,0,1] neg_lo:[1,0,0] neg_hi:[1,0,0]\n" \
    "v_pk_fma_f32 v[70:71], v[86:87], v[14:15], v[70:71] op_sel_hi:[1,0,1] neg_lo:[1,0,0] neg_hi:[1,0,0]\nv_pk_fma_f32 v[64:65], v[64:65], v[14:15], v[96:97] op_sel:[0,1,0]\n" \
    "v_pk_fma_f32 v[66:67], v[66:67], v[14:15], v[98:99] op_sel:[0,1,0]\nv_pk_fma_f32 v[68:69], v[68:69], v[14:15], v[100:101] op_sel:[0,1,0]\n" \
    "buffer_load_dwordx4 v0, s[64:67], s70 offen offset:1024 sc1 lds\nv_pk_fma_f32 v[70:71], v[70:71], v[14:15], v[102:103] op_sel:[0,1,0]\n" \
    "v_pk_mul_f32 v[112:113], v[64:65], v[64:65]\nv_pk_mul_f32 v[114:115], v[66:67], v[66:67]\nv_pk_mul_f32 v[116:117], v[68:69], v[68:69]\n" \
    "v_pk_mul_f32 v[118:119], v[70:71], v[70:71]\nv_pk_fma_f32 v[112:113], v[112:113], s[46:47], s[46:47] op_sel:[0,0,1] op_sel_hi:[1,0,1]\n" \
    "v_pk_fma_f32 v[114:115], v[114:115], s[46:47], s[46:47] op_sel:[0,0,1] op_sel_hi:[1,0,1]\nbuffer_load_dwordx4 v0, s[64:67], s70 offen offset:2048 sc1 lds\n" \
    "v_pk_fma_f32 v[116:117], v[116:117], s[46:47], s[46:47] op_sel:[0,0,1] op_sel_hi:[1,0,1]\n" \
    "v_pk_fma_f32 v[118:119], v[118:119], s[46:47], s[46:47] op_sel:[0,0,1] op_sel_hi:[1,0,1]\nv_pk_mul_f32 v[112:113], v[112:113], v[64:65]\n" \
    "v_pk_mul_f32 v[114:115], v[114:115], v[66:67]\nv_pk_mul_f32 v[116:117], v[116:117], v[68:69]\nv_pk_mul_f32 v[118:119], v[118:119], v[70:71]\nv_exp_f32 v112, v112\n" \
    "buffer_load_dwordx4 v0, s[64:67], s70 offen offset:3072 sc1 lds\nv_exp_f32 v113, v113\nv_exp_f32 v114, v114\nv_exp_f32 v115, v115\nv_exp_f32 v116, v116\n" \
    "v_exp_f32 v117, v117\nv_exp_f32 v118, v118\nv_exp_f32 v119, v119\nv_pk_add_f32 v[112:113], v[112:113], 1.0 op_sel_hi:[1,0]\n" \
    "v_pk_add_f32 v[114:115], v[114:115], 1.0 op_sel_hi:[1,0]\nv_pk_add_f32 v[116:117], v[116:117], 1.0 op_sel_hi:[1,0]\n" \
    "v_pk_add_f32 v[118:119], v[118:119], 1.0 op_sel_hi:[1,0]\nv_rcp_f32 v112, v112\nv_rcp_f32 v113, v113\nv_rcp_f32 v114, v114\nv_rcp_f32 v115, v115\nv_rcp_f32 v116, v116\n" \
    "v_rcp_f32 v117, v117\nv_rcp_f32 v118, v118\nv_rcp_f32 v119, v119\nv_pk_mul_f32 v[64:65], v[64:65], v[112:113]\nv_pk_mul_f32 v[66:67], v[66:67], v[114:115]\n" \
    "v_pk_mul_f32 v[68:69], v[68:69], v[116:117]\nv_pk_mul_f32 v[70:71], v[70:71], v[118:119]\nv_cvt_pk_bf16_f32 v64, v64, v65\nv_cvt_pk_bf16_f32 v65, v66, v67\n" \
    "v_cvt_pk_bf16_f32 v66, v68, v69\nv_cvt_pk_bf16_f32 v67, v70, v71\nv_min_u32 v64, -2, v64\nv_min_u32 v65, -2, v65\nv_min_u32 v66, -2, v66\nv_min_u32 v67, -2, v67\ns_nop 0\n" \
    "v_permlane32_swap_b32 v64, v66\nv_permlane32_swap_b32 v65, v67\nbuffer_store_dwordx4 v[64:67], v5, s[64:67], s77 offen offset:512 sc1\ns_mov_b32 s50, s71\n" \
    "s_mov_b32 s53, s74\ns_xor_b32 s51, s51, 0x10000\ns_mov_b32 s72, s75\ns_branch .Lf14_%=\n.Lf9_%=:\nv_mov_b32 v12, 0\ns_sub_u32 s69, s52, s79\ns_add_u32 s69, s69, s51\n" \
    "v_add_u32 v13, s69, v0\ns_mov_b32 s69, 64\n.Lf10_%=:\nds_read_b128 v[112:115], v13\nv_add_u32 v13, 0x400, v13\ns_waitcnt lgkmcnt(0)\nv_max3_u32 v12, v12, v112, v113\n" \
    "v_max3_u32 v12, v12, v114, v115\ns_sub_u32 s69, s69, 1\ns_cmp_lg_u32 s69, 0\ns_cbranch_scc1 .Lf10_%=\nv_cmp_eq_u32 vcc, -1, v12\ns_cbranch_vccz .Lf11_%=\n" \
    "s_waitcnt vmcnt(0)\ns_barrier\n.Lf12_%=:\ns_sleep 2\ns_mov_b32 s68, s80\ns_add_u32 s80, s80, 1\ns_add_u32 s68, s68, 1\ns_and_b32 s69, s68, 63\ns_cbranch_scc1 .Lf13_%=\n" \
    "s_cmp_gt_u32 s68, 0x200000\ns_cbranch_scc1 .Lf1_%=\nbuffer_load_dword v12, off, s[64:67], 0 sc1\ns_waitcnt vmcnt(0)\nv_readfirstlane_b32 s69, v12\ns_cmp_eq_u32 s69, 1\n" \
    "s_cbranch_scc0 .Lf13_%=\ns_endpgm\n.Lf16_%=:\ns_mov_b32 s68, 0\n.Lf18_%=:\ns_sleep 1\nbuffer_load_dwordx2 v[104:105], v2, s[64:67], s53 offen offset:0 sc1\n" \
    "buffer_load_dwordx2 v[106:107], v2, s[64:67], s53 offen offset:256 sc1\nbuffer_load_dwordx2 v[108:109], v2, s[64:67], s53 offen offset:512 sc1\n" \
    "buffer_load_dwordx2 v[110:111], v2, s[64:67], s53 offen offset:768 sc1\ns_waitcnt vmcnt(0)\nv_max3_u32 v12, v104, v105, v106\nv_max3_u32 v12, v12, v107, v108\n" \
    "v_max3_u32 v12, v12, v109, v110\nv_max_u32 v12, v12, v111\nv_cmp_eq_u32 vcc, -1, v12\ns_cbranch_vccz .Lf17_%=\ns_add_u32 s68, s68, 1\ns_and_b32 s69, s68, 63\n" \
    "s_cbranch_scc1 .Lf18_%=\ns_cmp_gt_u32 s68, 0x200000\ns_cbranch_scc1 .Lf1_%=\nbuffer_load_dword v12, off, s[64:67], 0 sc1\ns_waitcnt vmcnt(0)\nv_readfirstlane_b32 s69, v12\n" \
    "s_cmp_eq_u32 s69, 1\ns_cbranch_scc0 .Lf18_%=\ns_endpgm\n.Lf19_%=:\ns_mov_b32 s68, 0\n.Lf21_%=:\ns_sleep 1\n" \
    "buffer_load_dwordx2 v[104:105], v2, s[64:67], s53 offen offset:0 sc1\nbuffer_load_dwordx2 v[106:107], v2, s[64:67], s53 offen offset:256 sc1\n" \
    "buffer_load_dwordx2 v[108:109], v2, s[64:67], s53 offen offset:512 sc1\nbuffer_load_dwordx2 v[110:111], v2, s[64:67], s53 offen offset:768 sc1\ns_waitcnt vmcnt(0)\n" \
    "v_max3_u32 v12, v104, v105, v106\nv_max3_u32 v12, v12, v107, v108\nv_max3_u32 v12, v12, v109, v110\nv_max_u32 v12, v12, v111\nv_cmp_eq_u32 vcc, -1, v12\n" \
    "s_cbranch_vccz .Lf20_%=\ns_add_u32 s68, s68, 1\ns_and_b32 s69, s68, 63\ns_cbranch_scc1 .Lf21_%=\ns_cmp_gt_u32 s68, 0x200000\ns_cbranch_scc1 .Lf1_%=\n" \
    "buffer_load_dword v12, off, s[64:67], 0 sc1\ns_waitcnt vmcnt(0)\nv_readfirstlane_b32 s69, v12\ns_cmp_eq_u32 s69, 1\ns_cbranch_scc0 .Lf21_%=\ns_endpgm\n.Lf22_%=:\n" \
    "s_add_u32 s81, s50, 1\nv_mov_b32 v16, s81\ns_and_b32 s81, s50, 1\ns_lshl_b32 s81, s81, 2\nbuffer_atomic_umax v16, v6, s[64:67], s81 offen sc1\ns_branch .Lf23_%=\n" \
    ".Lf24_%=:\ns_add_u32 s81, s50, 1\nv_mov_b32 v16, s81\ns_and_b32 s81, s50, 1\ns_lshl_b32 s81, s81, 2\nbuffer_atomic_umax v16, v6, s[64:67], s81 offen sc1\n" \
    "s_branch .Lf25_%=\n.Lf1_%=:\nv_mov_b32 v12, 1\nbuffer_store_dword v12, off, s[64:67], 0 sc1\ns_endpgm\n.Lf2_%=:\n"
#define ROLE_F2 \
    "s_mov_b32 s80, 0\ns_mov_b32 s81, 0\nv_mov_b32 v18, 0x7f000000\nv_mov_b32 v19, 0x7f000000\nv_mov_b32 v20, 0x7f000000\n.Lg7_%=:\ns_mov_b32 s68, 0\n.Lg6_%=:\n" \
    "buffer_load_dword v17, v5, s[64:67], 0 offen sc1\ns_waitcnt vmcnt(0)\ns_and_b32 s69, s50, 1\ns_nop 3\nv_readlane_b32 s76, v17, s69\ns_cmp_gt_i32 s76, s50\n" \
    "s_cbranch_scc0 .Lg8_%=\n.Lg3_%=:\nbuffer_load_dword v21, v7, s[64:67], s51 offen sc1\ns_add_u32 s70, s51, s56\n" \
    "buffer_load_dwordx4 v[24:27], v0, s[64:67], s70 offen offset:0 sc1\nbuffer_load_dwordx4 v[28:31], v0, s[64:67], s70 offen offset:1024 sc1\n" \
    "buffer_load_dwordx4 v[32:35], v0, s[64:67], s70 offen offset:2048 sc1\nbuffer_load_dwordx4 v[36:39], v0, s[64:67], s70 offen offset:3072 sc1\ns_add_u32 s70, s51, s57\n" \
    "buffer_load_dwordx4 v[40:43], v0, s[64:67], s70 offen offset:0 sc1\nbuffer_load_dwordx4 v[44:47], v0, s[64:67], s70 offen offset:1024 sc1\n" \
    "buffer_load_dwordx4 v[48:51], v0, s[64:67], s70 offen offset:2048 sc1\nbuffer_load_dwordx4 v[52:55], v0, s[64:67], s70 offen offset:3072 sc1\ns_waitcnt vmcnt(8)\n" \
    "v_cmp_eq_u32 vcc, -1, v21\ns_cbranch_vccnz .Lg12_%=\n.Lg4_%=:\ns_waitcnt vmcnt(6)\nv_mfma_f32_16x16x32_bf16 v[136:139], a[0:3], v[24:27], 0\ns_add_u32 s70, s51, s58\n" \
    "buffer_load_dwordx4 v[56:59], v0, s[64:67], s70 offen offset:0 sc1\nv_mfma_f32_16x16x32_bf16 v[140:143], a[0:3], v[28:31], 0\ns_add_u32 s71, s50, 1\ns_cmp_ge_u32 s71, s55\n" \
    "s_addc_u32 s71, s71, 0\nv_mfma_f32_16x16x32_bf16 v[144:147], a[4:7], v[24:27], 0\ns_min_u32 s72, s71, 63\ns_lshl_b32 s72, s72, 20\ns_add_u32 s72, s72, s40\n" \
    "v_mfma_f32_16x16x32_bf16 v[148:151], a[4:7], v[28:31], 0\ns_add_u32 s74, s48, s72\ns_lshr_b32 s74, s74, 8\ns_xor_b32 s74, s74, s49\n" \
    "v_mfma_f32_16x16x32_bf16 v[152:155], a[8:11], v[24:27], 0\nbuffer_load_dwordx4 v[60:63], v0, s[64:67], s70 offen offset:1024 sc1\ns_and_b32 s74, s74, 0x2000\n" \
    "v_mfma_f32_16x16x32_bf16 v[156:159], a[8:11], v[28:31], 0\ns_add_u32 s74, s74, s72\ns_and_b32 s75, s71, 1\nv_mfma_f32_16x16x32_bf16 v[160:163], a[12:15], v[24:27], 0\n" \
    "v_mfma_f32_16x16x32_bf16 v[164:167], a[12:15], v[28:31], 0\ns_waitcnt vmcnt(6)\nv_mfma_f32_16x16x32_bf16 v[136:139], a[16:19], v[32:35], v[136:139]\n" \
    "buffer_load_dwordx4 v[64:67], v0, s[64:67], s70 offen offset:2048 sc1\nv_mfma_f32_16x16x32_bf16 v[140:143], a[16:19], v[36:39], v[140:143]\n" \
    "v_mfma_f32_16x16x32_bf16 v[144:147], a[20:23], v[32:35], v[144:147]\nv_mfma_f32_16x16x32_bf16 v[148:151], a[20:23], v[36:39], v[148:151]\n" \
    "v_mfma_f32_16x16x32_bf16 v[152:155], a[24:27], v[32:35], v[152:155]\nbuffer_load_dwordx4 v[68:71], v0, s[64:67], s70 offen offset:3072 sc1\n" \
    "v_mfma_f32_16x16x32_bf16 v[156:159], a[24:27], v[36:39], v[156:159]\nv_mfma_f32_16x16x32_bf16 v[160:163], a[28:31], v[32:35], v[160:163]\n" \
    "v_mfma_f32_16x16x32_bf16 v[164:167], a[28:31], v[36:39], v[164:167]\ns_waitcnt vmcnt(6)\nv_mfma_f32_16x16x32_bf16 v[136:139], a[32:35], v[40:43], v[136:139]\n" \
    "s_add_u32 s70, s51, s59\nbuffer_load_dwordx4 v[72:75], v0, s[64:67], s70 offen offset:0 sc1\nv_mfma_f32_16x16x32_bf16 v[140:143], a[32:35], v[44:47], v[140:143]\n" \
    "v_mfma_f32_16x16x32_bf16 v[144:147], a[36:39], v[40:43], v[144:147]\ns_add_u32 s73, s52, s41\nbuffer_load_dwordx4 v[172:175], v18, s[64:67], s73 offen sc1\n" \
    "v_mfma_f32_16x16x32_bf16 v[148:151], a[36:39], v[44:47], v[148:151]\nbuffer_load_dwordx2 v[176:177], v19, s[64:67], s52 offen sc1\n" \
    "v_mfma_f32_16x16x32_bf16 v[152:155], a[40:43], v[40:43], v[152:155]\nbuffer_load_dwordx4 v[76:79], v0, s[64:67], s70 offen offset:1024 sc1\n" \
    "v_mfma_f32_16x16x32_bf16 v[156:159], a[40:43], v[44:47], v[156:159]\nv_mfma_f32_16x16x32_bf16 v[160:163], a[44:47], v[40:43], v[160:163]\n" \
    "v_mfma_f32_16x16x32_bf16 v[164:167], a[44:47], v[44:47], v[164:167]\ns_waitcnt vmcnt(8)\nv_mfma_f32_16x16x32_bf16 v[136:139], a[48:51], v[48:51], v[136:139]\n" \
    "buffer_load_dwordx4 v[80:83], v0, s[64:67], s70 offen offset:2048 sc1\nv_mfma_f32_16x16x32_bf16 v[140:143], a[48:51], v[52:55], v[140:143]\n" \
    "v_mfma_f32_16x16x32_bf16 v[144:147], a[52:55], v[48:51], v[144:147]\nv_mfma_f32_16x16x32_bf16 v[148:151], a[52:55], v[52:55], v[148:151]\n" \
    "v_mfma_f32_16x16x32_bf16 v[152:155], a[56:59], v[48:51], v[152:155]\nbuffer_load_dwordx4 v[84:87], v0, s[64:67], s70 offen offset:3072 sc1\n" \
    "v_mfma_f32_16x16x32_bf16 v[156:159], a[56:59], v[52:55], v[156:159]\nv_mfma_f32_16x16x32_bf16 v[160:163], a[60:63], v[48:51], v[160:163]\n" \
    "v_mfma_f32_16x16x32_bf16 v[164:167], a[60:63], v[52:55], v[164:167]\ns_waitcnt vmcnt(8)\nv_mfma_f32_16x16x32_bf16 v[136:139], a[64:67], v[56:59], v[136:139]\n" \
    "s_add_u32 s70, s51, s60\nbuffer_load_dwordx4 v[88:91], v0, s[64:67], s70 offen offset:0 sc1\nv_mfma_f32_16x16x32_bf16 v[140:143], a[64:67], v[60:63], v[140:143]\n" \
    "v_mfma_f32_16x16x32_bf16 v[144:147], a[68:71], v[56:59], v[144:147]\nv_mfma_f32_16x16x32_bf16 v[148:151], a[68:71], v[60:63], v[148:151]\n" \
    "v_mfma_f32_16x16x32_bf16 v[152:155], a[72:75], v[56:59], v[152:155]\nbuffer_load_dwordx4 v[92:95], v0, s[64:67], s70 offen offset:1024 sc1\n" \
    "v_mfma_f32_16x16x32_bf16 v[156:159], a[72:75], v[60:63], v[156:159]\nv_mfma_f32_16x16x32_bf16 v[160:163], a[76:79], v[56:59], v[160:163]\n" \
    "v_mfma_f32_16x16x32_bf16 v[164:167], a[76:79], v[60:63], v[164:167]\ns_waitcnt vmcnt(8)\nv_mfma_f32_16x16x32_bf16 v[136:139], a[80:83], v[64:67], v[136:139]\n" \
    "buffer_load_dwordx4 v[96:99], v0, s[64:67], s70 offen offset:2048 sc1\nv_mfma_f32_16x16x32_bf16 v[140:143], a[80:83], v[68:71], v[140:143]\n" \
    "v_mfma_f32_16x16x32_bf16 v[144:147], a[84:87], v[64:67], v[144:147]\nbuffer_load_dword v17, v5, s[64:67], 0 offen sc1\n" \
    "v_mfma_f32_16x16x32_bf16 v[148:151], a[84:87], v[68:71], v[148:151]\nv_mfma_f32_16x16x32_bf16 v[152:155], a[88:91], v[64:67], v[152:155]\n" \
    "buffer_load_dwordx4 v[100:103], v0, s[64:67], s70 offen offset:3072 sc1\nv_mfma_f32_16x16x32_bf16 v[156:159], a[88:91], v[68:71], v[156:159]\n" \
    "v_mfma_f32_16x16x32_bf16 v[160:163], a[92:95], v[64:67], v[160:163]\nv_mfma_f32_16x16x32_bf16 v[164:167], a[92:95], v[68:71], v[164:167]\ns_waitcnt vmcnt(7)\n" \
    "v_mfma_f32_16x16x32_bf16 v[136:139], a[96:99], v[72:75], v[136:139]\ns_add_u32 s70, s51, s61\nbuffer_load_dwordx4 v[104:107], v0, s[64:67], s70 offen offset:0 sc1\n" \
    "v_mfma_f32_16x16x32_bf16 v[140:143], a[96:99], v[76:79], v[140:143]\nv_mfma_f32_16x16x32_bf16 v[144:147], a[100:103], v[72:75], v[144:147]\n" \
    "v_max3_u32 v21, v172, v173, v174\nv_max_u32 v21, v21, v175\nv_mfma_f32_16x16x32_bf16 v[148:151], a[100:103], v[76:79], v[148:151]\nv_cmp_eq_u32 vcc, -1, v21\n" \
    "v_mfma_f32_16x16x32_bf16 v[152:155], a[104:107], v[72:75], v[152:155]\nbuffer_load_dwordx4 v[108:111], v0, s[64:67], s70 offen offset:1024 sc1\n" \
    "v_mfma_f32_16x16x32_bf16 v[156:159], a[104:107], v[76:79], v[156:159]\ns_cbranch_vccnz .Lg15_%=\n.Lg16_%=:\nv_add_f32 v172, v172, v168\n" \
    "v_mfma_f32_16x16x32_bf16 v[160:163], a[108:111], v[72:75], v[160:163]\nv_add_f32 v173, v173, v169\nv_add_f32 v174, v174, v170\n" \
    "v_mfma_f32_16x16x32_bf16 v[164:167], a[108:111], v[76:79], v[164:167]\ns_waitcnt vmcnt(7)\nv_add_f32 v175, v175, v171\nv_lshlrev_b32 v168, 16, v176\n" \
    "v_mfma_f32_16x16x32_bf16 v[136:139], a[112:115], v[80:83], v[136:139]\nbuffer_load_dwordx4 v[112:115], v0, s[64:67], s70 offen offset:2048 sc1\n" \
    "v_and_b32 v169, 0xffff0000, v176\nv_mfma_f32_16x16x32_bf16 v[140:143], a[112:115], v[84:87], v[140:143]\nv_lshlrev_b32 v170, 16, v177\nv_and_b32 v171, 0xffff0000, v177\n" \
    "v_mfma_f32_16x16x32_bf16 v[144:147], a[116:119], v[80:83], v[144:147]\nv_fma_f32 v172, v172, v8, v168\nv_fma_f32 v173, v173, v9, v169\n" \
    "v_mfma_f32_16x16x32_bf16 v[148:151], a[116:119], v[84:87], v[148:151]\nv_fma_f32 v174, v174, v10, v170\nv_fma_f32 v175, v175, v11, v171\n" \
    "v_mfma_f32_16x16x32_bf16 v[152:155], a[120:123], v[80:83], v[152:155]\nbuffer_load_dwordx4 v[116:119], v0, s[64:67], s70 offen offset:3072 sc1\n" \
    "v_cvt_pk_bf16_f32 v176, v172, v173\nv_mfma_f32_16x16x32_bf16 v[156:159], a[120:123], v[84:87], v[156:159]\nv_cvt_pk_bf16_f32 v177, v174, v175\n" \
    "v_mfma_f32_16x16x32_bf16 v[160:163], a[124:127], v[80:83], v[160:163]\ns_lshl_b32 s73, s53, 16\nv_mfma_f32_16x16x32_bf16 v[164:167], a[124:127], v[84:87], v[164:167]\n" \
    "s_waitcnt vmcnt(7)\nv_mfma_f32_16x16x32_bf16 v[136:139], a[128:131], v[88:91], v[136:139]\ns_add_u32 s70, s51, s62\n" \
    "buffer_load_dwordx4 v[120:123], v0, s[64:67], s70 offen offset:0 sc1\nv_mfma_f32_16x16x32_bf16 v[140:143], a[128:131], v[92:95], v[140:143]\n" \
    "buffer_store_dwordx2 v[176:177], v20, s[44:47], s73 offen\nv_mfma_f32_16x16x32_bf16 v[144:147], a[132:135], v[88:91], v[144:147]\n" \
    "v_mfma_f32_16x16x32_bf16 v[148:151], a[132:135], v[92:95], v[148:151]\nv_mfma_f32_16x16x32_bf16 v[152:155], a[136:139], v[88:91], v[152:155]\n" \
    "buffer_load_dwordx4 v[124:127], v0, s[64:67], s70 offen offset:1024 sc1\nv_mfma_f32_16x16x32_bf16 v[156:159], a[136:139], v[92:95], v[156:159]\n" \
    "v_mfma_f32_16x16x32_bf16 v[160:163], a[140:143], v[88:91], v[160:163]\nv_mfma_f32_16x16x32_bf16 v[164:167], a[140:143], v[92:95], v[164:167]\ns_waitcnt vmcnt(7)\n" \
    "v_mfma_f32_16x16x32_bf16 v[136:139], a[144:147], v[96:99], v[136:139]\nbuffer_load_dwordx4 v[128:131], v0, s[64:67], s70 offen offset:2048 sc1\n" \
    "v_mfma_f32_16x16x32_bf16 v[140:143], a[144:147], v[100:103], v[140:143]\nv_mfma_f32_16x16x32_bf16 v[144:147], a[148:151], v[96:99], v[144:147]\n" \
    "v_readlane_b32 s76, v17, s75\nv_mfma_f32_16x16x32_bf16 v[148:151], a[148:151], v[100:103], v[148:151]\n" \
    "v_mfma_f32_16x16x32_bf16 v[152:155], a[152:155], v[96:99], v[152:155]\nbuffer_load_dwordx4 v[132:135], v0, s[64:67], s70 offen offset:3072 sc1\n" \
    "v_mfma_f32_16x16x32_bf16 v[156:159], a[152:155], v[100:103], v[156:159]\ns_cmp_gt_i32 s76, s71\ns_cselect_b32 s77, 0, 0x7f000000\nv_or_b32 v16, s77, v0\n" \
    "v_mfma_f32_16x16x32_bf16 v[160:163], a[156:159], v[96:99], v[160:163]\nv_mfma_f32_16x16x32_bf16 v[164:167], a[156:159], v[100:103], v[164:167]\ns_waitcnt vmcnt(7)\n" \
    "v_mfma_f32_16x16x32_bf16 v[136:139], a[160:163], v[104:107], v[136:139]\nv_mfma_f32_16x16x32_bf16 v[140:143], a[160:163], v[108:111], v[140:143]\ns_add_u32 s70, s74, s56\n" \
    "buffer_load_dwordx4 v[24:27], v16, s[64:67], s70 offen offset:0 sc1\nv_mfma_f32_16x16x32_bf16 v[144:147], a[164:167], v[104:107], v[144:147]\n" \
    "v_mfma_f32_16x16x32_bf16 v[148:151], a[164:167], v[108:111], v[148:151]\nbuffer_load_dword v17, v5, s[64:67], 0 offen sc1\n" \
    "v_mfma_f32_16x16x32_bf16 v[152:155], a[168:171], v[104:107], v[152:155]\nv_mfma_f32_16x16x32_bf16 v[156:159], a[168:171], v[108:111], v[156:159]\n" \
    "buffer_load_dwordx4 v[28:31], v16, s[64:67], s70 offen offset:1024 sc1\nv_mfma_f32_16x16x32_bf16 v[160:163], a[172:175], v[104:107], v[160:163]\n" \
    "v_mfma_f32_16x16x32_bf16 v[164:167], a[172:175], v[108:111], v[164:167]\ns_waitcnt vmcnt(8)\nv_mfma_f32_16x16x32_bf16 v[136:139], a[176:179], v[112:115], v[136:139]\n" \
    "v_mfma_f32_16x16x32_bf16 v[140:143], a[176:179], v[116:119], v[140:143]\nbuffer_load_dwordx4 v[32:35], v16, s[64:67], s70 offen offset:2048 sc1\n" \
    "v_mfma_f32_16x16x32_bf16 v[144:147], a[180:183], v[112:115], v[144:147]\nv_mfma_f32_16x16x32_bf16 v[148:151], a[180:183], v[116:119], v[148:151]\n" \
    "v_mfma_f32_16x16x32_bf16 v[152:155], a[184:187], v[112:115], v[152:155]\nv_mfma_f32_16x16x32_bf16 v[156:159], a[184:187], v[116:119], v[156:159]\n" \
    "buffer_load_dwordx4 v[36:39], v16, s[64:67], s70 offen offset:3072 sc1\nv_mfma_f32_16x16x32_bf16 v[160:163], a[188:191], v[112:115], v[160:163]\n" \
    "v_mfma_f32_16x16x32_bf16 v[164:167], a[188:191], v[116:119], v[164:167]\ns_waitcnt vmcnt(7)\nv_mfma_f32_16x16x32_bf16 v[136:139], a[192:195], v[120:123], v[136:139]\n" \
    "s_waitcnt vmcnt(5)\nv_mfma_f32_16x16x32_bf16 v[136:139], a[208:211], v[128:131], v[136:139]\ns_add_u32 s70, s74, s57\n" \
    "buffer_load_dwordx4 v[40:43], v16, s[64:67], s70 offen offset:0 sc1\nv_mfma_f32_16x16x32_bf16 v[140:143], a[192:195], v[124:127], v[140:143]\n" \
    "v_mfma_f32_16x16x32_bf16 v[140:143], a[208:211], v[132:135], v[140:143]\nv_mfma_f32_16x16x32_bf16 v[144:147], a[196:199], v[120:123], v[144:147]\n" \
    "v_mfma_f32_16x16x32_bf16 v[144:147], a[212:215], v[128:131], v[144:147]\nbuffer_load_dwordx4 v[44:47], v16, s[64:67], s70 offen offset:1024 sc1\n" \
    "v_mfma_f32_16x16x32_bf16 v[148:151], a[196:199], v[124:127], v[148:151]\nds_write_b128 v1, v[136:139] offset:0\n" \
    "v_mfma_f32_16x16x32_bf16 v[148:151], a[212:215], v[132:135], v[148:151]\nv_mfma_f32_16x16x32_bf16 v[152:155], a[200:203], v[120:123], v[152:155]\ns_nop 0\n" \
    "ds_write_b128 v1, v[140:143] offset:1024\nv_mfma_f32_16x16x32_bf16 v[152:155], a[216:219], v[128:131], v[152:155]\n" \
    "buffer_load_dwordx4 v[48:51], v16, s[64:67], s70 offen offset:2048 sc1\nv_mfma_f32_16x16x32_bf16 v[156:159], a[200:203], v[124:127], v[156:159]\n" \
    "ds_write_b128 v1, v[144:147] offset:2048\nv_mfma_f32_16x16x32_bf16 v[156:159], a[216:219], v[132:135], v[156:159]\n" \
    "v_mfma_f32_16x16x32_bf16 v[160:163], a[204:207], v[120:123], v[160:163]\nds_write_b128 v1, v[148:151] offset:3072\n" \
    "v_mfma_f32_16x16x32_bf16 v[160:163], a[220:223], v[128:131], v[160:163]\nbuffer_load_dwordx4 v[52:55], v16, s[64:67], s70 offen offset:3072 sc1\n" \
    "v_mfma_f32_16x16x32_bf16 v[164:167], a[204:207], v[124:127], v[164:167]\nds_write_b128 v1, v[152:155] offset:4096\n" \
    "v_mfma_f32_16x16x32_bf16 v[164:167], a[220:223], v[132:135], v[164:167]\ns_nop 0\nds_write_b128 v1, v[156:159] offset:5120\ns_nop 1\n" \
    "ds_write_b128 v1, v[160:163] offset:6144\ns_nop 2\nds_write_b128 v1, v[164:167] offset:7168\nv_cmp_u_f32 vcc, v136, v140\ns_cbranch_vccnz .Lg9_%=\n.Lg11_%=:\n" \
    "s_waitcnt lgkmcnt(0)\ns_barrier\nds_read_b128 v[136:139], v6 offset:0\nds_read_b128 v[140:143], v6 offset:8192\nds_read_b128 v[144:147], v6 offset:16384\n" \
    "ds_read_b128 v[148:151], v6 offset:24576\nds_read_b128 v[152:155], v2 offset:0\nds_read_b128 v[156:159], v2 offset:8192\nds_read_b128 v[160:163], v2 offset:16384\n" \
    "ds_read_b128 v[164:167], v2 offset:24576\ns_add_u32 s73, s51, s41\ns_xor_b32 s73, s73, 0x1000\ns_waitcnt lgkmcnt(4)\nv_pk_add_f32 v[136:137], v[136:137], v[140:141]\n" \
    "v_pk_add_f32 v[138:139], v[138:139], v[142:143]\nv_pk_add_f32 v[144:145], v[144:145], v[148:149]\nv_pk_add_f32 v[146:147], v[146:147], v[150:151]\n" \
    "v_pk_add_f32 v[136:137], v[136:137], v[144:145]\nv_pk_add_f32 v[138:139], v[138:139], v[146:147]\nbuffer_store_dwordx4 v[136:139], v0, s[64:67], s73 offen sc1\n" \
    "s_waitcnt lgkmcnt(0)\nv_pk_add_f32 v[152:153], v[152:153], v[156:157]\nv_pk_add_f32 v[154:155], v[154:155], v[158:159]\nv_pk_add_f32 v[160:161], v[160:161], v[164:165]\n" \
    "v_pk_add_f32 v[162:163], v[162:163], v[166:167]\nv_pk_add_f32 v[152:153], v[152:153], v[12:13]\nv_pk_add_f32 v[154:155], v[154:155], v[14:15]\n" \
    "v_pk_add_f32 v[168:169], v[152:153], v[160:161]\nv_pk_add_f32 v[170:171], v[154:155], v[162:163]\ns_memrealtime s[78:79]\nv_xor_b32 v1, 0x8000, v1\n" \
    "v_xor_b32 v2, 0x8000, v2\nv_xor_b32 v6, 0x8000, v6\nv_mov_b32 v18, v0\nv_mov_b32 v19, v3\nv_mov_b32 v20, v4\ns_mov_b32 s52, s51\ns_mov_b32 s53, s50\ns_mov_b32 s51, s74\n" \
    "s_mov_b32 s50, s71\ns_mov_b32 s80, 0\ns_cmp_eq_u32 s77, 0\ns_cbranch_scc1 .Lg4_%=\ns_waitcnt vmcnt(8)\ns_and_b32 s69, s50, 1\ns_nop 3\nv_readlane_b32 s76, v17, s69\n" \
    "s_cmp_gt_i32 s76, s50\ns_cbranch_scc1 .Lg3_%=\n.Lg14_%=:\ns_waitcnt lgkmcnt(0)\ns_add_u32 s78, s78, s42\n.Lg18_%=:\ns_memrealtime s[72:73]\ns_waitcnt lgkmcnt(0)\n" \
    "s_sub_u32 s72, s72, s78\ns_cmp_lt_i32 s72, 0\ns_cbranch_scc1 .Lg18_%=\ns_add_u32 s73, s52, s41\nbuffer_load_dwordx4 v[172:175], v0, s[64:67], s73 offen sc1\n" \
    "buffer_load_dwordx2 v[176:177], v3, s[64:67], s52 offen sc1\nbuffer_load_dword v17, v5, s[64:67], 0 offen sc1\ns_waitcnt vmcnt(1)\nv_max3_u32 v21, v172, v173, v174\n" \
    "v_max_u32 v21, v21, v175\nv_cmp_eq_u32 vcc, -1, v21\ns_cbranch_vccnz .Lg19_%=\n.Lg20_%=:\nv_add_f32 v172, v172, v168\nv_add_f32 v173, v173, v169\n" \
    "v_add_f32 v174, v174, v170\nv_add_f32 v175, v175, v171\nv_lshlrev_b32 v168, 16, v176\nv_and_b32 v169, 0xffff0000, v176\nv_lshlrev_b32 v170, 16, v177\n" \
    "v_and_b32 v171, 0xffff0000, v177\nv_fma_f32 v172, v172, v8, v168\nv_fma_f32 v173, v173, v9, v169\nv_fma_f32 v174, v174, v10, v170\nv_fma_f32 v175, v175, v11, v171\n" \
    "v_cvt_pk_bf16_f32 v176, v172, v173\nv_cvt_pk_bf16_f32 v177, v174, v175\ns_lshl_b32 s73, s53, 16\nbuffer_store_dwordx2 v[176:177], v4, s[44:47], s73 offen\n" \
    "v_mov_b32 v18, 0x7f000000\nv_mov_b32 v19, 0x7f000000\nv_mov_b32 v20, 0x7f000000\ns_cmp_ge_u32 s50, 64\ns_cbranch_scc1 .Lg2_%=\ns_waitcnt vmcnt(1)\ns_and_b32 s69, s50, 1\n" \
    "s_nop 3\nv_readlane_b32 s76, v17, s69\ns_cmp_gt_i32 s76, s50\ns_cbranch_scc1 .Lg3_%=\ns_branch .Lg7_%=\n.Lg8_%=:\ns_sleep 2\ns_add_u32 s68, s68, 1\ns_and_b32 s69, s68, 63\n" \
    "s_cbranch_scc1 .Lg6_%=\ns_cmp_gt_u32 s68, 0x200000\ns_cbranch_scc1 .Lg1_%=\nbuffer_load_dword v21, off, s[64:67], 0 sc1\ns_waitcnt vmcnt(0)\nv_readfirstlane_b32 s69, v21\n" \
    "s_cmp_eq_u32 s69, 1\ns_cbranch_scc0 .Lg6_%=\ns_endpgm\n.Lg12_%=:\ns_sleep 2\ns_mov_b32 s68, s81\ns_add_u32 s81, s81, 1\ns_add_u32 s68, s68, 1\ns_and_b32 s69, s68, 63\n" \
    "s_cbranch_scc1 .Lg3_%=\ns_cmp_gt_u32 s68, 0x200000\ns_cbranch_scc1 .Lg1_%=\nbuffer_load_dword v21, off, s[64:67], 0 sc1\ns_waitcnt vmcnt(0)\nv_readfirstlane_b32 s69, v21\n" \
    "s_cmp_eq_u32 s69, 1\ns_cbranch_scc0 .Lg3_%=\ns_endpgm\n.Lg9_%=:\ns_cmp_eq_u32 s80, 1\ns_cbranch_scc1 .Lg11_%=\ns_mov_b32 s68, 0\nv_mov_b32 v18, 0x7f000000\n" \
    "v_mov_b32 v19, 0x7f000000\nv_mov_b32 v20, 0x7f000000\n.Lg10_%=:\ns_add_u32 s70, s51, s56\nbuffer_load_dwordx4 v[24:27], v0, s[64:67], s70 offen offset:0 sc1\n" \
    "buffer_load_dwordx4 v[28:31], v0, s[64:67], s70 offen offset:1024 sc1\nbuffer_load_dwordx4 v[32:35], v0, s[64:67], s70 offen offset:2048 sc1\n" \
    "buffer_load_dwordx4 v[36:39], v0, s[64:67], s70 offen offset:3072 sc1\ns_add_u32 s70, s51, s57\nbuffer_load_dwordx4 v[40:43], v0, s[64:67], s70 offen offset:0 sc1\n" \
    "buffer_load_dwordx4 v[44:47], v0, s[64:67], s70 offen offset:1024 sc1\nbuffer_load_dwordx4 v[48:51], v0, s[64:67], s70 offen offset:2048 sc1\n" \
    "buffer_load_dwordx4 v[52:55], v0, s[64:67], s70 offen offset:3072 sc1\ns_add_u32 s70, s51, s58\nbuffer_load_dwordx4 v[56:59], v0, s[64:67], s70 offen offset:0 sc1\n" \
    "buffer_load_dwordx4 v[60:63], v0, s[64:67], s70 offen offset:1024 sc1\nbuffer_load_dwordx4 v[64:67], v0, s[64:67], s70 offen offset:2048 sc1\n" \
    "buffer_load_dwordx4 v[68:71], v0, s[64:67], s70 offen offset:3072 sc1\ns_add_u32 s70, s51, s59\nbuffer_load_dwordx4 v[72:75], v0, s[64:67], s70 offen offset:0 sc1\n" \
    "buffer_load_dwordx4 v[76:79], v0, s[64:67], s70 offen offset:1024 sc1\nbuffer_load_dwordx4 v[80:83], v0, s[64:67], s70 offen offset:2048 sc1\n" \
    "buffer_load_dwordx4 v[84:87], v0, s[64:67], s70 offen offset:3072 sc1\ns_add_u32 s70, s51, s60\nbuffer_load_dwordx4 v[88:91], v0, s[64:67], s70 offen offset:0 sc1\n" \
    "buffer_load_dwordx4 v[92:95], v0, s[64:67], s70 offen offset:1024 sc1\nbuffer_load_dwordx4 v[96:99], v0, s[64:67], s70 offen offset:2048 sc1\n" \
    "buffer_load_dwordx4 v[100:103], v0, s[64:67], s70 offen offset:3072 sc1\ns_add_u32 s70, s51, s61\nbuffer_load_dwordx4 v[104:107], v0, s[64:67], s70 offen offset:0 sc1\n" \
    "buffer_load_dwordx4 v[108:111], v0, s[64:67], s70 offen offset:1024 sc1\nbuffer_load_dwordx4 v[112:115], v0, s[64:67], s70 offen offset:2048 sc1\n" \
    "buffer_load_dwordx4 v[116:119], v0, s[64:67], s70 offen offset:3072 sc1\ns_add_u32 s70, s51, s62\nbuffer_load_dwordx4 v[120:123], v0, s[64:67], s70 offen offset:0 sc1\n" \
    "buffer_load_dwordx4 v[124:127], v0, s[64:67], s70 offen offset:1024 sc1\nbuffer_load_dwordx4 v[128:131], v0, s[64:67], s70 offen offset:2048 sc1\n" \
    "buffer_load_dwordx4 v[132:135], v0, s[64:67], s70 offen offset:3072 sc1\ns_waitcnt vmcnt(0) lgkmcnt(0)\nv_max3_u32 v21, v24, v25, v25\nv_max3_u32 v21, v21, v26, v27\n" \
    "v_max3_u32 v21, v21, v28, v29\nv_max3_u32 v21, v21, v30, v31\nv_max3_u32 v21, v21, v32, v33\nv_max3_u32 v21, v21, v34, v35\nv_max3_u32 v21, v21, v36, v37\n" \
    "v_max3_u32 v21, v21, v38, v39\nv_max3_u32 v21, v21, v40, v41\nv_max3_u32 v21, v21, v42, v43\nv_max3_u32 v21, v21, v44, v45\nv_max3_u32 v21, v21, v46, v47\n" \
    "v_max3_u32 v21, v21, v48, v49\nv_max3_u32 v21, v21, v50, v51\nv_max3_u32 v21, v21, v52, v53\nv_max3_u32 v21, v21, v54, v55\nv_max3_u32 v21, v21, v56, v57\n" \
    "v_max3_u32 v21, v21, v58, v59\nv_max3_u32 v21, v21, v60, v61\nv_max3_u32 v21, v21, v62, v63\nv_max3_u32 v21, v21, v64, v65\nv_max3_u32 v21, v21, v66, v67\n" \
    "v_max3_u32 v21, v21, v68, v69\nv_max3_u32 v21, v21, v70, v71\nv_max3_u32 v21, v21, v72, v73\nv_max3_u32 v21, v21, v74, v75\nv_max3_u32 v21, v21, v76, v77\n" \
    "v_max3_u32 v21, v21, v78, v79\nv_max3_u32 v21, v21, v80, v81\nv_max3_u32 v21, v21, v82, v83\nv_max3_u32 v21, v21, v84, v85\nv_max3_u32 v21, v21, v86, v87\n" \
    "v_max3_u32 v21, v21, v88, v89\nv_max3_u32 v21, v21, v90, v91\nv_max3_u32 v21, v21, v92, v93\nv_max3_u32 v21, v21, v94, v95\nv_max3_u32 v21, v21, v96, v97\n" \
    "v_max3_u32 v21, v21, v98, v99\nv_max3_u32 v21, v21, v100, v101\nv_max3_u32 v21, v21, v102, v103\nv_max3_u32 v21, v21, v104, v105\nv_max3_u32 v21, v21, v106, v107\n" \
    "v_max3_u32 v21, v21, v108, v109\nv_max3_u32 v21, v21, v110, v111\nv_max3_u32 v21, v21, v112, v113\nv_max3_u32 v21, v21, v114, v115\nv_max3_u32 v21, v21, v116, v117\n" \
    "v_max3_u32 v21, v21, v118, v119\nv_max3_u32 v21, v21, v120, v121\nv_max3_u32 v21, v21, v122, v123\nv_max3_u32 v21, v21, v124, v125\nv_max3_u32 v21, v21, v126, v127\n" \
    "v_max3_u32 v21, v21, v128, v129\nv_max3_u32 v21, v21, v130, v131\nv_max3_u32 v21, v21, v132, v133\nv_max3_u32 v21, v21, v134, v135\nv_cmp_eq_u32 vcc, -1, v21\n" \
    "s_mov_b32 s80, 1\ns_cbranch_vccz .Lg4_%=\ns_sleep 2\ns_add_u32 s68, s68, 1\ns_and_b32 s69, s68, 63\ns_cbranch_scc1 .Lg10_%=\ns_cmp_gt_u32 s68, 0x200000\n" \
    "s_cbranch_scc1 .Lg1_%=\nbuffer_load_dword v21, off, s[64:67], 0 sc1\ns_waitcnt vmcnt(0)\nv_readfirstlane_b32 s69, v21\ns_cmp_eq_u32 s69, 1\ns_cbranch_scc0 .Lg10_%=\n" \
    "s_endpgm\n.Lg15_%=:\ns_mov_b32 s68, 0\n.Lg17_%=:\ns_sleep 1\nbuffer_load_dwordx4 v[172:175], v0, s[64:67], s73 offen sc1\ns_waitcnt vmcnt(0)\n" \
    "v_max3_u32 v21, v172, v173, v174\nv_max_u32 v21, v21, v175\nv_cmp_eq_u32 vcc, -1, v21\ns_cbranch_vccz .Lg16_%=\ns_add_u32 s68, s68, 1\ns_and_b32 s69, s68, 63\n" \
    "s_cbranch_scc1 .Lg17_%=\ns_cmp_gt_u32 s68, 0x200000\ns_cbranch_scc1 .Lg1_%=\nbuffer_load_dword v21, off, s[64:67], 0 sc1\ns_waitcnt vmcnt(0)\nv_readfirstlane_b32 s69, v21\n" \
    "s_cmp_eq_u32 s69, 1\ns_cbranch_scc0 .Lg17_%=\ns_endpgm\n.Lg19_%=:\ns_mov_b32 s68, 0\n.Lg21_%=:\ns_sleep 1\nbuffer_load_dwordx4 v[172:175], v0, s[64:67], s73 offen sc1\n" \
    "s_waitcnt vmcnt(0)\nv_max3_u32 v21, v172, v173, v174\nv_max_u32 v21, v21, v175\nv_cmp_eq_u32 vcc, -1, v21\ns_cbranch_vccz .Lg20_%=\ns_add_u32 s68, s68, 1\n" \
    "s_and_b32 s69, s68, 63\ns_cbranch_scc1 .Lg21_%=\ns_cmp_gt_u32 s68, 0x200000\ns_cbranch_scc1 .Lg1_%=\nbuffer_load_dword v21, off, s[64:67], 0 sc1\ns_waitcnt vmcnt(0)\n" \
    "v_readfirstlane_b32 s69, v21\ns_cmp_eq_u32 s69, 1\ns_cbranch_scc0 .Lg21_%=\ns_endpgm\n.Lg1_%=:\nv_mov_b32 v21, 1\nbuffer_store_dword v21, off, s[64:67], 0 sc1\ns_endpgm\n" \
    ".Lg2_%=:\ns_waitcnt vmcnt(0)\n"
#define TD_F 57
#define TD_G 155
constexpr int DQ = LQ + 1, NR = 4, NQ = 8, L_Q = NR * 16384, L_KP = L_Q + NQ * 8192;
static_assert(DQ < NQ, "q slots");
__device__ __forceinline__ int keyof(int gg, int r, int t) { return 16 * (gg & 1) + 8 * (gg >> 1) + (r & 1) + 4 * (r >> 1) + 2 * t; }
template <class C8> __device__ __forceinline__ void qk_tail(const Args& a, rsrc_t rs, unsigned char* lds, int head, unsigned* ctl, C8 c, f32x2 ms) {
    const int lane = lane_id(), wave = a.pad0, l15 = lane & 15, g = lane >> 4;
    const unsigned lb = (unsigned)(size_t)(LAS unsigned char*)lds, q = l15 >> 2, p = lane & 3, l16 = lane * 16;
    const unsigned pol = __builtin_amdgcn_readfirstlane(*(const unsigned*)(a.ws + O_POL));
    const int qt = wave & 1, mk = (l15 & 1) + 4 * ((l15 >> 1) & 1) + 2 * ((l15 >> 2) & 1);
    const unsigned kxr = g * 512 + keyof(q, p, 0) * 16, kmr = mk * 64 + 16 * g;
    const unsigned po1 = (g >> 1) * 0x7F000000u + (8 + (head >> 4)) * 16384 + (((head >> 2) & 3) * 2 + qt) * 1024 + ((head & 3) * 16 + l15) * 16 + 8 * (g & 1);
    constexpr float SC2 = 0.08838834764831845f * 1.4426950408889634f;
    if (!wave_wait<8>(PW_V(head), 1, 1, NSEG, ctl + C_ABORT)) asm volatile("s_endpgm");
    v4u rA = mkrs(a.ws, WS_END - 16);
    asm volatile("s_nop 4" : "+s"(rA));
    for (int sg = NSEG - DQ + 1; sg < NSEG; ++sg) {
        set_m0(lb + (sg & 7) * 8192 + wave * 2048); asm volatile("s_nop 0");
        const unsigned kb = nearg(a.ws, pol, O_KV + RI(sg) * KV_SLOT + head * 16384, 1) + wave * 2048;
        dma1<0>(rA, l16, kb); dma1<1024>(rA, l16, kb);
    }
    asm volatile("s_waitcnt vmcnt(0)\n\ts_barrier" ::: "memory");
    v4u qf[4], kf[4];
    f32x4 sc;
    float mx = ms[0], sum = ms[1];
    auto smm = [&]() {
#pragma unroll
        for (int ks = 0; ks < 4; ++ks) { if (ks == 0) MFMA_V0(sc, kf[0], qf[0]); else MFMA_V(sc, kf[ks], qf[ks]); }
        asm volatile("s_nop 7" : "+v"(sc));
    };
    for (int ps = NSEG - DQ + (((NSEG - DQ) ^ (wave >> 1)) & 1); ps < NSEG; ps += 2) {
#pragma unroll
        for (int ks = 0; ks < 4; ++ks) qf[ks] = lds16(lds + L_Q + (ps & (NQ - 1)) * 8192 + (qt * 4 + ks) * 1024 + l16);
        if (ps > NSEG - DQ) {
            mx = -3e38f;
#pragma unroll
            for (int t = 0; t < 2; ++t) {
#pragma unroll
                for (int ks = 0; ks < 4; ++ks) kf[ks] = lds16(lds + (ps & 7) * 8192 + kxr + 32 * t + ks * 2048);
                smm();
#pragma unroll
                for (int r = 0; r < 4; ++r) { c[4 * t + r] = keyof(g, r, t) <= 16 * qt + l15 ? sc[r] : -INFINITY; mx = fmaxf(mx, c[4 * t + r]); }
            }
            sum = 0.f;
#pragma unroll
            for (int i = 0; i < 8; ++i) { c[i] = __builtin_amdgcn_exp2f(fmaf(c[i], SC2, -SC2 * mx)); sum += c[i]; }
        }
        const unsigned pb = nearg(a.ws, pol, O_D + (unsigned)ps * D_SLOT, 0), kb = kmof(a.ws, pol, ps, head);
        unsigned spins = 0;
        for (;;) {
            const unsigned cw = __builtin_amdgcn_readfirstlane(__hip_atomic_load((gu32*)(ctl + C_C + 32 * (head >> 2)), RLX_AGENT));
            if ((int)(cw & 127) > ps) { if ((int)(cw & 127) == ps + 1) cp_delay(ps == NSEG - 1 ? TD_P - TD_L : TD_P, cw); break; }
            __builtin_amdgcn_s_sleep(5);
            if (spin_fail(spins, ctl + C_ABORT)) asm volatile("s_endpgm");
        }
        RSTAMP(ps + DQ, 0);
        for (;;) {
#pragma unroll
            for (int ks = 0; ks < 4; ++ks) kf[ks] = sld16(rs, kmr + 512 * ks, kb);
            if (!__any(max(max(umax4(kf[0]), umax4(kf[1])), max(umax4(kf[2]), umax4(kf[3]))) == SENT)) break;
            __builtin_amdgcn_s_sleep(2);
            if (spin_fail(spins, ctl + C_ABORT)) asm volatile("s_endpgm");
        }
        RSTAMP(ps + DQ, 1);
        asm volatile("s_nop 1" ::: "memory");
        smm();
        float t1 = fmaxf(fmaxf(fmaxf(sc[0], sc[1]), fmaxf(sc[2], sc[3])), mx);
        { auto r = __builtin_amdgcn_permlane16_swap(__float_as_uint(t1), __float_as_uint(t1), false, false); t1 = fmaxf(__uint_as_float(r[0]), __uint_as_float(r[1])); }
        { auto r = __builtin_amdgcn_permlane32_swap(__float_as_uint(t1), __float_as_uint(t1), false, false); t1 = -SC2 * fmaxf(__uint_as_float(r[0]), __uint_as_float(r[1])); }
        const float ax = __builtin_amdgcn_exp2f(fmaf(mx, SC2, t1));
#pragma unroll
        for (int r = 0; r < 4; ++r) sc[r] = __builtin_amdgcn_exp2f(fmaf(sc[r], SC2, t1));
        const float rc = __builtin_amdgcn_rcpf(red32_add(red16_add(fmaf(0.5f, (sc[0] + sc[1]) + (sc[2] + sc[3]), sum * ax)))), fx = ax * rc;
        sst16((v4u){pack2(c[0] * fx, c[1] * fx), pack2(c[2] * fx, c[3] * fx), pack2(c[4] * fx, c[5] * fx), pack2(c[6] * fx, c[7] * fx)}, rs, l16, pb + (head >> 2) * 16384 + ((head & 3) * 2 + qt) * 1024, 16);
        __builtin_amdgcn_raw_buffer_store_b64((v2u){pack2(sc[0] * rc, sc[1] * rc), pack2(sc[2] * rc, sc[3] * rc)}, rs, po1, pb, 16);
        unsigned old = arrive(a.ws, 0, ps);
        asm volatile("s_waitcnt vmcnt(0)" : "+v"(old) :: "memory");
        tell<0>((unsigned*)(a.ws + nearg(a.ws, pol, C_P * 4, 0)) - GO(0, 0), ps, old, 64);
        RSTAMP(ps + DQ, 2);
    }
}
__device__ __forceinline__ v16u cat(v4u x0, v4u x1, v4u x2, v4u x3) { return __builtin_shufflevector(__builtin_shufflevector(x0, x1, 0, 1, 2, 3, 4, 5, 6, 7), __builtin_shufflevector(x2, x3, 0, 1, 2, 3, 4, 5, 6, 7), 0, 1, 2, 3, 4, 5, 6, 7, 8, 9, 10, 11, 12, 13, 14, 15); }
__device__ __forceinline__ v32u cat2(v16u x, v16u y) { return __builtin_shufflevector(x, y, 0, 1, 2, 3, 4, 5, 6, 7, 8, 9, 10, 11, 12, 13, 14, 15, 16, 17, 18, 19, 20, 21, 22, 23, 24, 25, 26, 27, 28, 29, 30, 31); }
template <bool isV> __device__ __forceinline__ void proj_role(const Args& a, rsrc_t rs, unsigned char* lds, int head, unsigned* ctl) {
    const int tid = otid(), lane = tid & 63, wave = a.pad0, l15 = lane & 15, g = lane >> 4;
    const unsigned lb = (unsigned)(size_t)(LAS unsigned char*)lds, q = l15 >> 2, p = lane & 3;
    const unsigned pol = __builtin_amdgcn_readfirstlane(*(const unsigned*)(a.ws + O_POL));
    v4u rA = mkrs(a.ws, WS_END);
    auto prm = [](unsigned x) { return isV ? ((x & 1) << 1) | (x >> 1) : x; };
    const unsigned ar = 8 * wave + (lane >> 3), ao = (2 * prm((ar & 15) >> 2) + (ar >> 4)) * 16384 + (ar & 3) * 2048 + 16 * ((lane & 7) ^ ((ar >> 1) & 7));
    const unsigned h0 = nearg(a.ws, pol, O_D + D_H1, 1);
    auto stage0 = [&](int t, unsigned oob) { set_m0(lb + wave * 4096 + t * 16384); asm volatile("s_nop 0"); const unsigned so = h0 + t * 512;
        dma1<0>(rA, ao | oob, so); dma1<1024>(rA, ao | oob, so - 896); dma1<2048>(rA, ao | oob, so - 1792); dma1<3072>(rA, ao | oob, so - 2688); };
    int hr = 0;
    v4u w[32][3];
    {
        v4u rX = mkrs(isV ? a.Wv : a.Wq, DM * HD * 2), rK = mkrs(a.Wk, DM * HD * 2);
        unsigned bo[2], ko[2];
#pragma unroll
        for (int i = 0; i < 2; ++i) {
            bo[i] = (g * HD + 16 * ((l15 >> 1) ^ (g | i << 2)) + 8 * (lane & 1)) * 2;
            ko[i] = ((lane >> 3) * HD + 16 * (((lane >> 1) & 3) ^ ((g & 1) | i << 1)) + 8 * (lane & 1)) * 2;
        }
        asm volatile("s_nop 4" : "+s"(rX), "+s"(rK), "+s"(rA));
        unsigned hc;
        auto issue = [&](int G) {
            const unsigned r0 = (4 * GO_(G) + wave) * 32, d = lb + ((G + 1) % 3) * 49152;
#pragma unroll
            for (int j = 0; j < 8; ++j) bdma<(NTW & 1)>(rX, bo[(j >> 1) & 1], ((r0 + 4 * j) * HD + head * DH) * 2, d + wave * 8192 + j * 1024);
#pragma unroll
            for (int j = 0; j < 4; ++j) bdma<(NTW & 1)>(rK, ko[j & 1], ((r0 + 8 * j) * HD + head * DH + (isV ? 64 : 0)) * 2, d + 32768 + wave * 4096 + j * 1024);
        };
        issue(0); issue(1); issue(2);
        const unsigned ox = (8 * g + q) * 256 + 8 * p, sx = q | (g & 1) << 2, ok = 32768 + (8 * g + q) * 128 + 32 * (wave ^ ((q >> 1) | (g & 1) << 1)) + 8 * p;
        sfor<0, 8>([&](auto gg) {
            constexpr int G = decltype(gg)::v;
            asm volatile("s_waitcnt vmcnt(%0)\n\ts_barrier" :: "n"(G < 7 ? 24 : 12) : "memory");
            if constexpr (G == 5) { asm volatile("" : "+v"(hc)); const u64 m = __ballot(hc < 32u); if (wave == 0) *(volatile LAS unsigned*)(lds + L_FLAG) = m ? __builtin_ctzll(m) : NSEG; }
#pragma unroll
            for (int k = 0; k < 4; ++k) {
#pragma unroll
                for (int n = 0; n < 3; ++n) {
                    const unsigned o = ((G + 1) % 3) * 49152 + (n < 2 ? k * 8192 + ox + 32 * ((2 * wave + n) ^ sx) : k * 4096 + ok);
                    const v2u lo = tr_rd(lds, o), hi = tr_rd(lds, o + (n < 2 ? 1024 : 512));
                    w[4 * GO_(G) + k][n] = (v4u){lo.x, lo.y, hi.x, hi.y};
                }
            }
#pragma unroll
            for (int k = 4 * GO_(G); k < 4 * GO_(G) + 4; ++k) asm volatile("" : "+a"(w[k][0]), "+a"(w[k][1]), "+v"(w[k][2]));
            lds_barrier();
            if constexpr (G == 3) asm volatile("buffer_load_dword %0, %1, %2, 0 offen sc1" : "=v"(hc) : "v"((C_H + 32 * lane) * 4), "s"(rA));
            if (G + 3 < 8) issue(G + 3);
            if constexpr (G == 5) hr = __builtin_amdgcn_readfirstlane(*(volatile LAS unsigned*)(lds + L_FLAG));
            if constexpr (G == 5) { for (int t = 0; t < 3; ++t) stage0(t, hr ? 0u : 0x7F000000u); }
        });
    }
    RSTAMP(74, 0);
    unsigned ra[2];
#pragma unroll
    for (int u = 0; u < 2; ++u) ra[u] = (l15 >> 3) * 4096 + (l15 & 7) * 128 + 16 * ((4 * u + g) ^ (l15 >> 1));
    if (!hr) {
        if (!wave_wait<5>(ctl + C_H, 1, 1, 32, ctl + C_ABORT)) asm volatile("s_endpgm");
        for (int t = 0; t < 3; ++t) stage0(t, 0u);
    }
    asm volatile("s_waitcnt vmcnt(0)\n\ts_barrier" ::: "memory");
    const unsigned tok = 8 * prm(q) + p, l16 = lane * 16;
    const int qt = wave & 1, mk = (l15 & 1) + 4 * ((l15 >> 1) & 1) + 2 * ((l15 >> 2) & 1);
    const unsigned kxr = g * 512 + keyof(q, p, 0) * 16, kmr = mk * 64 + 16 * g;
    const unsigned po1 = (g >> 1) * 0x7F000000u + (8 + (head >> 4)) * 16384 + (((head >> 2) & 3) * 2 + qt) * 1024 + ((head & 3) * 16 + l15) * 16 + 8 * (g & 1);
    constexpr float SC2 = 0.08838834764831845f * 1.4426950408889634f;
    constexpr unsigned OOB = 0x7F000000u;
    typedef float f32x8 __attribute__((ext_vector_type(8)));
    f32x8 c = {0.f, 0.f, 0.f, 0.f, 0.f, 0.f, 0.f, 0.f};
    f32x2 ms = {0.f, 0.f};
    {
        auto qwa = [&](int m) { return lb + L_Q + (((tok >> 4) * 4 + wave) * 64 + (2 * (g & 1) + (g >> 1)) * 16 + ((tok + 4 * m) & 15)) * 16; };
        v16u c0 = {l16, ao, lb + ra[0], lb + ra[1], ((isV ? 8 : 0) + 2 * wave + (g >> 1)) * 512 + (tok + 4 * (g & 1)) * 16, tid ? OOB : (C_X + 32 * (head + (isV ? 0 : 64))) * 4u,
                   tid > 1 ? OOB : (tid == 0 ? C_C + 32 * (head >> 2) : isV ? GO(1, head >> 2) : C_X + 32 * head) * 4u, lb + L_FLAG + (tid < 3 ? 4 * tid : 16),
                   isV ? tid * 16u : lb + L_KP + kxr, kmr, po1, qwa(0), qwa(1), lb + L_Q + qt * 4096 + l16, isV ? lb + L_FLAG : lane < 8 ? nearg(a.ws, pol, C_P * 4, 0) + 128 * lane : OOB, isV ? (lane & 1) * 4u : lb + L_FLAG};
        auto u = [](unsigned x) { return (unsigned)__builtin_amdgcn_readfirstlane(x); };
        v16u sv = {(unsigned)(size_t)a.ws, pol, 0u, (unsigned)hr, u(lb + wave * 4096), u(O_KV + head * 16384), h0, (unsigned)wave,
                   u(isV ? O_UM + head * 32768 : lb + L_KP + wave * 2048), u(isV ? (head < 27 ? head * 32768 : (head - 27) * 32768 + 8192) : (head >> 2) * 16384 + (head & 3) * 2048),
                   u(isV ? (head - 21u < 8u ? (head - 21) * 16384 + 8192 : head * 16384) : (head >> 2) * 16384 + ((head & 3) * 2 + qt) * 1024), u(isV ? O_E1 + (head >> 1) * 16384 : 0u), u(isV ? (head & 1) * 4096 : wave * 2048), 0u, 0u, 0u};
        const v16u sk = {RG - 1, ROFF & 1 ? 64 : RG, O_D + D_H1, D_H1, O_EP, O_D, C_N * 4, DQ, DQ - 1, (NQ - DQ + 1) * 8192, (NQ - DQ) * 8192, LVS, LV0, LV, TD_F << 8, TD_G << 8};
        v16u mk8;
#pragma unroll
        for (int i = 0; i < 8; ++i) { const u64 m = __ballot(keyof(g, i & 3, i >> 2) <= 16 * qt + l15); mk8[2 * i] = (unsigned)m; mk8[2 * i + 1] = (unsigned)(m >> 32); }
        v32u ww[12];
#pragma unroll
        for (int i = 0; i < 8; ++i) ww[i] = cat2(cat(w[4 * i][0], w[4 * i][1], w[4 * i + 1][0], w[4 * i + 1][1]), cat(w[4 * i + 2][0], w[4 * i + 2][1], w[4 * i + 3][0], w[4 * i + 3][1]));
#pragma unroll
        for (int i = 0; i < 4; ++i) ww[8 + i] = cat2(cat(w[8 * i][2], w[8 * i + 1][2], w[8 * i + 2][2], w[8 * i + 3][2]), cat(w[8 * i + 4][2], w[8 * i + 5][2], w[8 * i + 6][2], w[8 * i + 7][2]));
        v4u sn = sentv();
        const unsigned* dp = nullptr;
#define PROJ_CALL(TXT) asm volatile(TXT : "+{s[48:63]}"(sv), "+{v[0:15]}"(c0), "+{v[16:19]}"(sn), "+{v[112:119]}"(c), "+{v[120:121]}"(ms) \
            : "{s[16:31]}"(sk), "{s[64:67]}"(rA), "{s[80:95]}"(mk8), "{s32}"(SC2), "{s33}"(-3e38f), "{s[34:35]}"(dp), "{s36}"(C_H * 4), "{v22}"(-INFINITY), [sm] "n"(SM), [sv] "n"(SV), \
              "{a[0:31]}"(ww[0]), "{a[32:63]}"(ww[1]), "{a[64:95]}"(ww[2]), "{a[96:127]}"(ww[3]), "{a[128:159]}"(ww[4]), "{a[160:191]}"(ww[5]), "{a[192:223]}"(ww[6]), "{a[224:255]}"(ww[7]), \
              "{v[124:155]}"(ww[8]), "{v[156:187]}"(ww[9]), "{v[188:219]}"(ww[10]), "{v[220:251]}"(ww[11]) \
            : "memory", "vcc", "scc", "m0", "v20", "v21", "v24", "v25", "v26", "v27", "v28", "v29", "v30", "v31", "v32", "v33", "v34", "v35", "v36", "v37", "v38", "v39", "v40", "v41", "v42", "v43", "v44", "v45", "v46", "v47", "v48", "v49", "v50", "v51", "v52", "v53", "v54", "v55", "v56", "v57", "v58", "v59", "v60", "v61", "v62", "v63", "v64", "v65", "v66", "v67", "v68", "v69", "v70", "v71", "v72", "v73", "v74", "v75", "v76", "v77", "v78", "v79", "v80", "v81", "v82", "v83", "v84", "v85", "v86", "v87", "v88", "v89", "v90", "v91", "v92", "v93", "v94", "v95", "v96", "v97", "v98", "v99", "v100", "v101", "v102", "v103", "v104", "v105", "v106", "v107", "v108", "v109", "v110", "v111", "v122", "v123", "s37", "s38", "s39", "s40", "s41", "s42", "s43", "s68", "s69", "s70", "s71", "s72", "s73", "s74", "s75", "s76", "s77", "s78", "s79", "s96", "s97", "s98", "s99")
        if constexpr (isV) PROJ_CALL(PROJ_V); else PROJ_CALL(PROJ_Q);
    }
    auto signal = [&](int n) { if (otid() == 0) __hip_atomic_store((gu32*)(isV ? PW_V(head) : PW_Q(head)), (unsigned)n, RLX_AGENT); };
    asm volatile("s_waitcnt vmcnt(0) lgkmcnt(0)\n\ts_barrier" ::: "memory");
    signal(NSEG + 1);
    if constexpr (!isV) qk_tail(a, rs, lds, head, ctl, c, ms);
}

#define TD_T 24
__device__ __forceinline__ void r_role(const Args& a, unsigned char* lds, int xg, int cg, int set, unsigned par, unsigned ev) {
    const int tid = otid(), lane = tid & 63, wave = a.pad0, l15 = lane & 15, g = lane >> 4;
    const unsigned q = l15 >> 2, p = lane & 3, lb0 = (unsigned)(size_t)(LAS unsigned char*)lds;
    const unsigned pol = __builtin_amdgcn_readfirstlane(*(const unsigned*)(a.ws + O_POL));
    f32x4 ga[2];
    mod8(a, 2 * DM + 32 * cg + 8 * ((lane >> 3) & 3), ga);
    asm volatile("" : "+v"(ga[0]), "+v"(ga[1]));
    v4u w[8][8];
    {
        const unsigned lb = lb0 + wave * 32768;
        v4u rW = mkrs(a.Wo, DM * HD * 2);
        const unsigned bo = ((lane >> 2) * DM + 16 * (((lane >> 1) & 1) ^ (g & 1)) + 8 * (lane & 1)) * 2;
        asm volatile("s_nop 4" : "+s"(rW));
        auto issue = [&](int c) {
#pragma unroll
            for (int i = 0; i < 8; ++i) bdma<(NTW & 128)>(rW, bo, (((8 * wave + c) * DH + 16 * i) * DM + 32 * cg) * 2, lb + (c & 3) * 8192 + i * 1024);
        };
#define RTP ""
#define RHN 0
#define RH1 0
#define RTC 1
        if (RTC) {
            set_m0(lb0 + LDS_BYTES - 1024);
            for (int j = 0; j < 4; ++j) asm volatile("s_nop 0\n\tbuffer_load_dword %0, %1, %2 offen " RTP "lds" :: "v"((lane >> 2) * 2048 + (lane & 3) * 128), "s"(rW), "s"(((8 * wave + 2 * j + set) * DH + 16 * (cg & 7)) * 2048 + (cg >> 3) * 512));
        }
        auto help = [&](int j0, int j1) {
            const int W = ((cg & 7) * 2 + set) * 4 + wave;
            v4u rQ = mkrs(W & 1 ? a.Wq : a.Wk, DM * HD * 2);
            asm volatile("s_nop 4" : "+s"(rQ));
            set_m0(lb0 + LDS_BYTES - 1024);
            for (int j = j0; j < j1; ++j) {
                asm volatile("s_nop 0\n\tbuffer_load_dword %0, %1, %2 offen " RTP "lds" :: "v"((lane >> 4) * 8192 + (lane & 15) * 128), "s"(rQ), "s"((128 * (8 - RHN + j) + 4 * (W >> 1)) * 8192 + (cg >> 3) * 2048));
                asm volatile("s_nop 0\n\tbuffer_load_dword %0, %1, %2 offen " RTP "lds" :: "v"((lane >> 2) * 2048 + (lane & 3) * 512), "s"(rW), "s"(((cg >> 3) * 1024 + 16 * W) * 2048 + (4 - RHN + j) * 128));
            }
        };
        help(0, RH1);
        issue(0); issue(1); issue(2); issue(3);
        sfor<0, 8>([&](auto cc) {
            constexpr int c = decltype(cc)::v;
            asm volatile("s_waitcnt vmcnt(%0)" :: "n"(c < 5 ? 24 : 8 * (7 - c)) : "memory");
#pragma unroll
            for (int n = 0; n < 8; ++n) {
                const unsigned o = wave * 32768 + (c & 3) * 8192 + (n & 3) * 2048 + (4 * g + q) * 64 + 32 * ((n >> 2) ^ (g & 1)) + 8 * p;
                const v2u lo = tr_rd(lds, o), hi = tr_rd(lds, o + 1024);
                w[c][n] = (v4u){lo.x, lo.y, hi.x, hi.y};
            }
#pragma unroll
            for (int n = 0; n < 8; n += 4) asm volatile("" : "+a"(w[c][n]), "+a"(w[c][n + 1]), "+a"(w[c][n + 2]), "+a"(w[c][n + 3]));
            asm volatile("s_waitcnt lgkmcnt(0)" ::: "memory");
            if (c + 4 < 8) issue(c + 4);
        });
        help(RH1, RHN);
    }
    lds_barrier();
    constexpr unsigned OOB = 0x7F000000u, L_R = 0, L_O = 4608, L_M = L_O + 4 * 4608;
    RSTAMP(74, 0);
    const unsigned l16 = lane * 16, r = 8 * wave + (lane & 7), c4 = 8 * ((lane >> 3) & 3) + 4 * (lane >> 5), cr = xg >> 1, cb = 2 * cg;
    const v16u c0 = {l16, l16 + 4096, 0u, 0u, (lane & 8) ? 0xEEEEu : 0x4444u, (16 - 3 * cr - wave) * 16384 + wave * 1024 + 2 * g * 128 + (l15 & 7) * 16 + (l15 >> 3) * 8,
                     lb0 + L_R + wave * 1152 + 4 * (g & 1) * 144 + (16 * (g >> 1) + l15) * 4, lb0 + L_R + (l15 >> 1) * 144 + (16 * (g & 1) + 8 * (lane & 1) + 4 * (g >> 1)) * 4,
                     lane < 8 ? (C_X + 32 * (8 * wave + lane)) * 4u : lane - 9u < 2u ? nearg(a.ws, pol, (C_P + 32 * xg) * 4, 0) + 4 * (set ^ (lane & 1) ^ 1) : OOB,
                     lb0 + L_O + wave * 4608 + l15 * 144 + g * 16, lb0 + L_O + r * 144 + c4 * 4, (r * DM + 32 * cg + c4) * 2,
                     lane < 32 ? D_X2 - wave * 32768 + (cg >> 2) * 16384 + ((cg & 3) * 2 + (r >> 4)) * 1024 + ((lane >> 3) * 16 + (r & 15)) * 16 : OOB,
                     lane < 8 ? D_ST - wave * 32768 + cg * 256 + r * 8 : OOB, tid ? OOB : (C_X + 32 * (32 + cg) + 3072 * set) * 4u, lane < 8 ? (GO(1, lane) + set) * 4u : OOB};
    *(LAS f32x4*)(lds + L_M + wave * 3072 + l16) = ga[lane >> 5];
    auto u = [](unsigned x) { return (unsigned)__builtin_amdgcn_readfirstlane(x); };
    const u64 dp = 0;
    const v16u sk = {u(O_UM + cb * 16384 + wave * 2048), u(O_EP + (cr * 4 + wave) * 16384), u(O_E1 + (wave * 4 + ((cb >> 2) & 3)) * 16384 + (cb >> 4) * 1024 + (cb & 3) * 256), u(O_KV + wave * 131072),
                     u(O_D + wave * 32768), u((8 + (wave >> 1) - 2 * wave) * 16384 + (wave & 1) * 4096), u((20 - cr * 4 - wave) * 16384 + xg * 128), nearg(a.ws, pol, (C_R + 32 * xg + 1) * 4, 0), (C_N + 64) * 4, RG - 1, CH_S1, TD_O, TD_O - TD_L, 2 * (CH_A1 + CH_B1), (unsigned)dp, (unsigned)(dp >> 32)};
    v8u sv = {(unsigned)(size_t)a.ws, pol, u(set - 2), 0u, 0u, par >> 26, u(cg & 4 ? (par >> 16) & 31 : ev), (par & 255) + ((par >> 8) & 255)};
    v32u ww[8];
#pragma unroll
    for (int i = 0; i < 8; ++i) ww[i] = cat2(cat(w[i][0], w[i][1], w[i][2], w[i][3]), cat(w[i][4], w[i][5], w[i][6], w[i][7]));
    asm volatile("s_nop 4\n" ROLE_R : "+{s[48:55]}"(sv)
        : "{v[0:15]}"(c0), "{v16}"(lb0 + L_M + wave * 3072 + l16), "{s[32:47]}"(sk), "{s56}"(wave), "{s[60:63]}"(mkrs(a.x, S_TOK * DM * 2)), "{s[64:67]}"(mkrs(a.ws, WS_END)),
          "{a[0:31]}"(ww[0]), "{a[32:63]}"(ww[1]), "{a[64:95]}"(ww[2]), "{a[96:127]}"(ww[3]), "{a[128:159]}"(ww[4]), "{a[160:191]}"(ww[5]), "{a[192:223]}"(ww[6]), "{a[224:255]}"(ww[7])
        : "memory", "vcc", "scc", "m0", "v17", "v18", "v19", "v20", "v21", "v22", "v24", "v25", "v26", "v27", "v28", "v29", "v30", "v31", "v32", "v33", "v34", "v35", "v36", "v37", "v38", "v39", "v40", "v41", "v42", "v43", "v44", "v45", "v46", "v47", "v48", "v49", "v50", "v51", "v52", "v53", "v54", "v55", "v56", "v57", "v58", "v59", "v60", "v61", "v62", "v63", "v64", "v65", "v66", "v67", "v68", "v69", "v70", "v71", "v72", "v73", "v74", "v75", "v76", "v77", "v78", "v79", "v80", "v81", "v82", "v83", "v84", "v85", "v86", "v87", "v88", "v89", "v90", "v91", "v92", "v93", "v94", "v95", "v96", "v97", "v98", "v99", "v100", "v101", "v102", "v103", "v104", "v105", "v106", "v107", "v108", "v109", "v110", "v111", "v112", "v113", "v114", "v115", "v116", "v117", "v118", "v119", "v120", "v121", "v122", "v123", "v124", "v125", "v126", "v127", "v128", "v129", "v130", "v131", "v132", "v133", "v134", "v135", "v136", "v137", "v138", "v139", "v140", "v141", "v142", "v143", "v144", "v145", "v146", "v147", "v148", "v149", "v150", "v151", "v152", "v153", "v154", "v155", "v156", "v157", "v158", "v159", "v160", "v161", "v162", "v163", "v164", "v165", "v166", "v167", "v168", "v169", "v170", "v171", "v172", "v173", "v174", "v175", "v176", "v177", "v178", "v179", "v180", "v181", "v182", "v183", "v184", "v185", "v186", "v187", "v188", "v189", "v190", "v191", "v192", "v193", "v194", "v195", "v196", "v197", "v198", "v199", "v200", "v201", "v202", "v203", "v204", "v205", "v206", "v207", "v208", "v209", "v210", "v211", "v212", "v213", "v214", "v215", "v216", "v217", "v218", "v219", "v220", "v221", "v222", "v223", "v224", "v225", "v226", "v227", "v228", "v229", "v230", "v231", "v232", "v233", "v234", "v235", "v236", "v237", "v238", "v239", "v240", "v241", "v242", "v243", "v244", "v245", "v246", "v247", "v248", "v249", "v250", "v251", "v252", "v253", "s68", "s69", "s70", "s71", "s72", "s73", "s74", "s75", "s76", "s77", "s78", "s79", "s80", "s81", "s82", "s83", "s84", "s85", "s86", "s87", "s88", "s89");
}

template <int OFF> __device__ __forceinline__ void ld2(v4u (&d)[2], v4u r, unsigned vo, unsigned so) {
    asm volatile("buffer_load_dwordx4 %0, %2, %3, %4 offen offset:%5 sc1\n\tbuffer_load_dwordx4 %1, %2, %3, %4 offen offset:%6 sc1"
                 : "+v"(d[0]), "+v"(d[1]) : "v"(vo), "s"(r), "s"(so), "n"(OFF), "n"(OFF + 1024));
}
__device__ __forceinline__ v4u ld16w(v4u r, unsigned vo, unsigned so) {
    v4u d; asm volatile("buffer_load_dwordx4 %0, %1, %2, %3 offen sc1\n\ts_waitcnt vmcnt(0)" : "=&v"(d) : "v"(vo), "s"(r), "s"(so)); return d;
}
template <int OFF> __device__ __forceinline__ void ld2n(v4u (&d)[2], v4u r, unsigned vo, unsigned so) {
    asm volatile("buffer_load_dwordx4 %0, %2, %3, %4 offen offset:%5 sc1\n\tbuffer_load_dwordx4 %1, %2, %3, %4 offen offset:%6 sc1"
                 : "=&v"(d[0]), "=&v"(d[1]) : "v"(vo), "s"(r), "s"(so), "n"(OFF), "n"(OFF + 1024));
}
typedef __attribute__((ext_vector_type(16))) float f32x16;
__device__ __forceinline__ void mm32(f32x16& c, v4u x, v4u y) { asm volatile("s_nop 1\n\tv_mfma_f32_32x32x16_bf16 %0, %1, %2, %0" : "+v"(c) : "v"(x), "v"(y)); }
__device__ __forceinline__ void f1_role(const Args& a, rsrc_t rs, unsigned char* lds, int arg, int s0, unsigned* ctl) {
    const int tid = otid(), lane = tid & 63, wave = a.pad0, l15 = lane & 15, g = lane >> 4;
    const unsigned lb = (unsigned)(size_t)(LAS unsigned char*)lds, q = l15 >> 2, p = lane & 3, l16 = lane * 16;
    const unsigned pol = __builtin_amdgcn_readfirstlane(*(const unsigned*)(a.ws + O_POL));
    const int kh = arg >= 14, ia = arg - 14 * kh, rot = ia & 3;
    lds_barrier();
    v4u w[32][2];
    f32x16 bv, cs = {};
#pragma unroll
    for (int h = 0; h < 4; ++h) {
        const v2u br = *(const v2u*)(a.b1 + 128 * arg + 32 * wave + 8 * h + 4 * (g >> 1));
        bv[4 * h] = lo16f(br.x); bv[4 * h + 1] = hi16f(br.x); bv[4 * h + 2] = lo16f(br.y); bv[4 * h + 3] = hi16f(br.y);
    }
    asm volatile("" : "+v"(bv));
    {
        v4u rW = mkrs(a.w1, DM * FF * 2);
        unsigned bo[2];
#pragma unroll
        for (int i = 0; i < 2; ++i) bo[i] = (g * FF + 16 * ((l15 >> 1) ^ (g | i << 2)) + 8 * (lane & 1)) * 2;
        asm volatile("s_nop 4" : "+s"(rW));
        auto issue = [&](int G) {
            const unsigned r0 = ((4 * G + wave + 8 * rot) & 31) * 32;
#pragma unroll
            for (int j = 0; j < 8; ++j) bdma<(NTW & 8)>(rW, bo[(j >> 1) & 1], ((r0 + 4 * j) * FF + 128 * arg) * 2, lb + (G & 3) * 32768 + wave * 8192 + j * 1024);
        };
        {
            f32x4 m[2];
            ld_mod<2>(rs, ctl, {4 * DM + 4 * tid, 3 * DM + 4 * tid}, m);
            *(LAS f32x4*)(lds + 131072 + 16 * tid) = m[0]; *(LAS v2u*)(lds + 135168 + 8 * tid) = pack4(m[1]);
            asm volatile("s_waitcnt lgkmcnt(0)" ::: "memory");
        }
        issue(0); issue(1); issue(2); issue(3);
        const unsigned ox = (8 * (g >> 1) + q) * 256 + 32 * ((2 * wave + (g & 1)) ^ (q | (g >> 1) << 2)) + 8 * p;
        sfor<0, 8>([&](auto gg) {
            constexpr int G = decltype(gg)::v;
            asm volatile("s_waitcnt vmcnt(%0)\n\ts_barrier" :: "n"(G < 5 ? 24 : 8 * (7 - G)) : "memory");
#pragma unroll
            for (int k = 0; k < 4; ++k)
#pragma unroll
                for (int n = 0; n < 2; ++n) {
                    const unsigned o = (G & 3) * 32768 + k * 8192 + n * 4096 + ox;
                    const v2u lo = tr_rd(lds, o), hi = tr_rd(lds, o + 1024);
                    w[4 * G + k][n] = (v4u){lo.x, lo.y, hi.x, hi.y};
                }
            lds_barrier();
            if (G + 4 < 8) issue(G + 4);
#pragma unroll
            for (int i = 0; i < 8; ++i) {
                v4u& x = w[4 * G + (i >> 1)][i & 1];
                const unsigned k0 = ((4 * G + (i >> 1) + 8 * rot) & 31) * 32 + 16 * (i & 1) + 8 * (g >> 1);
                mm32(bv, x, lds16(lds + 135168 + 2 * k0));
#pragma unroll
                for (int j = 0; j < 4; ++j) { const f32x2 c1 = *(const LAS f32x2*)(lds + 131072 + 4 * k0 + 8 * j); x[j] = pack2(lo16f(x[j]) * c1[0], hi16f(x[j]) * c1[1]); }
                mm32(cs, x, (v4u){0x3F803F80u, 0x3F803F80u, 0x3F803F80u, 0x3F803F80u});
                asm volatile("" : "+a"(x));
            }
        });
    }
    asm volatile("s_nop 11" : "+v"(bv), "+v"(cs));
    RSTAMP(74, 0);
    constexpr unsigned OOB = 0x7F000000u, L_S = 139264;
    auto u = [](unsigned x) { return (unsigned)__builtin_amdgcn_readfirstlane(x); };
    auto st = [&](int t) { return u(D_X2 + (((t + rot) & 3) * 2 + (wave >> 1)) * 16384 + (wave & 1) * 4096); };
    const v8u c0 = {l16, lb + (g & 1) * 1024 + (g >> 1) * 256 + l15 * 16, D_ST + (lane >> 3) * 1024 + (8 * wave + (lane & 7)) * 8, lb + L_S + (8 * wave + (lane & 7)) * 8, lb + L_S + (lane & 31) * 8,
                    (g & 1) * 1024u + (g >> 1) * 256 + l15 * 16, lane < 4 ? GO(2, 4 * kh + lane) * 4u : OOB, lane ? OOB : GO(1, 4 * kh + (ia >> 2)) * 4u};
    v8u sv = {(unsigned)(size_t)a.ws, pol ^ 8192, (unsigned)s0, 0u, u(lb + wave * 4096), nearg(a.ws, pol, O_D + (unsigned)s0 * D_SLOT, 1), (unsigned)wave, u((C_N + 64 * (2 + kh)) * 4)};
    const v8u sk = {O_D, u(arg * 16384 + wave * 2048), st(0), st(1), st(2), st(3), __float_as_uint(-0.1029432397f), __float_as_uint(-2.302208198f)};
    const unsigned* dp = nullptr;
    v32u ww[8];
#pragma unroll
    for (int i = 0; i < 8; ++i) ww[i] = cat2(cat(w[4 * i][0], w[4 * i][1], w[4 * i + 1][0], w[4 * i + 1][1]), cat(w[4 * i + 2][0], w[4 * i + 2][1], w[4 * i + 3][0], w[4 * i + 3][1]));
    asm volatile("s_nop 11\n" ROLE_F1 : "+{s[48:55]}"(sv)
        : "{v[0:7]}"(c0), "{v[72:87]}"(cs), "{v[88:103]}"(bv), "{s[40:47]}"(sk), "{s[38:39]}"(dp), "{s56}"(XF1), "{s[64:67]}"(mkrs(a.ws, WS_END)), [sm] "n"(SM), [sv] "n"(SV),
          "{a[0:31]}"(ww[0]), "{a[32:63]}"(ww[1]), "{a[64:95]}"(ww[2]), "{a[96:127]}"(ww[3]), "{a[128:159]}"(ww[4]), "{a[160:191]}"(ww[5]), "{a[192:223]}"(ww[6]), "{a[224:255]}"(ww[7])
        : "memory", "vcc", "scc", "m0", "v8", "v9", "v10", "v11", "v12", "v13", "v14", "v15", "v16", "v17", "v18", "v24", "v25", "v26", "v27", "v28", "v29", "v30", "v31", "v32", "v33", "v34", "v35", "v36", "v37", "v38", "v39", "v40", "v41", "v42", "v43", "v44", "v45", "v46", "v47", "v48", "v49", "v50", "v51", "v52", "v53", "v54", "v55", "v56", "v57", "v58", "v59", "v60", "v61", "v62", "v63", "v64", "v65", "v66", "v67", "v68", "v69", "v70", "v71", "v104", "v105", "v106", "v107", "v108", "v109", "v110", "v111", "v112", "v113", "v114", "v115", "v116", "v117", "v118", "v119", "s68", "s69", "s70", "s71", "s72", "s73", "s74", "s75", "s76", "s77", "s78", "s79", "s80", "s81", "s82", "s83", "s84", "s85", "s86");
}

static_assert(LV < 16 && LV + 2 < RG && DQ + 2 < RG, "rings");
__device__ __forceinline__ void f2_role(const Args& a, rsrc_t rs, unsigned char* lds, int kh, int cg, int s0, unsigned* ctl) {
    constexpr int KS = 14;
    const int tid = otid(), lane = tid & 63, wave = a.pad0, l15 = lane & 15, g = lane >> 4;
    const int ks0 = (kh * 4 + wave) * KS, rot = (cg & 3) * 2;
    auto kp = [&](int k) { const int x = (k >> 1) + rot; return x >= 7 ? x - 7 : x; };
    auto ko = [&](int k) { const int pi = wave * 7 + kp(k); return (pi >> 1) * 16384 + (pi & 1) * 4096; };
    const unsigned pol = __builtin_amdgcn_readfirstlane(*(const unsigned*)(a.ws + O_POL));
    lds_barrier();
    v4u w[KS][4];
    {
        v4u rW = mkrs(a.w2, DM * FF * 2);
        const unsigned lb = (unsigned)(size_t)(LAS unsigned char*)lds + wave * 32768, q = l15 >> 2, p = lane & 3;
        unsigned bo[2];
#pragma unroll
        for (int i = 0; i < 2; ++i) bo[i] = ((lane >> 3) * DM + 16 * (((lane >> 1) & 3) ^ ((g & 1) | i << 1)) + 8 * (lane & 1)) * 2;
        asm volatile("s_nop 4" : "+s"(rW));
        sfor<0, 2>([&](auto hh) {
            constexpr int h0 = 7 * decltype(hh)::v;
#pragma unroll
            for (int k = h0; k < h0 + 7; ++k)
#pragma unroll
                for (int j = 0; j < 4; ++j) bdma<(NTW & 8)>(rW, bo[j & 1], (unsigned)(((ks0 + 2 * kp(k) + (k & 1)) * 32 + 8 * j) * DM + 64 * cg) * 2, lb + (k - h0) * 4096 + j * 1024);
            wait_vm0();
#pragma unroll
            for (int k = h0; k < h0 + 7; ++k)
#pragma unroll
                for (int n = 0; n < 4; ++n) {
                    const unsigned o = wave * 32768 + (k - h0) * 4096 + (8 * g + q) * 128 + 32 * (n ^ ((q >> 1) | (g & 1) << 1)) + 8 * p;
                    const v2u lo = tr_rd(lds, o), hi = tr_rd(lds, o + 512);
                    w[k][n] = (v4u){lo.x, lo.y, hi.x, hi.y};
                }
#pragma unroll
            for (int k = h0; k < h0 + 7; ++k) asm volatile("" : "+a"(w[k][0]), "+a"(w[k][1]), "+a"(w[k][2]), "+a"(w[k][3]));
            asm volatile("s_waitcnt lgkmcnt(0)" ::: "memory");
        });
    }
    const int colf = 64 * cg + 32 * kh + 16 * (wave & 1) + 4 * g;
    const v2u br = *(const v2u*)(a.b2 + colf);
    f32x4 bv = {lo16f(br.x), hi16f(br.x), lo16f(br.y), hi16f(br.y)};
    f32x4 gv;
    ld_mod<1>(rs, ctl, {5 * DM + colf}, &gv);
    asm volatile("" : "+v"(gv), "+v"(bv));
    lds_barrier();
    RSTAMP(74, 0);
    const unsigned l16 = lane * 16, lb = (unsigned)(size_t)(LAS unsigned char*)lds, t0 = (wave & 1) * 2 + (wave >> 1);
    auto u = [](unsigned x) { return (unsigned)__builtin_amdgcn_readfirstlane(x); };
    v8u c0 = {l16, lb + wave * 8192 + l16, lb + (4 * kh + t0) * 1024 + l16, D_X2 + (colf >> 7) * 16384 + (((colf >> 5) & 3) * 2 + (wave >> 1)) * 1024 + (((colf >> 3) & 3) * 16 + l15) * 16 + (colf & 4) * 2,
              (unsigned)((16 * (wave >> 1) + l15) * DM + colf) * 2, lane < 2 ? (GO(2, kh * 4 + (cg >> 2)) + lane) * 4u : 0x7F000000u, lb + (4 * (kh ^ 1) + t0) * 1024 + l16,
              lane < KS ? kh * 14 * 16384 + ((wave * KS + lane) >> 2) * 16384 + ((wave * KS + lane) & 3) * 2048 + 2044 : 0x7F000000u};
    v8u sv = {(unsigned)(size_t)a.ws, pol ^ 8192, (unsigned)s0, nearg(a.ws, pol, O_D + (unsigned)s0 * D_SLOT, 1), 0u, 0u, (unsigned)wave, XF2};
    v8u kk;
#pragma unroll
    for (int i = 0; i < 8; ++i) kk[i] = u(kh * 14 * 16384 + ko(2 * (i % 7)));
    const v4u sk = {O_D, u(D_SL + cg * 16384 + (kh * 4 + wave) * 1024), TD_S, 0u};
    const unsigned* dp = nullptr;
    v32u ww[7];
#pragma unroll
    for (int i = 0; i < 7; ++i) ww[i] = cat2(cat(w[2 * i][0], w[2 * i][1], w[2 * i][2], w[2 * i][3]), cat(w[2 * i + 1][0], w[2 * i + 1][1], w[2 * i + 1][2], w[2 * i + 1][3]));
    asm volatile("s_nop 4\n" ROLE_F2 : "+{s[48:55]}"(sv), "+{v[0:7]}"(c0)
        : "{v[8:11]}"(gv), "{v[12:15]}"(bv), "{s[40:43]}"(sk), "{s[44:47]}"(mkrs(a.out, S_TOK * DM * 2)), "{s[56:63]}"(kk), "{s[38:39]}"(dp), "{s[64:67]}"(mkrs(a.ws, WS_END)), [sm] "n"(SM), [sv] "n"(SV),
          "{a[0:31]}"(ww[0]), "{a[32:63]}"(ww[1]), "{a[64:95]}"(ww[2]), "{a[96:127]}"(ww[3]), "{a[128:159]}"(ww[4]), "{a[160:191]}"(ww[5]), "{a[192:223]}"(ww[6])
        : "memory", "vcc", "scc", "v16", "v17", "v18", "v19", "v20", "v21", "v24", "v25", "v26", "v27", "v28", "v29", "v30", "v31", "v32", "v33", "v34", "v35", "v36", "v37", "v38", "v39", "v40", "v41", "v42", "v43", "v44", "v45", "v46", "v47", "v48", "v49", "v50", "v51", "v52", "v53", "v54", "v55", "v56", "v57", "v58", "v59", "v60", "v61", "v62", "v63", "v64", "v65", "v66", "v67", "v68", "v69", "v70", "v71", "v72", "v73", "v74", "v75", "v76", "v77", "v78", "v79", "v80", "v81", "v82", "v83", "v84", "v85", "v86", "v87", "v88", "v89", "v90", "v91", "v92", "v93", "v94", "v95", "v96", "v97", "v98", "v99", "v100", "v101", "v102", "v103", "v104", "v105", "v106", "v107", "v108", "v109", "v110", "v111", "v112", "v113", "v114", "v115", "v116", "v117", "v118", "v119", "v120", "v121", "v122", "v123", "v124", "v125", "v126", "v127", "v128", "v129", "v130", "v131", "v132", "v133", "v134", "v135", "v136", "v137", "v138", "v139", "v140", "v141", "v142", "v143", "v144", "v145", "v146", "v147", "v148", "v149", "v150", "v151", "v152", "v153", "v154", "v155", "v156", "v157", "v158", "v159", "v160", "v161", "v162", "v163", "v164", "v165", "v166", "v167", "v168", "v169", "v170", "v171", "v172", "v173", "v174", "v175", "v176", "v177", "v178", "v179", "v180", "v181", "v182", "v183", "v184", "v185", "v186", "v187", "v188", "v189", "v190", "v191", "v192", "v193", "v194", "v195", "v196", "v197", "v198", "v199", "s68", "s69", "s70", "s71", "s72", "s73", "s74", "s75", "s76", "s77", "s78", "s79", "s80", "s81", "s82", "s83", "s84", "s85", "s86");
}

#define GM 0x76325410u
__global__ void __launch_bounds__(256, 1) k_main(Args a) {
    extern __shared__ __attribute__((aligned(16))) unsigned char lds[];
    const int px = blockIdx.x & 7, by = blockIdx.x >> 3;
    const int xg = 2 * (px & 3) + (by & 1), b2 = by >> 1;
    const int ii = ((GM >> (4 * ((b2 >> 2) + (px & 4)))) & 7) * 4 + (b2 & 3);
    int wv = __builtin_amdgcn_readfirstlane(threadIdx.x >> 6); asm volatile("" : "+s"(wv));
    if (blockIdx.x >= 224) code_touch(lds + LDS_BYTES - 1024, ((blockIdx.x >> 3) - 28) * 4 + wv);
    if (ii < 8) {
        if (ii & 1) chain_main<true>(a, lds, xg, ii, wv); else chain_main<false>(a, lds, xg, ii, wv);
        return;
    }
    if ((a.mode & 255) == 2) return;
    const unsigned par = a.pad0, ev = a.mode >> 8;
    a.pad0 = wv; a.mode = ii * 8 + xg;
    const rsrc_t rs = __builtin_amdgcn_make_buffer_rsrc((void*)a.ws, (short)0, (int)WS_END, 0x00020000);
    unsigned* const ctl = (unsigned*)(a.ws + O_CTL);
    const int role = (ii >> 2) - 2, i4 = ii & 3;
    RSTAMP(75, 0);
    if (role == 0) proj_role<true>(a, rs, lds, 4 * xg + i4, ctl);
    else if (role == 1) proj_role<false>(a, rs, lds, 4 * xg + i4, ctl);
    else if (role < 4) { r_role(a, lds, xg, 4 * xg + i4, role & 1, par, ev); return; }
    else {
        const unsigned t0 = rt_now();
        ln1_rows(a, rs, lds, (ii - 24) * 8 + xg);
        if (role > 3 && (ii < 30 || (xg & 3) < 3)) rt_delay(TD_W, t0);
        RSTAMP(75, 1);
    }
    if (ROFF & 2) return;
    else if (role == 4 || (role == 0 && XF2 < NSEG)) f2_role(a, rs, lds, xg >> 2, 4 * (xg & 3) + i4, role ? 0 : XF2, ctl);
    else if (role < 3 && (role || XF1 >= NSEG)) return;
    else if (4 * (xg & 3) + i4 < 14) f1_role(a, rs, lds, 14 * (xg >> 2) + 4 * (xg & 3) + i4, role ? 0 : XF1, ctl);
}

extern "C" void kernel_launch(void* const* d_in, const int* in_sizes, int n_in,
                              void* d_out, int out_size, void* d_ws, size_t ws_size,
                              hipStream_t stream) {
    Args a;
    a.x = (const u16*)d_in[0]; a.mem0 = (const u16*)d_in[1]; a.Wq = (const u16*)d_in[2]; a.Wk = (const u16*)d_in[3];
    a.Wv = (const u16*)d_in[4]; a.Wo = (const u16*)d_in[5];
    a.w1 = (const u16*)d_in[8]; a.b1 = (const u16*)d_in[9]; a.w2 = (const u16*)d_in[10]; a.b2 = (const u16*)d_in[11];
    a.out = (u16*)d_out; a.ws = (char*)d_ws + (size_t)WSB * MiB; a.mode = CH_EV << 8; a.pad0 = CH_PAR;
    char* ws = a.ws;
    a.c = (const u16*)d_in[12]; a.w_mod = (const u16*)d_in[13];
    k_pre<<<408, 256, 0, stream>>>(a.c, a.w_mod, a.mem0, ws);
    hipFuncSetAttribute((const void*)k_main, hipFuncAttributeMaxDynamicSharedMemorySize, LDS_BYTES);
    k_main<<<256, 256, LDS_BYTES, stream>>>(a);
}
```

```cpp
#include <hip/hip_runtime.h>
#include <hip/hip_bf16.h>
#include <stdint.h>

typedef unsigned short u16;
typedef unsigned long long u64;
typedef __attribute__((ext_vector_type(8))) short bf16x8;
typedef __attribute__((ext_vector_type(4))) short bf16x4;
typedef __attribute__((ext_vector_type(4))) __bf16 bf16x4_t;
typedef __attribute__((ext_vector_type(8))) __bf16 v8bf;
typedef __attribute__((ext_vector_type(16))) __bf16 v16bf;
typedef __attribute__((ext_vector_type(4))) float f32x4;
typedef __attribute__((ext_vector_type(2))) float f32x2;
typedef unsigned v8u __attribute__((ext_vector_type(8)));
typedef unsigned v4u __attribute__((ext_vector_type(4)));
typedef unsigned v2u __attribute__((ext_vector_type(2)));
#define GAS __attribute__((address_space(1)))
#define LAS __attribute__((address_space(3)))
typedef GAS unsigned gu32;
#define RLX_AGENT __ATOMIC_RELAXED, __HIP_MEMORY_SCOPE_AGENT

constexpr int S_TOK = 2048, DM = 1024, NH = 32, DH = 128, MEMR = 8, SEG = 32, NSEG = 64;
constexpr int HD = NH * DH;
constexpr int QKN = 2 * HD;
constexpr int FF = 3584;

#define NTW 111
#define HXM 0
#define RG 16
#define RI(s) ((unsigned)(s) & (RG - 1))
#define PLACE 9
constexpr unsigned MiB = 1u << 20;
constexpr unsigned O_CTL = 0;
constexpr unsigned CTL_BYTES = 65536;
constexpr unsigned O_MOD = 65536;
constexpr unsigned O_POL = O_MOD + 32768;
constexpr unsigned O_MODP = 131072;
constexpr unsigned O_EP = 4 * MiB, EP_SLOT = 524288;
constexpr unsigned O_UM = O_EP + RG * EP_SLOT;
constexpr unsigned UM_SLOT = 64 * 16384;
constexpr unsigned O_E1 = O_UM + RG * UM_SLOT;
constexpr unsigned E1_SLOT = 262144;
constexpr unsigned O_KV = O_E1 + RG * E1_SLOT, KV_SLOT = 524288;
constexpr unsigned O_D = O_KV + RG * KV_SLOT, D_SLOT = MiB, D_SL = 28 * 16384, D_H1 = 54 * 16384, D_X2 = 44 * 16384, D_ST = 52 * 16384;
constexpr unsigned WS_END = O_D + 64 * D_SLOT;
#define WSB 0
static_assert(WSB * MiB + WS_END <= (RG == 64 ? 512 : 256) * MiB && O_D % (2 * MiB) == 0, "ws");
__device__ __forceinline__ unsigned nearg(const char* ws, unsigned pol, unsigned off, unsigned h) {
    unsigned r;
    asm("s_add_u32 %0, %1, %2\n\ts_lshr_b32 %0, %0, 8\n\ts_xor_b32 %0, %0, %3\n\ts_and_b32 %0, %0, 0x2000\n\ts_add_u32 %0, %0, %2" : "=&s"(r) : "s"((unsigned)(size_t)ws), "s"(__builtin_amdgcn_readfirstlane(off)), "s"(pol ^ h << 13) : "scc");
    return r;
}
__device__ __forceinline__ unsigned kmof(const char* ws, unsigned pol, int seg, int h) { return nearg(ws, pol, O_EP + RI(seg) * EP_SLOT, 1) + (h >> 2) * 16384 + (h & 3) * 2048; }
constexpr int C_ABORT = 0;
constexpr int C_HA = 32;
constexpr int C_T = 12256;
constexpr int C_X = 64;
#define PW_V(h) (ctl + C_X + 32 * (h))
#define PW_U(i) (ctl + C_X + 32 * (32 + (i)))
#define PW_Q(h) (ctl + C_X + 32 * (64 + (h)))
constexpr int C_H = C_X + 5120;
constexpr int C_R = 12288, C_P = C_R + 256;
constexpr int C_C = C_H + 2048;
constexpr int C_N = C_C + 288;
constexpr int C_G = C_N + 512;

__device__ __forceinline__ float bf2f(u16 v) { return __uint_as_float(((unsigned)v) << 16); }
__device__ __forceinline__ u16 f2bf(float f) {
    __hip_bfloat16 h = __float2bfloat16(f);
    return __builtin_bit_cast(u16, h);
}
__device__ __forceinline__ float rbf(float f) { return bf2f(f2bf(f)); }

#define NPR 6
#define CTC 3
__device__ __forceinline__ void code_touch(unsigned char* lds, int part) {
    u64 pc; const unsigned sz = 120 * 1024;
    asm volatile("s_getpc_b64 %0" : "=s"(pc));
    const v4u r = {(unsigned)pc, (unsigned)(pc >> 32) & 0xFFFFu, sz, 0x00020000u};
    asm volatile("s_mov_b32 m0, %2\n\ts_nop 4\n\tbuffer_load_dword %0, %1, 0 offen lds" :: "v"(((threadIdx.x & 63) + 64 * part) * 128), "s"(r), "s"((unsigned)(size_t)(LAS unsigned char*)lds) : "m0");
}
__global__ __launch_bounds__(256) void k_pre(const u16* __restrict__ c, const u16* __restrict__ w_mod, const u16* __restrict__ mem0, char* __restrict__ ws) {
    __shared__ float red[32][65];
    const int tid = threadIdx.x;
    const unsigned bid = blockIdx.x;
    if (bid >= 24) {
        const int cg = (bid - 24) % 48, kq = (bid - 24) / 48, c8 = tid & 7, kr = 128 * kq + (tid >> 3);
        v4u wv[4];
#pragma unroll
        for (int i = 0; i < 4; ++i) wv[i] = NTW & 4 ? __builtin_nontemporal_load((const v4u*)(w_mod + (size_t)(kr + 32 * i) * 6144 + cg * 64 + 8 * c8)) : *(const v4u*)(w_mod + (size_t)(kr + 32 * i) * 6144 + cg * 64 + 8 * c8);
        float acc[8] = {0.f, 0.f, 0.f, 0.f, 0.f, 0.f, 0.f, 0.f};
#pragma unroll
        for (int i = 0; i < 4; ++i) {
            const float v = bf2f(c[kr + 32 * i]), sv = rbf(v / (1.f + __expf(-v)));
#pragma unroll
            for (int j = 0; j < 4; ++j) { acc[2 * j] += sv * __uint_as_float(wv[i][j] << 16); acc[2 * j + 1] += sv * __uint_as_float(wv[i][j] & 0xFFFF0000u); }
        }
#pragma unroll
        for (int j = 0; j < 8; ++j) red[tid >> 3][8 * c8 + j] = acc[j];
        __syncthreads();
        if (tid < 64) {
            float v = 0.f;
#pragma unroll
            for (int i = 0; i < 32; ++i) v += red[i][tid];
            ((float*)(ws + O_MODP))[kq * 3072 + cg * 64 + tid] = v;
        }
        return;
    }
    if (bid >= 16) {
        if (bid < 22) *(uint4*)(ws + O_MOD + (bid - 16) * 4096 + tid * 16) = make_uint4(~0u, ~0u, ~0u, ~0u);
        if (bid == 23) { for (int i = 0; i < 4; ++i) code_touch((unsigned char*)red, i * 4 + (tid >> 6)); asm volatile("s_waitcnt vmcnt(0)"); }
        if (bid != 22 || tid) return;
        unsigned best[2] = {~0u, ~0u};
        for (int r = 0; r < NPR; ++r) {
            const u64 t0 = __builtin_readcyclecounter();
            asm volatile("global_store_dword %0, %1, %2 sc1\n\ts_waitcnt vmcnt(0)" :: "v"(O_POL + 128 + (r & 1) * 8192 + (r & 2) * 2048), "v"(0u), "s"(ws) : "memory");
            best[r & 1] = min(best[r & 1], (unsigned)(__builtin_readcyclecounter() - t0));
        }
        const unsigned xcc = __builtin_amdgcn_s_getreg((3 << 11) | 20);
        *(unsigned*)(ws + O_POL) = (((best[1] < best[0]) ^ (xcc >> 2) ^ (((unsigned)(size_t)ws + O_POL) >> 21)) & 1) << 13;
        return;
    }
    ((uint4*)(ws + O_CTL))[bid * 256 + tid] = make_uint4(0u, 0u, 0u, 0u);
    const unsigned u = (tid >> 6) * 256 + (bid & 3) * 64 + (tid & 63);
    uint4 m = *(const uint4*)(mem0 + ((u >> 1) & 7) * DM + (u >> 4) * 16 + (u & 1) * 8);
    m.x = min(m.x, 0xFFFFFFFEu); m.y = min(m.y, 0xFFFFFFFEu); m.z = min(m.z, 0xFFFFFFFEu); m.w = min(m.w, 0xFFFFFFFEu);
    for (int i = 0; i < 4; ++i) *(uint4*)(ws + O_E1 + bid * 16384 + i * 4096 + tid * 16) = m;
}

struct Args {
    const u16* Wq; const u16* Wk; const u16* Wv; const u16* Wo; const u16* mem0;
    const u16* c; const u16* w_mod;
    const u16* x; const u16* w1; const u16* b1; const u16* w2; const u16* b2;
    u16* out; char* ws;
    unsigned mode, pad0;
};
#define RSTAMP(sg, i) do { } while (0)
#define STAMP(i) do { } while (0)
#define XSTAMP(i) do { } while (0)
#define CSTAMP(i, v) do { } while (0)
#define STAMP_WAVE 0
constexpr unsigned SPIN_CAP = 1u << 22;
constexpr unsigned SENT = 0xFFFFFFFFu;

__device__ __forceinline__ void lds_barrier() { asm volatile("s_waitcnt lgkmcnt(0)\n\ts_barrier" ::: "memory"); }
__device__ __forceinline__ void wait_vm0() { asm volatile("s_waitcnt vmcnt(0)" ::: "memory"); }
typedef __bf16 bf16x2_t __attribute__((ext_vector_type(2)));
__device__ __forceinline__ unsigned pack2(float a, float b) { const f32x2 v = {a, b}; return __builtin_bit_cast(unsigned, __builtin_convertvector(v, bf16x2_t)); }
__device__ __forceinline__ float lo16f(unsigned u) { return __uint_as_float(u << 16); }
__device__ __forceinline__ float hi16f(unsigned u) { return __uint_as_float(u & 0xFFFF0000u); }
__device__ __forceinline__ unsigned umax4(v4u x) { return max(max(x.x, x.y), max(x.z, x.w)); }
__device__ __forceinline__ unsigned unsent(unsigned x) { return min(x, 0xFFFFFFFEu); }

__device__ __forceinline__ unsigned ldg4_sc1(const void* sb, unsigned vo) {
    unsigned r;
    asm volatile("global_load_dword %0, %1, %2 sc1\n\ts_waitcnt vmcnt(0)" : "=&v"(r) : "v"(vo), "s"(sb) : "memory");
    return r;
}
__device__ __forceinline__ bool spin_fail(unsigned& spins, unsigned* abortw) {
    if (++spins > SPIN_CAP) { __hip_atomic_store((gu32*)abortw, 1u, RLX_AGENT); return true; }
    if ((spins & 63u) == 0 && __builtin_amdgcn_readfirstlane(__hip_atomic_load((gu32*)abortw, RLX_AGENT)) == 1u) return true;
    return false;
}
__device__ __forceinline__ float red16_add(float x) { auto r = __builtin_amdgcn_permlane16_swap(__float_as_uint(x), __float_as_uint(x), false, false); return __uint_as_float(r[0]) + __uint_as_float(r[1]); }
__device__ __forceinline__ float red32_add(float x) { auto r = __builtin_amdgcn_permlane32_swap(__float_as_uint(x), __float_as_uint(x), false, false); return __uint_as_float(r[0]) + __uint_as_float(r[1]); }
__device__ __forceinline__ v2u tr_rd(const unsigned char* lds_base, unsigned off) {
    return __builtin_bit_cast(v2u, __builtin_amdgcn_ds_read_tr16_b64_v4bf16((LAS bf16x4_t*)(lds_base + off)));
}
__device__ __forceinline__ v4u lds16(const unsigned char* p) { return *(const LAS v4u*)p; }
__device__ __forceinline__ void dma16(const void* src, unsigned char* dst, int aux) {
    if (aux) __builtin_amdgcn_global_load_lds((const unsigned*)src, (unsigned*)dst, 16, 0, 2);
    else __builtin_amdgcn_global_load_lds((const unsigned*)src, (unsigned*)dst, 16, 0, 0);
}

__device__ __forceinline__ v4u mkrs(const void* p, unsigned bytes) { return (v4u){(unsigned)(u64)p, (unsigned)((u64)p >> 32) & 0xFFFFu, bytes, 0x00020000u}; }
template <int AUX> __device__ __forceinline__ void bdma(v4u r, unsigned vo, unsigned so, unsigned ldsaddr) {
    if (AUX == 16) asm volatile("s_mov_b32 m0, %0\n\ts_nop 0\n\tbuffer_load_dwordx4 %1, %2, %3 offen sc1 lds" :: "s"(ldsaddr), "v"(vo), "s"(r), "s"(so) : "m0");
    else if (AUX) asm volatile("s_mov_b32 m0, %0\n\ts_nop 0\n\tbuffer_load_dwordx4 %1, %2, %3 offen nt lds" :: "s"(ldsaddr), "v"(vo), "s"(r), "s"(so) : "m0");
    else asm volatile("s_mov_b32 m0, %0\n\ts_nop 0\n\tbuffer_load_dwordx4 %1, %2, %3 offen lds" :: "s"(ldsaddr), "v"(vo), "s"(r), "s"(so) : "m0");
}

constexpr int L_W = 0;
constexpr int L_KX = 131072;
constexpr int L_MEM = L_KX + 8192;
constexpr int L_T = L_MEM + 16384;
constexpr int L_RED = L_T + 5120;
constexpr int LDS_BYTES = 163840;

#define CH_A 144
#define CH_B 81
#define CH_EU 12
#define CH_EV 22
#define CH_DMIN 0
#define CH_R 60
#define CH_S1 8
#define CH_MA 128
#define CH_MB 4
#define FINE_PAR
constexpr unsigned CH_PAR = CH_A | CH_B << 8 | CH_EU << 16 | CH_DMIN << 21 | (unsigned)CH_R << 26;

#define KRV 0
#define WOF 0
constexpr bool iswo(bool isV, int u) { return isV && (u >= 8) != (WOF != 0); }
constexpr int frag(bool isV, int u) { return isV ? 8 * (u & 1) + (((u >> 1) & 3) ^ KRV) : 8 * ((u >> 1) & 1) + ((u >> 2) ^ KRV); }
constexpr int tile(bool isV, int u, int n) {
    if (iswo(isV, u)) return 4 * (8 + (u & 7)) + n;
    const int i = frag(isV, u) + 4 * (n >> 1);
    return isV ? 2 * i + (n & 1) : 4 * i + 2 * ((u & 1) ^ (i > 3 && (i & 3) == 3)) + (n & 1);
}
constexpr int flat(bool isV, int j) { int u = 0; while (tile(isV, u >> 2, u & 3) != 4 * j + 3) ++u; u >>= 2; return u > 12 + (j >> 2) ? u : 12 + (j >> 2); }
template <int I> struct IC { static constexpr int v = I; };
template <int B, int E, class F> __device__ __forceinline__ void sfor(F&& f) { if constexpr (B < E) { f(IC<B>{}); sfor<B + 1, E>(f); } }
#define SB() __builtin_amdgcn_sched_barrier(0)

__device__ __forceinline__ int lane_id() { int l; asm volatile("v_mbcnt_lo_u32_b32 %0, -1, 0\n\tv_mbcnt_hi_u32_b32 %0, -1, %0" : "=v"(l)); return l; }
#define CHAIN_QK \
    "s_mov_b32 m0, s58\ns_sub_u32 s70, s44, 14\ns_mov_b32 s71, 0\n.Lchq1_%=:\ns_mov_b32 s68, 0\nds_read_b128 v[220:223], v7 offset:0\nds_read_b128 v[224:227], v7 offset:1024\n" \
    "ds_read_b128 v[228:231], v7 offset:2048\nds_read_b128 v[232:235], v7 offset:3072\nds_read_b128 v[236:239], v7 offset:4096\nds_read_b128 v[240:243], v7 offset:5120\n" \
    "ds_read_b128 v[244:247], v7 offset:6144\nds_read_b128 v[248:251], v7 offset:7168\nds_read_b128 v[84:87], v7 offset:8192\nds_read_b128 v[88:91], v7 offset:9216\n" \
    "ds_read_b128 v[60:63], v7 offset:10240\nds_read_b128 v[64:67], v7 offset:11264\nds_read_b128 v[68:71], v7 offset:16384\nds_read_b128 v[72:75], v7 offset:17408\n" \
    "s_waitcnt lgkmcnt(12)\nds_read_b128 v[76:79], v7 offset:18432\nds_read_b128 v[80:83], v7 offset:19456\n.Lchq10_%=:\ns_memrealtime s[72:73]\ns_waitcnt lgkmcnt(0)\n" \
    ".Lchq11_%=:\ns_sub_u32 s72, s72, s63\ns_cmp_lt_i32 s72, -3\ns_cbranch_scc1 .Lchq10_%=\ns_cmp_ge_i32 s72, 0\ns_cbranch_scc1 .Lchq12_%=\ns_nop 2\ns_cmp_ge_i32 s72, -1\n" \
    "s_cbranch_scc1 .Lchq12_%=\ns_nop 2\ns_cmp_ge_i32 s72, -2\ns_cbranch_scc1 .Lchq12_%=\ns_nop 2\n.Lchq12_%=:\n" \
    "buffer_load_dwordx4 v[40:43], v2, s[64:67], s48 offen offset:0 sc1\ns_nop 12\nbuffer_load_dwordx4 v[44:47], v2, s[64:67], s48 offen offset:1024 sc1\ns_nop 12\n" \
    "buffer_load_dwordx4 v[48:51], v2, s[64:67], s48 offen offset:2048 sc1\ns_nop 12\nbuffer_load_dwordx4 v[52:55], v2, s[64:67], s48 offen offset:3072 sc1\n.Lchq9_%=:\n" \
    "v_mov_b64 v[38:39], 0\nv_mov_b64 v[36:37], 0\nv_mov_b64 v[34:35], 0\nv_mov_b64 v[32:33], 0\nv_mov_b64 v[30:31], 0\nv_mov_b64 v[28:29], 0\nv_mov_b64 v[26:27], 0\n" \
    "v_mov_b64 v[24:25], 0\ns_waitcnt vmcnt(3)\nv_smfmac_f32_16x16x64_bf16 v[36:39], v[40:43], v[220:227], v0\nds_write_b128 v3, v[40:43] offset:0\n" \
    "v_smfmac_f32_16x16x64_bf16 v[32:35], v[40:43], a[16:23], v0\nds_read_b128 v[220:223], v7 offset:24576\nv_max3_u32 v252, v40, v41, v41\n" \
    "v_smfmac_f32_16x16x64_bf16 v[28:31], v[40:43], a[8:15], v0\nds_read_b128 v[224:227], v7 offset:25600\nv_max3_u32 v252, v252, v42, v43\n" \
    "v_smfmac_f32_16x16x64_bf16 v[24:27], v[40:43], a[0:7], v0\ns_waitcnt vmcnt(2)\nv_smfmac_f32_16x16x64_bf16 v[36:39], v[44:47], v[228:235], v0\n" \
    "ds_write_b128 v3, v[44:47] offset:1024\nv_smfmac_f32_16x16x64_bf16 v[32:35], v[44:47], a[40:47], v0\nds_read_b128 v[228:231], v7 offset:26624\n" \
    "v_max3_u32 v252, v252, v44, v45\nv_smfmac_f32_16x16x64_bf16 v[28:31], v[44:47], a[32:39], v0\nds_read_b128 v[232:235], v7 offset:27648\nv_max3_u32 v252, v252, v46, v47\n" \
    "v_smfmac_f32_16x16x64_bf16 v[24:27], v[44:47], a[24:31], v0\ns_waitcnt vmcnt(1)\nv_smfmac_f32_16x16x64_bf16 v[36:39], v[48:51], v[236:243], v0\n" \
    "ds_write_b128 v3, v[48:51] offset:2048\nv_smfmac_f32_16x16x64_bf16 v[32:35], v[48:51], a[64:71], v0\ns_waitcnt lgkmcnt(3)\ns_barrier\nv_max3_u32 v252, v252, v48, v49\n" \
    "v_smfmac_f32_16x16x64_bf16 v[28:31], v[48:51], a[56:63], v0\nds_read_b128 v[56:59], v4 offset:0\nv_max3_u32 v252, v252, v50, v51\n" \
    "v_smfmac_f32_16x16x64_bf16 v[24:27], v[48:51], a[48:55], v0\nds_read_b128 v[40:43], v4 offset:1024\ns_waitcnt vmcnt(0)\n" \
    "v_smfmac_f32_16x16x64_bf16 v[36:39], v[52:55], v[244:251], v0\nds_write_b128 v3, v[52:55] offset:3072\nv_smfmac_f32_16x16x64_bf16 v[32:35], v[52:55], a[88:95], v0\n" \
    "ds_read_b128 v[44:47], v5 offset:0\nv_max3_u32 v252, v252, v52, v53\nv_smfmac_f32_16x16x64_bf16 v[28:31], v[52:55], a[80:87], v0\nds_read_b128 v[48:51], v5 offset:1024\n" \
    "v_max3_u32 v252, v252, v54, v55\nv_smfmac_f32_16x16x64_bf16 v[24:27], v[52:55], a[72:79], v0\ns_waitcnt lgkmcnt(4)\nds_write_b32 v16, v252\n" \
    "v_smfmac_f32_16x16x64_bf16 v[36:39], v[56:59], v[84:91], v0\nds_read_b128 v[52:55], v6 offset:0\nv_smfmac_f32_16x16x64_bf16 v[32:35], v[56:59], a[112:119], v0\n" \
    "ds_read_b128 v[84:87], v6 offset:1024\ns_waitcnt lgkmcnt(6)\nv_smfmac_f32_16x16x64_bf16 v[36:39], v[40:43], v[60:67], v0\nds_read_b128 v[236:239], v7 offset:12288\n" \
    "v_smfmac_f32_16x16x64_bf16 v[32:35], v[40:43], a[136:143], v0\ns_waitcnt lgkmcnt(5)\ns_waitcnt lgkmcnt(3)\ns_barrier\n" \
    "v_smfmac_f32_16x16x64_bf16 v[36:39], v[44:47], v[68:75], v0\nds_read_b128 v[18:21], v17\nv_smfmac_f32_16x16x64_bf16 v[32:35], v[44:47], a[208:215], v0\n" \
    "ds_read_b128 v[88:91], v4 offset:2048\nv_smfmac_f32_16x16x64_bf16 v[36:39], v[48:51], v[76:83], v0\nds_read_b128 v[240:243], v7 offset:13312\n" \
    "v_smfmac_f32_16x16x64_bf16 v[32:35], v[48:51], a[232:239], v0\nds_read_b128 v[56:59], v4 offset:3072\ns_waitcnt lgkmcnt(6)\n" \
    "v_smfmac_f32_16x16x64_bf16 v[36:39], v[52:55], v[220:227], v0\nds_read_b128 v[40:43], v5 offset:2048\nv_smfmac_f32_16x16x64_bf16 v[32:35], v[52:55], v[140:147], v0\n" \
    "s_waitcnt lgkmcnt(6)\ns_waitcnt lgkmcnt(4)\nv_max3_u32 v18, v18, v19, v20\nv_smfmac_f32_16x16x64_bf16 v[36:39], v[84:87], v[228:235], v0\n" \
    "ds_read_b128 v[244:247], v7 offset:20480\nv_max_u32 v18, v18, v21\nv_smfmac_f32_16x16x64_bf16 v[32:35], v[84:87], v[164:171], v0\nds_read_b128 v[248:251], v7 offset:21504\n" \
    "s_waitcnt lgkmcnt(4)\nv_cmp_eq_u32 vcc, -1, v18\nv_smfmac_f32_16x16x64_bf16 v[36:39], v[88:91], v[236:243], v0\ns_cbranch_vccnz .Lchq4_%=\n" \
    "v_smfmac_f32_16x16x64_bf16 v[32:35], v[88:91], a[160:167], v0\nds_read_b128 v[44:47], v5 offset:3072\ns_waitcnt lgkmcnt(4)\n" \
    "v_smfmac_f32_16x16x64_bf16 v[36:39], v[56:59], a[176:183], v0\nds_read_b128 v[48:51], v6 offset:2048\nv_smfmac_f32_16x16x64_bf16 v[32:35], v[56:59], a[168:175], v0\n" \
    "ds_read_b128 v[60:63], v7 offset:28672\ns_waitcnt lgkmcnt(3)\nds_read_b128 v[64:67], v7 offset:29696\nv_smfmac_f32_16x16x64_bf16 v[36:39], v[40:43], v[244:251], v0\n" \
    "ds_read_b128 v[52:55], v6 offset:3072\nv_smfmac_f32_16x16x64_bf16 v[32:35], v[40:43], v[92:99], v0\nds_read_b128 v[76:79], v7 offset:14336\ns_waitcnt lgkmcnt(5)\n" \
    "v_smfmac_f32_16x16x64_bf16 v[36:39], v[44:47], v[108:115], v0\nds_read_b128 v[80:83], v7 offset:15360\nv_smfmac_f32_16x16x64_bf16 v[32:35], v[44:47], v[100:107], v0\n" \
    "ds_read_b128 v[68:71], v7 offset:22528\ns_waitcnt lgkmcnt(4)\nv_smfmac_f32_16x16x64_bf16 v[36:39], v[48:51], v[60:67], v0\nds_read_b128 v[72:75], v7 offset:23552\n" \
    "v_smfmac_f32_16x16x64_bf16 v[32:35], v[48:51], v[188:195], v0\nds_read_b128 v[60:63], v7 offset:30720\ns_waitcnt lgkmcnt(5)\n" \
    "v_smfmac_f32_16x16x64_bf16 v[36:39], v[52:55], v[204:211], v0\nds_read_b128 v[64:67], v7 offset:31744\nv_smfmac_f32_16x16x64_bf16 v[32:35], v[52:55], v[196:203], v0\n" \
    "ds_read_b128 v[220:223], v8 offset:0\nv_smfmac_f32_16x16x64_bf16 v[24:27], v[84:87], v[148:155], v0\nds_read_b128 v[224:227], v8 offset:256\ns_nop 0\n" \
    "v_smfmac_f32_16x16x64_bf16 v[28:31], v[84:87], v[156:163], v0\nds_read_b128 v[84:87], v4 offset:0\ns_nop 0\nv_smfmac_f32_16x16x64_bf16 v[24:27], v[88:91], a[144:151], v0\n" \
    "ds_read_b128 v[228:231], v8 offset:2048\nv_permlane32_swap_b32 v32, v36\nv_smfmac_f32_16x16x64_bf16 v[28:31], v[88:91], a[152:159], v0\n" \
    "ds_read_b128 v[88:91], v4 offset:1024\nv_permlane32_swap_b32 v33, v37\nv_smfmac_f32_16x16x64_bf16 v[24:27], v[56:59], a[184:191], v0\ns_waitcnt lgkmcnt(9)\n" \
    "v_add_f32 v32, v32, v36\nv_smfmac_f32_16x16x64_bf16 v[28:31], v[56:59], v[76:83], v0\nds_read_b128 v[56:59], v5 offset:0\nv_add_f32 v33, v33, v37\n" \
    "v_smfmac_f32_16x16x64_bf16 v[24:27], v[40:43], a[240:247], v0\nds_read_b128 v[232:235], v8 offset:2304\nv_permlane32_swap_b32 v34, v38\n" \
    "v_smfmac_f32_16x16x64_bf16 v[28:31], v[40:43], a[248:255], v0\nds_read_b128 v[40:43], v5 offset:1024\nv_permlane32_swap_b32 v35, v39\n" \
    "v_smfmac_f32_16x16x64_bf16 v[24:27], v[44:47], v[116:123], v0\ns_waitcnt lgkmcnt(10)\nv_add_f32 v34, v34, v38\nv_smfmac_f32_16x16x64_bf16 v[28:31], v[44:47], v[68:75], v0\n" \
    "ds_read_b128 v[44:47], v6 offset:0\nv_add_f32 v35, v35, v39\nv_smfmac_f32_16x16x64_bf16 v[24:27], v[48:51], v[172:179], v0\nds_read_b128 v[236:239], v8 offset:4096\n" \
    "v_cvt_pk_bf16_f32 v32, v32, v33\nv_smfmac_f32_16x16x64_bf16 v[28:31], v[48:51], v[180:187], v0\nds_read_b128 v[240:243], v8 offset:4352\nv_cvt_pk_bf16_f32 v33, v34, v35\n" \
    "v_smfmac_f32_16x16x64_bf16 v[24:27], v[52:55], v[212:219], v0\ns_waitcnt lgkmcnt(11)\nds_write_b64 v9, v[32:33]\n" \
    "v_smfmac_f32_16x16x64_bf16 v[28:31], v[52:55], v[60:67], v0\nds_read_b128 v[244:247], v8 offset:6144\ns_waitcnt lgkmcnt(8)\n" \
    "v_smfmac_f32_16x16x64_bf16 v[24:27], v[84:87], a[96:103], v0\nds_read_b128 v[248:251], v8 offset:6400\nv_smfmac_f32_16x16x64_bf16 v[28:31], v[84:87], a[104:111], v0\n" \
    "v_smfmac_f32_16x16x64_bf16 v[24:27], v[88:91], a[120:127], v0\ns_waitcnt lgkmcnt(2)\ns_barrier\nv_smfmac_f32_16x16x64_bf16 v[28:31], v[88:91], a[128:135], v0\n" \
    "ds_read_b64_tr_b16 v[60:61], v10 offset:0\nv_smfmac_f32_16x16x64_bf16 v[24:27], v[56:59], a[192:199], v0\nds_read_b64_tr_b16 v[62:63], v10 offset:32\n" \
    "v_smfmac_f32_16x16x64_bf16 v[28:31], v[56:59], a[200:207], v0\nds_read_b64_tr_b16 v[64:65], v10 offset:256\nv_smfmac_f32_16x16x64_bf16 v[24:27], v[40:43], a[216:223], v0\n" \
    "ds_read_b64_tr_b16 v[66:67], v10 offset:288\nv_smfmac_f32_16x16x64_bf16 v[28:31], v[40:43], a[224:231], v0\nds_read_b64_tr_b16 v[68:69], v10 offset:512\n" \
    "v_smfmac_f32_16x16x64_bf16 v[24:27], v[44:47], v[124:131], v0\nds_read_b64_tr_b16 v[70:71], v10 offset:544\nv_smfmac_f32_16x16x64_bf16 v[28:31], v[44:47], v[132:139], v0\n" \
    "ds_read_b64_tr_b16 v[72:73], v10 offset:768\ns_waitcnt lgkmcnt(5)\nv_mfma_f32_16x16x32_bf16 v[32:35], v[220:223], v[60:63], 0\nds_read_b64_tr_b16 v[74:75], v10 offset:800\n" \
    "s_nop 0\nv_mfma_f32_16x16x32_bf16 v[36:39], v[224:227], v[60:63], 0\ns_nop 1\nv_permlane32_swap_b32 v24, v28\nv_permlane32_swap_b32 v25, v29\n" \
    "v_permlane32_swap_b32 v26, v30\nv_permlane32_swap_b32 v27, v31\nv_pk_add_f32 v[24:25], v[24:25], v[28:29]\nv_pk_add_f32 v[26:27], v[26:27], v[30:31]\n" \
    "v_cvt_pk_bf16_f32 v24, v24, v25\nv_cvt_pk_bf16_f32 v25, v26, v27\nds_write_b64 v9, v[24:25] offset:2560\ns_waitcnt lgkmcnt(5)\n" \
    "v_mfma_f32_16x16x32_bf16 v[32:35], v[228:231], v[64:67], v[32:35]\nv_mfma_f32_16x16x32_bf16 v[36:39], v[232:235], v[64:67], v[36:39]\ns_waitcnt lgkmcnt(3)\n" \
    "v_mfma_f32_16x16x32_bf16 v[32:35], v[236:239], v[68:71], v[32:35]\ns_xor_b32 s74, s50, 0x1000\ns_cmp_eq_u32 s61, 63\ns_waitcnt lgkmcnt(0)\ns_barrier\n" \
    "ds_read_b64_tr_b16 v[40:41], v10 offset:2560\nds_read_b64_tr_b16 v[42:43], v10 offset:2592\nds_read_b64_tr_b16 v[44:45], v10 offset:2816\n" \
    "v_mfma_f32_16x16x32_bf16 v[36:39], v[240:243], v[68:71], v[36:39]\nds_read_b64_tr_b16 v[46:47], v10 offset:2848\nds_read_b64_tr_b16 v[48:49], v10 offset:3072\n" \
    "ds_read_b64_tr_b16 v[50:51], v10 offset:3104\nv_mfma_f32_16x16x32_bf16 v[32:35], v[244:247], v[72:75], v[32:35]\nds_read_b64_tr_b16 v[52:53], v10 offset:3328\n" \
    "ds_read_b64_tr_b16 v[54:55], v10 offset:3360\nv_mfma_f32_16x16x32_bf16 v[36:39], v[248:251], v[72:75], v[36:39]\ns_cbranch_scc1 .Lchq7_%=\ns_waitcnt lgkmcnt(6)\n" \
    "v_mfma_f32_16x16x32_bf16 v[28:31], v[40:43], v[60:63], 0\ns_waitcnt lgkmcnt(4)\nv_mfma_f32_16x16x32_bf16 v[28:31], v[44:47], v[64:67], v[28:31]\ns_waitcnt lgkmcnt(2)\n" \
    "s_nop 1\nv_mov_b32_dpp v32, v36 quad_perm:[0,1,2,3] row_mask:0xf bank_mask:0xc\nv_mov_b32_dpp v33, v37 quad_perm:[0,1,2,3] row_mask:0xf bank_mask:0xc\n" \
    "v_mfma_f32_16x16x32_bf16 v[28:31], v[48:51], v[68:71], v[28:31]\ns_waitcnt lgkmcnt(0)\nv_mov_b32_dpp v34, v38 quad_perm:[0,1,2,3] row_mask:0xf bank_mask:0xc\n" \
    "v_mov_b32_dpp v35, v39 quad_perm:[0,1,2,3] row_mask:0xf bank_mask:0xc\nv_mfma_f32_16x16x32_bf16 v[28:31], v[52:55], v[72:75], v[28:31]\nv_max3_f32 v36, v32, v33, v34\n" \
    "v_max_f32 v36, v36, v35\ns_nop 1\nv_max_f32_dpp v37, v36, v36 row_ror:8 row_mask:0xf bank_mask:0xf\nv_max_f32_dpp v38, v36, v36 row_ror:8 row_mask:0xf bank_mask:0xf\n" \
    "s_nop 1\nv_permlane32_swap_b32 v37, v38\nv_max3_f32 v36, v28, v29, v30\nv_max3_f32 v39, v31, v37, v38\nv_max_f32 v37, v36, v39\nv_max_f32 v36, v36, v39\ns_nop 1\n" \
    "v_permlane16_swap_b32 v36, v37\nv_max_f32 v36, v36, v37\nv_mul_f32_e64 v36, -s32, v36\nv_pk_fma_f32 v[32:33], v[32:33], s[32:33], v[36:37] op_sel_hi:[1,0,0]\n" \
    "v_pk_fma_f32 v[34:35], v[34:35], s[32:33], v[36:37] op_sel_hi:[1,0,0]\nv_pk_fma_f32 v[28:29], v[28:29], s[32:33], v[36:37] op_sel_hi:[1,0,0]\n" \
    "v_pk_fma_f32 v[30:31], v[30:31], s[32:33], v[36:37] op_sel_hi:[1,0,0]\nv_exp_f32 v32, v32\nv_exp_f32 v33, v33\nv_exp_f32 v34, v34\nv_exp_f32 v35, v35\nv_exp_f32 v28, v28\n" \
    "v_pk_add_f32 v[36:37], v[32:33], v[34:35]\nv_add_f32 v36, v36, v37\nv_exp_f32 v29, v29\nv_exp_f32 v30, v30\n" \
    "v_add_f32_dpp v37, v36, v36 row_ror:8 row_mask:0xf bank_mask:0xf\nv_add_f32_dpp v38, v36, v36 row_ror:8 row_mask:0xf bank_mask:0xf\nv_exp_f32 v31, v31\n" \
    "v_add_f32 v36, v28, v29\nv_permlane32_swap_b32 v37, v38\nv_add_f32 v39, v30, v31\nv_pk_add_f32 v[36:37], v[36:37], v[38:39] op_sel:[0,1] op_sel_hi:[1,0]\n" \
    "v_add_f32 v38, v36, v37\nv_add_f32 v36, v36, v37\ns_nop 1\nv_permlane16_swap_b32 v36, v38\nv_add_f32 v36, v36, v38\nv_rcp_f32 v36, v36\ns_nop 0\n" \
    "v_pk_mul_f32 v[28:29], v[28:29], v[36:37] op_sel_hi:[1,0]\nv_pk_mul_f32 v[30:31], v[30:31], v[36:37] op_sel_hi:[1,0]\nv_cvt_pk_bf16_f32 v76, v28, v29\n" \
    "v_cvt_pk_bf16_f32 v77, v30, v31\nv_pk_mul_f32 v[32:33], v[32:33], v[36:37] op_sel_hi:[1,0]\nv_pk_mul_f32 v[34:35], v[34:35], v[36:37] op_sel_hi:[1,0]\n" \
    "v_permlane16_swap_b32 v76, v78\nv_cvt_pk_bf16_f32 v32, v32, v33\nv_permlane16_swap_b32 v77, v79\nv_cvt_pk_bf16_f32 v33, v34, v35\n" \
    "v_mov_b32_dpp v76, v32 quad_perm:[0,1,2,3] row_mask:0xf bank_mask:0x3\nv_mov_b32_dpp v78, v32 row_ror:8 row_mask:0xf bank_mask:0x3\n" \
    "v_mov_b32_dpp v77, v33 quad_perm:[0,1,2,3] row_mask:0xf bank_mask:0x3\nv_mov_b32_dpp v79, v33 row_ror:8 row_mask:0xf bank_mask:0x3\ns_mov_b64 exec, s[46:47]\n" \
    "buffer_store_dwordx4 v[76:79], v11, s[64:67], s50 offen sc1\nbuffer_store_dwordx4 v[76:79], v11, s[64:67], s74 offen sc1\ns_mov_b64 exec, -1\ns_memrealtime s[72:73]\n" \
    "s_add_u32 s63, s63, s44\ns_waitcnt lgkmcnt(0)\ns_sub_u32 s69, s63, s72\ns_cmp_lt_i32 s69, s37\ns_cbranch_scc1 .Lchq13_%=\n.Lchq14_%=:\ns_sub_u32 s69, s44, s69\n" \
    "s_cmp_le_u32 s68, 0\ns_cselect_b32 s70, s69, s70\ns_cmp_ge_u32 s61, s80\ns_cselect_b32 s69, s70, 0\ns_add_u32 s71, s71, s69\nds_read_b64_tr_b16 v[56:57], v13\n" \
    "ds_read_b64_tr_b16 v[58:59], v13 offset:32\ns_waitcnt lgkmcnt(0)\ns_mov_b32 exec_lo, 0x00ff00ff\ns_mov_b32 exec_hi, 0x00ff00ff\n" \
    "buffer_store_dwordx4 v[56:59], v14, s[64:67], s51 offen sc1\ns_mov_b64 exec, -1\ns_lshl_b32 s69, s59, 8\ns_add_u32 s69, s69, s80\ns_add_u32 s69, s69, 7\n" \
    "s_cmp_lg_u32 s61, s69\ns_cbranch_scc1 .Lchq15_%=\nv_mov_b32 v253, s71\ns_mov_b64 exec, 1\nbuffer_atomic_umax v253, off, s[64:67], s83 sc1\ns_mov_b64 exec, -1\n.Lchq15_%=:\n" \
    "s_add_u32 s63, s63, s45\ns_mov_b32 s68, 0\ns_cmp_gt_i32 s60, s61\ns_cbranch_scc0 .Lchq5_%=\n.Lchq6_%=:\nbuffer_load_dwordx4 v1, s[64:67], s56 offen offset:0 sc1 lds\n" \
    "buffer_load_dwordx4 v1, s[64:67], s56 offen offset:1024 sc1 lds\ns_add_u32 s61, s61, 1\ns_and_b32 s69, s61, %[rm]\ns_cselect_b32 s78, 1, %[rw]\ns_cmp_lg_u32 s69, %[rm]\n" \
    "s_cselect_b32 s79, 1, %[rw]\ns_lshl_b32 s77, s78, 18\ns_add_u32 s74, s40, s77\ns_xor_b32 s69, s74, s40\ns_mov_b32 s40, s74\ns_lshr_b32 s69, s69, 8\n" \
    "s_and_b32 s69, s69, 0x2000\ns_add_u32 s48, s48, s77\ns_xor_b32 s48, s48, s69\ns_add_u32 s69, s61, 2\ns_and_b32 s69, s69, %[rm]\ns_lshl_b32 s69, s69, 19\n" \
    "s_add_u32 s69, s69, s52\ns_add_u32 s74, s53, s69\ns_lshr_b32 s74, s74, 8\ns_xor_b32 s74, s74, s54\ns_and_b32 s74, s74, 0x2000\ns_add_u32 s49, s74, s69\n" \
    "s_lshl_b32 s77, s78, 19\ns_add_u32 s74, s41, s77\ns_xor_b32 s69, s74, s41\ns_mov_b32 s41, s74\ns_lshr_b32 s69, s69, 8\ns_and_b32 s69, s69, 0x2000\ns_add_u32 s50, s50, s77\n" \
    "s_xor_b32 s50, s50, s69\ns_add_u32 s51, s51, s77\ns_xor_b32 s51, s51, s69\ns_lshl_b32 s77, s79, 19\ns_add_u32 s74, s35, s77\ns_xor_b32 s69, s74, s35\ns_mov_b32 s35, s74\n" \
    "s_lshr_b32 s69, s69, 8\ns_and_b32 s69, s69, 0x2000\ns_add_u32 s56, s56, s77\ns_xor_b32 s56, s56, s69\ns_add_u32 s69, s80, 10\ns_cmp_lg_u32 s61, s69\n" \
    "s_cbranch_scc1 .Lchq16_%=\nbuffer_load_dword v252, off, s[64:67], s83 sc1\ns_waitcnt vmcnt(0)\nv_readfirstlane_b32 s69, v252\ns_sub_u32 s45, s45, s82\n" \
    "s_add_u32 s69, s69, s81\ns_lshr_b32 s69, s69, 3\ns_add_u32 s77, s44, 8\ns_min_u32 s69, s69, s77\ns_sub_u32 s77, s44, 12\ns_max_u32 s44, s69, s77\n.Lchq16_%=:\n" \
    "s_branch .Lchq1_%=\n.Lchq4_%=:\ns_waitcnt lgkmcnt(0)\ns_barrier\ns_add_u32 s63, s63, s38\ns_xor_b32 s48, s48, 0x1000\ns_xor_b32 s50, s50, 0x1000\ns_add_u32 s68, s68, 1\n" \
    "s_and_b32 s69, s68, 63\ns_cbranch_scc1 .Lchq3_%=\ns_cmp_gt_u32 s68, 0x100000\ns_cbranch_scc1 .Lchq8_%=\nv_mov_b32 v252, 0\n" \
    "buffer_load_dword v253, v252, s[64:67], 0 offen sc1\ns_waitcnt vmcnt(0)\nv_readfirstlane_b32 s69, v253\ns_cmp_eq_u32 s69, 1\ns_cbranch_scc0 .Lchq3_%=\ns_endpgm\n" \
    ".Lchq3_%=:\n.Lchq17_%=:\ns_memrealtime s[72:73]\ns_waitcnt lgkmcnt(0)\n.Lchq18_%=:\ns_sub_u32 s72, s72, s63\ns_cmp_lt_i32 s72, -3\ns_cbranch_scc1 .Lchq17_%=\n" \
    "s_cmp_ge_i32 s72, 0\ns_cbranch_scc1 .Lchq19_%=\ns_nop 2\ns_cmp_ge_i32 s72, -1\ns_cbranch_scc1 .Lchq19_%=\ns_nop 2\ns_cmp_ge_i32 s72, -2\ns_cbranch_scc1 .Lchq19_%=\n" \
    "s_nop 2\n.Lchq19_%=:\nbuffer_load_dwordx4 v[40:43], v2, s[64:67], s48 offen offset:0 sc1\ns_nop 12\nbuffer_load_dwordx4 v[44:47], v2, s[64:67], s48 offen offset:1024 sc1\n" \
    "s_nop 12\nbuffer_load_dwordx4 v[48:51], v2, s[64:67], s48 offen offset:2048 sc1\ns_nop 12\nbuffer_load_dwordx4 v[52:55], v2, s[64:67], s48 offen offset:3072 sc1\n" \
    "ds_read_b128 v[220:223], v7 offset:0\nds_read_b128 v[224:227], v7 offset:1024\nds_read_b128 v[228:231], v7 offset:2048\nds_read_b128 v[232:235], v7 offset:3072\n" \
    "ds_read_b128 v[236:239], v7 offset:4096\nds_read_b128 v[240:243], v7 offset:5120\nds_read_b128 v[244:247], v7 offset:6144\nds_read_b128 v[248:251], v7 offset:7168\n" \
    "ds_read_b128 v[84:87], v7 offset:8192\nds_read_b128 v[88:91], v7 offset:9216\nds_read_b128 v[60:63], v7 offset:10240\nds_read_b128 v[64:67], v7 offset:11264\n" \
    "ds_read_b128 v[68:71], v7 offset:16384\nds_read_b128 v[72:75], v7 offset:17408\ns_waitcnt lgkmcnt(12)\nds_read_b128 v[76:79], v7 offset:18432\n" \
    "ds_read_b128 v[80:83], v7 offset:19456\ns_waitcnt lgkmcnt(0)\ns_branch .Lchq9_%=\n.Lchq5_%=:\nbuffer_load_dword v22, v12, s[64:67], 0 offen sc1\ns_waitcnt vmcnt(0)\n" \
    "v_readfirstlane_b32 s74, v22\ns_max_i32 s60, s60, s74\ns_cmp_gt_i32 s60, s61\ns_cbranch_scc1 .Lchq20_%=\ns_sleep 4\ns_add_u32 s68, s68, 1\ns_and_b32 s69, s68, 63\n" \
    "s_cbranch_scc1 .Lchq5_%=\ns_cmp_gt_u32 s68, 0x100000\ns_cbranch_scc1 .Lchq8_%=\nv_mov_b32 v252, 0\nbuffer_load_dword v253, v252, s[64:67], 0 offen sc1\ns_waitcnt vmcnt(0)\n" \
    "v_readfirstlane_b32 s69, v253\ns_cmp_eq_u32 s69, 1\ns_cbranch_scc0 .Lchq5_%=\ns_endpgm\n.Lchq20_%=:\ns_memrealtime s[72:73]\ns_waitcnt lgkmcnt(0)\n.Lchq21_%=:\n" \
    "s_sub_u32 s69, s63, s72\ns_cmp_ge_i32 s69, 20\ns_cbranch_scc1 .Lchq6_%=\ns_add_u32 s63, s63, s38\ns_xor_b32 s48, s48, 0x1000\ns_xor_b32 s50, s50, 0x1000\n" \
    "s_branch .Lchq21_%=\n.Lchq13_%=:\ns_mov_b32 s68, 1\ns_add_u32 s63, s63, s38\ns_xor_b32 s48, s48, 0x1000\ns_xor_b32 s50, s50, 0x1000\ns_sub_u32 s69, s63, s72\n" \
    "s_cmp_lt_i32 s69, s37\ns_cbranch_scc1 .Lchq13_%=\ns_branch .Lchq14_%=\n.Lchq8_%=:\nv_mov_b32 v252, 0\nv_mov_b32 v253, 1\n" \
    "buffer_store_dword v253, v252, s[64:67], 0 offen sc1\ns_endpgm\n.Lchq7_%=:\nds_read_b64_tr_b16 v[56:57], v13\nds_read_b64_tr_b16 v[58:59], v13 offset:32\n" \
    "s_waitcnt lgkmcnt(0)\ns_mov_b32 exec_lo, 0x00ff00ff\ns_mov_b32 exec_hi, 0x00ff00ff\nbuffer_store_dwordx4 v[56:59], v14, s[64:67], s51 offen sc1\ns_mov_b64 exec, -1\n" \
    ".Lchq2_%=:\ns_waitcnt vmcnt(0)\n"
#define CHAIN_V \
    "s_mov_b32 m0, s58\ns_sub_u32 s70, s44, 14\ns_mov_b32 s71, 0\n.Lchv1_%=:\ns_mov_b32 s68, 0\nds_read_b128 v[220:223], v7 offset:0\nds_read_b128 v[224:227], v7 offset:1024\n" \
    "ds_read_b128 v[228:231], v7 offset:2048\nds_read_b128 v[232:235], v7 offset:3072\nds_read_b128 v[236:239], v7 offset:4096\nds_read_b128 v[240:243], v7 offset:5120\n" \
    "ds_read_b128 v[244:247], v7 offset:8192\nds_read_b128 v[248:251], v7 offset:9216\nds_read_b128 v[60:63], v7 offset:12288\nds_read_b128 v[64:67], v7 offset:13312\n" \
    "ds_read_b128 v[68:71], v7 offset:6144\nds_read_b128 v[72:75], v7 offset:7168\nds_read_b128 v[76:79], v7 offset:10240\nds_read_b128 v[80:83], v7 offset:11264\n" \
    "s_waitcnt lgkmcnt(12)\nds_read_b128 v[84:87], v7 offset:14336\nds_read_b128 v[88:91], v7 offset:15360\n.Lchv10_%=:\ns_memrealtime s[72:73]\ns_waitcnt lgkmcnt(0)\n" \
    ".Lchv11_%=:\ns_sub_u32 s72, s72, s63\ns_cmp_lt_i32 s72, -3\ns_cbranch_scc1 .Lchv10_%=\ns_cmp_ge_i32 s72, 0\ns_cbranch_scc1 .Lchv12_%=\ns_nop 2\ns_cmp_ge_i32 s72, -1\n" \
    "s_cbranch_scc1 .Lchv12_%=\ns_nop 2\ns_cmp_ge_i32 s72, -2\ns_cbranch_scc1 .Lchv12_%=\ns_nop 2\n.Lchv12_%=:\n" \
    "buffer_load_dwordx4 v[32:35], v2, s[64:67], s48 offen offset:0 sc1\ns_nop 12\nbuffer_load_dwordx4 v[36:39], v2, s[64:67], s48 offen offset:1024 sc1\ns_nop 12\n" \
    "buffer_load_dwordx4 v[40:43], v2, s[64:67], s48 offen offset:2048 sc1\ns_nop 12\nbuffer_load_dwordx4 v[44:47], v2, s[64:67], s48 offen offset:3072 sc1\n.Lchv9_%=:\n" \
    "v_mov_b64 v[30:31], 0\nv_mov_b64 v[28:29], 0\nv_mov_b64 v[26:27], 0\nv_mov_b64 v[24:25], 0\ns_add_u32 s77, s63, s44\ns_lshl_b32 s77, s77, 8\ns_add_u32 s77, s77, s61\n" \
    "s_sub_u32 s77, s77, 45055\ns_lshr_b32 s74, s48, 5\ns_and_b32 s74, s74, 128\ns_or_b32 s77, s77, s74\nv_mov_b32 v56, s77\ns_waitcnt vmcnt(3)\n" \
    "v_smfmac_f32_16x16x64_bf16 v[24:27], v[32:35], a[0:7], v0\nds_write_b128 v3, v[32:35] offset:0\nv_max3_u32 v252, v32, v33, v33\n" \
    "v_smfmac_f32_16x16x64_bf16 v[28:31], v[32:35], a[8:15], v0\ns_waitcnt vmcnt(2)\nv_max3_u32 v252, v252, v34, v35\n" \
    "v_smfmac_f32_16x16x64_bf16 v[24:27], v[36:39], a[16:23], v0\nds_write_b128 v3, v[36:39] offset:1024\nv_max3_u32 v252, v252, v36, v37\n" \
    "v_smfmac_f32_16x16x64_bf16 v[28:31], v[36:39], v[220:227], v0\ns_waitcnt vmcnt(1)\nv_max3_u32 v252, v252, v38, v39\n" \
    "v_smfmac_f32_16x16x64_bf16 v[24:27], v[40:43], a[24:31], v0\nds_write_b128 v3, v[40:43] offset:2048\nv_max3_u32 v252, v252, v40, v41\n" \
    "v_smfmac_f32_16x16x64_bf16 v[28:31], v[40:43], a[32:39], v0\ns_waitcnt lgkmcnt(1)\ns_barrier\nds_read_b128 v[48:51], v4 offset:0\nds_read_b128 v[52:55], v4 offset:1024\n" \
    "v_max3_u32 v252, v252, v42, v43\ns_waitcnt vmcnt(0)\nv_smfmac_f32_16x16x64_bf16 v[24:27], v[44:47], a[40:47], v0\nds_write_b128 v3, v[44:47] offset:3072\n" \
    "v_max3_u32 v252, v252, v44, v45\nv_smfmac_f32_16x16x64_bf16 v[28:31], v[44:47], v[228:235], v0\nds_read_b128 v[32:35], v5 offset:0\ns_waitcnt lgkmcnt(3)\n" \
    "v_max3_u32 v252, v252, v46, v47\nv_smfmac_f32_16x16x64_bf16 v[24:27], v[48:51], a[48:55], v0\nds_write_b32 v16, v252\n" \
    "v_smfmac_f32_16x16x64_bf16 v[28:31], v[48:51], a[56:63], v0\nds_read_b128 v[36:39], v5 offset:1024\ns_waitcnt lgkmcnt(4)\n" \
    "v_smfmac_f32_16x16x64_bf16 v[24:27], v[52:55], a[64:71], v0\nds_read_b128 v[40:43], v6 offset:0\nv_smfmac_f32_16x16x64_bf16 v[28:31], v[52:55], v[236:243], v0\n" \
    "ds_read_b128 v[44:47], v6 offset:1024\ns_waitcnt lgkmcnt(4)\nv_smfmac_f32_16x16x64_bf16 v[24:27], v[32:35], a[96:103], v0\n" \
    "v_smfmac_f32_16x16x64_bf16 v[28:31], v[32:35], a[104:111], v0\ns_waitcnt lgkmcnt(2)\ns_barrier\nv_smfmac_f32_16x16x64_bf16 v[24:27], v[36:39], a[112:119], v0\n" \
    "ds_read_b128 v[18:21], v17\nv_smfmac_f32_16x16x64_bf16 v[28:31], v[36:39], v[244:251], v0\nds_read_b128 v[48:51], v4 offset:2048\ns_waitcnt lgkmcnt(3)\n" \
    "v_smfmac_f32_16x16x64_bf16 v[24:27], v[40:43], a[144:151], v0\nds_read_b128 v[52:55], v4 offset:3072\nv_smfmac_f32_16x16x64_bf16 v[28:31], v[40:43], a[152:159], v0\n" \
    "ds_read_b128 v[32:35], v5 offset:2048\ns_waitcnt lgkmcnt(4)\nv_smfmac_f32_16x16x64_bf16 v[24:27], v[44:47], a[160:167], v0\ns_waitcnt lgkmcnt(3)\n" \
    "v_max3_u32 v18, v18, v19, v20\nv_smfmac_f32_16x16x64_bf16 v[28:31], v[44:47], v[60:67], v0\ns_waitcnt lgkmcnt(2)\nv_max_u32 v18, v18, v21\n" \
    "v_smfmac_f32_16x16x64_bf16 v[24:27], v[48:51], a[72:79], v0\nds_read_b128 v[36:39], v5 offset:3072\nv_cmp_eq_u32 vcc, -1, v18\n" \
    "v_smfmac_f32_16x16x64_bf16 v[28:31], v[48:51], a[80:87], v0\ns_waitcnt lgkmcnt(2)\ns_cbranch_vccnz .Lchv4_%=\nv_smfmac_f32_16x16x64_bf16 v[24:27], v[52:55], a[88:95], v0\n" \
    "buffer_store_dword v56, v14, s[64:67], s51 offen sc1\nv_smfmac_f32_16x16x64_bf16 v[28:31], v[52:55], v[68:75], v0\nds_read_b128 v[40:43], v6 offset:2048\n" \
    "s_waitcnt lgkmcnt(2)\nv_smfmac_f32_16x16x64_bf16 v[24:27], v[32:35], a[120:127], v0\nds_read_b128 v[44:47], v6 offset:3072\n" \
    "v_smfmac_f32_16x16x64_bf16 v[28:31], v[32:35], a[128:135], v0\nds_read_b128 v[60:63], v7 offset:16384\ns_waitcnt lgkmcnt(3)\n" \
    "v_smfmac_f32_16x16x64_bf16 v[24:27], v[36:39], a[136:143], v0\nbuffer_store_dword v56, v8, s[64:67], 0 offen sc1\n" \
    "v_smfmac_f32_16x16x64_bf16 v[28:31], v[36:39], v[76:83], v0\nds_read_b128 v[64:67], v7 offset:17408\ns_waitcnt lgkmcnt(3)\n" \
    "v_smfmac_f32_16x16x64_bf16 v[24:27], v[40:43], a[168:175], v0\nds_read_b128 v[68:71], v7 offset:18432\nv_smfmac_f32_16x16x64_bf16 v[28:31], v[40:43], a[176:183], v0\n" \
    "ds_read_b128 v[72:75], v7 offset:19456\ns_waitcnt lgkmcnt(4)\nv_smfmac_f32_16x16x64_bf16 v[24:27], v[44:47], a[184:191], v0\nds_read_b128 v[76:79], v7 offset:20480\n" \
    "v_smfmac_f32_16x16x64_bf16 v[28:31], v[44:47], v[84:91], v0\nds_read_b128 v[80:83], v7 offset:21504\nds_read_b128 v[84:87], v7 offset:22528\n" \
    "ds_read_b128 v[88:91], v7 offset:23552\nv_mov_b64 v[220:221], 0\nv_mov_b64 v[222:223], 0\nv_mov_b64 v[224:225], 0\nv_mov_b64 v[226:227], 0\nv_mov_b64 v[228:229], 0\n" \
    "v_mov_b64 v[230:231], 0\nv_permlane32_swap_b32 v24, v28\nv_permlane32_swap_b32 v25, v29\nv_permlane32_swap_b32 v26, v30\nv_permlane32_swap_b32 v27, v31\n" \
    "v_pk_add_f32 v[24:25], v[24:25], v[28:29]\nv_pk_add_f32 v[26:27], v[26:27], v[30:31]\nv_cvt_pk_bf16_f32 v24, v24, v25\nv_cvt_pk_bf16_f32 v25, v26, v27\n" \
    "ds_write_b64 v9, v[24:25]\nv_mov_b64 v[232:233], 0\nv_mov_b64 v[234:235], 0\nv_mov_b64 v[236:237], 0\nv_mov_b64 v[238:239], 0\nv_mov_b64 v[240:241], 0\n" \
    "v_mov_b64 v[242:243], 0\ns_xor_b32 s74, s50, 0x10000\ns_xor_b32 s75, s50, 0x20000\ns_xor_b32 s76, s50, 0x30000\ns_waitcnt lgkmcnt(0)\ns_barrier\n" \
    "ds_read_b64_tr_b16 v[32:33], v10 offset:0\nds_read_b64_tr_b16 v[34:35], v10 offset:32\nds_read_b64_tr_b16 v[36:37], v10 offset:256\n" \
    "ds_read_b64_tr_b16 v[38:39], v10 offset:288\nds_read_b64_tr_b16 v[40:41], v10 offset:512\nds_read_b64_tr_b16 v[42:43], v10 offset:544\ns_waitcnt lgkmcnt(4)\n" \
    "v_smfmac_f32_16x16x64_bf16 v[220:223], v[32:35], a[192:199], v0\nds_read_b64_tr_b16 v[44:45], v10 offset:768\n" \
    "v_smfmac_f32_16x16x64_bf16 v[224:227], v[32:35], a[208:215], v0\ns_waitcnt lgkmcnt(3)\nds_read_b64_tr_b16 v[46:47], v10 offset:800\n" \
    "v_smfmac_f32_16x16x64_bf16 v[220:223], v[36:39], a[200:207], v0\nv_smfmac_f32_16x16x64_bf16 v[224:227], v[36:39], v[60:67], v0\nds_read_b128 v[60:63], v7 offset:24576\n" \
    "s_waitcnt lgkmcnt(3)\nv_smfmac_f32_16x16x64_bf16 v[220:223], v[40:43], a[216:223], v0\nds_read_b128 v[64:67], v7 offset:25600\nv_mov_b64 v[244:245], 0\n" \
    "v_smfmac_f32_16x16x64_bf16 v[224:227], v[40:43], a[232:239], v0\ns_waitcnt lgkmcnt(2)\nv_mov_b64 v[246:247], 0\n" \
    "v_smfmac_f32_16x16x64_bf16 v[220:223], v[44:47], a[224:231], v0\nv_mov_b64 v[248:249], 0\nv_smfmac_f32_16x16x64_bf16 v[224:227], v[44:47], v[68:75], v0\n" \
    "ds_read_b128 v[68:71], v7 offset:26624\nv_mov_b64 v[250:251], 0\nv_smfmac_f32_16x16x64_bf16 v[228:231], v[32:35], a[240:247], v0\ns_nop 1\n" \
    "v_smfmac_f32_16x16x64_bf16 v[232:235], v[32:35], v[92:99], v0\ns_nop 1\nv_smfmac_f32_16x16x64_bf16 v[228:231], v[36:39], a[248:255], v0\n" \
    "ds_read_b128 v[72:75], v7 offset:27648\nv_cvt_pk_bf16_f32 v52, v220, v221\nv_smfmac_f32_16x16x64_bf16 v[232:235], v[36:39], v[76:83], v0\n" \
    "ds_read_b128 v[76:79], v7 offset:28672\nv_cvt_pk_bf16_f32 v53, v224, v225\nv_smfmac_f32_16x16x64_bf16 v[228:231], v[40:43], v[100:107], v0\n" \
    "ds_read_b128 v[80:83], v7 offset:29696\nv_cvt_pk_bf16_f32 v54, v222, v223\nv_smfmac_f32_16x16x64_bf16 v[232:235], v[40:43], v[116:123], v0\n" \
    "v_cvt_pk_bf16_f32 v55, v226, v227\nv_smfmac_f32_16x16x64_bf16 v[228:231], v[44:47], v[108:115], v0\nv_permlane16_swap_b32 v52, v53\n" \
    "v_smfmac_f32_16x16x64_bf16 v[232:235], v[44:47], v[84:91], v0\nds_read_b128 v[84:87], v7 offset:30720\nv_permlane16_swap_b32 v54, v55\n" \
    "v_smfmac_f32_16x16x64_bf16 v[236:239], v[32:35], v[124:131], v0\nbuffer_store_dwordx4 v[52:55], v11, s[64:67], s50 offen sc1\n" \
    "v_smfmac_f32_16x16x64_bf16 v[240:243], v[32:35], v[140:147], v0\ns_nop 1\nv_smfmac_f32_16x16x64_bf16 v[236:239], v[36:39], v[132:139], v0\ns_waitcnt lgkmcnt(3)\n" \
    "v_cvt_pk_bf16_f32 v48, v228, v229\nv_smfmac_f32_16x16x64_bf16 v[240:243], v[36:39], v[60:67], v0\nds_read_b128 v[88:91], v7 offset:31744\n" \
    "v_cvt_pk_bf16_f32 v49, v232, v233\nv_smfmac_f32_16x16x64_bf16 v[236:239], v[40:43], v[148:155], v0\nv_cvt_pk_bf16_f32 v50, v230, v231\n" \
    "v_smfmac_f32_16x16x64_bf16 v[240:243], v[40:43], v[164:171], v0\nv_cvt_pk_bf16_f32 v51, v234, v235\nv_smfmac_f32_16x16x64_bf16 v[236:239], v[44:47], v[156:163], v0\n" \
    "v_permlane16_swap_b32 v48, v49\nv_smfmac_f32_16x16x64_bf16 v[240:243], v[44:47], v[68:75], v0\nv_permlane16_swap_b32 v50, v51\n" \
    "v_smfmac_f32_16x16x64_bf16 v[244:247], v[32:35], v[172:179], v0\nbuffer_store_dwordx4 v[48:51], v11, s[64:67], s74 offen sc1\n" \
    "v_smfmac_f32_16x16x64_bf16 v[244:247], v[36:39], v[180:187], v0\ns_nop 1\nv_smfmac_f32_16x16x64_bf16 v[244:247], v[40:43], v[196:203], v0\n" \
    "v_cvt_pk_bf16_f32 v52, v236, v237\nv_smfmac_f32_16x16x64_bf16 v[244:247], v[44:47], v[204:211], v0\nv_cvt_pk_bf16_f32 v53, v240, v241\n" \
    "v_smfmac_f32_16x16x64_bf16 v[248:251], v[32:35], v[188:195], v0\ns_waitcnt lgkmcnt(0)\nv_cvt_pk_bf16_f32 v54, v238, v239\n" \
    "v_smfmac_f32_16x16x64_bf16 v[248:251], v[36:39], v[76:83], v0\nv_cvt_pk_bf16_f32 v55, v242, v243\nv_smfmac_f32_16x16x64_bf16 v[248:251], v[40:43], v[212:219], v0\n" \
    "v_permlane16_swap_b32 v52, v53\nv_smfmac_f32_16x16x64_bf16 v[248:251], v[44:47], v[84:91], v0\nv_permlane16_swap_b32 v54, v55\n" \
    "buffer_store_dwordx4 v[52:55], v11, s[64:67], s75 offen sc1\nv_cvt_pk_bf16_f32 v56, v244, v245\nv_cvt_pk_bf16_f32 v58, v246, v247\ns_nop 3\n" \
    "v_cvt_pk_bf16_f32 v57, v248, v249\nv_cvt_pk_bf16_f32 v59, v250, v251\ns_nop 0\nv_permlane16_swap_b32 v56, v57\nv_permlane16_swap_b32 v58, v59\n" \
    "buffer_store_dwordx4 v[56:59], v11, s[64:67], s76 offen sc1\ns_cmp_eq_u32 s61, 63\ns_cbranch_scc1 .Lchv2_%=\ns_memrealtime s[72:73]\ns_add_u32 s63, s63, s44\n" \
    "s_waitcnt lgkmcnt(0)\ns_sub_u32 s69, s63, s72\ns_cmp_lt_i32 s69, s37\ns_cbranch_scc1 .Lchv13_%=\n.Lchv14_%=:\ns_add_u32 s63, s63, s45\ns_mov_b32 s68, 0\n" \
    "s_cmp_gt_i32 s60, s61\ns_cbranch_scc0 .Lchv5_%=\n.Lchv6_%=:\ns_add_u32 s61, s61, 1\ns_and_b32 s69, s61, %[rm]\ns_cselect_b32 s78, 1, %[rw]\ns_cmp_lg_u32 s69, %[rm]\n" \
    "s_cselect_b32 s79, 1, %[rw]\ns_lshl_b32 s77, s78, 18\ns_add_u32 s74, s40, s77\ns_xor_b32 s69, s74, s40\ns_mov_b32 s40, s74\ns_lshr_b32 s69, s69, 8\n" \
    "s_and_b32 s69, s69, 0x2000\ns_add_u32 s48, s48, s77\ns_xor_b32 s48, s48, s69\ns_sub_u32 s69, s61, 2\ns_max_i32 s69, s69, 0\ns_lshl_b32 s69, s69, 20\n" \
    "s_add_u32 s69, s69, s52\ns_add_u32 s74, s53, s69\ns_lshr_b32 s74, s74, 8\ns_xor_b32 s74, s74, s54\ns_and_b32 s74, s74, 0x2000\ns_add_u32 s49, s74, s69\n" \
    "s_lshl_b32 s77, s78, 20\ns_add_u32 s74, s42, s77\ns_xor_b32 s69, s74, s42\ns_mov_b32 s42, s74\ns_lshr_b32 s69, s69, 8\ns_and_b32 s69, s69, 0x2000\ns_add_u32 s50, s50, s77\n" \
    "s_xor_b32 s50, s50, s69\ns_lshl_b32 s77, s78, 19\ns_add_u32 s74, s41, s77\ns_xor_b32 s69, s74, s41\ns_mov_b32 s41, s74\ns_lshr_b32 s69, s69, 8\ns_and_b32 s69, s69, 0x2000\n" \
    "s_add_u32 s51, s51, s77\ns_xor_b32 s51, s51, s69\ns_add_u32 s69, s80, 10\ns_cmp_lg_u32 s61, s69\ns_cbranch_scc1 .Lchv15_%=\nbuffer_load_dword v252, off, s[64:67], s83 sc1\n" \
    "s_waitcnt vmcnt(0)\nv_readfirstlane_b32 s69, v252\ns_sub_u32 s45, s45, s82\ns_add_u32 s69, s69, s81\ns_lshr_b32 s69, s69, 3\ns_add_u32 s77, s44, 8\n" \
    "s_min_u32 s69, s69, s77\ns_sub_u32 s77, s44, 12\ns_max_u32 s44, s69, s77\n.Lchv15_%=:\ns_branch .Lchv1_%=\n.Lchv4_%=:\ns_waitcnt lgkmcnt(0)\ns_barrier\n" \
    "s_add_u32 s63, s63, s38\ns_xor_b32 s48, s48, 0x1000\ns_add_u32 s68, s68, 1\ns_and_b32 s69, s68, 63\ns_cbranch_scc1 .Lchv3_%=\ns_cmp_gt_u32 s68, 0x100000\n" \
    "s_cbranch_scc1 .Lchv8_%=\nv_mov_b32 v252, 0\nbuffer_load_dword v253, v252, s[64:67], 0 offen sc1\ns_waitcnt vmcnt(0)\nv_readfirstlane_b32 s69, v253\ns_cmp_eq_u32 s69, 1\n" \
    "s_cbranch_scc0 .Lchv3_%=\ns_endpgm\n.Lchv3_%=:\n.Lchv16_%=:\ns_memrealtime s[72:73]\ns_waitcnt lgkmcnt(0)\n.Lchv17_%=:\ns_sub_u32 s72, s72, s63\ns_cmp_lt_i32 s72, -3\n" \
    "s_cbranch_scc1 .Lchv16_%=\ns_cmp_ge_i32 s72, 0\ns_cbranch_scc1 .Lchv18_%=\ns_nop 2\ns_cmp_ge_i32 s72, -1\ns_cbranch_scc1 .Lchv18_%=\ns_nop 2\ns_cmp_ge_i32 s72, -2\n" \
    "s_cbranch_scc1 .Lchv18_%=\ns_nop 2\n.Lchv18_%=:\nbuffer_load_dwordx4 v[32:35], v2, s[64:67], s48 offen offset:0 sc1\ns_nop 12\n" \
    "buffer_load_dwordx4 v[36:39], v2, s[64:67], s48 offen offset:1024 sc1\ns_nop 12\nbuffer_load_dwordx4 v[40:43], v2, s[64:67], s48 offen offset:2048 sc1\ns_nop 12\n" \
    "buffer_load_dwordx4 v[44:47], v2, s[64:67], s48 offen offset:3072 sc1\nds_read_b128 v[220:223], v7 offset:0\nds_read_b128 v[224:227], v7 offset:1024\n" \
    "ds_read_b128 v[228:231], v7 offset:2048\nds_read_b128 v[232:235], v7 offset:3072\nds_read_b128 v[236:239], v7 offset:4096\nds_read_b128 v[240:243], v7 offset:5120\n" \
    "ds_read_b128 v[244:247], v7 offset:8192\nds_read_b128 v[248:251], v7 offset:9216\nds_read_b128 v[60:63], v7 offset:12288\nds_read_b128 v[64:67], v7 offset:13312\n" \
    "ds_read_b128 v[68:71], v7 offset:6144\nds_read_b128 v[72:75], v7 offset:7168\nds_read_b128 v[76:79], v7 offset:10240\nds_read_b128 v[80:83], v7 offset:11264\n" \
    "s_waitcnt lgkmcnt(12)\nds_read_b128 v[84:87], v7 offset:14336\nds_read_b128 v[88:91], v7 offset:15360\ns_waitcnt lgkmcnt(0)\ns_branch .Lchv9_%=\n.Lchv5_%=:\n" \
    "buffer_load_dword v22, v12, s[64:67], 0 offen sc1\ns_waitcnt vmcnt(0)\nv_readfirstlane_b32 s74, v22\ns_max_i32 s60, s60, s74\ns_cmp_gt_i32 s60, s61\n" \
    "s_cbranch_scc1 .Lchv19_%=\ns_sleep 4\ns_add_u32 s68, s68, 1\ns_and_b32 s69, s68, 63\ns_cbranch_scc1 .Lchv5_%=\ns_cmp_gt_u32 s68, 0x100000\ns_cbranch_scc1 .Lchv8_%=\n" \
    "v_mov_b32 v252, 0\nbuffer_load_dword v253, v252, s[64:67], 0 offen sc1\ns_waitcnt vmcnt(0)\nv_readfirstlane_b32 s69, v253\ns_cmp_eq_u32 s69, 1\ns_cbranch_scc0 .Lchv5_%=\n" \
    "s_endpgm\n.Lchv19_%=:\ns_memrealtime s[72:73]\ns_waitcnt lgkmcnt(0)\n.Lchv20_%=:\ns_sub_u32 s69, s63, s72\ns_cmp_ge_i32 s69, 20\ns_cbranch_scc1 .Lchv6_%=\n" \
    "s_add_u32 s63, s63, s38\ns_xor_b32 s48, s48, 0x1000\ns_branch .Lchv20_%=\n.Lchv13_%=:\ns_mov_b32 s68, 1\ns_add_u32 s63, s63, s38\ns_xor_b32 s48, s48, 0x1000\n" \
    "s_sub_u32 s69, s63, s72\ns_cmp_lt_i32 s69, s37\ns_cbranch_scc1 .Lchv13_%=\ns_branch .Lchv14_%=\n.Lchv8_%=:\nv_mov_b32 v252, 0\nv_mov_b32 v253, 1\n" \
    "buffer_store_dword v253, v252, s[64:67], 0 offen sc1\ns_endpgm\n.Lchv2_%=:\ns_waitcnt vmcnt(0)\n"
typedef unsigned v32u __attribute__((ext_vector_type(32)));
typedef unsigned v16u __attribute__((ext_vector_type(16)));
template <bool isV>
__device__ __forceinline__ void chain_main(const Args& a, unsigned char* lds, const int xg, const int ii, const int cr, int wave) {
    asm volatile("" : "+s"(wave));
    int tid = wave * 64 + lane_id(); asm volatile("" : "+v"(tid));
    const int head = xg * 4 + (ii >> 1);
    char* const ws = a.ws;
    unsigned* const ctl = (unsigned*)(ws + O_CTL);
    v16bf w[48];
    {
        const int lane = tid & 63, l15 = lane & 15, gb = lane >> 4;
        auto bufp = [&](int u, int wv) { const int b = (u + 4) % 5; return lds + (b < 4 ? L_W + wv * 32768 + b * 8192 : L_KX + wv * 8192); };
        v4u rw[2] = {mkrs(isV ? a.Wv : a.Wk, DM * HD * 2), mkrs(isV ? a.Wo : a.Wq, DM * HD * 2)};
        asm volatile("s_nop 4" : "+s"(rw[0]), "+s"(rw[1]));
        auto issue = [&](int u) {
            const unsigned buf = (unsigned)(size_t)(LAS unsigned char*)bufp(u, wave);
            const int c = u & 7;
#pragma unroll
            for (int r8 = 0; r8 < 8; ++r8) {
                const unsigned ch = (lane & 7) ^ (((2 * r8 + (lane >> 5)) & 3) << 1);
                if (!iswo(isV, u)) bdma<(NTW & (isV ? 64 : 2))>(rw[!isV && (u & 1)], ((lane >> 3) * HD + 8 * ch) * 2, ((64 * (4 * (frag(isV, u) & 3) + (wave ^ (frag(isV, u) >> 2))) + 8 * r8) * HD + head * DH + 64 * (wave >> 1)) * 2, buf + r8 * 1024);
                else bdma<(NTW & 32)>(rw[1], ((lane >> 3) * DM + 8 * ch) * 2, ((head * DH + 64 * (c & 1) + 8 * r8) * DM + 256 * wave + 64 * (c >> 1)) * 2, buf + r8 * 1024);
            }
        };
        const unsigned q = l15 >> 2, p = lane & 3;
        const unsigned row0 = 16 * (gb >> 1) + 4 * (gb & 1) + (q & 1) + 8 * (q >> 1);
        const unsigned sw = ((gb & 1) | ((q >> 1) << 1)) << 1;
        v16bf pend[16];
        auto flush = [&](int j) {
            const v8u v = __builtin_bit_cast(v8u, pend[j]);
            unsigned char* d = lds + L_W + wave * 32768 + j * 2048 + lane * 16;
            *(LAS v4u*)d = (v4u){v[0], v[1], v[2], v[3]};
            *(LAS v4u*)(d + 1024) = (v4u){v[4], v[5], v[6], v[7]};
        };
        issue(0); issue(1); issue(2); issue(3);
        sfor<0, 17>([&](auto cc) {
            constexpr int u = decltype(cc)::v;
            asm volatile("s_waitcnt vmcnt(%0)\n\ts_barrier" :: "n"(u < 13 ? 24 : u < 16 ? 8 * (15 - u) : 0) : "memory");
            if (u + 4 < 16) issue(u + 4);
            if constexpr (u < 16) {
                v16bf t[4];
#pragma unroll
                for (int n = 0; n < 4; ++n) {
                    constexpr bool pk = iswo(isV, u);
                    unsigned char* buf = bufp(u, pk ? wave : wave ^ (n >> 1));
                    const unsigned o = pk ? (32 * (n & 1) + 4 * gb + (q & 1)) * 128 + (((4 * (n >> 1) + 2 * (q >> 1) + (p >> 1)) ^ (gb << 1)) << 4) + (p & 1) * 8
                                          : row0 * 128 + (((4 * (wave & 1) + 2 * (n & 1) + (p >> 1)) ^ sw) << 4) + (p & 1) * 8;
                    const v2u r0 = tr_rd(buf, o), r1 = tr_rd(buf, o + 2 * 128), r2 = tr_rd(buf, o + (pk ? 16 : 32) * 128), r3 = tr_rd(buf, o + (pk ? 18 : 34) * 128);
                    const v8u v = {r0.x, r0.y, r1.x, r1.y, r2.x, r2.y, r3.x, r3.y};
                    t[n] = __builtin_bit_cast(v16bf, v);
                }
                asm volatile("s_waitcnt lgkmcnt(0)" ::: "memory");
                sfor<0, 4>([&](auto nn) {
                    constexpr int n = decltype(nn)::v, c = tile(isV, u, n) >> 2, r = 3 * c + (tile(isV, u, n) & 3);
                    if constexpr ((tile(isV, u, n) & 3) == 3) { pend[c] = t[n]; asm volatile("" : "+v"(pend[c])); }
                    else {
                        w[r] = t[n];
                        if constexpr (r < 32) asm volatile("" : "+a"(w[r])); else asm volatile("" : "+v"(w[r]));
                    }
                });
            }
            sfor<0, 16>([&](auto jj) { if constexpr (flat(isV, decltype(jj)::v) == u) flush(decltype(jj)::v); });
        });
        asm volatile("s_waitcnt vmcnt(0) lgkmcnt(0)\n\ts_barrier" ::: "memory");
    }
    { const int s = 0; STAMP(8); }
    auto olane = [&]() { int l = tid & 63; asm volatile("" : "+v"(l)); return l; };
    const unsigned pol = __builtin_amdgcn_readfirstlane(*(const unsigned*)(ws + O_POL));
    const v4u rws = mkrs(ws, WS_END);
    const unsigned lb0 = (unsigned)(size_t)(LAS unsigned char*)lds;
    auto pwo = [&](int l) { return (C_X + 32 * (l < 32 ? l : 64 + head)) * 4; };
    int rdy = 0;
    if (wave == 0) {
        const int l = olane();
        for (unsigned spins = 0;;) {
            const int c = (int)ldg4_sc1(ws, pwo(l));
            if (!__any(c < 1)) { rdy = 1; break; }
            if (spin_fail(spins, ctl + C_ABORT)) break;
            __builtin_amdgcn_s_sleep(8);
        }
        *(volatile LAS int*)(lds + L_RED) = rdy;
    }
    lds_barrier();
    rdy = __builtin_amdgcn_readfirstlane(*(volatile LAS int*)(lds + L_RED));
    if (!rdy) asm volatile("s_endpgm");
    { const int s = 0; STAMP(9); }
    const unsigned kx0 = O_KV + head * 16384;
    if (!isV) { const int l = olane(); for (int i = 0; i < 2; ++i) bdma<16>(rws, l * 16, nearg(ws, pol, kx0, 1) + wave * 2048 + i * 1024, lb0 + L_KX + wave * 2048 + i * 1024); }
    {
        const unsigned lane = olane(), l15 = lane & 15, g = lane >> 4, q = l15 >> 2, p = lane & 3, l16 = lane * 16, to = (8 * g + q) * 8 + (p & 1) * 1152;
        const v16u c0 = {(lane & 8) ? 0xEEEEu : 0x4444u, l16, g * 256 + (l15 & 7) * 32 + (l15 >> 3) * 16, lb0 + L_MEM + wave * 4096 + l16,
                         lb0 + L_MEM + (wave ^ 1) * 4096 + l16, lb0 + L_MEM + (wave ^ 2) * 4096 + l16, lb0 + L_MEM + (wave ^ 3) * 4096 + l16,
                         lb0 + L_W + wave * 32768 + l16, !isV ? lb0 + L_KX + g * 512 + l15 * 16 : ii == 1 && wave == 0 && lane == 0 ? (C_C + 32 * xg) * 4 : 0x7F000000u, lb0 + L_T + (32 * wave + 16 * (g >> 1) + l15) * 8 + (g & 1) * 1152,
                         lb0 + L_T + to,
                         isV ? (((g & 1) << 1) | (g >> 1)) * 16384 + l15 * 16 : l15 < 8 ? (4 * wave + (head >> 3)) * 16384 + (head & 7) * 512 + g * 128 + l15 * 16 : (16 + wave) * 16384 + head * 128 + (l15 & 7) * 16,
                         nearg(ws, pol, (C_R + 32 * (2 * cr + (xg & 1))) * 4, 0), lb0 + L_T + 2560 + to + wave * 256, !isV ? l15 * 64 + g * 16 : ii == 1 && wave == 1 && lane == 0 ? xg * 128 : 0x7F000000u, isV && ((head & 7) * 4 + wave >= 20 || lane >= 32) ? 0x7F000000u : lane * 128};
        const unsigned par = a.pad0, R = par >> 26, blo = (unsigned)(size_t)ws;
        const unsigned n0 = __builtin_amdgcn_readfirstlane((unsigned)__builtin_amdgcn_s_memrealtime() / R), tc = n0 & 1;
        v16u sv = {nearg(ws, pol, O_E1 + (cr * 4 + wave) * 16384, 0) + tc * 4096, MiB,
                   isV ? nearg(ws, pol, O_UM + wave * 262144, 0) + head * 256 : nearg(ws, pol, O_EP, 0) + tc * 4096,
                   isV ? nearg(ws, pol, O_EP + 20 * 16384, 0) : kmof(ws, pol, 0, head) + wave * 512, isV ? O_D + ((head & 7) * 2 + (wave >> 1)) * 16384 + (wave & 1) * 4096 : O_KV + ((head & 7) * 4 + wave) * 16384, blo, pol, 384 * MiB + ((xg * 8 + ii) * 4 + wave) * 262144u,
                   nearg(ws, pol, kx0 + KV_SLOT, 1) + wave * 2048, 0u, lb0 + L_KX + wave * 2048,
                   (unsigned)wave, 0u, 0u, lb0 + L_RED + 1024 + (wave == STAMP_WAVE ? 0 : MiB), n0 * R};
        v4u sa = {0u, (par >> 21) & 31, R, 1u};
        unsigned sk = blo + O_KV + KV_SLOT;
        v8u sb = {blo + O_E1, blo + O_EP, blo + O_UM, 0u, par & 255, (par >> 8) & 255, 0x00FFFFFFu, 0x00FF00FFu};
        v32u ww[12];
#pragma unroll
        for (int i = 0; i < 12; ++i) {
            const v8u x0 = __builtin_bit_cast(v8u, w[4 * i]), x1 = __builtin_bit_cast(v8u, w[4 * i + 1]), x2 = __builtin_bit_cast(v8u, w[4 * i + 2]), x3 = __builtin_bit_cast(v8u, w[4 * i + 3]);
            ww[i] = __builtin_shufflevector(__builtin_shufflevector(x0, x1, 0, 1, 2, 3, 4, 5, 6, 7, 8, 9, 10, 11, 12, 13, 14, 15), __builtin_shufflevector(x2, x3, 0, 1, 2, 3, 4, 5, 6, 7, 8, 9, 10, 11, 12, 13, 14, 15),
                                            0, 1, 2, 3, 4, 5, 6, 7, 8, 9, 10, 11, 12, 13, 14, 15, 16, 17, 18, 19, 20, 21, 22, 23, 24, 25, 26, 27, 28, 29, 30, 31);
        }
#define CHAIN_CALL(TXT) asm volatile(TXT : "+{s[48:63]}"(sv), "+{s[36:39]}"(sa), "+{s[40:47]}"(sb), "+{s35}"(sk) \
            : "{v[0:15]}"(c0), "{v16}"(lb0 + L_RED + l16 + 4 * wave), "{v17}"(lb0 + L_RED + l16), "{s[64:67]}"(rws), "{s[80:83]}"((v4u){CH_S1, CH_MA, CH_MB, C_T * 4}), [rm] "n"(RG - 1), [rw] "n"(1 - RG), "{s[32:33]}"(0x3e0293eeull), \
              "{a[0:31]}"(ww[0]), "{a[32:63]}"(ww[1]), "{a[64:95]}"(ww[2]), "{a[96:127]}"(ww[3]), "{a[128:159]}"(ww[4]), "{a[160:191]}"(ww[5]), "{a[192:223]}"(ww[6]), "{a[224:255]}"(ww[7]), \
              "{v[92:123]}"(ww[8]), "{v[124:155]}"(ww[9]), "{v[156:187]}"(ww[10]), "{v[188:219]}"(ww[11]) \
            : "memory", "vcc", "scc", "m0", "v18", "v19", "v20", "v21", "v22", "v24", "v25", "v26", "v27", "v28", "v29", "v30", "v31", "v32", "v33", "v34", "v35", "v36", "v37", "v38", "v39", "v40", "v41", "v42", "v43", "v44", "v45", "v46", "v47", "v48", "v49", "v50", "v51", "v52", "v53", "v54", "v55", "v56", "v57", "v58", "v59", "v60", "v61", "v62", "v63", "v64", "v65", "v66", "v67", "v68", "v69", "v70", "v71", "v72", "v73", "v74", "v75", "v76", "v77", "v78", "v79", "v80", "v81", "v82", "v83", "v84", "v85", "v86", "v87", "v88", "v89", "v90", "v91", "v220", "v221", "v222", "v223", "v224", "v225", "v226", "v227", "v228", "v229", "v230", "v231", "v232", "v233", "v234", "v235", "v236", "v237", "v238", "v239", "v240", "v241", "v242", "v243", "v244", "v245", "v246", "v247", "v248", "v249", "v250", "v251", "v252", "v253", "s68", "s69", "s70", "s71", "s72", "s73", "s74", "s75", "s76", "s77", "s78", "s79")
        if constexpr (isV) CHAIN_CALL(CHAIN_V); else CHAIN_CALL(CHAIN_QK);
    }
    tid = wave * 64 + lane_id();
    asm volatile("s_waitcnt vmcnt(0)\n\ts_barrier" ::: "memory");
}

typedef __amdgpu_buffer_rsrc_t rsrc_t;
__device__ __forceinline__ v4u bld16(rsrc_t r, unsigned off) { return __builtin_bit_cast(v4u, __builtin_amdgcn_raw_buffer_load_b128(r, off, 0, 16)); }
__device__ __forceinline__ v2u bld8(rsrc_t r, unsigned off) { return __builtin_bit_cast(v2u, __builtin_amdgcn_raw_buffer_load_b64(r, off, 0, 16)); }
__device__ __forceinline__ void bst16(rsrc_t r, unsigned off, v4u v) { __builtin_amdgcn_raw_buffer_store_b128(__builtin_bit_cast(__attribute__((ext_vector_type(4))) unsigned, v), r, off, 0, 16); }
__device__ __forceinline__ void bst8(rsrc_t r, unsigned off, v2u v) { __builtin_amdgcn_raw_buffer_store_b64(__builtin_bit_cast(__attribute__((ext_vector_type(2))) unsigned, v), r, off, 0, 16); }

constexpr int L_FLAG = LDS_BYTES - 64;
#define otid() (a.pad0 * 64 + lane_id())
__device__ __forceinline__ v4u sld16(rsrc_t r, unsigned vo, unsigned so) { return __builtin_bit_cast(v4u, __builtin_amdgcn_raw_buffer_load_b128(r, vo, so, 16)); }
__device__ __forceinline__ v4u sentv() { unsigned x = SENT; asm volatile("" : "+v"(x)); return (v4u){x, x, x, x}; }
#define BW_SLEEP 16
template <int SLP = BW_SLEEP> __device__ __forceinline__ bool wave_wait(const unsigned* p, int stride, int n, unsigned target, unsigned* abortw, unsigned char* lds = nullptr) {
    const int lane = lane_id();
    for (unsigned spins = 0;;) {
        const unsigned v = lane < n ? __hip_atomic_load((gu32*)(p + lane * stride), RLX_AGENT) : target;
        if (!__any(v < target)) { if (lds && lane == 0) *(volatile LAS unsigned*)(lds + L_FLAG + 32) = v; return true; }
        __builtin_amdgcn_s_sleep(SLP);
        if (++spins > (SPIN_CAP << 1)) { __hip_atomic_store((gu32*)abortw, 1u, RLX_AGENT); return false; }
        if ((spins & 63u) == 0 && __builtin_amdgcn_readfirstlane(__hip_atomic_load((gu32*)abortw, RLX_AGENT)) == 1u) return false;
    }
}
__device__ __forceinline__ float gelu_tanh(float v) {
    const float e = v * (-2.302208198f - 0.1029432397f * (v * v));
    return v * __builtin_amdgcn_rcpf(1.f + __builtin_amdgcn_exp2f(e));
}
__device__ __forceinline__ v4u widen(v2u x, v2u y) {
    const auto s0 = __builtin_amdgcn_permlane16_swap(x.x, y.x, false, false);
    const auto s1 = __builtin_amdgcn_permlane16_swap(x.y, y.y, false, false);
    return (v4u){s0[0], s1[0], s0[1], s1[1]};
}
#define TD_P 190
#define TD_O 260
__device__ __forceinline__ v4u unsent4(v4u v) { return (v4u){unsent(v.x), unsent(v.y), unsent(v.z), unsent(v.w)}; }
__device__ __forceinline__ unsigned rt_now() { return (unsigned)__builtin_amdgcn_s_memrealtime(); }
__device__ __forceinline__ void rt_delay(unsigned ticks  , unsigned t0 = rt_now()) {
    while ((unsigned)__builtin_amdgcn_s_memrealtime() - t0 < ticks) __builtin_amdgcn_s_sleep(2);
}
__device__ __forceinline__ float wave_sum(float x) {
    x += __uint_as_float(__builtin_amdgcn_update_dpp(0, __float_as_uint(x), 0xB1, 0xF, 0xF, false));
    x += __uint_as_float(__builtin_amdgcn_update_dpp(0, __float_as_uint(x), 0x4E, 0xF, 0xF, false));
    x += __uint_as_float(__builtin_amdgcn_update_dpp(0, __float_as_uint(x), 0x124, 0xF, 0xF, false));
    x += __uint_as_float(__builtin_amdgcn_update_dpp(0, __float_as_uint(x), 0x128, 0xF, 0xF, false));
    return red32_add(red16_add(x));
}
__device__ __forceinline__ void ln_row(const v4u (&xr)[2], const f32x4 (&c1)[4], const f32x4 (&sh)[4], v4u (&o)[2]) {
    f32x2 v[8];
    f32x2 s2 = {0.f, 0.f};
#pragma unroll
    for (int j = 0; j < 8; ++j) { v[j] = (f32x2){lo16f(xr[j >> 2][j & 3]), hi16f(xr[j >> 2][j & 3])}; s2 += v[j]; }
    const float mean = wave_sum(s2[0] + s2[1]) * (1.f / DM);
    f32x2 q2 = {0.f, 0.f};
#pragma unroll
    for (int j = 0; j < 8; ++j) { const f32x2 d = v[j] - mean; q2 += d * d; }
    const float vs = wave_sum(q2[0] + q2[1]);
    const float mb = rbf(mean), rsd = __builtin_amdgcn_rcpf(rbf(__builtin_amdgcn_sqrtf(rbf(rbf(vs * (1.f / DM)) + 1e-5f))));
#pragma unroll
    for (int j = 0; j < 8; ++j) {
        const f32x2 cc = {c1[j >> 1][2 * (j & 1)], c1[j >> 1][2 * (j & 1) + 1]}, ss = {sh[j >> 1][2 * (j & 1)], sh[j >> 1][2 * (j & 1) + 1]};
        const f32x2 y = (v[j] - mb) * rsd * cc + ss;
        o[j >> 2][j & 3] = pack2(y[0], y[1]);
    }
}
template <int N> __device__ __forceinline__ void ld_mod(rsrc_t rs, unsigned* ctl, const int (&col)[N], f32x4* m) {
    for (unsigned spins = 0;;) {
        unsigned mx = 0;
#pragma unroll
        for (int i = 0; i < N; ++i) { const v4u v = bld16(rs, O_MOD + col[i] * 4); mx = max(mx, umax4(v)); m[i] = __builtin_bit_cast(f32x4, v); }
        if (!__any(mx == SENT)) break;
        __builtin_amdgcn_s_sleep(6);
        if (spin_fail(spins, ctl + C_ABORT)) asm volatile("s_endpgm");
    }
}
__device__ __forceinline__ void cp_delay(unsigned ticks, unsigned cw) {
    while ((((unsigned)__builtin_amdgcn_s_memrealtime() - (cw >> 8)) & 0xFFFFFFu) < ticks) __builtin_amdgcn_s_sleep(1);
}
__device__ __forceinline__ void set_m0(unsigned v) { asm volatile("s_mov_b32 m0, %0" :: "s"(v) : "m0"); }
__device__ __forceinline__ float sum8(float x) {
    x += __uint_as_float(__builtin_amdgcn_update_dpp(0, __float_as_uint(x), 0x128, 0xF, 0xF, false));
    return red32_add(red16_add(x));
}
__device__ __forceinline__ void mod8(const Args& a, int col, f32x4 (&m)[2]) {
    f32x4 p[8][2];
#pragma unroll
    for (int k = 0; k < 16; ++k) p[k >> 1][k & 1] = *(const f32x4*)(a.ws + O_MODP + ((k >> 1) * 3072 + col + 4 * (k & 1)) * 4);
#pragma unroll
    for (int h = 0; h < 2; ++h) {
        const f32x4 t = ((p[0][h] + p[1][h]) + (p[2][h] + p[3][h])) + ((p[4][h] + p[5][h]) + (p[6][h] + p[7][h]));
#pragma unroll
        for (int i = 0; i < 4; ++i) { float v = rbf(t[i]); if ((col >> 10) == 1) v = rbf(1.f + v); m[h][i] = v; }
    }
}
__device__ __forceinline__ void mod_cols(const Args& a, rsrc_t rs, unsigned char* lds, int cg) {
    const int tid = otid();
    v4u wv[32];
#pragma unroll
    for (int i = 0; i < 32; ++i) wv[i] = __builtin_nontemporal_load((const v4u*)(a.w_mod + (size_t)((tid >> 3) + 32 * i) * 6144 + cg * 64 + 8 * (tid & 7)));
    f32x2 acc[4] = {{0.f, 0.f}, {0.f, 0.f}, {0.f, 0.f}, {0.f, 0.f}};
#pragma unroll
    for (int i = 0; i < 32; ++i) {
        const float sv = *(const LAS float*)(lds + 16384 + ((tid >> 3) + 32 * i) * 4);
#pragma unroll
        for (int j = 0; j < 4; ++j) acc[j] += sv * (f32x2){lo16f(wv[i][j]), hi16f(wv[i][j])};
    }
    unsigned char* red = lds + 20480;
    lds_barrier();
    *(LAS f32x4*)(red + (tid >> 3) * 272 + (tid & 7) * 32) = (f32x4){acc[0][0], acc[0][1], acc[1][0], acc[1][1]};
    *(LAS f32x4*)(red + (tid >> 3) * 272 + (tid & 7) * 32 + 16) = (f32x4){acc[2][0], acc[2][1], acc[3][0], acc[3][1]};
    lds_barrier();
    if (tid < 64) {
        f32x4 t = {0.f, 0.f, 0.f, 0.f};
#pragma unroll
        for (int i = 0; i < 8; ++i) t += *(const LAS f32x4*)(red + ((tid >> 4) * 8 + i) * 272 + (tid & 15) * 16);
#pragma unroll
        for (int i = 0; i < 4; ++i) { float v = rbf(red32_add(red16_add(t[i]))); if ((cg >> 4) == 4) v = rbf(1.f + v); t[i] = v; }
        if (tid < 16) bst16(rs, O_MOD + (cg * 64 + 4 * tid) * 4, unsent4(__builtin_bit_cast(v4u, t)));
    }
}
#define TCH 1
#define TOF 0
#define TK0 320
#define TM 3
#define TE 0
__device__ __forceinline__ void touch(const Args& a, unsigned char* lds, int xg, int wi) {
    const int lane = lane_id();
    constexpr int NU = (1024 - TK0) / 8 * TM;
    set_m0((unsigned)(size_t)(LAS unsigned char*)lds + LDS_BYTES - 1024);
    for (int u = wi; u < NU; u += 32) {
        const int m = u % TM;
        v4u r = mkrs(m == 0 ? a.Wv : m == 1 ? a.Wk : a.Wq, DM * HD * 2);
        asm volatile("s_nop 4\n\tbuffer_load_dword %0, %1, %2 offen lds" :: "v"((lane >> 3) * 8192 + (lane & 7) * 128), "s"(r), "s"((TK0 + 8 * (u / TM)) * 8192 + xg * 1024));
    }
}
#define GO(kind, x) (C_G + ((kind) * 8 + (x)) * 32)
__device__ __forceinline__ void monitor(const Args& a, unsigned* ctl) {
    const int lane = lane_id();
    unsigned* const cr = (unsigned*)(a.ws + nearg(a.ws, __builtin_amdgcn_readfirstlane(*(const unsigned*)(a.ws + O_POL)), C_R * 4, 0));
    int last = 0, lv = 0;
    for (unsigned spins = 0; last < NSEG;) {
        const int v = (int)__hip_atomic_load((gu32*)PW_V(lane + (lane & 32)), RLX_AGENT) - 1;
        int r = last, rv = lv;
        while (r < NSEG && !__any(v <= r)) ++r;
        while (rv <= NSEG && !__any(lane < 32 && v < rv)) ++rv;
        if (r > last || rv > lv) { last = r; lv = rv; spins = 0; if (lane < 8) __hip_atomic_store((GAS u64*)(cr + 32 * lane), (u64)rv << 32 | (unsigned)r, RLX_AGENT); }
        if (last < 6) __builtin_amdgcn_s_sleep(2); else __builtin_amdgcn_s_sleep(12);
        if (spin_fail(spins, ctl + C_ABORT)) return;
    }
}
#define TD_N 1300
#define M2D 150
__device__ __forceinline__ void monitor2(unsigned* ctl) {
    const int lane = lane_id();
    for (int sg = 0; sg < NSEG - RG; ++sg) {
        unsigned spins = 0;
        while (__any((int)__hip_atomic_load((gu32*)(PW_U(lane & 31) + 1024 * 3 * (sg & 1)), RLX_AGENT) <= sg)) {
            __builtin_amdgcn_s_sleep(8);
            if (spin_fail(spins, ctl + C_ABORT)) return;
        }
        if (lane < 8) __hip_atomic_fetch_max((gu32*)(ctl + GO(1, lane) + (sg & 1)), (unsigned)sg + 1, RLX_AGENT);
        if (spins) rt_delay(M2D);
    }
}

__device__ __forceinline__ void ln1_rows(const Args& a, rsrc_t rs, unsigned char* lds, int idx  ) {
    const int tid = otid(), lane = tid & 63, wave = a.pad0;
    unsigned* const ctl = (unsigned*)(a.ws + O_CTL);
    const unsigned pol = __builtin_amdgcn_readfirstlane(*(const unsigned*)(a.ws + O_POL));
    const unsigned t0 = rt_now();
    const int r0 = 4 * idx + wave;
    v4u xr[2];
#pragma unroll
    for (int c = 0; c < 2; ++c) xr[c] = *(const v4u*)(a.x + (size_t)r0 * DM + 512 * c + 8 * lane);
    {
        f32x4 m[2];
        mod8(a, 8 * tid, m);
        *(LAS f32x4*)(lds + 32 * tid) = m[0]; *(LAS f32x4*)(lds + 32 * tid + 16) = m[1];
        const v2u cr = *(const v2u*)(a.c + 4 * tid);
        f32x4 sv = {lo16f(cr.x), hi16f(cr.x), lo16f(cr.y), hi16f(cr.y)};
#pragma unroll
        for (int i = 0; i < 4; ++i) sv[i] = rbf(sv[i] / (1.f + __expf(-sv[i])));
        *(LAS f32x4*)(lds + 16384 + tid * 16) = sv;
    }
    lds_barrier();
    f32x4 c1[4], sh[4];
#pragma unroll
    for (int i = 0; i < 4; ++i) { const int col = 512 * (i >> 1) + 8 * lane + 4 * (i & 1); sh[i] = *(const LAS f32x4*)(lds + col * 4); c1[i] = *(const LAS f32x4*)(lds + (DM + col) * 4); }
    constexpr int MON = 51;
    auto put = [&](int row, const v4u (&x)[2]) {
        v4u o[2];
        ln_row(x, c1, sh, o);
#pragma unroll
        for (int c = 0; c < 2; ++c) bst16(rs, nearg(a.ws, pol, O_D + (unsigned)(row >> 5) * D_SLOT + D_H1 + ((row >> 2) & 7) * 16384, 1) + (row & 3) * 2048 + 1024 * c + 16 * lane, o[c]);
    };
    put(r0, xr);
    wait_vm0();
    if (lane == 0) __hip_atomic_fetch_add((gu32*)(ctl + C_H + 32 * (r0 >> 5)), 1u, RLX_AGENT);
    RSTAMP(75, 2);
    if (TCH) touch(a, lds, idx & 7, (idx >> 3) * 4 + wave);
    if (r0 == 4 * MON) { monitor(a, ctl); asm volatile("s_endpgm"); }
    rt_delay(TD_N, t0);
    if (r0 == 4 * MON + 1 && wave_wait<64>(ctl + C_H, 32, 64, 32, ctl + C_ABORT) && lane == 0) __hip_atomic_store((gu32*)(ctl + C_HA), 384u, RLX_AGENT);
    if (r0 == 4 * MON + 2) { monitor2(ctl); asm volatile("s_endpgm"); }
    if (idx == 16) mod_cols(a, rs, lds, MON + 16);
    if (idx < 32) {
        v4u xn[14][2];
#pragma unroll
        for (int k = 0; k < 28; ++k) xn[k >> 1][k & 1] = *(const v4u*)(a.x + (size_t)(r0 + 128 * (2 + (k >> 1))) * DM + 512 * (k & 1) + 8 * lane);
#pragma unroll
        for (int k = 0; k < 14; ++k) {
            put(r0 + 128 * (k + 2), xn[k]);
            if (k == 3 || k == 13) { wait_vm0(); if (lane <= k && lane >= (k & 4)) __hip_atomic_fetch_add((gu32*)(ctl + C_H + 32 * ((r0 + 128 * (lane + 2)) >> 5)), 1u, RLX_AGENT); }
        }
    }
    if (idx < 16 || (idx >= 32 && idx != MON)) mod_cols(a, rs, lds, idx < 16 ? idx + 80 : idx + 16);
    lds_barrier();
}
__device__ __forceinline__ void sst16(v4u v, rsrc_t r, unsigned vo, unsigned so, int) { __builtin_amdgcn_raw_buffer_store_b128(v, r, vo, so, 16); asm volatile("s_nop 1" :: "v"(v) : "memory"); }
#define MFMA_A(acc, wv, xv) asm volatile("v_mfma_f32_16x16x32_bf16 %0, %1, %2, %0" : "+v"(acc) : "a"(wv), "v"(xv))
#define MFMA_V(acc, wv, xv) asm volatile("v_mfma_f32_16x16x32_bf16 %0, %1, %2, %0" : "+v"(acc) : "v"(wv), "v"(xv))
#define MFMA_A0(acc, wv, xv) asm volatile("v_mfma_f32_16x16x32_bf16 %0, %1, %2, 0" : "=&v"(acc) : "a"(wv), "v"(xv))
#define MFMA_V0(acc, wv, xv) asm volatile("v_mfma_f32_16x16x32_bf16 %0, %1, %2, 0" : "=&v"(acc) : "v"(wv), "v"(xv))
__device__ __forceinline__ float rb1(float v) { return __uint_as_float(pack2(0.f, v)); }
#define TD_L 36
#define TD_W 1000
#define XF1 59
#define XF2 64
#define XO 64
#define TLF 2
#define TD_S 16
__device__ __forceinline__ unsigned arrive(const char* ws, int cnt, int seg, int wave = 3) {
    unsigned old = 1u;
    asm volatile("s_cmp_eq_u32 %3, 3\n\ts_cselect_b64 exec, 1, 0\n\tglobal_atomic_add %0, %1, %0, %2 sc0\n\ts_mov_b64 exec, -1" : "+v"(old) : "v"((C_N + 64 * cnt + seg) * 4), "s"(ws), "s"(wave) : "memory", "scc");
    return old;
}
template <int N> __device__ __forceinline__ unsigned rdw(unsigned v) { unsigned r; asm volatile("s_waitcnt vmcnt(%2)\n\tv_readfirstlane_b32 %0, %1" : "=s"(r) : "v"(v), "n"(N) : "memory"); return r; }
template <int KIND> __device__ __forceinline__ void tell(unsigned* ctl, int seg, unsigned old, unsigned total, int n = 8, int x0 = 0) {
    const int lane = lane_id();
    if (__builtin_amdgcn_readfirstlane(old) == total - 1 && lane < n) __hip_atomic_fetch_max((gu32*)(ctl + GO(KIND, x0 + lane) + (seg & 1)), (unsigned)seg + 1, RLX_AGENT);
}
template <int KIND> __device__ __forceinline__ void tell_now(const Args& a, unsigned* ctl, int cnt, int seg, unsigned total, int n = 8, int x0 = 0) {
    if (a.pad0 == 3) { unsigned old = arrive(a.ws, cnt, seg); asm volatile("s_waitcnt vmcnt(0)" : "+v"(old)); tell<KIND>(ctl, seg, old, total, n, x0); }
}
__device__ __forceinline__ v2u pack4(f32x4 v) { return (v2u){pack2(v[0], v[1]), pack2(v[2], v[3])}; }

template <int OFF> __device__ __forceinline__ void dma1(v4u r, unsigned vo, unsigned so) { asm volatile("buffer_load_dwordx4 %0, %1, %2 offen offset:%3 sc1 lds" :: "v"(vo), "s"(r), "s"(so), "n"(OFF)); }
template <int O0, int ST> __device__ __forceinline__ void ld4(v4u (&d)[4], v4u r, unsigned vo, unsigned so) {
    asm volatile("buffer_load_dwordx4 %0, %4, %5, %6 offen offset:%7 sc1\n\tbuffer_load_dwordx4 %1, %4, %5, %6 offen offset:%8 sc1\n\t"
                 "buffer_load_dwordx4 %2, %4, %5, %6 offen offset:%9 sc1\n\tbuffer_load_dwordx4 %3, %4, %5, %6 offen offset:%10 sc1"
                 : "=&v"(d[0]), "=&v"(d[1]), "=&v"(d[2]), "=&v"(d[3]) : "v"(vo), "s"(r), "s"(so), "n"(O0), "n"(O0 + ST), "n"(O0 + 2 * ST), "n"(O0 + 3 * ST));
}
#define ROFF 0
#define XB 0
#define GO_(G) (G)
#define UPF 0
#define UPO 1
#define AFD(v) ((v) ? 2 : 1)
#define LV0 7
#define LVS 3
#define KTL 29
#define LQ 4
#define LV 12
constexpr int SM = 0, SV = 0;
#define PROJ_Q \
    "ds_read_b128 v[48:51], v2 offset:0\nds_read_b128 v[52:55], v2 offset:8192\nds_read_b128 v[56:59], v3 offset:0\nds_read_b128 v[60:63], v3 offset:8192\ns_mov_b32 s72, 0\n" \
    "s_mov_b32 s73, 0\nv_mov_b32 v21, 0\ns_add_u32 s97, s50, 1\ns_min_u32 s97, s97, 63\ns_sub_u32 s98, s50, s23\ns_add_u32 s99, s98, 1\ns_branch .Lprqslow_%=\n.Lprqgo_%=:\n" \
    "s_lshr_b32 s68, s55, 1\ns_xor_b32 s68, s68, s98\ns_bitcmp1_b32 s68, 0\ns_cbranch_scc0 .Lprqb2_%=\n.Lprqb1_%=:\ns_waitcnt lgkmcnt(4)\n" \
    "v_mfma_f32_16x16x32_bf16 v[24:27], a[0:3], v[48:51], 0\nds_read_b128 v[64:67], v2 offset:1024\ns_sub_u32 s38, s50, s24\n" \
    "v_mfma_f32_16x16x32_bf16 v[36:39], a[0:3], v[52:55], 0\nds_read_b128 v[68:71], v2 offset:9216\ns_max_i32 s38, s38, 0\n" \
    "v_mfma_f32_16x16x32_bf16 v[28:31], a[4:7], v[48:51], 0\ns_add_u32 m0, s52, 49152\ns_add_u32 s69, s54, 1536\ns_and_b32 s38, s38, s16\n" \
    "v_mfma_f32_16x16x32_bf16 v[40:43], a[4:7], v[52:55], 0\nbuffer_load_dwordx4 v1, s[64:67], s69 offen offset:0 sc1 lds\ns_lshl_b32 s38, s38, 19\n" \
    "v_mfma_f32_16x16x32_bf16 v[32:35], v[124:127], v[48:51], 0\ns_add_u32 s38, s38, s53\ns_add_u32 s74, s48, s38\ns_lshr_b32 s74, s74, 8\n" \
    "v_mfma_f32_16x16x32_bf16 v[44:47], v[124:127], v[52:55], 0\ns_waitcnt lgkmcnt(4)\ns_xnor_b32 s74, s74, s49\ns_and_b32 s74, s74, 0x2000\n" \
    "v_mfma_f32_16x16x32_bf16 v[24:27], a[8:11], v[56:59], v[24:27]\nds_read_b128 v[72:75], v3 offset:1024\ns_add_u32 s74, s74, s38\n" \
    "v_mfma_f32_16x16x32_bf16 v[36:39], a[8:11], v[60:63], v[36:39]\nds_read_b128 v[76:79], v3 offset:9216\ns_add_u32 s74, s74, s60\n" \
    "v_mfma_f32_16x16x32_bf16 v[28:31], a[12:15], v[56:59], v[28:31]\ns_add_u32 s68, s50, 1\ns_min_u32 s68, s68, 63\ns_lshl_b32 s68, s68, 20\n" \
    "v_mfma_f32_16x16x32_bf16 v[40:43], a[12:15], v[60:63], v[40:43]\ns_add_u32 s68, s68, s18\ns_add_u32 s70, s48, s68\ns_lshr_b32 s70, s70, 8\n" \
    "v_mfma_f32_16x16x32_bf16 v[32:35], v[128:131], v[56:59], v[32:35]\ns_xnor_b32 s70, s70, s49\ns_and_b32 s70, s70, 0x2000\ns_add_u32 s70, s70, s68\n" \
    "v_mfma_f32_16x16x32_bf16 v[44:47], v[128:131], v[60:63], v[44:47]\ns_waitcnt lgkmcnt(2)\ns_add_u32 s69, s54, 640\n" \
    "v_mfma_f32_16x16x32_bf16 v[24:27], a[16:19], v[64:67], v[24:27]\nds_read_b128 v[48:51], v2 offset:2048\nv_mfma_f32_16x16x32_bf16 v[36:39], a[16:19], v[68:71], v[36:39]\n" \
    "ds_read_b128 v[52:55], v2 offset:10240\nv_mfma_f32_16x16x32_bf16 v[28:31], a[20:23], v[64:67], v[28:31]\nbuffer_load_dwordx4 v1, s[64:67], s69 offen offset:1024 sc1 lds\n" \
    "s_add_u32 s69, s54, -256\nv_mfma_f32_16x16x32_bf16 v[40:43], a[20:23], v[68:71], v[40:43]\nbuffer_load_dwordx4 v1, s[64:67], s69 offen offset:2048 sc1 lds\n" \
    "v_mfma_f32_16x16x32_bf16 v[32:35], v[132:135], v[64:67], v[32:35]\nv_mfma_f32_16x16x32_bf16 v[44:47], v[132:135], v[68:71], v[44:47]\ns_waitcnt lgkmcnt(2)\n" \
    "v_mfma_f32_16x16x32_bf16 v[24:27], a[24:27], v[72:75], v[24:27]\nds_read_b128 v[56:59], v3 offset:2048\nv_mfma_f32_16x16x32_bf16 v[36:39], a[24:27], v[76:79], v[36:39]\n" \
    "ds_read_b128 v[60:63], v3 offset:10240\nv_mfma_f32_16x16x32_bf16 v[28:31], a[28:31], v[72:75], v[28:31]\ns_add_u32 s69, s54, -1152\n" \
    "buffer_load_dwordx4 v1, s[64:67], s69 offen offset:3072 sc1 lds\nv_mfma_f32_16x16x32_bf16 v[40:43], a[28:31], v[76:79], v[40:43]\ns_waitcnt vmcnt(7)\n" \
    "v_readfirstlane_b32 s42, v21\nv_mfma_f32_16x16x32_bf16 v[32:35], v[136:139], v[72:75], v[32:35]\nv_mfma_f32_16x16x32_bf16 v[44:47], v[136:139], v[76:79], v[44:47]\n" \
    "s_waitcnt lgkmcnt(2)\nv_mfma_f32_16x16x32_bf16 v[24:27], a[32:35], v[48:51], v[24:27]\nds_read_b128 v[64:67], v2 offset:3072\n" \
    "v_mfma_f32_16x16x32_bf16 v[36:39], a[32:35], v[52:55], v[36:39]\nds_read_b128 v[68:71], v2 offset:11264\nv_mfma_f32_16x16x32_bf16 v[28:31], a[36:39], v[48:51], v[28:31]\n" \
    "s_and_b32 s37, s50, s16\ns_lshl_b32 s37, s37, 19\ns_add_u32 s37, s37, s53\nv_mfma_f32_16x16x32_bf16 v[40:43], a[36:39], v[52:55], v[40:43]\ns_add_u32 s71, s48, s37\n" \
    "s_lshr_b32 s71, s71, 8\ns_xnor_b32 s71, s71, s49\nv_mfma_f32_16x16x32_bf16 v[32:35], v[140:143], v[48:51], v[32:35]\ns_and_b32 s71, s71, 0x2000\ns_add_u32 s71, s71, s37\n" \
    "s_mov_b32 m0, s56\nv_mfma_f32_16x16x32_bf16 v[44:47], v[140:143], v[52:55], v[44:47]\ns_waitcnt lgkmcnt(2)\nbuffer_load_dwordx4 v0, s[64:67], s74 offen offset:0 sc1 lds\n" \
    "v_mfma_f32_16x16x32_bf16 v[24:27], a[40:43], v[56:59], v[24:27]\nds_read_b128 v[72:75], v3 offset:3072\nv_mfma_f32_16x16x32_bf16 v[36:39], a[40:43], v[60:63], v[36:39]\n" \
    "ds_read_b128 v[76:79], v3 offset:11264\nv_mfma_f32_16x16x32_bf16 v[28:31], a[44:47], v[56:59], v[28:31]\nbuffer_load_dwordx4 v0, s[64:67], s74 offen offset:1024 sc1 lds\n" \
    "v_mfma_f32_16x16x32_bf16 v[40:43], a[44:47], v[60:63], v[40:43]\ns_cmp_eq_u32 s42, 63\ns_cbranch_scc1 .Lprqtl_%=\n.Lprqtlb_%=:\n" \
    "v_mfma_f32_16x16x32_bf16 v[32:35], v[144:147], v[56:59], v[32:35]\nv_mfma_f32_16x16x32_bf16 v[44:47], v[144:147], v[60:63], v[44:47]\ns_waitcnt lgkmcnt(0)\ns_barrier\n" \
    "v_mfma_f32_16x16x32_bf16 v[24:27], a[48:51], v[64:67], v[24:27]\nds_read_b128 v[48:51], v2 offset:16384\nv_mfma_f32_16x16x32_bf16 v[36:39], a[48:51], v[68:71], v[36:39]\n" \
    "ds_read_b128 v[52:55], v2 offset:24576\nv_mfma_f32_16x16x32_bf16 v[28:31], a[52:55], v[64:67], v[28:31]\ns_add_u32 s39, s50, 1\ns_add_u32 s97, s39, 1\n" \
    "s_min_u32 s97, s97, 63\nv_mfma_f32_16x16x32_bf16 v[40:43], a[52:55], v[68:71], v[40:43]\ns_sub_u32 s98, s39, s23\ns_add_u32 s99, s98, 1\n" \
    "v_mfma_f32_16x16x32_bf16 v[32:35], v[148:151], v[64:67], v[32:35]\ns_cmp_gt_i32 s51, s97\ns_cselect_b32 s96, s98, 0x7fffffff\ns_sub_u32 s39, s39, 2\n" \
    "v_mfma_f32_16x16x32_bf16 v[44:47], v[148:151], v[68:71], v[44:47]\ns_cmp_lt_u32 s39, 62\ns_cselect_b32 s96, s96, 0x7fffffff\n" \
    "v_mfma_f32_16x16x32_bf16 v[24:27], a[56:59], v[72:75], v[24:27]\nds_read_b128 v[56:59], v3 offset:16384\nv_mfma_f32_16x16x32_bf16 v[36:39], a[56:59], v[76:79], v[36:39]\n" \
    "ds_read_b128 v[60:63], v3 offset:24576\nv_mfma_f32_16x16x32_bf16 v[28:31], a[60:63], v[72:75], v[28:31]\nv_mfma_f32_16x16x32_bf16 v[40:43], a[60:63], v[76:79], v[40:43]\n" \
    "v_mfma_f32_16x16x32_bf16 v[32:35], v[152:155], v[72:75], v[32:35]\nv_mfma_f32_16x16x32_bf16 v[44:47], v[152:155], v[76:79], v[44:47]\ns_waitcnt lgkmcnt(2)\n" \
    "v_mfma_f32_16x16x32_bf16 v[24:27], a[64:67], v[48:51], v[24:27]\nds_read_b128 v[64:67], v2 offset:17408\nv_mfma_f32_16x16x32_bf16 v[36:39], a[64:67], v[52:55], v[36:39]\n" \
    "ds_read_b128 v[68:71], v2 offset:25600\nv_mfma_f32_16x16x32_bf16 v[28:31], a[68:71], v[48:51], v[28:31]\ns_add_u32 m0, s52, 0\ns_add_u32 s69, s70, 0\n" \
    "v_mfma_f32_16x16x32_bf16 v[40:43], a[68:71], v[52:55], v[40:43]\nbuffer_load_dwordx4 v1, s[64:67], s69 offen offset:0 sc1 lds\n" \
    "v_mfma_f32_16x16x32_bf16 v[32:35], v[156:159], v[48:51], v[32:35]\nv_mfma_f32_16x16x32_bf16 v[44:47], v[156:159], v[52:55], v[44:47]\ns_waitcnt lgkmcnt(2)\n" \
    "v_mfma_f32_16x16x32_bf16 v[24:27], a[72:75], v[56:59], v[24:27]\nds_read_b128 v[72:75], v3 offset:17408\nv_mfma_f32_16x16x32_bf16 v[36:39], a[72:75], v[60:63], v[36:39]\n" \
    "ds_read_b128 v[76:79], v3 offset:25600\nv_mfma_f32_16x16x32_bf16 v[28:31], a[76:79], v[56:59], v[28:31]\ns_add_u32 s69, s70, -896\n" \
    "buffer_load_dwordx4 v1, s[64:67], s69 offen offset:1024 sc1 lds\nv_mfma_f32_16x16x32_bf16 v[40:43], a[76:79], v[60:63], v[40:43]\n" \
    "v_mfma_f32_16x16x32_bf16 v[32:35], v[160:163], v[56:59], v[32:35]\nv_mfma_f32_16x16x32_bf16 v[44:47], v[160:163], v[60:63], v[44:47]\ns_waitcnt lgkmcnt(2)\n" \
    "v_mfma_f32_16x16x32_bf16 v[24:27], a[80:83], v[64:67], v[24:27]\nds_read_b128 v[48:51], v2 offset:18432\nv_mfma_f32_16x16x32_bf16 v[36:39], a[80:83], v[68:71], v[36:39]\n" \
    "ds_read_b128 v[52:55], v2 offset:26624\nv_mfma_f32_16x16x32_bf16 v[28:31], a[84:87], v[64:67], v[28:31]\ns_add_u32 s69, s70, -1792\n" \
    "buffer_load_dwordx4 v1, s[64:67], s69 offen offset:2048 sc1 lds\nv_mfma_f32_16x16x32_bf16 v[40:43], a[84:87], v[68:71], v[40:43]\ns_add_u32 s68, s59, s25\n" \
    "s_and_b32 s68, s68, 0xffff\nv_add_u32 v122, s68, v13\nv_mfma_f32_16x16x32_bf16 v[32:35], v[164:167], v[64:67], v[32:35]\nds_read_b128 v[80:83], v122 offset:0\n" \
    "v_mfma_f32_16x16x32_bf16 v[44:47], v[164:167], v[68:71], v[44:47]\ns_waitcnt lgkmcnt(3)\nds_read_b128 v[84:87], v122 offset:1024\n" \
    "v_mfma_f32_16x16x32_bf16 v[24:27], a[88:91], v[72:75], v[24:27]\nds_read_b128 v[56:59], v3 offset:18432\nv_mfma_f32_16x16x32_bf16 v[36:39], a[88:91], v[76:79], v[36:39]\n" \
    "ds_read_b128 v[60:63], v3 offset:26624\nv_mfma_f32_16x16x32_bf16 v[28:31], a[92:95], v[72:75], v[28:31]\ns_add_u32 s69, s70, -2688\n" \
    "buffer_load_dwordx4 v1, s[64:67], s69 offen offset:3072 sc1 lds\nv_mfma_f32_16x16x32_bf16 v[40:43], a[92:95], v[76:79], v[40:43]\nds_read_b128 v[88:91], v122 offset:2048\n" \
    "v_mfma_f32_16x16x32_bf16 v[32:35], v[168:171], v[72:75], v[32:35]\nds_read_b128 v[92:95], v122 offset:3072\n" \
    "v_mfma_f32_16x16x32_bf16 v[44:47], v[168:171], v[76:79], v[44:47]\ns_waitcnt lgkmcnt(6)\nv_mfma_f32_16x16x32_bf16 v[24:27], a[96:99], v[48:51], v[24:27]\n" \
    "ds_read_b128 v[64:67], v2 offset:19456\nv_mfma_f32_16x16x32_bf16 v[36:39], a[96:99], v[52:55], v[36:39]\nds_read_b128 v[68:71], v2 offset:27648\n" \
    "v_mfma_f32_16x16x32_bf16 v[28:31], a[100:103], v[48:51], v[28:31]\nbuffer_load_dword v20, v6, s[64:67], 0 offen sc1\n" \
    "v_mfma_f32_16x16x32_bf16 v[40:43], a[100:103], v[52:55], v[40:43]\nv_mfma_f32_16x16x32_bf16 v[32:35], v[172:175], v[48:51], v[32:35]\n" \
    "v_mfma_f32_16x16x32_bf16 v[44:47], v[172:175], v[52:55], v[44:47]\ns_waitcnt lgkmcnt(4)\nv_mfma_f32_16x16x32_bf16 v[24:27], a[104:107], v[56:59], v[24:27]\n" \
    "ds_read_b128 v[72:75], v3 offset:19456\nv_mfma_f32_16x16x32_bf16 v[36:39], a[104:107], v[60:63], v[36:39]\nds_read_b128 v[76:79], v3 offset:27648\n" \
    "v_mfma_f32_16x16x32_bf16 v[28:31], a[108:111], v[56:59], v[28:31]\ns_memrealtime s[72:73]\nv_mfma_f32_16x16x32_bf16 v[40:43], a[108:111], v[60:63], v[40:43]\n" \
    "v_mfma_f32_16x16x32_bf16 v[32:35], v[176:179], v[56:59], v[32:35]\nv_mfma_f32_16x16x32_bf16 v[44:47], v[176:179], v[60:63], v[44:47]\ns_waitcnt vmcnt(5) lgkmcnt(0)\n" \
    "s_barrier\nv_mfma_f32_16x16x32_bf16 v[24:27], a[112:115], v[64:67], v[24:27]\nds_read_b128 v[48:51], v2 offset:32768\nv_mov_b32 v123, s50\n" \
    "v_mfma_f32_16x16x32_bf16 v[36:39], a[112:115], v[68:71], v[36:39]\nds_read_b128 v[52:55], v2 offset:40960\n" \
    "v_mfma_f32_16x16x32_bf16 v[28:31], a[116:119], v[64:67], v[28:31]\nbuffer_store_dword v123, v5, s[64:67], 0 offen sc1\n" \
    "v_mfma_f32_16x16x32_bf16 v[40:43], a[116:119], v[68:71], v[40:43]\nds_read_b128 v[96:99], v8 offset:0\nv_mfma_f32_16x16x32_bf16 v[32:35], v[180:183], v[64:67], v[32:35]\n" \
    "ds_read_b128 v[100:103], v8 offset:2048\nv_mfma_f32_16x16x32_bf16 v[44:47], v[180:183], v[68:71], v[44:47]\nds_read_b128 v[104:107], v8 offset:4096\n" \
    "v_mfma_f32_16x16x32_bf16 v[24:27], a[120:123], v[72:75], v[24:27]\nds_read_b128 v[56:59], v3 offset:32768\n" \
    "v_mfma_f32_16x16x32_bf16 v[36:39], a[120:123], v[76:79], v[36:39]\nds_read_b128 v[60:63], v3 offset:40960\n" \
    "v_mfma_f32_16x16x32_bf16 v[28:31], a[124:127], v[72:75], v[28:31]\nds_read_b128 v[108:111], v8 offset:6144\n" \
    "v_mfma_f32_16x16x32_bf16 v[40:43], a[124:127], v[76:79], v[40:43]\nv_mfma_f32_16x16x32_bf16 v[32:35], v[184:187], v[72:75], v[32:35]\n" \
    "v_mfma_f32_16x16x32_bf16 v[44:47], v[184:187], v[76:79], v[44:47]\ns_waitcnt lgkmcnt(6)\nv_mfma_f32_16x16x32_bf16 v[24:27], a[128:131], v[48:51], v[24:27]\n" \
    "ds_read_b128 v[64:67], v2 offset:33792\nv_mfma_f32_16x16x32_bf16 v[36:39], a[128:131], v[52:55], v[36:39]\nds_read_b128 v[68:71], v2 offset:41984\n" \
    "v_mfma_f32_16x16x32_bf16 v[28:31], a[132:135], v[48:51], v[28:31]\ns_waitcnt lgkmcnt(2)\ns_add_u32 m0, s52, 16384\ns_add_u32 s69, s70, 512\n" \
    "v_mfma_f32_16x16x32_bf16 v[112:115], v[96:99], v[80:83], 0\nbuffer_load_dwordx4 v1, s[64:67], s69 offen offset:0 sc1 lds\n" \
    "v_mfma_f32_16x16x32_bf16 v[40:43], a[132:135], v[52:55], v[40:43]\nds_read_b128 v[96:99], v8 offset:32\n" \
    "v_mfma_f32_16x16x32_bf16 v[112:115], v[100:103], v[84:87], v[112:115]\nds_read_b128 v[100:103], v8 offset:2080\n" \
    "v_mfma_f32_16x16x32_bf16 v[32:35], v[188:191], v[48:51], v[32:35]\nv_mfma_f32_16x16x32_bf16 v[112:115], v[104:107], v[88:91], v[112:115]\n" \
    "ds_read_b128 v[104:107], v8 offset:4128\nv_mfma_f32_16x16x32_bf16 v[44:47], v[188:191], v[52:55], v[44:47]\n" \
    "v_mfma_f32_16x16x32_bf16 v[112:115], v[108:111], v[92:95], v[112:115]\nds_read_b128 v[108:111], v8 offset:6176\n" \
    "v_mfma_f32_16x16x32_bf16 v[24:27], a[136:139], v[56:59], v[24:27]\nds_read_b128 v[72:75], v3 offset:33792\n" \
    "v_mfma_f32_16x16x32_bf16 v[36:39], a[136:139], v[60:63], v[36:39]\nds_read_b128 v[76:79], v3 offset:41984\n" \
    "v_mfma_f32_16x16x32_bf16 v[28:31], a[140:143], v[56:59], v[28:31]\ns_add_u32 s69, s70, -384\nbuffer_load_dwordx4 v1, s[64:67], s69 offen offset:1024 sc1 lds\n" \
    "v_mfma_f32_16x16x32_bf16 v[40:43], a[140:143], v[60:63], v[40:43]\nv_cndmask_b32_e64 v112, v22, v112, s[80:81]\nv_cndmask_b32_e64 v113, v22, v113, s[82:83]\n" \
    "v_mfma_f32_16x16x32_bf16 v[32:35], v[192:195], v[56:59], v[32:35]\nv_cndmask_b32_e64 v114, v22, v114, s[84:85]\nv_cndmask_b32_e64 v115, v22, v115, s[86:87]\n" \
    "v_mfma_f32_16x16x32_bf16 v[44:47], v[192:195], v[60:63], v[44:47]\ns_waitcnt lgkmcnt(6)\nv_max3_f32 v120, v112, v113, v114\nv_max3_f32 v120, v120, v115, s33\n" \
    "v_mfma_f32_16x16x32_bf16 v[24:27], a[144:147], v[64:67], v[24:27]\nds_read_b128 v[48:51], v2 offset:34816\n" \
    "v_mfma_f32_16x16x32_bf16 v[36:39], a[144:147], v[68:71], v[36:39]\nds_read_b128 v[52:55], v2 offset:43008\n" \
    "v_mfma_f32_16x16x32_bf16 v[28:31], a[148:151], v[64:67], v[28:31]\ns_waitcnt lgkmcnt(4)\ns_add_u32 s69, s70, -1280\n" \
    "v_mfma_f32_16x16x32_bf16 v[116:119], v[96:99], v[80:83], 0\nbuffer_load_dwordx4 v1, s[64:67], s69 offen offset:2048 sc1 lds\n" \
    "v_mfma_f32_16x16x32_bf16 v[40:43], a[148:151], v[68:71], v[40:43]\nv_mfma_f32_16x16x32_bf16 v[116:119], v[100:103], v[84:87], v[116:119]\n" \
    "v_mfma_f32_16x16x32_bf16 v[32:35], v[196:199], v[64:67], v[32:35]\nv_mfma_f32_16x16x32_bf16 v[116:119], v[104:107], v[88:91], v[116:119]\n" \
    "v_mfma_f32_16x16x32_bf16 v[44:47], v[196:199], v[68:71], v[44:47]\nv_mfma_f32_16x16x32_bf16 v[116:119], v[108:111], v[92:95], v[116:119]\ns_waitcnt lgkmcnt(2)\n" \
    "v_mfma_f32_16x16x32_bf16 v[24:27], a[152:155], v[72:75], v[24:27]\nds_read_b128 v[56:59], v3 offset:34816\n" \
    "v_mfma_f32_16x16x32_bf16 v[36:39], a[152:155], v[76:79], v[36:39]\nds_read_b128 v[60:63], v3 offset:43008\n" \
    "v_mfma_f32_16x16x32_bf16 v[28:31], a[156:159], v[72:75], v[28:31]\ns_add_u32 s69, s70, -2176\nbuffer_load_dwordx4 v1, s[64:67], s69 offen offset:3072 sc1 lds\n" \
    "v_mfma_f32_16x16x32_bf16 v[40:43], a[156:159], v[76:79], v[40:43]\nv_cndmask_b32_e64 v116, v22, v116, s[88:89]\nv_cndmask_b32_e64 v117, v22, v117, s[90:91]\n" \
    "v_mfma_f32_16x16x32_bf16 v[32:35], v[200:203], v[72:75], v[32:35]\nv_cndmask_b32_e64 v118, v22, v118, s[92:93]\nv_cndmask_b32_e64 v119, v22, v119, s[94:95]\n" \
    "v_mfma_f32_16x16x32_bf16 v[44:47], v[200:203], v[76:79], v[44:47]\ns_waitcnt lgkmcnt(2)\nv_max3_f32 v120, v120, v116, v117\nv_max3_f32 v120, v120, v118, v119\n" \
    "v_mfma_f32_16x16x32_bf16 v[24:27], a[160:163], v[48:51], v[24:27]\nds_read_b128 v[64:67], v2 offset:35840\nv_mul_f32_e64 v122, -s32, v120\n" \
    "v_mfma_f32_16x16x32_bf16 v[36:39], a[160:163], v[52:55], v[36:39]\nds_read_b128 v[68:71], v2 offset:44032\nv_fma_f32 v112, v112, s32, v122\n" \
    "v_mfma_f32_16x16x32_bf16 v[28:31], a[164:167], v[48:51], v[28:31]\ns_waitcnt vmcnt(5)\nv_writelane_b32 v20, s72, 2\nv_fma_f32 v113, v113, s32, v122\n" \
    "v_mfma_f32_16x16x32_bf16 v[40:43], a[164:167], v[52:55], v[40:43]\nds_write_b32 v7, v20\nv_mfma_f32_16x16x32_bf16 v[32:35], v[204:207], v[48:51], v[32:35]\n" \
    "v_fma_f32 v114, v114, s32, v122\nv_fma_f32 v115, v115, s32, v122\nv_mfma_f32_16x16x32_bf16 v[44:47], v[204:207], v[52:55], v[44:47]\ns_waitcnt lgkmcnt(3)\n" \
    "v_fma_f32 v116, v116, s32, v122\nv_fma_f32 v117, v117, s32, v122\nv_mfma_f32_16x16x32_bf16 v[24:27], a[168:171], v[56:59], v[24:27]\n" \
    "ds_read_b128 v[72:75], v3 offset:35840\nv_fma_f32 v118, v118, s32, v122\nv_mfma_f32_16x16x32_bf16 v[36:39], a[168:171], v[60:63], v[36:39]\n" \
    "ds_read_b128 v[76:79], v3 offset:44032\nv_fma_f32 v119, v119, s32, v122\nv_mfma_f32_16x16x32_bf16 v[28:31], a[172:175], v[56:59], v[28:31]\nv_exp_f32 v112, v112\n" \
    "v_mfma_f32_16x16x32_bf16 v[40:43], a[172:175], v[60:63], v[40:43]\nv_exp_f32 v113, v113\nv_mfma_f32_16x16x32_bf16 v[32:35], v[208:211], v[56:59], v[32:35]\n" \
    "v_exp_f32 v114, v114\nv_mfma_f32_16x16x32_bf16 v[44:47], v[208:211], v[60:63], v[44:47]\ns_waitcnt lgkmcnt(0)\ns_barrier\n" \
    "v_mfma_f32_16x16x32_bf16 v[24:27], a[176:179], v[64:67], v[24:27]\nds_read_b128 v[48:51], v2 offset:49152\n" \
    "v_mfma_f32_16x16x32_bf16 v[36:39], a[176:179], v[68:71], v[36:39]\nds_read_b128 v[52:55], v2 offset:57344\n" \
    "v_mfma_f32_16x16x32_bf16 v[28:31], a[180:183], v[64:67], v[28:31]\nv_exp_f32 v115, v115\nv_mfma_f32_16x16x32_bf16 v[40:43], a[180:183], v[68:71], v[40:43]\n" \
    "v_exp_f32 v116, v116\nv_mfma_f32_16x16x32_bf16 v[32:35], v[212:215], v[64:67], v[32:35]\nv_exp_f32 v117, v117\n" \
    "v_mfma_f32_16x16x32_bf16 v[44:47], v[212:215], v[68:71], v[44:47]\nv_exp_f32 v118, v118\nv_mfma_f32_16x16x32_bf16 v[24:27], a[184:187], v[72:75], v[24:27]\n" \
    "ds_read_b128 v[56:59], v3 offset:49152\nv_mfma_f32_16x16x32_bf16 v[36:39], a[184:187], v[76:79], v[36:39]\nds_read_b128 v[60:63], v3 offset:57344\n" \
    "v_mfma_f32_16x16x32_bf16 v[28:31], a[188:191], v[72:75], v[28:31]\nv_exp_f32 v119, v119\nv_mfma_f32_16x16x32_bf16 v[40:43], a[188:191], v[76:79], v[40:43]\n" \
    "v_add_f32 v121, v112, v113\nv_add_f32 v121, v121, v114\nv_mfma_f32_16x16x32_bf16 v[32:35], v[216:219], v[72:75], v[32:35]\nv_add_f32 v121, v121, v115\n" \
    "v_add_f32 v121, v121, v116\nv_mfma_f32_16x16x32_bf16 v[44:47], v[216:219], v[76:79], v[44:47]\ns_waitcnt lgkmcnt(2)\nv_add_f32 v121, v121, v117\n" \
    "v_add_f32 v121, v121, v118\nv_mfma_f32_16x16x32_bf16 v[24:27], a[192:195], v[48:51], v[24:27]\nds_read_b128 v[64:67], v2 offset:50176\nv_add_f32 v121, v121, v119\n" \
    "v_mfma_f32_16x16x32_bf16 v[36:39], a[192:195], v[52:55], v[36:39]\nds_read_b128 v[68:71], v2 offset:58368\n" \
    "v_mfma_f32_16x16x32_bf16 v[28:31], a[196:199], v[48:51], v[28:31]\ns_add_u32 m0, s52, 32768\ns_add_u32 s69, s70, 1024\n" \
    "v_mfma_f32_16x16x32_bf16 v[40:43], a[196:199], v[52:55], v[40:43]\nbuffer_load_dwordx4 v1, s[64:67], s69 offen offset:0 sc1 lds\n" \
    "v_mfma_f32_16x16x32_bf16 v[32:35], v[220:223], v[48:51], v[32:35]\nds_read_b128 v[108:111], v15\nv_mfma_f32_16x16x32_bf16 v[44:47], v[220:223], v[52:55], v[44:47]\n" \
    "s_waitcnt lgkmcnt(3)\nv_mfma_f32_16x16x32_bf16 v[24:27], a[200:203], v[56:59], v[24:27]\nds_read_b128 v[72:75], v3 offset:50176\n" \
    "v_mfma_f32_16x16x32_bf16 v[36:39], a[200:203], v[60:63], v[36:39]\nds_read_b128 v[76:79], v3 offset:58368\n" \
    "v_mfma_f32_16x16x32_bf16 v[28:31], a[204:207], v[56:59], v[28:31]\ns_add_u32 s69, s70, 128\nbuffer_load_dwordx4 v1, s[64:67], s69 offen offset:1024 sc1 lds\n" \
    "v_mfma_f32_16x16x32_bf16 v[40:43], a[204:207], v[60:63], v[40:43]\nv_mfma_f32_16x16x32_bf16 v[32:35], v[224:227], v[56:59], v[32:35]\ns_waitcnt lgkmcnt(2)\n" \
    "v_readfirstlane_b32 s72, v108\nv_readfirstlane_b32 s73, v109\nv_mfma_f32_16x16x32_bf16 v[44:47], v[224:227], v[60:63], v[44:47]\nv_readfirstlane_b32 s37, v111\n" \
    "v_readfirstlane_b32 s40, v110\nv_mfma_f32_16x16x32_bf16 v[24:27], a[208:211], v[64:67], v[24:27]\nds_read_b128 v[48:51], v2 offset:51200\n" \
    "v_mfma_f32_16x16x32_bf16 v[36:39], a[208:211], v[68:71], v[36:39]\nds_read_b128 v[52:55], v2 offset:59392\ns_lshr_b32 s41, s72, 8\n" \
    "v_mfma_f32_16x16x32_bf16 v[28:31], a[212:215], v[64:67], v[28:31]\ns_add_u32 s69, s70, -768\nbuffer_load_dwordx4 v1, s[64:67], s69 offen offset:2048 sc1 lds\n" \
    "v_mfma_f32_16x16x32_bf16 v[40:43], a[212:215], v[68:71], v[40:43]\ns_sub_u32 s41, s40, s41\ns_lshl_b32 s41, s41, 8\ns_and_b32 s72, s72, 127\n" \
    "v_mfma_f32_16x16x32_bf16 v[32:35], v[228:231], v[64:67], v[32:35]\ns_cmp_lt_i32 s41, s30\ns_subb_u32 s72, s72, 0\ns_max_i32 s72, s72, 0\n" \
    "v_mfma_f32_16x16x32_bf16 v[44:47], v[228:231], v[68:71], v[44:47]\ns_waitcnt lgkmcnt(2)\ns_cmp_gt_i32 s72, s96\ns_cselect_b32 s41, s73, 0x80000000\n" \
    "v_mfma_f32_16x16x32_bf16 v[24:27], a[216:219], v[72:75], v[24:27]\nds_read_b128 v[56:59], v3 offset:51200\n" \
    "v_mfma_f32_16x16x32_bf16 v[36:39], a[216:219], v[76:79], v[36:39]\nds_read_b128 v[60:63], v3 offset:59392\n" \
    "v_mfma_f32_16x16x32_bf16 v[28:31], a[220:223], v[72:75], v[28:31]\ns_add_u32 s69, s70, -1664\nbuffer_load_dwordx4 v1, s[64:67], s69 offen offset:3072 sc1 lds\n" \
    "v_mfma_f32_16x16x32_bf16 v[40:43], a[220:223], v[76:79], v[40:43]\nbuffer_load_dword v20, v6, s[64:67], 0 offen sc1\n" \
    "v_mfma_f32_16x16x32_bf16 v[32:35], v[232:235], v[72:75], v[32:35]\ns_cmp_eq_u32 s37, 384\ns_cselect_b32 s51, 64, s51\n" \
    "v_mfma_f32_16x16x32_bf16 v[44:47], v[232:235], v[76:79], v[44:47]\ns_waitcnt lgkmcnt(2)\nv_mfma_f32_16x16x32_bf16 v[24:27], a[224:227], v[48:51], v[24:27]\n" \
    "ds_read_b128 v[64:67], v2 offset:52224\nv_mfma_f32_16x16x32_bf16 v[36:39], a[224:227], v[52:55], v[36:39]\nds_read_b128 v[68:71], v2 offset:60416\n" \
    "v_mfma_f32_16x16x32_bf16 v[28:31], a[228:231], v[48:51], v[28:31]\nds_read_b128 v[72:75], v3 offset:52224\n" \
    "v_mfma_f32_16x16x32_bf16 v[40:43], a[228:231], v[52:55], v[40:43]\nds_read_b128 v[76:79], v3 offset:60416\n" \
    "v_mfma_f32_16x16x32_bf16 v[32:35], v[236:239], v[48:51], v[32:35]\nv_mfma_f32_16x16x32_bf16 v[44:47], v[236:239], v[52:55], v[44:47]\ns_waitcnt lgkmcnt(4)\n" \
    "v_mfma_f32_16x16x32_bf16 v[32:35], v[240:243], v[56:59], v[32:35]\nv_add_u32 v122, s59, v11\nv_add_u32 v123, s59, v12\n" \
    "v_mfma_f32_16x16x32_bf16 v[44:47], v[240:243], v[60:63], v[44:47]\ns_waitcnt lgkmcnt(2)\ns_add_u32 s59, s59, 0x2000\ns_and_b32 s59, s59, 0xffff\n" \
    "v_mfma_f32_16x16x32_bf16 v[32:35], v[244:247], v[64:67], v[32:35]\ns_waitcnt lgkmcnt(0)\ns_barrier\nv_mfma_f32_16x16x32_bf16 v[44:47], v[244:247], v[68:71], v[44:47]\n" \
    "ds_read_b128 v[48:51], v2 offset:0\nv_mfma_f32_16x16x32_bf16 v[32:35], v[248:251], v[72:75], v[32:35]\nds_read_b128 v[52:55], v2 offset:8192\n" \
    "v_mfma_f32_16x16x32_bf16 v[44:47], v[248:251], v[76:79], v[44:47]\ns_mov_b32 s54, s70\ns_add_u32 s50, s50, 1\n" \
    "v_mfma_f32_16x16x32_bf16 v[24:27], a[232:235], v[56:59], v[24:27]\ns_nop 1\nv_mfma_f32_16x16x32_bf16 v[24:27], a[240:243], v[64:67], v[24:27]\ns_nop 0\n" \
    "v_cvt_pk_bf16_f32 v80, v32, v33\nv_cvt_pk_bf16_f32 v81, v34, v35\nv_mfma_f32_16x16x32_bf16 v[24:27], a[248:251], v[72:75], v[24:27]\nv_cvt_pk_bf16_f32 v82, v44, v45\n" \
    "v_cvt_pk_bf16_f32 v83, v46, v47\nv_mfma_f32_16x16x32_bf16 v[28:31], a[236:239], v[56:59], v[28:31]\nv_permlane16_swap_b32 v80, v82\n" \
    "v_mfma_f32_16x16x32_bf16 v[28:31], a[244:247], v[64:67], v[28:31]\nds_read_b128 v[56:59], v3 offset:0\nv_mfma_f32_16x16x32_bf16 v[28:31], a[252:255], v[72:75], v[28:31]\n" \
    "v_permlane16_swap_b32 v81, v83\nv_mfma_f32_16x16x32_bf16 v[36:39], a[232:235], v[60:63], v[36:39]\nbuffer_store_dwordx4 v[80:83], v4, s[64:67], s71 offen sc1\n" \
    "v_mfma_f32_16x16x32_bf16 v[36:39], a[240:243], v[68:71], v[36:39]\nv_cvt_pk_bf16_f32 v84, v24, v25\nv_cvt_pk_bf16_f32 v85, v26, v27\n" \
    "v_mfma_f32_16x16x32_bf16 v[36:39], a[248:251], v[76:79], v[36:39]\ns_nop 0\nv_cvt_pk_bf16_f32 v86, v28, v29\nv_cvt_pk_bf16_f32 v87, v30, v31\n" \
    "v_mfma_f32_16x16x32_bf16 v[40:43], a[236:239], v[60:63], v[40:43]\nv_permlane16_swap_b32 v84, v86\nv_mfma_f32_16x16x32_bf16 v[40:43], a[244:247], v[68:71], v[40:43]\n" \
    "ds_read_b128 v[60:63], v3 offset:8192\nv_mfma_f32_16x16x32_bf16 v[40:43], a[252:255], v[76:79], v[40:43]\nv_permlane16_swap_b32 v85, v87\nds_write_b128 v122, v[84:87]\n" \
    "s_cmp_gt_i32 s41, s99\nv_cvt_pk_bf16_f32 v88, v36, v37\nv_cvt_pk_bf16_f32 v89, v38, v39\ns_nop 2\nv_cvt_pk_bf16_f32 v90, v40, v41\nv_cvt_pk_bf16_f32 v91, v42, v43\n" \
    "s_nop 0\nv_permlane16_swap_b32 v88, v90\nv_permlane16_swap_b32 v89, v91\nds_write_b128 v123, v[88:91]\ns_cbranch_scc0 .Lprqmid_%=\n.Lprqb2_%=:\ns_waitcnt lgkmcnt(4)\n" \
    "v_mfma_f32_16x16x32_bf16 v[24:27], a[0:3], v[48:51], 0\nds_read_b128 v[64:67], v2 offset:1024\ns_sub_u32 s38, s50, s24\n" \
    "v_mfma_f32_16x16x32_bf16 v[36:39], a[0:3], v[52:55], 0\nds_read_b128 v[68:71], v2 offset:9216\ns_max_i32 s38, s38, 0\n" \
    "v_mfma_f32_16x16x32_bf16 v[28:31], a[4:7], v[48:51], 0\ns_add_u32 m0, s52, 49152\ns_add_u32 s69, s54, 1536\ns_and_b32 s38, s38, s16\n" \
    "v_mfma_f32_16x16x32_bf16 v[40:43], a[4:7], v[52:55], 0\nbuffer_load_dwordx4 v1, s[64:67], s69 offen offset:0 sc1 lds\ns_lshl_b32 s38, s38, 19\n" \
    "v_mfma_f32_16x16x32_bf16 v[32:35], v[124:127], v[48:51], 0\ns_add_u32 s38, s38, s53\ns_add_u32 s74, s48, s38\ns_lshr_b32 s74, s74, 8\n" \
    "v_mfma_f32_16x16x32_bf16 v[44:47], v[124:127], v[52:55], 0\ns_waitcnt lgkmcnt(4)\ns_xnor_b32 s74, s74, s49\ns_and_b32 s74, s74, 0x2000\n" \
    "v_mfma_f32_16x16x32_bf16 v[24:27], a[8:11], v[56:59], v[24:27]\nds_read_b128 v[72:75], v3 offset:1024\ns_add_u32 s74, s74, s38\n" \
    "v_mfma_f32_16x16x32_bf16 v[36:39], a[8:11], v[60:63], v[36:39]\nds_read_b128 v[76:79], v3 offset:9216\ns_add_u32 s74, s74, s60\n" \
    "v_mfma_f32_16x16x32_bf16 v[28:31], a[12:15], v[56:59], v[28:31]\ns_add_u32 s68, s50, 1\ns_min_u32 s68, s68, 63\ns_lshl_b32 s68, s68, 20\n" \
    "v_mfma_f32_16x16x32_bf16 v[40:43], a[12:15], v[60:63], v[40:43]\ns_add_u32 s68, s68, s18\ns_add_u32 s70, s48, s68\ns_lshr_b32 s70, s70, 8\n" \
    "v_mfma_f32_16x16x32_bf16 v[32:35], v[128:131], v[56:59], v[32:35]\ns_xnor_b32 s70, s70, s49\ns_and_b32 s70, s70, 0x2000\ns_add_u32 s70, s70, s68\n" \
    "v_mfma_f32_16x16x32_bf16 v[44:47], v[128:131], v[60:63], v[44:47]\ns_waitcnt lgkmcnt(2)\ns_add_u32 s69, s54, 640\n" \
    "v_mfma_f32_16x16x32_bf16 v[24:27], a[16:19], v[64:67], v[24:27]\nds_read_b128 v[48:51], v2 offset:2048\nv_mfma_f32_16x16x32_bf16 v[36:39], a[16:19], v[68:71], v[36:39]\n" \
    "ds_read_b128 v[52:55], v2 offset:10240\nv_mfma_f32_16x16x32_bf16 v[28:31], a[20:23], v[64:67], v[28:31]\nbuffer_load_dwordx4 v1, s[64:67], s69 offen offset:1024 sc1 lds\n" \
    "s_add_u32 s69, s54, -256\nv_mfma_f32_16x16x32_bf16 v[40:43], a[20:23], v[68:71], v[40:43]\nbuffer_load_dwordx4 v1, s[64:67], s69 offen offset:2048 sc1 lds\n" \
    "s_sub_u32 s77, s50, s23\nv_mfma_f32_16x16x32_bf16 v[32:35], v[132:135], v[64:67], v[32:35]\ns_max_i32 s76, s77, 0\ns_and_b32 s76, s76, s16\ns_lshl_b32 s76, s76, 19\n" \
    "v_mfma_f32_16x16x32_bf16 v[44:47], v[132:135], v[68:71], v[44:47]\ns_waitcnt lgkmcnt(2)\ns_add_u32 s76, s76, s20\ns_add_u32 s75, s48, s76\n" \
    "v_mfma_f32_16x16x32_bf16 v[24:27], a[24:27], v[72:75], v[24:27]\nds_read_b128 v[56:59], v3 offset:2048\ns_lshr_b32 s75, s75, 8\n" \
    "v_mfma_f32_16x16x32_bf16 v[36:39], a[24:27], v[76:79], v[36:39]\nds_read_b128 v[60:63], v3 offset:10240\ns_xnor_b32 s75, s75, s49\n" \
    "v_mfma_f32_16x16x32_bf16 v[28:31], a[28:31], v[72:75], v[28:31]\ns_add_u32 s69, s54, -1152\nbuffer_load_dwordx4 v1, s[64:67], s69 offen offset:3072 sc1 lds\n" \
    "v_mfma_f32_16x16x32_bf16 v[40:43], a[28:31], v[76:79], v[40:43]\ns_and_b32 s75, s75, 0x2000\ns_add_u32 s75, s75, s76\ns_add_u32 s75, s75, s57\n" \
    "v_mfma_f32_16x16x32_bf16 v[32:35], v[136:139], v[72:75], v[32:35]\nv_mfma_f32_16x16x32_bf16 v[44:47], v[136:139], v[76:79], v[44:47]\ns_waitcnt lgkmcnt(2)\n" \
    "v_mfma_f32_16x16x32_bf16 v[24:27], a[32:35], v[48:51], v[24:27]\nds_read_b128 v[64:67], v2 offset:3072\nv_mfma_f32_16x16x32_bf16 v[36:39], a[32:35], v[52:55], v[36:39]\n" \
    "ds_read_b128 v[68:71], v2 offset:11264\nv_mfma_f32_16x16x32_bf16 v[28:31], a[36:39], v[48:51], v[28:31]\ns_and_b32 s37, s50, s16\ns_lshl_b32 s37, s37, 19\n" \
    "s_add_u32 s37, s37, s53\nv_mfma_f32_16x16x32_bf16 v[40:43], a[36:39], v[52:55], v[40:43]\ns_add_u32 s71, s48, s37\ns_lshr_b32 s71, s71, 8\ns_xnor_b32 s71, s71, s49\n" \
    "v_mfma_f32_16x16x32_bf16 v[32:35], v[140:143], v[48:51], v[32:35]\ns_and_b32 s71, s71, 0x2000\ns_add_u32 s71, s71, s37\ns_mov_b32 m0, s56\n" \
    "v_mfma_f32_16x16x32_bf16 v[44:47], v[140:143], v[52:55], v[44:47]\ns_waitcnt lgkmcnt(2)\nbuffer_load_dwordx4 v0, s[64:67], s74 offen offset:0 sc1 lds\n" \
    "v_mfma_f32_16x16x32_bf16 v[24:27], a[40:43], v[56:59], v[24:27]\nds_read_b128 v[72:75], v3 offset:3072\nv_mfma_f32_16x16x32_bf16 v[36:39], a[40:43], v[60:63], v[36:39]\n" \
    "ds_read_b128 v[76:79], v3 offset:11264\nv_mfma_f32_16x16x32_bf16 v[28:31], a[44:47], v[56:59], v[28:31]\nbuffer_load_dwordx4 v0, s[64:67], s74 offen offset:1024 sc1 lds\n" \
    "v_mfma_f32_16x16x32_bf16 v[40:43], a[44:47], v[60:63], v[40:43]\nbuffer_load_dwordx4 v[96:99], v9, s[64:67], s75 offen offset:0 sc1\n" \
    "v_mfma_f32_16x16x32_bf16 v[32:35], v[144:147], v[56:59], v[32:35]\nbuffer_load_dwordx4 v[100:103], v9, s[64:67], s75 offen offset:512 sc1\n" \
    "v_mfma_f32_16x16x32_bf16 v[44:47], v[144:147], v[60:63], v[44:47]\ns_waitcnt vmcnt(14) lgkmcnt(0)\ns_barrier\n" \
    "v_mfma_f32_16x16x32_bf16 v[24:27], a[48:51], v[64:67], v[24:27]\nds_read_b128 v[48:51], v2 offset:16384\nv_mfma_f32_16x16x32_bf16 v[36:39], a[48:51], v[68:71], v[36:39]\n" \
    "ds_read_b128 v[52:55], v2 offset:24576\nv_mfma_f32_16x16x32_bf16 v[28:31], a[52:55], v[64:67], v[28:31]\n" \
    "buffer_load_dwordx4 v[104:107], v9, s[64:67], s75 offen offset:1024 sc1\ns_add_u32 s39, s50, 1\nv_mfma_f32_16x16x32_bf16 v[40:43], a[52:55], v[68:71], v[40:43]\n" \
    "buffer_load_dwordx4 v[108:111], v9, s[64:67], s75 offen offset:1536 sc1\ns_add_u32 s97, s39, 1\nv_mfma_f32_16x16x32_bf16 v[32:35], v[148:151], v[64:67], v[32:35]\n" \
    "s_lshl_b32 s78, s77, 20\ns_add_u32 s78, s78, s21\ns_add_u32 s76, s48, s78\nv_mfma_f32_16x16x32_bf16 v[44:47], v[148:151], v[68:71], v[44:47]\ns_lshr_b32 s76, s76, 8\n" \
    "s_xor_b32 s76, s76, s49\ns_and_b32 s76, s76, 0x2000\nv_mfma_f32_16x16x32_bf16 v[24:27], a[56:59], v[72:75], v[24:27]\nds_read_b128 v[56:59], v3 offset:16384\n" \
    "s_add_u32 s76, s76, s78\nv_mfma_f32_16x16x32_bf16 v[36:39], a[56:59], v[76:79], v[36:39]\nds_read_b128 v[60:63], v3 offset:24576\ns_min_u32 s97, s97, 63\n" \
    "v_mfma_f32_16x16x32_bf16 v[28:31], a[60:63], v[72:75], v[28:31]\ns_cmp_lt_i32 s77, 0\ns_cselect_b32 s76, 0x100000, s76\ns_add_u32 s78, s76, s58\n" \
    "v_mfma_f32_16x16x32_bf16 v[40:43], a[60:63], v[76:79], v[40:43]\ns_lshl_b32 s79, s77, 2\ns_add_u32 s79, s79, s22\ns_sub_u32 s98, s39, s23\n" \
    "v_mfma_f32_16x16x32_bf16 v[32:35], v[152:155], v[72:75], v[32:35]\ns_add_u32 s99, s98, 1\ns_cmp_gt_i32 s51, s97\ns_cselect_b32 s96, s98, 0x7fffffff\n" \
    "v_mfma_f32_16x16x32_bf16 v[44:47], v[152:155], v[76:79], v[44:47]\ns_waitcnt lgkmcnt(2)\ns_sub_u32 s39, s39, 2\n" \
    "v_mfma_f32_16x16x32_bf16 v[24:27], a[64:67], v[48:51], v[24:27]\nds_read_b128 v[64:67], v2 offset:17408\nv_mfma_f32_16x16x32_bf16 v[36:39], a[64:67], v[52:55], v[36:39]\n" \
    "ds_read_b128 v[68:71], v2 offset:25600\nv_mfma_f32_16x16x32_bf16 v[28:31], a[68:71], v[48:51], v[28:31]\ns_add_u32 m0, s52, 0\ns_add_u32 s69, s70, 0\n" \
    "v_mfma_f32_16x16x32_bf16 v[40:43], a[68:71], v[52:55], v[40:43]\nbuffer_load_dwordx4 v1, s[64:67], s69 offen offset:0 sc1 lds\n" \
    "v_mfma_f32_16x16x32_bf16 v[32:35], v[156:159], v[48:51], v[32:35]\ns_cmp_lt_u32 s39, 62\ns_cselect_b32 s96, s96, 0x7fffffff\n" \
    "v_mfma_f32_16x16x32_bf16 v[44:47], v[156:159], v[52:55], v[44:47]\ns_waitcnt lgkmcnt(2)\nv_mfma_f32_16x16x32_bf16 v[24:27], a[72:75], v[56:59], v[24:27]\n" \
    "ds_read_b128 v[72:75], v3 offset:17408\nv_mfma_f32_16x16x32_bf16 v[36:39], a[72:75], v[60:63], v[36:39]\nds_read_b128 v[76:79], v3 offset:25600\n" \
    "v_mfma_f32_16x16x32_bf16 v[28:31], a[76:79], v[56:59], v[28:31]\ns_add_u32 s69, s70, -896\nbuffer_load_dwordx4 v1, s[64:67], s69 offen offset:1024 sc1 lds\n" \
    "v_mfma_f32_16x16x32_bf16 v[40:43], a[76:79], v[60:63], v[40:43]\nv_mfma_f32_16x16x32_bf16 v[32:35], v[160:163], v[56:59], v[32:35]\n" \
    "v_mfma_f32_16x16x32_bf16 v[44:47], v[160:163], v[60:63], v[44:47]\ns_waitcnt lgkmcnt(2)\nv_mfma_f32_16x16x32_bf16 v[24:27], a[80:83], v[64:67], v[24:27]\n" \
    "ds_read_b128 v[48:51], v2 offset:18432\nv_mfma_f32_16x16x32_bf16 v[36:39], a[80:83], v[68:71], v[36:39]\nds_read_b128 v[52:55], v2 offset:26624\n" \
    "v_mfma_f32_16x16x32_bf16 v[28:31], a[84:87], v[64:67], v[28:31]\ns_add_u32 s69, s70, -1792\nbuffer_load_dwordx4 v1, s[64:67], s69 offen offset:2048 sc1 lds\n" \
    "v_mfma_f32_16x16x32_bf16 v[40:43], a[84:87], v[68:71], v[40:43]\ns_add_u32 s68, s59, s26\ns_and_b32 s68, s68, 0xffff\nv_add_u32 v122, s68, v13\n" \
    "v_mfma_f32_16x16x32_bf16 v[32:35], v[164:167], v[64:67], v[32:35]\nds_read_b128 v[80:83], v122 offset:0\nv_mfma_f32_16x16x32_bf16 v[44:47], v[164:167], v[68:71], v[44:47]\n" \
    "s_waitcnt lgkmcnt(3)\nds_read_b128 v[84:87], v122 offset:1024\nv_mfma_f32_16x16x32_bf16 v[24:27], a[88:91], v[72:75], v[24:27]\nds_read_b128 v[56:59], v3 offset:18432\n" \
    "v_mfma_f32_16x16x32_bf16 v[36:39], a[88:91], v[76:79], v[36:39]\nds_read_b128 v[60:63], v3 offset:26624\nv_mfma_f32_16x16x32_bf16 v[28:31], a[92:95], v[72:75], v[28:31]\n" \
    "s_add_u32 s69, s70, -2688\nbuffer_load_dwordx4 v1, s[64:67], s69 offen offset:3072 sc1 lds\nv_mfma_f32_16x16x32_bf16 v[40:43], a[92:95], v[76:79], v[40:43]\n" \
    "ds_read_b128 v[88:91], v122 offset:2048\nv_mfma_f32_16x16x32_bf16 v[32:35], v[168:171], v[72:75], v[32:35]\nds_read_b128 v[92:95], v122 offset:3072\n" \
    "v_mfma_f32_16x16x32_bf16 v[44:47], v[168:171], v[76:79], v[44:47]\ns_waitcnt lgkmcnt(6)\nv_mfma_f32_16x16x32_bf16 v[24:27], a[96:99], v[48:51], v[24:27]\n" \
    "ds_read_b128 v[64:67], v2 offset:19456\nv_mfma_f32_16x16x32_bf16 v[36:39], a[96:99], v[52:55], v[36:39]\nds_read_b128 v[68:71], v2 offset:27648\n" \
    "v_mfma_f32_16x16x32_bf16 v[28:31], a[100:103], v[48:51], v[28:31]\nbuffer_load_dword v20, v6, s[64:67], 0 offen sc1\n" \
    "v_mfma_f32_16x16x32_bf16 v[40:43], a[100:103], v[52:55], v[40:43]\nv_mfma_f32_16x16x32_bf16 v[32:35], v[172:175], v[48:51], v[32:35]\ns_waitcnt vmcnt(5)\n" \
    "v_max3_u32 v123, v96, v97, v98\nv_max3_u32 v123, v123, v99, v100\nv_mfma_f32_16x16x32_bf16 v[44:47], v[172:175], v[52:55], v[44:47]\ns_waitcnt lgkmcnt(4)\n" \
    "v_max3_u32 v123, v123, v101, v102\nv_max3_u32 v123, v123, v103, v104\nv_mfma_f32_16x16x32_bf16 v[24:27], a[104:107], v[56:59], v[24:27]\n" \
    "ds_read_b128 v[72:75], v3 offset:19456\nv_max3_u32 v123, v123, v105, v106\nv_mfma_f32_16x16x32_bf16 v[36:39], a[104:107], v[60:63], v[36:39]\n" \
    "ds_read_b128 v[76:79], v3 offset:27648\nv_max3_u32 v123, v123, v107, v108\nv_mfma_f32_16x16x32_bf16 v[28:31], a[108:111], v[56:59], v[28:31]\ns_memrealtime s[72:73]\n" \
    "v_max3_u32 v123, v123, v109, v110\nv_max_u32 v123, v123, v111\nv_mfma_f32_16x16x32_bf16 v[40:43], a[108:111], v[60:63], v[40:43]\nv_cmp_eq_u32 vcc, -1, v123\n" \
    "v_mfma_f32_16x16x32_bf16 v[32:35], v[176:179], v[56:59], v[32:35]\ns_cbranch_vccnz .Lprqkm_%=\n.Lprqkmb_%=:\n" \
    "v_mfma_f32_16x16x32_bf16 v[44:47], v[176:179], v[60:63], v[44:47]\ns_waitcnt lgkmcnt(0)\ns_barrier\nv_mfma_f32_16x16x32_bf16 v[24:27], a[112:115], v[64:67], v[24:27]\n" \
    "ds_read_b128 v[48:51], v2 offset:32768\nv_mov_b32 v123, s50\nv_mfma_f32_16x16x32_bf16 v[36:39], a[112:115], v[68:71], v[36:39]\nds_read_b128 v[52:55], v2 offset:40960\n" \
    "v_mfma_f32_16x16x32_bf16 v[28:31], a[116:119], v[64:67], v[28:31]\nv_mfma_f32_16x16x32_bf16 v[16:19], v[96:99], v[80:83], 0\n" \
    "buffer_store_dword v123, v5, s[64:67], 0 offen sc1\nv_mfma_f32_16x16x32_bf16 v[40:43], a[116:119], v[68:71], v[40:43]\n" \
    "v_mfma_f32_16x16x32_bf16 v[16:19], v[100:103], v[84:87], v[16:19]\nv_mfma_f32_16x16x32_bf16 v[32:35], v[180:183], v[64:67], v[32:35]\n" \
    "v_mfma_f32_16x16x32_bf16 v[16:19], v[104:107], v[88:91], v[16:19]\nv_mfma_f32_16x16x32_bf16 v[44:47], v[180:183], v[68:71], v[44:47]\n" \
    "v_mfma_f32_16x16x32_bf16 v[16:19], v[108:111], v[92:95], v[16:19]\nv_mfma_f32_16x16x32_bf16 v[24:27], a[120:123], v[72:75], v[24:27]\n" \
    "ds_read_b128 v[56:59], v3 offset:32768\nv_mfma_f32_16x16x32_bf16 v[36:39], a[120:123], v[76:79], v[36:39]\nds_read_b128 v[60:63], v3 offset:40960\n" \
    "v_mfma_f32_16x16x32_bf16 v[28:31], a[124:127], v[72:75], v[28:31]\nv_mfma_f32_16x16x32_bf16 v[40:43], a[124:127], v[76:79], v[40:43]\n" \
    "v_mfma_f32_16x16x32_bf16 v[32:35], v[184:187], v[72:75], v[32:35]\ns_nop 0\nv_max3_f32 v122, v120, v16, v17\nv_max3_f32 v122, v122, v18, v19\n" \
    "v_mfma_f32_16x16x32_bf16 v[44:47], v[184:187], v[76:79], v[44:47]\ns_waitcnt lgkmcnt(2)\nv_mov_b32 v123, v122\n" \
    "v_mfma_f32_16x16x32_bf16 v[24:27], a[128:131], v[48:51], v[24:27]\nds_read_b128 v[64:67], v2 offset:33792\n" \
    "v_mfma_f32_16x16x32_bf16 v[36:39], a[128:131], v[52:55], v[36:39]\nds_read_b128 v[68:71], v2 offset:41984\n" \
    "v_mfma_f32_16x16x32_bf16 v[28:31], a[132:135], v[48:51], v[28:31]\ns_add_u32 m0, s52, 16384\ns_add_u32 s69, s70, 512\n" \
    "v_mfma_f32_16x16x32_bf16 v[40:43], a[132:135], v[52:55], v[40:43]\nbuffer_load_dwordx4 v1, s[64:67], s69 offen offset:0 sc1 lds\n" \
    "v_mfma_f32_16x16x32_bf16 v[32:35], v[188:191], v[48:51], v[32:35]\nv_permlane16_swap_b32 v122, v123\nv_mfma_f32_16x16x32_bf16 v[44:47], v[188:191], v[52:55], v[44:47]\n" \
    "s_waitcnt lgkmcnt(2)\nv_max_f32 v122, v122, v123\nv_mov_b32 v123, v122\nv_mfma_f32_16x16x32_bf16 v[24:27], a[136:139], v[56:59], v[24:27]\n" \
    "ds_read_b128 v[72:75], v3 offset:33792\nv_mfma_f32_16x16x32_bf16 v[36:39], a[136:139], v[60:63], v[36:39]\nds_read_b128 v[76:79], v3 offset:41984\n" \
    "v_mfma_f32_16x16x32_bf16 v[28:31], a[140:143], v[56:59], v[28:31]\ns_add_u32 s69, s70, -384\nbuffer_load_dwordx4 v1, s[64:67], s69 offen offset:1024 sc1 lds\n" \
    "v_mfma_f32_16x16x32_bf16 v[40:43], a[140:143], v[60:63], v[40:43]\nv_permlane32_swap_b32 v122, v123\nv_mfma_f32_16x16x32_bf16 v[32:35], v[192:195], v[56:59], v[32:35]\n" \
    "v_max_f32 v122, v122, v123\nv_mul_f32_e64 v122, -s32, v122\nv_mfma_f32_16x16x32_bf16 v[44:47], v[192:195], v[60:63], v[44:47]\ns_waitcnt lgkmcnt(2)\n" \
    "v_fma_f32 v120, v120, s32, v122\nv_fma_f32 v16, v16, s32, v122\nv_mfma_f32_16x16x32_bf16 v[24:27], a[144:147], v[64:67], v[24:27]\nds_read_b128 v[48:51], v2 offset:34816\n" \
    "v_fma_f32 v17, v17, s32, v122\nv_mfma_f32_16x16x32_bf16 v[36:39], a[144:147], v[68:71], v[36:39]\nds_read_b128 v[52:55], v2 offset:43008\nv_fma_f32 v18, v18, s32, v122\n" \
    "v_mfma_f32_16x16x32_bf16 v[28:31], a[148:151], v[64:67], v[28:31]\ns_add_u32 s69, s70, -1280\nbuffer_load_dwordx4 v1, s[64:67], s69 offen offset:2048 sc1 lds\n" \
    "v_mfma_f32_16x16x32_bf16 v[40:43], a[148:151], v[68:71], v[40:43]\nv_fma_f32 v19, v19, s32, v122\nv_mfma_f32_16x16x32_bf16 v[32:35], v[196:199], v[64:67], v[32:35]\n" \
    "v_exp_f32 v120, v120\nv_mfma_f32_16x16x32_bf16 v[44:47], v[196:199], v[68:71], v[44:47]\ns_waitcnt lgkmcnt(2)\nv_exp_f32 v16, v16\n" \
    "v_mfma_f32_16x16x32_bf16 v[24:27], a[152:155], v[72:75], v[24:27]\nds_read_b128 v[56:59], v3 offset:34816\n" \
    "v_mfma_f32_16x16x32_bf16 v[36:39], a[152:155], v[76:79], v[36:39]\nds_read_b128 v[60:63], v3 offset:43008\n" \
    "v_mfma_f32_16x16x32_bf16 v[28:31], a[156:159], v[72:75], v[28:31]\ns_add_u32 s69, s70, -2176\nbuffer_load_dwordx4 v1, s[64:67], s69 offen offset:3072 sc1 lds\n" \
    "v_mfma_f32_16x16x32_bf16 v[40:43], a[156:159], v[76:79], v[40:43]\nv_exp_f32 v17, v17\nv_mfma_f32_16x16x32_bf16 v[32:35], v[200:203], v[72:75], v[32:35]\n" \
    "v_exp_f32 v18, v18\nv_mfma_f32_16x16x32_bf16 v[44:47], v[200:203], v[76:79], v[44:47]\ns_waitcnt lgkmcnt(2)\nv_exp_f32 v19, v19\n" \
    "v_mfma_f32_16x16x32_bf16 v[24:27], a[160:163], v[48:51], v[24:27]\nds_read_b128 v[64:67], v2 offset:35840\nv_add_f32 v122, v16, v17\n" \
    "v_mfma_f32_16x16x32_bf16 v[36:39], a[160:163], v[52:55], v[36:39]\nds_read_b128 v[68:71], v2 offset:44032\nv_add_f32 v123, v18, v19\n" \
    "v_mfma_f32_16x16x32_bf16 v[28:31], a[164:167], v[48:51], v[28:31]\ns_waitcnt vmcnt(5)\nv_writelane_b32 v20, s72, 2\nv_mul_f32 v121, v121, v120\n" \
    "v_mfma_f32_16x16x32_bf16 v[40:43], a[164:167], v[52:55], v[40:43]\nds_write_b32 v7, v20\nv_mfma_f32_16x16x32_bf16 v[32:35], v[204:207], v[48:51], v[32:35]\n" \
    "v_add_f32 v122, v122, v123\nv_fma_f32 v121, 0.5, v122, v121\nv_mfma_f32_16x16x32_bf16 v[44:47], v[204:207], v[52:55], v[44:47]\ns_waitcnt lgkmcnt(3)\nv_mov_b32 v123, v121\n" \
    "v_mfma_f32_16x16x32_bf16 v[24:27], a[168:171], v[56:59], v[24:27]\nds_read_b128 v[72:75], v3 offset:35840\n" \
    "v_mfma_f32_16x16x32_bf16 v[36:39], a[168:171], v[60:63], v[36:39]\nds_read_b128 v[76:79], v3 offset:44032\n" \
    "v_mfma_f32_16x16x32_bf16 v[28:31], a[172:175], v[56:59], v[28:31]\nv_permlane16_swap_b32 v121, v123\nv_mfma_f32_16x16x32_bf16 v[40:43], a[172:175], v[60:63], v[40:43]\n" \
    "v_add_f32 v121, v121, v123\nv_mov_b32 v123, v121\nv_mfma_f32_16x16x32_bf16 v[32:35], v[208:211], v[56:59], v[32:35]\n" \
    "v_mfma_f32_16x16x32_bf16 v[44:47], v[208:211], v[60:63], v[44:47]\ns_waitcnt lgkmcnt(0)\ns_barrier\nv_mfma_f32_16x16x32_bf16 v[24:27], a[176:179], v[64:67], v[24:27]\n" \
    "ds_read_b128 v[48:51], v2 offset:49152\nv_mfma_f32_16x16x32_bf16 v[36:39], a[176:179], v[68:71], v[36:39]\nds_read_b128 v[52:55], v2 offset:57344\n" \
    "v_mfma_f32_16x16x32_bf16 v[28:31], a[180:183], v[64:67], v[28:31]\nv_permlane32_swap_b32 v121, v123\nv_mfma_f32_16x16x32_bf16 v[40:43], a[180:183], v[68:71], v[40:43]\n" \
    "ds_read_b128 v[108:111], v15\nv_add_f32 v121, v121, v123\nv_mfma_f32_16x16x32_bf16 v[32:35], v[212:215], v[64:67], v[32:35]\nv_rcp_f32 v121, v121\n" \
    "v_mfma_f32_16x16x32_bf16 v[44:47], v[212:215], v[68:71], v[44:47]\nv_mul_f32 v120, v120, v121\nv_mul_f32 v16, v16, v121\n" \
    "v_mfma_f32_16x16x32_bf16 v[24:27], a[184:187], v[72:75], v[24:27]\nds_read_b128 v[56:59], v3 offset:49152\nv_mul_f32 v17, v17, v121\n" \
    "v_mfma_f32_16x16x32_bf16 v[36:39], a[184:187], v[76:79], v[36:39]\nds_read_b128 v[60:63], v3 offset:57344\nv_mul_f32 v18, v18, v121\n" \
    "v_mfma_f32_16x16x32_bf16 v[28:31], a[188:191], v[72:75], v[28:31]\nv_mul_f32 v19, v19, v121\nv_mul_f32 v112, v112, v120\n" \
    "v_mfma_f32_16x16x32_bf16 v[40:43], a[188:191], v[76:79], v[40:43]\nv_mul_f32 v113, v113, v120\nv_mul_f32 v114, v114, v120\n" \
    "v_mfma_f32_16x16x32_bf16 v[32:35], v[216:219], v[72:75], v[32:35]\nv_mul_f32 v115, v115, v120\nv_mul_f32 v116, v116, v120\n" \
    "v_mfma_f32_16x16x32_bf16 v[44:47], v[216:219], v[76:79], v[44:47]\ns_waitcnt lgkmcnt(3)\nv_mul_f32 v117, v117, v120\nv_mul_f32 v118, v118, v120\n" \
    "v_mfma_f32_16x16x32_bf16 v[24:27], a[192:195], v[48:51], v[24:27]\nds_read_b128 v[64:67], v2 offset:50176\nv_mul_f32 v119, v119, v120\n" \
    "v_mfma_f32_16x16x32_bf16 v[36:39], a[192:195], v[52:55], v[36:39]\nds_read_b128 v[68:71], v2 offset:58368\nv_cvt_pk_bf16_f32 v16, v16, v17\n" \
    "v_mfma_f32_16x16x32_bf16 v[28:31], a[196:199], v[48:51], v[28:31]\ns_add_u32 m0, s52, 32768\ns_add_u32 s69, s70, 1024\nv_cvt_pk_bf16_f32 v17, v18, v19\n" \
    "v_mfma_f32_16x16x32_bf16 v[40:43], a[196:199], v[52:55], v[40:43]\nbuffer_load_dwordx4 v1, s[64:67], s69 offen offset:0 sc1 lds\nv_cvt_pk_bf16_f32 v112, v112, v113\n" \
    "v_mfma_f32_16x16x32_bf16 v[32:35], v[220:223], v[48:51], v[32:35]\nv_cvt_pk_bf16_f32 v113, v114, v115\nv_cvt_pk_bf16_f32 v114, v116, v117\n" \
    "v_mfma_f32_16x16x32_bf16 v[44:47], v[220:223], v[52:55], v[44:47]\ns_waitcnt lgkmcnt(2)\nv_cvt_pk_bf16_f32 v115, v118, v119\n" \
    "v_mfma_f32_16x16x32_bf16 v[24:27], a[200:203], v[56:59], v[24:27]\nds_read_b128 v[72:75], v3 offset:50176\n" \
    "v_mfma_f32_16x16x32_bf16 v[36:39], a[200:203], v[60:63], v[36:39]\nds_read_b128 v[76:79], v3 offset:58368\n" \
    "v_mfma_f32_16x16x32_bf16 v[28:31], a[204:207], v[56:59], v[28:31]\ns_add_u32 s69, s70, 128\nbuffer_load_dwordx4 v1, s[64:67], s69 offen offset:1024 sc1 lds\n" \
    "v_mfma_f32_16x16x32_bf16 v[40:43], a[204:207], v[60:63], v[40:43]\nbuffer_store_dwordx4 v[112:115], v0, s[64:67], s78 offen sc1\n" \
    "v_mfma_f32_16x16x32_bf16 v[32:35], v[224:227], v[56:59], v[32:35]\nbuffer_store_dwordx2 v[16:17], v10, s[64:67], s76 offen sc1\n" \
    "v_mfma_f32_16x16x32_bf16 v[44:47], v[224:227], v[60:63], v[44:47]\ns_waitcnt lgkmcnt(2)\nv_mov_b32 v21, 1\n" \
    "v_mfma_f32_16x16x32_bf16 v[24:27], a[208:211], v[64:67], v[24:27]\nds_read_b128 v[48:51], v2 offset:51200\n" \
    "v_mfma_f32_16x16x32_bf16 v[36:39], a[208:211], v[68:71], v[36:39]\nds_read_b128 v[52:55], v2 offset:59392\n" \
    "v_mfma_f32_16x16x32_bf16 v[28:31], a[212:215], v[64:67], v[28:31]\ns_add_u32 s69, s70, -768\nbuffer_load_dwordx4 v1, s[64:67], s69 offen offset:2048 sc1 lds\n" \
    "v_mfma_f32_16x16x32_bf16 v[40:43], a[212:215], v[68:71], v[40:43]\ns_mov_b64 exec, 1\nbuffer_atomic_add v21, off, s[64:67], s79 sc0\ns_mov_b64 exec, -1\n" \
    "v_mfma_f32_16x16x32_bf16 v[32:35], v[228:231], v[64:67], v[32:35]\nv_readfirstlane_b32 s72, v108\nv_readfirstlane_b32 s73, v109\n" \
    "v_mfma_f32_16x16x32_bf16 v[44:47], v[228:231], v[68:71], v[44:47]\ns_waitcnt lgkmcnt(2)\nv_readfirstlane_b32 s37, v111\nv_readfirstlane_b32 s40, v110\n" \
    "v_mfma_f32_16x16x32_bf16 v[24:27], a[216:219], v[72:75], v[24:27]\nds_read_b128 v[56:59], v3 offset:51200\n" \
    "v_mfma_f32_16x16x32_bf16 v[36:39], a[216:219], v[76:79], v[36:39]\nds_read_b128 v[60:63], v3 offset:59392\ns_lshr_b32 s41, s72, 8\n" \
    "v_mfma_f32_16x16x32_bf16 v[28:31], a[220:223], v[72:75], v[28:31]\ns_add_u32 s69, s70, -1664\nbuffer_load_dwordx4 v1, s[64:67], s69 offen offset:3072 sc1 lds\n" \
    "v_mfma_f32_16x16x32_bf16 v[40:43], a[220:223], v[76:79], v[40:43]\nbuffer_load_dword v20, v6, s[64:67], 0 offen sc1\ns_sub_u32 s41, s40, s41\n" \
    "v_mfma_f32_16x16x32_bf16 v[32:35], v[232:235], v[72:75], v[32:35]\ns_lshl_b32 s41, s41, 8\ns_and_b32 s72, s72, 127\n" \
    "v_mfma_f32_16x16x32_bf16 v[44:47], v[232:235], v[76:79], v[44:47]\ns_waitcnt lgkmcnt(2)\ns_cmp_lt_i32 s41, s30\ns_subb_u32 s72, s72, 0\n" \
    "v_mfma_f32_16x16x32_bf16 v[24:27], a[224:227], v[48:51], v[24:27]\nds_read_b128 v[64:67], v2 offset:52224\ns_max_i32 s72, s72, 0\n" \
    "v_mfma_f32_16x16x32_bf16 v[36:39], a[224:227], v[52:55], v[36:39]\nds_read_b128 v[68:71], v2 offset:60416\n" \
    "v_mfma_f32_16x16x32_bf16 v[28:31], a[228:231], v[48:51], v[28:31]\nds_read_b128 v[72:75], v3 offset:52224\n" \
    "v_mfma_f32_16x16x32_bf16 v[40:43], a[228:231], v[52:55], v[40:43]\nds_read_b128 v[76:79], v3 offset:60416\n" \
    "v_mfma_f32_16x16x32_bf16 v[32:35], v[236:239], v[48:51], v[32:35]\ns_cmp_gt_i32 s72, s96\ns_cselect_b32 s41, s73, 0x80000000\n" \
    "v_mfma_f32_16x16x32_bf16 v[44:47], v[236:239], v[52:55], v[44:47]\ns_waitcnt lgkmcnt(4)\ns_cmp_eq_u32 s37, 384\ns_cselect_b32 s51, 64, s51\n" \
    "v_mfma_f32_16x16x32_bf16 v[32:35], v[240:243], v[56:59], v[32:35]\nv_add_u32 v122, s59, v11\nv_add_u32 v123, s59, v12\n" \
    "v_mfma_f32_16x16x32_bf16 v[44:47], v[240:243], v[60:63], v[44:47]\ns_waitcnt lgkmcnt(2)\ns_add_u32 s59, s59, 0x2000\ns_and_b32 s59, s59, 0xffff\n" \
    "v_mfma_f32_16x16x32_bf16 v[32:35], v[244:247], v[64:67], v[32:35]\ns_waitcnt lgkmcnt(0)\ns_barrier\nv_mfma_f32_16x16x32_bf16 v[44:47], v[244:247], v[68:71], v[44:47]\n" \
    "ds_read_b128 v[48:51], v2 offset:0\nv_mfma_f32_16x16x32_bf16 v[32:35], v[248:251], v[72:75], v[32:35]\nds_read_b128 v[52:55], v2 offset:8192\n" \
    "v_mfma_f32_16x16x32_bf16 v[44:47], v[248:251], v[76:79], v[44:47]\ns_mov_b32 s54, s70\ns_add_u32 s50, s50, 1\n" \
    "v_mfma_f32_16x16x32_bf16 v[24:27], a[232:235], v[56:59], v[24:27]\ns_nop 1\nv_mfma_f32_16x16x32_bf16 v[24:27], a[240:243], v[64:67], v[24:27]\ns_nop 0\n" \
    "v_cvt_pk_bf16_f32 v80, v32, v33\nv_cvt_pk_bf16_f32 v81, v34, v35\nv_mfma_f32_16x16x32_bf16 v[24:27], a[248:251], v[72:75], v[24:27]\nv_cvt_pk_bf16_f32 v82, v44, v45\n" \
    "v_cvt_pk_bf16_f32 v83, v46, v47\nv_mfma_f32_16x16x32_bf16 v[28:31], a[236:239], v[56:59], v[28:31]\nv_permlane16_swap_b32 v80, v82\n" \
    "v_mfma_f32_16x16x32_bf16 v[28:31], a[244:247], v[64:67], v[28:31]\nds_read_b128 v[56:59], v3 offset:0\nv_mfma_f32_16x16x32_bf16 v[28:31], a[252:255], v[72:75], v[28:31]\n" \
    "v_permlane16_swap_b32 v81, v83\nv_mfma_f32_16x16x32_bf16 v[36:39], a[232:235], v[60:63], v[36:39]\nbuffer_store_dwordx4 v[80:83], v4, s[64:67], s71 offen sc1\n" \
    "v_mfma_f32_16x16x32_bf16 v[36:39], a[240:243], v[68:71], v[36:39]\nv_cvt_pk_bf16_f32 v84, v24, v25\nv_cvt_pk_bf16_f32 v85, v26, v27\n" \
    "v_mfma_f32_16x16x32_bf16 v[36:39], a[248:251], v[76:79], v[36:39]\ns_nop 0\nv_cvt_pk_bf16_f32 v86, v28, v29\nv_cvt_pk_bf16_f32 v87, v30, v31\n" \
    "v_mfma_f32_16x16x32_bf16 v[40:43], a[236:239], v[60:63], v[40:43]\nv_permlane16_swap_b32 v84, v86\nv_mfma_f32_16x16x32_bf16 v[40:43], a[244:247], v[68:71], v[40:43]\n" \
    "ds_read_b128 v[60:63], v3 offset:8192\nv_mfma_f32_16x16x32_bf16 v[40:43], a[252:255], v[76:79], v[40:43]\nv_permlane16_swap_b32 v85, v87\nds_write_b128 v122, v[84:87]\n" \
    "s_cmp_gt_i32 s41, s99\nv_cvt_pk_bf16_f32 v88, v36, v37\nv_cvt_pk_bf16_f32 v89, v38, v39\ns_nop 2\nv_cvt_pk_bf16_f32 v90, v40, v41\nv_cvt_pk_bf16_f32 v91, v42, v43\n" \
    "s_nop 0\nv_permlane16_swap_b32 v88, v90\nv_permlane16_swap_b32 v89, v91\nds_write_b128 v123, v[88:91]\ns_cbranch_scc1 .Lprqb1_%=\n.Lprqmid_%=:\n" \
    "s_cmp_eq_u32 s96, 0x7fffffff\ns_cbranch_scc1 .Lprqslow_%=\ns_waitcnt vmcnt(1)\nds_write_b32 v7, v20\nv_readfirstlane_b32 s42, v21\ns_cmp_eq_u32 s42, 63\n" \
    "s_cbranch_scc1 .Lprqtn_%=\n.Lprqtnb_%=:\ns_waitcnt lgkmcnt(0)\ns_barrier\nds_read_b64 v[96:97], v15\ns_waitcnt lgkmcnt(0)\nv_readfirstlane_b32 s78, v96\n" \
    "v_readfirstlane_b32 s73, v97\ns_and_b32 s72, s78, 127\ns_cmp_gt_i32 s51, s97\ns_cbranch_scc0 .Lprqslow_%=\ns_cmp_gt_i32 s72, s98\ns_cbranch_scc0 .Lprqslow_%=\n" \
    "s_cmp_gt_i32 s73, s99\ns_cbranch_scc0 .Lprqslow_%=\n.Lprqclk_%=:\ns_memrealtime s[40:41]\ns_mov_b32 s72, s78\ns_waitcnt lgkmcnt(0)\ns_lshr_b32 s41, s72, 8\n" \
    "s_sub_u32 s41, s40, s41\ns_lshl_b32 s41, s41, 8\ns_and_b32 s72, s72, 127\ns_cmp_lt_i32 s41, s31\ns_subb_u32 s72, s72, 0\ns_max_i32 s72, s72, 0\ns_cmp_gt_i32 s72, s98\n" \
    "s_cbranch_scc0 .Lprqclk_%=\ns_branch .Lprqgo_%=\n.Lprqslow_%=:\ns_waitcnt vmcnt(0) lgkmcnt(0)\nv_readfirstlane_b32 s42, v21\ns_cmp_eq_u32 s42, 63\n" \
    "s_cbranch_scc1 .Lprqtm_%=\n.Lprqtmb_%=:\ns_cmp_eq_u32 s50, 64\ns_cbranch_scc1 .Lprqdone_%=\ns_barrier\nv_mov_b32 v96, s50\n" \
    "buffer_store_dword v96, v5, s[64:67], 0 offen sc1\ns_cmp_gt_i32 s51, s97\ns_cbranch_scc0 .Lprqpoll_%=\ns_cmp_gt_i32 s72, s98\ns_cbranch_scc0 .Lprqpoll_%=\n" \
    "s_cmp_gt_i32 s73, s99\ns_cbranch_scc0 .Lprqpoll_%=\ns_branch .Lprqgo_%=\n.Lprqpoll_%=:\ns_mov_b32 s68, 0\ns_cmp_lg_u32 s55, 0\ns_cbranch_scc1 .Lprqjoin_%=\n.Lprqpl2_%=:\n" \
    "s_cmp_gt_i32 s51, s97\ns_cbranch_scc1 .Lprqpl3_%=\nv_lshlrev_b32 v96, 3, v0\nbuffer_load_dword v96, v96, s[64:67], s36 offen sc1\ns_waitcnt vmcnt(0)\n" \
    "v_cmp_gt_u32 vcc, 32, v96\ns_nop 4\ns_ff1_i32_b64 s51, vcc\ns_min_u32 s51, s51, 64\n.Lprqpl3_%=:\nbuffer_load_dword v96, v6, s[64:67], 0 offen sc1\ns_waitcnt vmcnt(0)\n" \
    "v_readlane_b32 s73, v96, 1\n.Lprqpl4_%=:\nv_readlane_b32 s72, v96, 0\ns_memrealtime s[70:71]\ns_and_b32 s78, s72, 127\ns_waitcnt lgkmcnt(0)\ns_mov_b32 s40, s70\n" \
    "s_lshr_b32 s41, s72, 8\ns_sub_u32 s41, s40, s41\ns_lshl_b32 s41, s41, 8\ns_and_b32 s72, s72, 127\ns_cmp_lt_i32 s41, s31\ns_subb_u32 s72, s72, 0\ns_max_i32 s72, s72, 0\n" \
    "s_cmp_gt_i32 s51, s97\ns_cbranch_scc0 .Lprqok_%=\ns_cmp_gt_i32 s72, s98\ns_cbranch_scc0 .Lprqok_%=\ns_cmp_gt_i32 s73, s99\ns_cbranch_scc0 .Lprqok_%=\nv_mov_b32 v96, s51\n" \
    "ds_write_b32 v7, v96\n.Lprqjoin_%=:\ns_waitcnt lgkmcnt(0)\ns_barrier\nds_read_b32 v96, v15\ns_waitcnt lgkmcnt(0)\nv_readfirstlane_b32 s51, v96\ns_branch .Lprqgo_%=\n" \
    ".Lprqok_%=:\ns_sleep 1\ns_cmp_gt_i32 s78, s98\ns_cselect_b32 s69, s73, s99\ns_cmp_gt_i32 s51, s97\ns_cselect_b32 s69, s69, s99\ns_cmp_gt_i32 s69, s99\n" \
    "s_cbranch_scc1 .Lprqpl4_%=\ns_sleep 5\ns_add_u32 s68, s68, 1\ns_and_b32 s43, s68, 63\ns_cbranch_scc1 .Lprqpl2_%=\ns_cmp_gt_u32 s68, 0x200000\ns_cbranch_scc1 .Lprqabort_%=\n" \
    "buffer_load_dword v97, off, s[64:67], 0 sc1\ns_waitcnt vmcnt(0)\nv_readfirstlane_b32 s43, v97\ns_cmp_eq_u32 s43, 1\ns_cbranch_scc0 .Lprqpl2_%=\ns_endpgm\n.Lprqtl_%=:\n" \
    "v_mov_b32 v21, 0\ns_cmp_lt_i32 s77, 0\ns_cbranch_scc1 .Lprqtlb_%=\ns_add_u32 s42, s77, 1\nv_mov_b32 v123, s42\ns_and_b32 s42, s77, 1\ns_lshl_b32 s42, s42, 2\n" \
    "buffer_atomic_umax v123, v14, s[64:67], s42 offen sc1\ns_branch .Lprqtlb_%=\n.Lprqkm_%=:\ns_cmp_lt_i32 s77, 0\ns_cbranch_scc1 .Lprqkmb_%=\ns_mov_b32 s42, 0\n.Lprqkm2_%=:\n" \
    "buffer_load_dwordx4 v[96:99], v9, s[64:67], s75 offen offset:0 sc1\nbuffer_load_dwordx4 v[100:103], v9, s[64:67], s75 offen offset:512 sc1\n" \
    "buffer_load_dwordx4 v[104:107], v9, s[64:67], s75 offen offset:1024 sc1\nbuffer_load_dwordx4 v[108:111], v9, s[64:67], s75 offen offset:1536 sc1\ns_waitcnt vmcnt(0)\n" \
    "v_max3_u32 v123, v96, v97, v98\nv_max3_u32 v123, v123, v99, v100\nv_max3_u32 v123, v123, v101, v102\nv_max3_u32 v123, v123, v103, v104\nv_max3_u32 v123, v123, v105, v106\n" \
    "v_max3_u32 v123, v123, v107, v108\nv_max3_u32 v123, v123, v109, v110\nv_max_u32 v123, v123, v111\nv_cmp_eq_u32 vcc, -1, v123\ns_cbranch_vccz .Lprqkmb_%=\n" \
    "s_add_u32 s42, s42, 1\ns_and_b32 s43, s42, 63\ns_cbranch_scc1 .Lprqkm2_%=\ns_cmp_gt_u32 s42, 0x200000\ns_cbranch_scc1 .Lprqabort_%=\n" \
    "buffer_load_dword v123, off, s[64:67], 0 sc1\ns_waitcnt vmcnt(0)\nv_readfirstlane_b32 s43, v123\ns_cmp_eq_u32 s43, 1\ns_cbranch_scc0 .Lprqkm2_%=\ns_endpgm\n.Lprqtn_%=:\n" \
    "v_mov_b32 v21, 0\ns_cmp_lt_i32 s77, 0\ns_cbranch_scc1 .Lprqtnb_%=\ns_add_u32 s42, s77, 1\nv_mov_b32 v123, s42\ns_and_b32 s42, s77, 1\ns_lshl_b32 s42, s42, 2\n" \
    "buffer_atomic_umax v123, v14, s[64:67], s42 offen sc1\ns_branch .Lprqtnb_%=\n.Lprqtm_%=:\nv_mov_b32 v21, 0\ns_cmp_lt_i32 s77, 0\ns_cbranch_scc1 .Lprqtmb_%=\n" \
    "s_add_u32 s42, s77, 1\nv_mov_b32 v123, s42\ns_and_b32 s42, s77, 1\ns_lshl_b32 s42, s42, 2\nbuffer_atomic_umax v123, v14, s[64:67], s42 offen sc1\ns_branch .Lprqtmb_%=\n" \
    ".Lprqabort_%=:\nv_mov_b32 v96, 1\nbuffer_store_dword v96, off, s[64:67], 0 sc1\ns_endpgm\n.Lprqdone_%=:\n"
#define PROJ_V \
    "ds_read_b128 v[48:51], v2 offset:0\nds_read_b128 v[52:55], v2 offset:8192\nds_read_b128 v[56:59], v3 offset:0\nds_read_b128 v[60:63], v3 offset:8192\ns_mov_b32 s72, 0\n" \
    "s_mov_b32 s73, 0\nv_mov_b32 v21, 0\ns_add_u32 s97, s50, 1\ns_min_u32 s97, s97, 63\ns_lshr_b32 s98, s50, s27\ns_add_u32 s98, s98, s28\ns_min_u32 s98, s98, s29\n" \
    "s_sub_u32 s98, s50, s98\ns_sub_u32 s99, s50, s17\ns_branch .Lprvslow_%=\n.Lprvgo_%=:\ns_waitcnt lgkmcnt(2)\nv_mfma_f32_16x16x32_bf16 v[24:27], a[0:3], v[48:51], 0\n" \
    "ds_read_b128 v[64:67], v2 offset:1024\nv_mfma_f32_16x16x32_bf16 v[36:39], a[0:3], v[52:55], 0\nds_read_b128 v[68:71], v2 offset:9216\n" \
    "v_mfma_f32_16x16x32_bf16 v[28:31], a[4:7], v[48:51], 0\ns_add_u32 m0, s52, 49152\ns_add_u32 s69, s54, 1536\ns_and_b32 s76, s50, s16\n" \
    "v_mfma_f32_16x16x32_bf16 v[40:43], a[4:7], v[52:55], 0\nbuffer_load_dwordx4 v1, s[64:67], s69 offen offset:0 sc1 lds\ns_lshl_b32 s76, s76, 20\n" \
    "v_mfma_f32_16x16x32_bf16 v[32:35], v[124:127], v[48:51], 0\ns_add_u32 s76, s76, s56\ns_add_u32 s74, s48, s76\ns_lshr_b32 s74, s74, 8\n" \
    "v_mfma_f32_16x16x32_bf16 v[44:47], v[124:127], v[52:55], 0\ns_waitcnt lgkmcnt(2)\ns_xor_b32 s74, s74, s49\ns_and_b32 s74, s74, 0x2000\n" \
    "v_mfma_f32_16x16x32_bf16 v[24:27], a[8:11], v[56:59], v[24:27]\nds_read_b128 v[72:75], v3 offset:1024\ns_add_u32 s74, s74, s76\n" \
    "v_mfma_f32_16x16x32_bf16 v[36:39], a[8:11], v[60:63], v[36:39]\nds_read_b128 v[76:79], v3 offset:9216\nv_mfma_f32_16x16x32_bf16 v[28:31], a[12:15], v[56:59], v[28:31]\n" \
    "s_add_u32 s68, s50, 1\ns_min_u32 s68, s68, 63\ns_lshl_b32 s68, s68, 20\nv_mfma_f32_16x16x32_bf16 v[40:43], a[12:15], v[60:63], v[40:43]\ns_add_u32 s68, s68, s18\n" \
    "s_add_u32 s70, s48, s68\ns_lshr_b32 s70, s70, 8\nv_mfma_f32_16x16x32_bf16 v[32:35], v[128:131], v[56:59], v[32:35]\ns_xnor_b32 s70, s70, s49\ns_and_b32 s70, s70, 0x2000\n" \
    "s_add_u32 s70, s70, s68\nv_mfma_f32_16x16x32_bf16 v[44:47], v[128:131], v[60:63], v[44:47]\ns_waitcnt lgkmcnt(2)\ns_add_u32 s69, s54, 640\n" \
    "v_mfma_f32_16x16x32_bf16 v[24:27], a[16:19], v[64:67], v[24:27]\nds_read_b128 v[48:51], v2 offset:2048\nv_mfma_f32_16x16x32_bf16 v[36:39], a[16:19], v[68:71], v[36:39]\n" \
    "ds_read_b128 v[52:55], v2 offset:10240\nv_mfma_f32_16x16x32_bf16 v[28:31], a[20:23], v[64:67], v[28:31]\nbuffer_load_dwordx4 v1, s[64:67], s69 offen offset:1024 sc1 lds\n" \
    "s_add_u32 s69, s54, -256\nv_mfma_f32_16x16x32_bf16 v[40:43], a[20:23], v[68:71], v[40:43]\nbuffer_load_dwordx4 v1, s[64:67], s69 offen offset:2048 sc1 lds\n" \
    "v_mfma_f32_16x16x32_bf16 v[32:35], v[132:135], v[64:67], v[32:35]\nbuffer_store_dwordx4 v[16:19], v8, s[64:67], s74 offen sc1\n" \
    "v_mfma_f32_16x16x32_bf16 v[44:47], v[132:135], v[68:71], v[44:47]\ns_waitcnt lgkmcnt(2)\nv_mfma_f32_16x16x32_bf16 v[24:27], a[24:27], v[72:75], v[24:27]\n" \
    "ds_read_b128 v[56:59], v3 offset:2048\nv_mfma_f32_16x16x32_bf16 v[36:39], a[24:27], v[76:79], v[36:39]\nds_read_b128 v[60:63], v3 offset:10240\n" \
    "v_mfma_f32_16x16x32_bf16 v[28:31], a[28:31], v[72:75], v[28:31]\ns_add_u32 s69, s54, -1152\nbuffer_load_dwordx4 v1, s[64:67], s69 offen offset:3072 sc1 lds\n" \
    "v_mfma_f32_16x16x32_bf16 v[40:43], a[28:31], v[76:79], v[40:43]\nv_mfma_f32_16x16x32_bf16 v[32:35], v[136:139], v[72:75], v[32:35]\n" \
    "v_mfma_f32_16x16x32_bf16 v[44:47], v[136:139], v[76:79], v[44:47]\ns_waitcnt lgkmcnt(2)\nv_mfma_f32_16x16x32_bf16 v[24:27], a[32:35], v[48:51], v[24:27]\n" \
    "ds_read_b128 v[64:67], v2 offset:3072\nv_mfma_f32_16x16x32_bf16 v[36:39], a[32:35], v[52:55], v[36:39]\nds_read_b128 v[68:71], v2 offset:11264\n" \
    "v_mfma_f32_16x16x32_bf16 v[28:31], a[36:39], v[48:51], v[28:31]\ns_and_b32 s37, s50, s16\ns_lshl_b32 s37, s37, 19\ns_add_u32 s37, s37, s53\n" \
    "v_mfma_f32_16x16x32_bf16 v[40:43], a[36:39], v[52:55], v[40:43]\ns_add_u32 s71, s48, s37\ns_lshr_b32 s71, s71, 8\ns_xnor_b32 s71, s71, s49\n" \
    "v_mfma_f32_16x16x32_bf16 v[32:35], v[140:143], v[48:51], v[32:35]\ns_and_b32 s71, s71, 0x2000\ns_add_u32 s71, s71, s37\n" \
    "v_mfma_f32_16x16x32_bf16 v[44:47], v[140:143], v[52:55], v[44:47]\ns_waitcnt lgkmcnt(2)\nv_mfma_f32_16x16x32_bf16 v[24:27], a[40:43], v[56:59], v[24:27]\n" \
    "ds_read_b128 v[72:75], v3 offset:3072\nv_mfma_f32_16x16x32_bf16 v[36:39], a[40:43], v[60:63], v[36:39]\nds_read_b128 v[76:79], v3 offset:11264\ns_add_u32 s75, s74, 4096\n" \
    "v_mfma_f32_16x16x32_bf16 v[28:31], a[44:47], v[56:59], v[28:31]\nbuffer_store_dwordx4 v[16:19], v8, s[64:67], s75 offen sc1\n" \
    "v_mfma_f32_16x16x32_bf16 v[40:43], a[44:47], v[60:63], v[40:43]\nv_mfma_f32_16x16x32_bf16 v[32:35], v[144:147], v[56:59], v[32:35]\n" \
    "v_mfma_f32_16x16x32_bf16 v[44:47], v[144:147], v[60:63], v[44:47]\ns_waitcnt vmcnt(17) lgkmcnt(0)\ns_barrier\n" \
    "v_mfma_f32_16x16x32_bf16 v[24:27], a[48:51], v[64:67], v[24:27]\nds_read_b128 v[48:51], v2 offset:16384\nv_mfma_f32_16x16x32_bf16 v[36:39], a[48:51], v[68:71], v[36:39]\n" \
    "ds_read_b128 v[52:55], v2 offset:24576\nv_mfma_f32_16x16x32_bf16 v[28:31], a[52:55], v[64:67], v[28:31]\ns_add_u32 s39, s50, 1\ns_add_u32 s97, s39, 1\n" \
    "s_min_u32 s97, s97, 63\nv_mfma_f32_16x16x32_bf16 v[40:43], a[52:55], v[68:71], v[40:43]\ns_lshr_b32 s98, s39, s27\ns_add_u32 s98, s98, s28\ns_min_u32 s98, s98, s29\n" \
    "v_mfma_f32_16x16x32_bf16 v[32:35], v[148:151], v[64:67], v[32:35]\ns_sub_u32 s98, s39, s98\ns_sub_u32 s99, s39, s17\n" \
    "v_mfma_f32_16x16x32_bf16 v[44:47], v[148:151], v[68:71], v[44:47]\ns_cmp_gt_i32 s51, s97\ns_cselect_b32 s96, s98, 0x7fffffff\ns_sub_u32 s39, s39, 3\n" \
    "v_mfma_f32_16x16x32_bf16 v[24:27], a[56:59], v[72:75], v[24:27]\nds_read_b128 v[56:59], v3 offset:16384\nv_mfma_f32_16x16x32_bf16 v[36:39], a[56:59], v[76:79], v[36:39]\n" \
    "ds_read_b128 v[60:63], v3 offset:24576\nv_mfma_f32_16x16x32_bf16 v[28:31], a[60:63], v[72:75], v[28:31]\ns_cmp_lt_u32 s39, 61\ns_cselect_b32 s96, s96, 0x7fffffff\n" \
    "v_mfma_f32_16x16x32_bf16 v[40:43], a[60:63], v[76:79], v[40:43]\nv_mfma_f32_16x16x32_bf16 v[32:35], v[152:155], v[72:75], v[32:35]\ns_add_u32 s75, s74, 16384\n" \
    "v_mfma_f32_16x16x32_bf16 v[44:47], v[152:155], v[76:79], v[44:47]\ns_waitcnt lgkmcnt(2)\nv_mfma_f32_16x16x32_bf16 v[24:27], a[64:67], v[48:51], v[24:27]\n" \
    "ds_read_b128 v[64:67], v2 offset:17408\nv_mfma_f32_16x16x32_bf16 v[36:39], a[64:67], v[52:55], v[36:39]\nds_read_b128 v[68:71], v2 offset:25600\n" \
    "v_mfma_f32_16x16x32_bf16 v[28:31], a[68:71], v[48:51], v[28:31]\ns_add_u32 m0, s52, 0\ns_add_u32 s69, s70, 0\n" \
    "v_mfma_f32_16x16x32_bf16 v[40:43], a[68:71], v[52:55], v[40:43]\nbuffer_load_dwordx4 v1, s[64:67], s69 offen offset:0 sc1 lds\n" \
    "v_mfma_f32_16x16x32_bf16 v[32:35], v[156:159], v[48:51], v[32:35]\nbuffer_store_dwordx4 v[16:19], v8, s[64:67], s75 offen sc1\n" \
    "v_mfma_f32_16x16x32_bf16 v[44:47], v[156:159], v[52:55], v[44:47]\ns_waitcnt lgkmcnt(2)\nv_mfma_f32_16x16x32_bf16 v[24:27], a[72:75], v[56:59], v[24:27]\n" \
    "ds_read_b128 v[72:75], v3 offset:17408\nv_mfma_f32_16x16x32_bf16 v[36:39], a[72:75], v[60:63], v[36:39]\nds_read_b128 v[76:79], v3 offset:25600\n" \
    "v_mfma_f32_16x16x32_bf16 v[28:31], a[76:79], v[56:59], v[28:31]\ns_add_u32 s69, s70, -896\nbuffer_load_dwordx4 v1, s[64:67], s69 offen offset:1024 sc1 lds\n" \
    "v_mfma_f32_16x16x32_bf16 v[40:43], a[76:79], v[60:63], v[40:43]\nv_mfma_f32_16x16x32_bf16 v[32:35], v[160:163], v[56:59], v[32:35]\n" \
    "v_mfma_f32_16x16x32_bf16 v[44:47], v[160:163], v[60:63], v[44:47]\ns_waitcnt lgkmcnt(2)\nv_mfma_f32_16x16x32_bf16 v[24:27], a[80:83], v[64:67], v[24:27]\n" \
    "ds_read_b128 v[48:51], v2 offset:18432\nv_mfma_f32_16x16x32_bf16 v[36:39], a[80:83], v[68:71], v[36:39]\nds_read_b128 v[52:55], v2 offset:26624\ns_add_u32 s75, s74, 20480\n" \
    "v_mfma_f32_16x16x32_bf16 v[28:31], a[84:87], v[64:67], v[28:31]\ns_add_u32 s69, s70, -1792\nbuffer_load_dwordx4 v1, s[64:67], s69 offen offset:2048 sc1 lds\n" \
    "v_mfma_f32_16x16x32_bf16 v[40:43], a[84:87], v[68:71], v[40:43]\nv_xor_b32 v6, v15, v6\nv_mfma_f32_16x16x32_bf16 v[32:35], v[164:167], v[64:67], v[32:35]\n" \
    "buffer_store_dwordx4 v[16:19], v8, s[64:67], s75 offen sc1\nv_mfma_f32_16x16x32_bf16 v[44:47], v[164:167], v[68:71], v[44:47]\ns_waitcnt lgkmcnt(2)\n" \
    "s_sub_u32 s74, s54, s19\ns_xor_b32 s74, s74, s57\nv_mfma_f32_16x16x32_bf16 v[24:27], a[88:91], v[72:75], v[24:27]\nds_read_b128 v[56:59], v3 offset:18432\n" \
    "v_mfma_f32_16x16x32_bf16 v[36:39], a[88:91], v[76:79], v[36:39]\nds_read_b128 v[60:63], v3 offset:26624\nv_mfma_f32_16x16x32_bf16 v[28:31], a[92:95], v[72:75], v[28:31]\n" \
    "s_add_u32 s69, s70, -2688\nbuffer_load_dwordx4 v1, s[64:67], s69 offen offset:3072 sc1 lds\nv_mfma_f32_16x16x32_bf16 v[40:43], a[92:95], v[76:79], v[40:43]\n" \
    "v_mfma_f32_16x16x32_bf16 v[32:35], v[168:171], v[72:75], v[32:35]\nv_mfma_f32_16x16x32_bf16 v[44:47], v[168:171], v[76:79], v[44:47]\ns_waitcnt lgkmcnt(2)\n" \
    "v_mfma_f32_16x16x32_bf16 v[24:27], a[96:99], v[48:51], v[24:27]\nds_read_b128 v[64:67], v2 offset:19456\nv_mfma_f32_16x16x32_bf16 v[36:39], a[96:99], v[52:55], v[36:39]\n" \
    "ds_read_b128 v[68:71], v2 offset:27648\nv_mfma_f32_16x16x32_bf16 v[28:31], a[100:103], v[48:51], v[28:31]\nbuffer_load_dword v20, v6, s[64:67], 0 offen sc1\n" \
    "v_mfma_f32_16x16x32_bf16 v[40:43], a[100:103], v[52:55], v[40:43]\nv_mfma_f32_16x16x32_bf16 v[32:35], v[172:175], v[48:51], v[32:35]\n" \
    "buffer_store_dwordx4 v[16:19], v8, s[64:67], s74 offen sc1\nv_mfma_f32_16x16x32_bf16 v[44:47], v[172:175], v[52:55], v[44:47]\ns_waitcnt lgkmcnt(2)\n" \
    "v_mfma_f32_16x16x32_bf16 v[24:27], a[104:107], v[56:59], v[24:27]\nds_read_b128 v[72:75], v3 offset:19456\n" \
    "v_mfma_f32_16x16x32_bf16 v[36:39], a[104:107], v[60:63], v[36:39]\nds_read_b128 v[76:79], v3 offset:27648\n" \
    "v_mfma_f32_16x16x32_bf16 v[28:31], a[108:111], v[56:59], v[28:31]\nv_mfma_f32_16x16x32_bf16 v[40:43], a[108:111], v[60:63], v[40:43]\n" \
    "v_mfma_f32_16x16x32_bf16 v[32:35], v[176:179], v[56:59], v[32:35]\nv_mfma_f32_16x16x32_bf16 v[44:47], v[176:179], v[60:63], v[44:47]\ns_waitcnt vmcnt(14) lgkmcnt(0)\n" \
    "s_barrier\nv_mfma_f32_16x16x32_bf16 v[24:27], a[112:115], v[64:67], v[24:27]\nds_read_b128 v[48:51], v2 offset:32768\nv_mov_b32 v123, s50\n" \
    "v_mfma_f32_16x16x32_bf16 v[36:39], a[112:115], v[68:71], v[36:39]\nds_read_b128 v[52:55], v2 offset:40960\n" \
    "v_mfma_f32_16x16x32_bf16 v[28:31], a[116:119], v[64:67], v[28:31]\nbuffer_store_dword v123, v5, s[64:67], 0 offen sc1\n" \
    "v_mfma_f32_16x16x32_bf16 v[40:43], a[116:119], v[68:71], v[40:43]\nv_mfma_f32_16x16x32_bf16 v[32:35], v[180:183], v[64:67], v[32:35]\n" \
    "v_mfma_f32_16x16x32_bf16 v[44:47], v[180:183], v[68:71], v[44:47]\nv_mfma_f32_16x16x32_bf16 v[24:27], a[120:123], v[72:75], v[24:27]\n" \
    "ds_read_b128 v[56:59], v3 offset:32768\nv_mfma_f32_16x16x32_bf16 v[36:39], a[120:123], v[76:79], v[36:39]\nds_read_b128 v[60:63], v3 offset:40960\n" \
    "s_add_u32 s75, s74, 4096\nv_mfma_f32_16x16x32_bf16 v[28:31], a[124:127], v[72:75], v[28:31]\nbuffer_store_dwordx4 v[16:19], v8, s[64:67], s75 offen sc1\n" \
    "v_mfma_f32_16x16x32_bf16 v[40:43], a[124:127], v[76:79], v[40:43]\nv_mfma_f32_16x16x32_bf16 v[32:35], v[184:187], v[72:75], v[32:35]\n" \
    "v_mfma_f32_16x16x32_bf16 v[44:47], v[184:187], v[76:79], v[44:47]\ns_waitcnt lgkmcnt(2)\nv_mfma_f32_16x16x32_bf16 v[24:27], a[128:131], v[48:51], v[24:27]\n" \
    "ds_read_b128 v[64:67], v2 offset:33792\nv_mfma_f32_16x16x32_bf16 v[36:39], a[128:131], v[52:55], v[36:39]\nds_read_b128 v[68:71], v2 offset:41984\n" \
    "v_mfma_f32_16x16x32_bf16 v[28:31], a[132:135], v[48:51], v[28:31]\ns_add_u32 m0, s52, 16384\ns_add_u32 s69, s70, 512\n" \
    "v_mfma_f32_16x16x32_bf16 v[40:43], a[132:135], v[52:55], v[40:43]\nbuffer_load_dwordx4 v1, s[64:67], s69 offen offset:0 sc1 lds\n" \
    "v_mfma_f32_16x16x32_bf16 v[32:35], v[188:191], v[48:51], v[32:35]\nv_mfma_f32_16x16x32_bf16 v[44:47], v[188:191], v[52:55], v[44:47]\ns_waitcnt lgkmcnt(2)\n" \
    "v_mfma_f32_16x16x32_bf16 v[24:27], a[136:139], v[56:59], v[24:27]\nds_read_b128 v[72:75], v3 offset:33792\n" \
    "v_mfma_f32_16x16x32_bf16 v[36:39], a[136:139], v[60:63], v[36:39]\nds_read_b128 v[76:79], v3 offset:41984\n" \
    "v_mfma_f32_16x16x32_bf16 v[28:31], a[140:143], v[56:59], v[28:31]\ns_add_u32 s69, s70, -384\nbuffer_load_dwordx4 v1, s[64:67], s69 offen offset:1024 sc1 lds\n" \
    "v_mfma_f32_16x16x32_bf16 v[40:43], a[140:143], v[60:63], v[40:43]\nv_mfma_f32_16x16x32_bf16 v[32:35], v[192:195], v[56:59], v[32:35]\ns_add_u32 s75, s74, 16384\n" \
    "v_mfma_f32_16x16x32_bf16 v[44:47], v[192:195], v[60:63], v[44:47]\ns_waitcnt lgkmcnt(2)\nv_mfma_f32_16x16x32_bf16 v[24:27], a[144:147], v[64:67], v[24:27]\n" \
    "ds_read_b128 v[48:51], v2 offset:34816\nv_mfma_f32_16x16x32_bf16 v[36:39], a[144:147], v[68:71], v[36:39]\nds_read_b128 v[52:55], v2 offset:43008\n" \
    "v_mfma_f32_16x16x32_bf16 v[28:31], a[148:151], v[64:67], v[28:31]\ns_add_u32 s69, s70, -1280\nbuffer_load_dwordx4 v1, s[64:67], s69 offen offset:2048 sc1 lds\n" \
    "v_mfma_f32_16x16x32_bf16 v[40:43], a[148:151], v[68:71], v[40:43]\nbuffer_store_dwordx4 v[16:19], v8, s[64:67], s75 offen sc1\n" \
    "v_mfma_f32_16x16x32_bf16 v[32:35], v[196:199], v[64:67], v[32:35]\nv_mfma_f32_16x16x32_bf16 v[44:47], v[196:199], v[68:71], v[44:47]\ns_waitcnt lgkmcnt(2)\n" \
    "v_mfma_f32_16x16x32_bf16 v[24:27], a[152:155], v[72:75], v[24:27]\nds_read_b128 v[56:59], v3 offset:34816\n" \
    "v_mfma_f32_16x16x32_bf16 v[36:39], a[152:155], v[76:79], v[36:39]\nds_read_b128 v[60:63], v3 offset:43008\n" \
    "v_mfma_f32_16x16x32_bf16 v[28:31], a[156:159], v[72:75], v[28:31]\ns_add_u32 s69, s70, -2176\nbuffer_load_dwordx4 v1, s[64:67], s69 offen offset:3072 sc1 lds\n" \
    "v_mfma_f32_16x16x32_bf16 v[40:43], a[156:159], v[76:79], v[40:43]\nv_mfma_f32_16x16x32_bf16 v[32:35], v[200:203], v[72:75], v[32:35]\n" \
    "v_mfma_f32_16x16x32_bf16 v[44:47], v[200:203], v[76:79], v[44:47]\ns_waitcnt lgkmcnt(2)\nv_mfma_f32_16x16x32_bf16 v[24:27], a[160:163], v[48:51], v[24:27]\n" \
    "ds_read_b128 v[64:67], v2 offset:35840\nv_mfma_f32_16x16x32_bf16 v[36:39], a[160:163], v[52:55], v[36:39]\nds_read_b128 v[68:71], v2 offset:44032\n" \
    "s_add_u32 s75, s74, 20480\nv_mfma_f32_16x16x32_bf16 v[28:31], a[164:167], v[48:51], v[28:31]\ns_waitcnt vmcnt(8)\nds_write_b32 v7, v20\n" \
    "v_mfma_f32_16x16x32_bf16 v[40:43], a[164:167], v[52:55], v[40:43]\nbuffer_store_dwordx4 v[16:19], v8, s[64:67], s75 offen sc1\n" \
    "v_mfma_f32_16x16x32_bf16 v[32:35], v[204:207], v[48:51], v[32:35]\ns_and_b32 s76, s50, s16\ns_lshl_b32 s76, s76, 19\ns_add_u32 s76, s76, s20\n" \
    "v_mfma_f32_16x16x32_bf16 v[44:47], v[204:207], v[52:55], v[44:47]\ns_waitcnt lgkmcnt(3)\ns_add_u32 s74, s48, s76\ns_lshr_b32 s74, s74, 8\n" \
    "v_mfma_f32_16x16x32_bf16 v[24:27], a[168:171], v[56:59], v[24:27]\nds_read_b128 v[72:75], v3 offset:35840\ns_xor_b32 s74, s74, s49\n" \
    "v_mfma_f32_16x16x32_bf16 v[36:39], a[168:171], v[60:63], v[36:39]\nds_read_b128 v[76:79], v3 offset:44032\ns_and_b32 s74, s74, 0x2000\n" \
    "v_mfma_f32_16x16x32_bf16 v[28:31], a[172:175], v[56:59], v[28:31]\ns_add_u32 s74, s74, s76\ns_xor_b32 s74, s74, s58\n" \
    "v_mfma_f32_16x16x32_bf16 v[40:43], a[172:175], v[60:63], v[40:43]\nv_mfma_f32_16x16x32_bf16 v[32:35], v[208:211], v[56:59], v[32:35]\n" \
    "v_mfma_f32_16x16x32_bf16 v[44:47], v[208:211], v[60:63], v[44:47]\ns_waitcnt lgkmcnt(0)\ns_barrier\nv_mfma_f32_16x16x32_bf16 v[24:27], a[176:179], v[64:67], v[24:27]\n" \
    "ds_read_b128 v[48:51], v2 offset:49152\nv_mfma_f32_16x16x32_bf16 v[36:39], a[176:179], v[68:71], v[36:39]\nds_read_b128 v[52:55], v2 offset:57344\n" \
    "v_mfma_f32_16x16x32_bf16 v[28:31], a[180:183], v[64:67], v[28:31]\nds_read_b128 v[108:111], v14\nv_mfma_f32_16x16x32_bf16 v[40:43], a[180:183], v[68:71], v[40:43]\n" \
    "v_mfma_f32_16x16x32_bf16 v[32:35], v[212:215], v[64:67], v[32:35]\nbuffer_store_dwordx4 v[16:19], v8, s[64:67], s74 offen sc1\n" \
    "v_mfma_f32_16x16x32_bf16 v[44:47], v[212:215], v[68:71], v[44:47]\nv_mfma_f32_16x16x32_bf16 v[24:27], a[184:187], v[72:75], v[24:27]\n" \
    "ds_read_b128 v[56:59], v3 offset:49152\nv_mfma_f32_16x16x32_bf16 v[36:39], a[184:187], v[76:79], v[36:39]\nds_read_b128 v[60:63], v3 offset:57344\n" \
    "v_mfma_f32_16x16x32_bf16 v[28:31], a[188:191], v[72:75], v[28:31]\ns_waitcnt lgkmcnt(2)\nv_readfirstlane_b32 s72, v108\nv_readfirstlane_b32 s73, v109\n" \
    "v_mfma_f32_16x16x32_bf16 v[40:43], a[188:191], v[76:79], v[40:43]\nv_readfirstlane_b32 s37, v111\nv_mfma_f32_16x16x32_bf16 v[32:35], v[216:219], v[72:75], v[32:35]\n" \
    "v_mfma_f32_16x16x32_bf16 v[44:47], v[216:219], v[76:79], v[44:47]\ns_and_b32 s72, s72, 127\nv_mfma_f32_16x16x32_bf16 v[24:27], a[192:195], v[48:51], v[24:27]\n" \
    "ds_read_b128 v[64:67], v2 offset:50176\nv_mfma_f32_16x16x32_bf16 v[36:39], a[192:195], v[52:55], v[36:39]\nds_read_b128 v[68:71], v2 offset:58368\n" \
    "v_mfma_f32_16x16x32_bf16 v[28:31], a[196:199], v[48:51], v[28:31]\ns_add_u32 m0, s52, 32768\ns_add_u32 s69, s70, 1024\n" \
    "v_mfma_f32_16x16x32_bf16 v[40:43], a[196:199], v[52:55], v[40:43]\nbuffer_load_dwordx4 v1, s[64:67], s69 offen offset:0 sc1 lds\n" \
    "v_mfma_f32_16x16x32_bf16 v[32:35], v[220:223], v[48:51], v[32:35]\ns_cmp_gt_i32 s72, s96\ns_cselect_b32 s41, s73, 0x80000000\n" \
    "v_mfma_f32_16x16x32_bf16 v[44:47], v[220:223], v[52:55], v[44:47]\ns_waitcnt lgkmcnt(2)\ns_cmp_eq_u32 s37, 384\ns_cselect_b32 s51, 64, s51\n" \
    "v_mfma_f32_16x16x32_bf16 v[24:27], a[200:203], v[56:59], v[24:27]\nds_read_b128 v[72:75], v3 offset:50176\n" \
    "v_mfma_f32_16x16x32_bf16 v[36:39], a[200:203], v[60:63], v[36:39]\nds_read_b128 v[76:79], v3 offset:58368\ns_add_u32 s75, s74, 4096\n" \
    "v_mfma_f32_16x16x32_bf16 v[28:31], a[204:207], v[56:59], v[28:31]\ns_add_u32 s69, s70, 128\nbuffer_load_dwordx4 v1, s[64:67], s69 offen offset:1024 sc1 lds\n" \
    "v_mfma_f32_16x16x32_bf16 v[40:43], a[204:207], v[60:63], v[40:43]\nbuffer_store_dwordx4 v[16:19], v8, s[64:67], s75 offen sc1\n" \
    "v_mfma_f32_16x16x32_bf16 v[32:35], v[224:227], v[56:59], v[32:35]\ns_add_u32 s76, s50, 1\ns_and_b32 s76, s76, s16\ns_lshl_b32 s76, s76, 18\n" \
    "v_mfma_f32_16x16x32_bf16 v[44:47], v[224:227], v[60:63], v[44:47]\ns_waitcnt lgkmcnt(2)\ns_add_u32 s76, s76, s59\ns_add_u32 s77, s48, s76\n" \
    "v_mfma_f32_16x16x32_bf16 v[24:27], a[208:211], v[64:67], v[24:27]\nds_read_b128 v[48:51], v2 offset:51200\ns_lshr_b32 s77, s77, 8\n" \
    "v_mfma_f32_16x16x32_bf16 v[36:39], a[208:211], v[68:71], v[36:39]\nds_read_b128 v[52:55], v2 offset:59392\ns_xor_b32 s77, s77, s49\n" \
    "v_mfma_f32_16x16x32_bf16 v[28:31], a[212:215], v[64:67], v[28:31]\ns_add_u32 s69, s70, -768\nbuffer_load_dwordx4 v1, s[64:67], s69 offen offset:2048 sc1 lds\n" \
    "v_mfma_f32_16x16x32_bf16 v[40:43], a[212:215], v[68:71], v[40:43]\ns_and_b32 s77, s77, 0x2000\ns_add_u32 s77, s77, s76\ns_add_u32 s77, s77, s60\n" \
    "v_mfma_f32_16x16x32_bf16 v[32:35], v[228:231], v[64:67], v[32:35]\ns_cmp_lt_u32 s50, 63\ns_cselect_b32 s74, s77, s74\n" \
    "v_mfma_f32_16x16x32_bf16 v[44:47], v[228:231], v[68:71], v[44:47]\ns_waitcnt lgkmcnt(2)\nv_mfma_f32_16x16x32_bf16 v[24:27], a[216:219], v[72:75], v[24:27]\n" \
    "ds_read_b128 v[56:59], v3 offset:51200\nv_mfma_f32_16x16x32_bf16 v[36:39], a[216:219], v[76:79], v[36:39]\nds_read_b128 v[60:63], v3 offset:59392\n" \
    "v_mfma_f32_16x16x32_bf16 v[28:31], a[220:223], v[72:75], v[28:31]\ns_add_u32 s69, s70, -1664\nbuffer_load_dwordx4 v1, s[64:67], s69 offen offset:3072 sc1 lds\n" \
    "v_mfma_f32_16x16x32_bf16 v[40:43], a[220:223], v[76:79], v[40:43]\nv_mfma_f32_16x16x32_bf16 v[32:35], v[232:235], v[72:75], v[32:35]\n" \
    "buffer_store_dwordx4 v[16:19], v8, s[64:67], s74 offen sc1\nv_mfma_f32_16x16x32_bf16 v[44:47], v[232:235], v[76:79], v[44:47]\ns_waitcnt lgkmcnt(2)\n" \
    "v_mfma_f32_16x16x32_bf16 v[24:27], a[224:227], v[48:51], v[24:27]\nds_read_b128 v[64:67], v2 offset:52224\n" \
    "v_mfma_f32_16x16x32_bf16 v[36:39], a[224:227], v[52:55], v[36:39]\nds_read_b128 v[68:71], v2 offset:60416\n" \
    "v_mfma_f32_16x16x32_bf16 v[28:31], a[228:231], v[48:51], v[28:31]\nds_read_b128 v[72:75], v3 offset:52224\n" \
    "v_mfma_f32_16x16x32_bf16 v[40:43], a[228:231], v[52:55], v[40:43]\nds_read_b128 v[76:79], v3 offset:60416\n" \
    "v_mfma_f32_16x16x32_bf16 v[32:35], v[236:239], v[48:51], v[32:35]\nv_mfma_f32_16x16x32_bf16 v[44:47], v[236:239], v[52:55], v[44:47]\ns_waitcnt lgkmcnt(4)\n" \
    "v_mfma_f32_16x16x32_bf16 v[32:35], v[240:243], v[56:59], v[32:35]\ns_xor_b32 s75, s71, 0x2000\ns_add_u32 s76, s75, 0x1000\n" \
    "v_mfma_f32_16x16x32_bf16 v[44:47], v[240:243], v[60:63], v[44:47]\ns_waitcnt lgkmcnt(2)\nv_mfma_f32_16x16x32_bf16 v[32:35], v[244:247], v[64:67], v[32:35]\n" \
    "s_waitcnt lgkmcnt(0)\ns_barrier\nv_mfma_f32_16x16x32_bf16 v[44:47], v[244:247], v[68:71], v[44:47]\nds_read_b128 v[48:51], v2 offset:0\n" \
    "v_mfma_f32_16x16x32_bf16 v[32:35], v[248:251], v[72:75], v[32:35]\nds_read_b128 v[52:55], v2 offset:8192\n" \
    "v_mfma_f32_16x16x32_bf16 v[44:47], v[248:251], v[76:79], v[44:47]\ns_mov_b32 s54, s70\ns_add_u32 s50, s50, 1\n" \
    "v_mfma_f32_16x16x32_bf16 v[24:27], a[232:235], v[56:59], v[24:27]\ns_nop 1\nv_mfma_f32_16x16x32_bf16 v[24:27], a[240:243], v[64:67], v[24:27]\ns_nop 0\n" \
    "v_cvt_pk_bf16_f32 v80, v32, v33\nv_cvt_pk_bf16_f32 v81, v34, v35\nv_mfma_f32_16x16x32_bf16 v[24:27], a[248:251], v[72:75], v[24:27]\nv_cvt_pk_bf16_f32 v82, v44, v45\n" \
    "v_cvt_pk_bf16_f32 v83, v46, v47\nv_mfma_f32_16x16x32_bf16 v[28:31], a[236:239], v[56:59], v[28:31]\nv_permlane16_swap_b32 v80, v82\n" \
    "v_mfma_f32_16x16x32_bf16 v[28:31], a[244:247], v[64:67], v[28:31]\nds_read_b128 v[56:59], v3 offset:0\nv_mfma_f32_16x16x32_bf16 v[28:31], a[252:255], v[72:75], v[28:31]\n" \
    "v_permlane16_swap_b32 v81, v83\nv_mfma_f32_16x16x32_bf16 v[36:39], a[232:235], v[60:63], v[36:39]\nbuffer_store_dwordx4 v[80:83], v4, s[64:67], s71 offen sc1\n" \
    "v_mfma_f32_16x16x32_bf16 v[36:39], a[240:243], v[68:71], v[36:39]\nv_cvt_pk_bf16_f32 v84, v24, v25\nv_cvt_pk_bf16_f32 v85, v26, v27\n" \
    "v_mfma_f32_16x16x32_bf16 v[36:39], a[248:251], v[76:79], v[36:39]\ns_nop 0\nv_cvt_pk_bf16_f32 v86, v28, v29\nv_cvt_pk_bf16_f32 v87, v30, v31\n" \
    "v_mfma_f32_16x16x32_bf16 v[40:43], a[236:239], v[60:63], v[40:43]\nbuffer_store_dwordx4 v[84:87], v8, s[64:67], s75 offen sc1\n" \
    "v_mfma_f32_16x16x32_bf16 v[40:43], a[244:247], v[68:71], v[40:43]\nds_read_b128 v[60:63], v3 offset:8192\n" \
    "v_mfma_f32_16x16x32_bf16 v[40:43], a[252:255], v[76:79], v[40:43]\ns_cmp_gt_i32 s41, s99\nv_cvt_pk_bf16_f32 v88, v36, v37\nv_cvt_pk_bf16_f32 v89, v38, v39\ns_nop 4\n" \
    "v_cvt_pk_bf16_f32 v90, v40, v41\nv_cvt_pk_bf16_f32 v91, v42, v43\nbuffer_store_dwordx4 v[88:91], v8, s[64:67], s76 offen sc1\ns_cbranch_scc1 .Lprvgo_%=\n.Lprvslow_%=:\n" \
    "s_waitcnt vmcnt(0) lgkmcnt(0)\ns_cmp_eq_u32 s50, 64\ns_cbranch_scc1 .Lprvdone_%=\ns_barrier\nv_mov_b32 v96, s50\nbuffer_store_dword v96, v5, s[64:67], 0 offen sc1\n" \
    "s_cmp_gt_i32 s51, s97\ns_cbranch_scc0 .Lprvpoll_%=\ns_cmp_gt_i32 s72, s98\ns_cbranch_scc0 .Lprvpoll_%=\ns_cmp_gt_i32 s73, s99\ns_cbranch_scc0 .Lprvpoll_%=\n" \
    "s_branch .Lprvgo_%=\n.Lprvpoll_%=:\ns_mov_b32 s68, 0\ns_cmp_lg_u32 s55, 0\ns_cbranch_scc1 .Lprvjoin_%=\n.Lprvpl2_%=:\ns_cmp_gt_i32 s51, s97\ns_cbranch_scc1 .Lprvpl3_%=\n" \
    "v_lshlrev_b32 v96, 3, v0\nbuffer_load_dword v96, v96, s[64:67], s36 offen sc1\ns_waitcnt vmcnt(0)\nv_cmp_gt_u32 vcc, 32, v96\ns_nop 4\ns_ff1_i32_b64 s51, vcc\n" \
    "s_min_u32 s51, s51, 64\n.Lprvpl3_%=:\nbuffer_load_dword v96, v6, s[64:67], 0 offen sc1\ns_waitcnt vmcnt(0)\nv_readlane_b32 s73, v96, 1\n.Lprvpl4_%=:\n" \
    "v_readlane_b32 s72, v96, 0\ns_and_b32 s72, s72, 127\ns_cmp_gt_i32 s51, s97\ns_cbranch_scc0 .Lprvok_%=\ns_cmp_gt_i32 s72, s98\ns_cbranch_scc0 .Lprvok_%=\n" \
    "s_cmp_gt_i32 s73, s99\ns_cbranch_scc0 .Lprvok_%=\nv_mov_b32 v96, s51\nds_write_b32 v7, v96\n.Lprvjoin_%=:\ns_waitcnt lgkmcnt(0)\ns_barrier\nds_read_b32 v96, v14\n" \
    "s_waitcnt lgkmcnt(0)\nv_readfirstlane_b32 s51, v96\ns_branch .Lprvgo_%=\n.Lprvok_%=:\ns_sleep 5\ns_add_u32 s68, s68, 1\ns_and_b32 s43, s68, 63\ns_cbranch_scc1 .Lprvpl2_%=\n" \
    "s_cmp_gt_u32 s68, 0x200000\ns_cbranch_scc1 .Lprvabort_%=\nbuffer_load_dword v97, off, s[64:67], 0 sc1\ns_waitcnt vmcnt(0)\nv_readfirstlane_b32 s43, v97\n" \
    "s_cmp_eq_u32 s43, 1\ns_cbranch_scc0 .Lprvpl2_%=\ns_endpgm\n.Lprvabort_%=:\nv_mov_b32 v96, 1\nbuffer_store_dword v96, off, s[64:67], 0 sc1\ns_endpgm\n.Lprvdone_%=:\n"
#define ROLE_R \
    "v_mov_b32 v17, 0\ns_add_u32 s70, s50, 2\ns_min_i32 s70, s70, 63\ns_add_u32 s71, s50, -2\ns_mov_b32 s68, 0\ns_branch .Lr9_%=\n.Lr8_%=:\n" \
    "buffer_load_dword v17, v8, s[64:67], 0 offen sc1\ns_waitcnt vmcnt(0)\n.Lr9_%=:\nv_cmp_lt_i32 vcc, s70, v17\ns_nop 3\ns_and_b32 s69, vcc_lo, 0xff\ns_cmp_eq_u32 s69, 0xff\n" \
    "s_cbranch_scc0 .Lr10_%=\ns_mov_b32 s68, 0\ns_add_u32 s69, s50, 2\ns_and_b32 s69, s69, s41\ns_lshl_b32 s69, s69, 19\ns_add_u32 s69, s69, s35\ns_add_u32 s84, s48, s69\n" \
    "s_lshr_b32 s84, s84, 8\ns_xor_b32 s84, s84, s49\ns_and_b32 s84, s84, 0x2000\ns_add_u32 s84, s84, s69\n.Lr13_%=:\ns_add_u32 s85, s84, 0\n" \
    "buffer_load_dwordx4 v[104:107], v2, s[64:67], s85 offen offset:0 sc1\nbuffer_load_dwordx4 v[108:111], v2, s[64:67], s85 offen offset:1024 sc1\n" \
    "buffer_load_dwordx4 v[112:115], v2, s[64:67], s85 offen offset:2048 sc1\nbuffer_load_dwordx4 v[116:119], v2, s[64:67], s85 offen offset:3072 sc1\n" \
    "buffer_load_dwordx4 v[72:75], v3, s[64:67], s85 offen offset:0 sc1\nbuffer_load_dwordx4 v[76:79], v3, s[64:67], s85 offen offset:1024 sc1\n" \
    "buffer_load_dwordx4 v[80:83], v3, s[64:67], s85 offen offset:2048 sc1\nbuffer_load_dwordx4 v[84:87], v3, s[64:67], s85 offen offset:3072 sc1\ns_add_u32 s85, s84, 16384\n" \
    "buffer_load_dwordx4 v[88:91], v2, s[64:67], s85 offen offset:0 sc1\nbuffer_load_dwordx4 v[92:95], v2, s[64:67], s85 offen offset:1024 sc1\n" \
    "buffer_load_dwordx4 v[96:99], v2, s[64:67], s85 offen offset:2048 sc1\n.Lr11_%=:\nv_mov_b32 v22, 0\ns_waitcnt vmcnt(10)\n" \
    "v_mfma_f32_16x16x32_bf16 v[24:27], v[104:107], a[0:3], 0\nbuffer_load_dwordx4 v[100:103], v2, s[64:67], s85 offen offset:3072 sc1\n" \
    "v_mfma_f32_16x16x32_bf16 v[28:31], v[104:107], a[16:19], 0\ns_waitcnt vmcnt(10)\nv_mfma_f32_16x16x32_bf16 v[24:27], v[108:111], a[4:7], v[24:27]\n" \
    "buffer_load_dwordx4 v[104:107], v3, s[64:67], s85 offen offset:0 sc1\nv_mfma_f32_16x16x32_bf16 v[28:31], v[108:111], a[20:23], v[28:31]\ns_waitcnt vmcnt(10)\n" \
    "v_mfma_f32_16x16x32_bf16 v[24:27], v[112:115], a[8:11], v[24:27]\nbuffer_load_dwordx4 v[108:111], v3, s[64:67], s85 offen offset:1024 sc1\n" \
    "v_mfma_f32_16x16x32_bf16 v[28:31], v[112:115], a[24:27], v[28:31]\ns_waitcnt vmcnt(10)\nv_mfma_f32_16x16x32_bf16 v[24:27], v[116:119], a[12:15], v[24:27]\n" \
    "buffer_load_dwordx4 v[112:115], v3, s[64:67], s85 offen offset:2048 sc1\nv_mfma_f32_16x16x32_bf16 v[28:31], v[116:119], a[28:31], v[28:31]\ns_waitcnt vmcnt(10)\n" \
    "v_mfma_f32_16x16x32_bf16 v[32:35], v[72:75], a[0:3], 0\nbuffer_load_dwordx4 v[116:119], v3, s[64:67], s85 offen offset:3072 sc1\n" \
    "v_mfma_f32_16x16x32_bf16 v[36:39], v[72:75], a[16:19], 0\ns_waitcnt vmcnt(10)\nv_mfma_f32_16x16x32_bf16 v[32:35], v[76:79], a[4:7], v[32:35]\ns_add_u32 s85, s84, 32768\n" \
    "buffer_load_dwordx4 v[72:75], v2, s[64:67], s85 offen offset:0 sc1\nv_mfma_f32_16x16x32_bf16 v[36:39], v[76:79], a[20:23], v[36:39]\nv_cvt_pk_bf16_f32 v120, v24, v25\n" \
    "v_cvt_pk_bf16_f32 v122, v26, v27\ns_waitcnt vmcnt(10)\nv_mfma_f32_16x16x32_bf16 v[32:35], v[80:83], a[8:11], v[32:35]\n" \
    "buffer_load_dwordx4 v[76:79], v2, s[64:67], s85 offen offset:1024 sc1\nv_mfma_f32_16x16x32_bf16 v[36:39], v[80:83], a[24:27], v[36:39]\nv_cvt_pk_bf16_f32 v152, v28, v29\n" \
    "v_cvt_pk_bf16_f32 v154, v30, v31\ns_waitcnt vmcnt(10)\nv_mfma_f32_16x16x32_bf16 v[32:35], v[84:87], a[12:15], v[32:35]\n" \
    "buffer_load_dwordx4 v[80:83], v2, s[64:67], s85 offen offset:2048 sc1\nv_mfma_f32_16x16x32_bf16 v[36:39], v[84:87], a[28:31], v[36:39]\ns_waitcnt vmcnt(10)\n" \
    "v_mfma_f32_16x16x32_bf16 v[24:27], v[88:91], a[32:35], 0\nbuffer_load_dwordx4 v[84:87], v2, s[64:67], s85 offen offset:3072 sc1\n" \
    "v_mfma_f32_16x16x32_bf16 v[28:31], v[88:91], a[48:51], 0\ns_waitcnt vmcnt(10)\nv_mfma_f32_16x16x32_bf16 v[24:27], v[92:95], a[36:39], v[24:27]\n" \
    "buffer_load_dwordx4 v[88:91], v3, s[64:67], s85 offen offset:0 sc1\nv_mfma_f32_16x16x32_bf16 v[28:31], v[92:95], a[52:55], v[28:31]\nv_cvt_pk_bf16_f32 v121, v32, v33\n" \
    "v_cvt_pk_bf16_f32 v123, v34, v35\ns_waitcnt vmcnt(10)\nv_mfma_f32_16x16x32_bf16 v[24:27], v[96:99], a[40:43], v[24:27]\n" \
    "buffer_load_dwordx4 v[92:95], v3, s[64:67], s85 offen offset:1024 sc1\nv_mfma_f32_16x16x32_bf16 v[28:31], v[96:99], a[56:59], v[28:31]\nv_cvt_pk_bf16_f32 v153, v36, v37\n" \
    "v_cvt_pk_bf16_f32 v155, v38, v39\ns_waitcnt vmcnt(10)\nv_mfma_f32_16x16x32_bf16 v[24:27], v[100:103], a[44:47], v[24:27]\n" \
    "buffer_load_dwordx4 v[96:99], v3, s[64:67], s85 offen offset:2048 sc1\nv_mfma_f32_16x16x32_bf16 v[28:31], v[100:103], a[60:63], v[28:31]\ns_waitcnt vmcnt(10)\n" \
    "v_mfma_f32_16x16x32_bf16 v[32:35], v[104:107], a[32:35], 0\nbuffer_load_dwordx4 v[100:103], v3, s[64:67], s85 offen offset:3072 sc1\n" \
    "v_mfma_f32_16x16x32_bf16 v[36:39], v[104:107], a[48:51], 0\ns_waitcnt vmcnt(10)\nv_mfma_f32_16x16x32_bf16 v[32:35], v[108:111], a[36:39], v[32:35]\n" \
    "s_add_u32 s85, s84, 49152\nbuffer_load_dwordx4 v[104:107], v2, s[64:67], s85 offen offset:0 sc1\nv_mfma_f32_16x16x32_bf16 v[36:39], v[108:111], a[52:55], v[36:39]\n" \
    "v_cvt_pk_bf16_f32 v124, v24, v25\nv_cvt_pk_bf16_f32 v126, v26, v27\ns_waitcnt vmcnt(10)\nv_mfma_f32_16x16x32_bf16 v[32:35], v[112:115], a[40:43], v[32:35]\n" \
    "buffer_load_dwordx4 v[108:111], v2, s[64:67], s85 offen offset:1024 sc1\nv_mfma_f32_16x16x32_bf16 v[36:39], v[112:115], a[56:59], v[36:39]\n" \
    "v_cvt_pk_bf16_f32 v156, v28, v29\nv_cvt_pk_bf16_f32 v158, v30, v31\ns_waitcnt vmcnt(10)\nv_mfma_f32_16x16x32_bf16 v[32:35], v[116:119], a[44:47], v[32:35]\n" \
    "buffer_load_dwordx4 v[112:115], v2, s[64:67], s85 offen offset:2048 sc1\nv_mfma_f32_16x16x32_bf16 v[36:39], v[116:119], a[60:63], v[36:39]\ns_waitcnt vmcnt(10)\n" \
    "v_mfma_f32_16x16x32_bf16 v[24:27], v[72:75], a[64:67], 0\nbuffer_load_dwordx4 v[116:119], v2, s[64:67], s85 offen offset:3072 sc1\n" \
    "v_mfma_f32_16x16x32_bf16 v[28:31], v[72:75], a[80:83], 0\ns_waitcnt vmcnt(10)\nv_mfma_f32_16x16x32_bf16 v[24:27], v[76:79], a[68:71], v[24:27]\n" \
    "buffer_load_dwordx4 v[72:75], v3, s[64:67], s85 offen offset:0 sc1\nv_mfma_f32_16x16x32_bf16 v[28:31], v[76:79], a[84:87], v[28:31]\nv_cvt_pk_bf16_f32 v125, v32, v33\n" \
    "v_cvt_pk_bf16_f32 v127, v34, v35\ns_waitcnt vmcnt(10)\nv_mfma_f32_16x16x32_bf16 v[24:27], v[80:83], a[72:75], v[24:27]\n" \
    "buffer_load_dwordx4 v[76:79], v3, s[64:67], s85 offen offset:1024 sc1\nv_mfma_f32_16x16x32_bf16 v[28:31], v[80:83], a[88:91], v[28:31]\nv_cvt_pk_bf16_f32 v157, v36, v37\n" \
    "v_cvt_pk_bf16_f32 v159, v38, v39\ns_waitcnt vmcnt(10)\nv_mfma_f32_16x16x32_bf16 v[24:27], v[84:87], a[76:79], v[24:27]\n" \
    "buffer_load_dwordx4 v[80:83], v3, s[64:67], s85 offen offset:2048 sc1\nv_mfma_f32_16x16x32_bf16 v[28:31], v[84:87], a[92:95], v[28:31]\ns_waitcnt vmcnt(10)\n" \
    "v_mfma_f32_16x16x32_bf16 v[32:35], v[88:91], a[64:67], 0\nbuffer_load_dwordx4 v[84:87], v3, s[64:67], s85 offen offset:3072 sc1\n" \
    "v_mfma_f32_16x16x32_bf16 v[36:39], v[88:91], a[80:83], 0\ns_waitcnt vmcnt(10)\nv_mfma_f32_16x16x32_bf16 v[32:35], v[92:95], a[68:71], v[32:35]\ns_add_u32 s85, s84, 65536\n" \
    "buffer_load_dwordx4 v[88:91], v2, s[64:67], s85 offen offset:0 sc1\nv_mfma_f32_16x16x32_bf16 v[36:39], v[92:95], a[84:87], v[36:39]\nv_cvt_pk_bf16_f32 v128, v24, v25\n" \
    "v_cvt_pk_bf16_f32 v130, v26, v27\ns_waitcnt vmcnt(10)\nv_mfma_f32_16x16x32_bf16 v[32:35], v[96:99], a[72:75], v[32:35]\n" \
    "buffer_load_dwordx4 v[92:95], v2, s[64:67], s85 offen offset:1024 sc1\nv_mfma_f32_16x16x32_bf16 v[36:39], v[96:99], a[88:91], v[36:39]\nv_cvt_pk_bf16_f32 v160, v28, v29\n" \
    "v_cvt_pk_bf16_f32 v162, v30, v31\ns_waitcnt vmcnt(10)\nv_mfma_f32_16x16x32_bf16 v[32:35], v[100:103], a[76:79], v[32:35]\n" \
    "buffer_load_dwordx4 v[96:99], v2, s[64:67], s85 offen offset:2048 sc1\nv_mfma_f32_16x16x32_bf16 v[36:39], v[100:103], a[92:95], v[36:39]\ns_waitcnt vmcnt(10)\n" \
    "v_mfma_f32_16x16x32_bf16 v[24:27], v[104:107], a[96:99], 0\nbuffer_load_dwordx4 v[100:103], v2, s[64:67], s85 offen offset:3072 sc1\n" \
    "v_mfma_f32_16x16x32_bf16 v[28:31], v[104:107], a[112:115], 0\ns_waitcnt vmcnt(10)\nv_mfma_f32_16x16x32_bf16 v[24:27], v[108:111], a[100:103], v[24:27]\n" \
    "buffer_load_dwordx4 v[104:107], v3, s[64:67], s85 offen offset:0 sc1\nv_mfma_f32_16x16x32_bf16 v[28:31], v[108:111], a[116:119], v[28:31]\n" \
    "v_cvt_pk_bf16_f32 v129, v32, v33\nv_cvt_pk_bf16_f32 v131, v34, v35\ns_waitcnt vmcnt(10)\nv_mfma_f32_16x16x32_bf16 v[24:27], v[112:115], a[104:107], v[24:27]\n" \
    "buffer_load_dwordx4 v[108:111], v3, s[64:67], s85 offen offset:1024 sc1\nv_mfma_f32_16x16x32_bf16 v[28:31], v[112:115], a[120:123], v[28:31]\n" \
    "v_cvt_pk_bf16_f32 v161, v36, v37\nv_cvt_pk_bf16_f32 v163, v38, v39\ns_waitcnt vmcnt(10)\nv_mfma_f32_16x16x32_bf16 v[24:27], v[116:119], a[108:111], v[24:27]\n" \
    "buffer_load_dwordx4 v[112:115], v3, s[64:67], s85 offen offset:2048 sc1\nv_mfma_f32_16x16x32_bf16 v[28:31], v[116:119], a[124:127], v[28:31]\ns_waitcnt vmcnt(10)\n" \
    "v_mfma_f32_16x16x32_bf16 v[32:35], v[72:75], a[96:99], 0\nbuffer_load_dwordx4 v[116:119], v3, s[64:67], s85 offen offset:3072 sc1\n" \
    "v_mfma_f32_16x16x32_bf16 v[36:39], v[72:75], a[112:115], 0\ns_waitcnt vmcnt(10)\nv_mfma_f32_16x16x32_bf16 v[32:35], v[76:79], a[100:103], v[32:35]\n" \
    "s_add_u32 s85, s84, 81920\nbuffer_load_dwordx4 v[72:75], v2, s[64:67], s85 offen offset:0 sc1\nv_mfma_f32_16x16x32_bf16 v[36:39], v[76:79], a[116:119], v[36:39]\n" \
    "v_cvt_pk_bf16_f32 v132, v24, v25\nv_cvt_pk_bf16_f32 v134, v26, v27\ns_waitcnt vmcnt(10)\nv_mfma_f32_16x16x32_bf16 v[32:35], v[80:83], a[104:107], v[32:35]\n" \
    "buffer_load_dwordx4 v[76:79], v2, s[64:67], s85 offen offset:1024 sc1\nv_mfma_f32_16x16x32_bf16 v[36:39], v[80:83], a[120:123], v[36:39]\nv_cvt_pk_bf16_f32 v164, v28, v29\n" \
    "v_cvt_pk_bf16_f32 v166, v30, v31\ns_waitcnt vmcnt(10)\nv_mfma_f32_16x16x32_bf16 v[32:35], v[84:87], a[108:111], v[32:35]\n" \
    "buffer_load_dwordx4 v[80:83], v2, s[64:67], s85 offen offset:2048 sc1\nv_mfma_f32_16x16x32_bf16 v[36:39], v[84:87], a[124:127], v[36:39]\ns_waitcnt vmcnt(10)\n" \
    "v_mfma_f32_16x16x32_bf16 v[24:27], v[88:91], a[128:131], 0\nbuffer_load_dwordx4 v[84:87], v2, s[64:67], s85 offen offset:3072 sc1\n" \
    "v_mfma_f32_16x16x32_bf16 v[28:31], v[88:91], a[144:147], 0\ns_waitcnt vmcnt(10)\nv_mfma_f32_16x16x32_bf16 v[24:27], v[92:95], a[132:135], v[24:27]\n" \
    "buffer_load_dwordx4 v[88:91], v3, s[64:67], s85 offen offset:0 sc1\nv_mfma_f32_16x16x32_bf16 v[28:31], v[92:95], a[148:151], v[28:31]\nv_cvt_pk_bf16_f32 v133, v32, v33\n" \
    "v_cvt_pk_bf16_f32 v135, v34, v35\ns_waitcnt vmcnt(10)\nv_mfma_f32_16x16x32_bf16 v[24:27], v[96:99], a[136:139], v[24:27]\n" \
    "buffer_load_dwordx4 v[92:95], v3, s[64:67], s85 offen offset:1024 sc1\nv_mfma_f32_16x16x32_bf16 v[28:31], v[96:99], a[152:155], v[28:31]\nv_cvt_pk_bf16_f32 v165, v36, v37\n" \
    "v_cvt_pk_bf16_f32 v167, v38, v39\ns_waitcnt vmcnt(10)\nv_mfma_f32_16x16x32_bf16 v[24:27], v[100:103], a[140:143], v[24:27]\n" \
    "buffer_load_dwordx4 v[96:99], v3, s[64:67], s85 offen offset:2048 sc1\nv_mfma_f32_16x16x32_bf16 v[28:31], v[100:103], a[156:159], v[28:31]\ns_waitcnt vmcnt(10)\n" \
    "v_mfma_f32_16x16x32_bf16 v[32:35], v[104:107], a[128:131], 0\nbuffer_load_dwordx4 v[100:103], v3, s[64:67], s85 offen offset:3072 sc1\n" \
    "v_mfma_f32_16x16x32_bf16 v[36:39], v[104:107], a[144:147], 0\ns_waitcnt vmcnt(10)\nv_mfma_f32_16x16x32_bf16 v[32:35], v[108:111], a[132:135], v[32:35]\n" \
    "s_add_u32 s85, s84, 98304\nbuffer_load_dwordx4 v[104:107], v2, s[64:67], s85 offen offset:0 sc1\nv_mfma_f32_16x16x32_bf16 v[36:39], v[108:111], a[148:151], v[36:39]\n" \
    "v_cvt_pk_bf16_f32 v136, v24, v25\nv_cvt_pk_bf16_f32 v138, v26, v27\ns_waitcnt vmcnt(10)\nv_mfma_f32_16x16x32_bf16 v[32:35], v[112:115], a[136:139], v[32:35]\n" \
    "buffer_load_dwordx4 v[108:111], v2, s[64:67], s85 offen offset:1024 sc1\nv_mfma_f32_16x16x32_bf16 v[36:39], v[112:115], a[152:155], v[36:39]\n" \
    "v_cvt_pk_bf16_f32 v168, v28, v29\nv_cvt_pk_bf16_f32 v170, v30, v31\ns_waitcnt vmcnt(10)\nv_mfma_f32_16x16x32_bf16 v[32:35], v[116:119], a[140:143], v[32:35]\n" \
    "buffer_load_dwordx4 v[112:115], v2, s[64:67], s85 offen offset:2048 sc1\nv_mfma_f32_16x16x32_bf16 v[36:39], v[116:119], a[156:159], v[36:39]\ns_waitcnt vmcnt(10)\n" \
    "v_mfma_f32_16x16x32_bf16 v[24:27], v[72:75], a[160:163], 0\nbuffer_load_dwordx4 v[116:119], v2, s[64:67], s85 offen offset:3072 sc1\n" \
    "v_mfma_f32_16x16x32_bf16 v[28:31], v[72:75], a[176:179], 0\ns_waitcnt vmcnt(10)\nv_mfma_f32_16x16x32_bf16 v[24:27], v[76:79], a[164:167], v[24:27]\n" \
    "buffer_load_dwordx4 v[72:75], v3, s[64:67], s85 offen offset:0 sc1\nv_mfma_f32_16x16x32_bf16 v[28:31], v[76:79], a[180:183], v[28:31]\nv_cvt_pk_bf16_f32 v137, v32, v33\n" \
    "v_cvt_pk_bf16_f32 v139, v34, v35\ns_waitcnt vmcnt(10)\nv_mfma_f32_16x16x32_bf16 v[24:27], v[80:83], a[168:171], v[24:27]\n" \
    "buffer_load_dwordx4 v[76:79], v3, s[64:67], s85 offen offset:1024 sc1\nv_mfma_f32_16x16x32_bf16 v[28:31], v[80:83], a[184:187], v[28:31]\nv_cvt_pk_bf16_f32 v169, v36, v37\n" \
    "v_cvt_pk_bf16_f32 v171, v38, v39\ns_waitcnt vmcnt(10)\nv_mfma_f32_16x16x32_bf16 v[24:27], v[84:87], a[172:175], v[24:27]\n" \
    "buffer_load_dwordx4 v[80:83], v3, s[64:67], s85 offen offset:2048 sc1\nv_mfma_f32_16x16x32_bf16 v[28:31], v[84:87], a[188:191], v[28:31]\ns_waitcnt vmcnt(10)\n" \
    "v_mfma_f32_16x16x32_bf16 v[32:35], v[88:91], a[160:163], 0\nbuffer_load_dwordx4 v[84:87], v3, s[64:67], s85 offen offset:3072 sc1\n" \
    "v_mfma_f32_16x16x32_bf16 v[36:39], v[88:91], a[176:179], 0\ns_waitcnt vmcnt(10)\nv_mfma_f32_16x16x32_bf16 v[32:35], v[92:95], a[164:167], v[32:35]\n" \
    "s_add_u32 s85, s84, 114688\nbuffer_load_dwordx4 v[88:91], v2, s[64:67], s85 offen offset:0 sc1\nv_mfma_f32_16x16x32_bf16 v[36:39], v[92:95], a[180:183], v[36:39]\n" \
    "v_cvt_pk_bf16_f32 v140, v24, v25\nv_cvt_pk_bf16_f32 v142, v26, v27\ns_waitcnt vmcnt(10)\nv_mfma_f32_16x16x32_bf16 v[32:35], v[96:99], a[168:171], v[32:35]\n" \
    "buffer_load_dwordx4 v[92:95], v2, s[64:67], s85 offen offset:1024 sc1\nv_mfma_f32_16x16x32_bf16 v[36:39], v[96:99], a[184:187], v[36:39]\nv_cvt_pk_bf16_f32 v172, v28, v29\n" \
    "v_cvt_pk_bf16_f32 v174, v30, v31\ns_waitcnt vmcnt(10)\nv_mfma_f32_16x16x32_bf16 v[32:35], v[100:103], a[172:175], v[32:35]\n" \
    "buffer_load_dwordx4 v[96:99], v2, s[64:67], s85 offen offset:2048 sc1\nv_mfma_f32_16x16x32_bf16 v[36:39], v[100:103], a[188:191], v[36:39]\ns_waitcnt vmcnt(10)\n" \
    "v_mfma_f32_16x16x32_bf16 v[24:27], v[104:107], a[192:195], 0\nbuffer_load_dwordx4 v[100:103], v2, s[64:67], s85 offen offset:3072 sc1\n" \
    "v_mfma_f32_16x16x32_bf16 v[28:31], v[104:107], a[208:211], 0\ns_waitcnt vmcnt(10)\nv_mfma_f32_16x16x32_bf16 v[24:27], v[108:111], a[196:199], v[24:27]\n" \
    "buffer_load_dwordx4 v[104:107], v3, s[64:67], s85 offen offset:0 sc1\nv_mfma_f32_16x16x32_bf16 v[28:31], v[108:111], a[212:215], v[28:31]\n" \
    "v_cvt_pk_bf16_f32 v141, v32, v33\nv_cvt_pk_bf16_f32 v143, v34, v35\ns_waitcnt vmcnt(10)\nv_mfma_f32_16x16x32_bf16 v[24:27], v[112:115], a[200:203], v[24:27]\n" \
    "buffer_load_dwordx4 v[108:111], v3, s[64:67], s85 offen offset:1024 sc1\nv_mfma_f32_16x16x32_bf16 v[28:31], v[112:115], a[216:219], v[28:31]\n" \
    "v_cvt_pk_bf16_f32 v173, v36, v37\nv_cvt_pk_bf16_f32 v175, v38, v39\ns_waitcnt vmcnt(10)\nv_mfma_f32_16x16x32_bf16 v[24:27], v[116:119], a[204:207], v[24:27]\n" \
    "buffer_load_dwordx4 v[112:115], v3, s[64:67], s85 offen offset:2048 sc1\nv_mfma_f32_16x16x32_bf16 v[28:31], v[116:119], a[220:223], v[28:31]\ns_waitcnt vmcnt(10)\n" \
    "v_mfma_f32_16x16x32_bf16 v[32:35], v[72:75], a[192:195], 0\nbuffer_load_dwordx4 v[116:119], v3, s[64:67], s85 offen offset:3072 sc1\n" \
    "v_mfma_f32_16x16x32_bf16 v[36:39], v[72:75], a[208:211], 0\ns_waitcnt vmcnt(10)\nv_mfma_f32_16x16x32_bf16 v[32:35], v[76:79], a[196:199], v[32:35]\n" \
    "v_mfma_f32_16x16x32_bf16 v[36:39], v[76:79], a[212:215], v[36:39]\nv_cvt_pk_bf16_f32 v144, v24, v25\nv_cvt_pk_bf16_f32 v146, v26, v27\ns_waitcnt vmcnt(9)\n" \
    "v_mfma_f32_16x16x32_bf16 v[32:35], v[80:83], a[200:203], v[32:35]\nv_mfma_f32_16x16x32_bf16 v[36:39], v[80:83], a[216:219], v[36:39]\nv_cvt_pk_bf16_f32 v176, v28, v29\n" \
    "v_cvt_pk_bf16_f32 v178, v30, v31\ns_waitcnt vmcnt(8)\nv_mfma_f32_16x16x32_bf16 v[32:35], v[84:87], a[204:207], v[32:35]\n" \
    "v_mfma_f32_16x16x32_bf16 v[36:39], v[84:87], a[220:223], v[36:39]\ns_waitcnt vmcnt(7)\nv_mfma_f32_16x16x32_bf16 v[24:27], v[88:91], a[224:227], 0\n" \
    "v_mfma_f32_16x16x32_bf16 v[28:31], v[88:91], a[240:243], 0\ns_waitcnt vmcnt(6)\nv_mfma_f32_16x16x32_bf16 v[24:27], v[92:95], a[228:231], v[24:27]\n" \
    "v_mfma_f32_16x16x32_bf16 v[28:31], v[92:95], a[244:247], v[28:31]\ns_nop 0\nv_cvt_pk_bf16_f32 v145, v32, v33\nv_cvt_pk_bf16_f32 v147, v34, v35\ns_waitcnt vmcnt(5)\n" \
    "v_mfma_f32_16x16x32_bf16 v[24:27], v[96:99], a[232:235], v[24:27]\nv_mfma_f32_16x16x32_bf16 v[28:31], v[96:99], a[248:251], v[28:31]\nv_cvt_pk_bf16_f32 v177, v36, v37\n" \
    "v_cvt_pk_bf16_f32 v179, v38, v39\ns_waitcnt vmcnt(4)\nv_mfma_f32_16x16x32_bf16 v[24:27], v[100:103], a[236:239], v[24:27]\n" \
    "v_mfma_f32_16x16x32_bf16 v[28:31], v[100:103], a[252:255], v[28:31]\ns_waitcnt vmcnt(3)\nv_mfma_f32_16x16x32_bf16 v[32:35], v[104:107], a[224:227], 0\n" \
    "v_mfma_f32_16x16x32_bf16 v[36:39], v[104:107], a[240:243], 0\ns_waitcnt vmcnt(2)\nv_mfma_f32_16x16x32_bf16 v[32:35], v[108:111], a[228:231], v[32:35]\n" \
    "v_mfma_f32_16x16x32_bf16 v[36:39], v[108:111], a[244:247], v[36:39]\ns_nop 0\nv_cvt_pk_bf16_f32 v148, v24, v25\nv_cvt_pk_bf16_f32 v150, v26, v27\ns_waitcnt vmcnt(1)\n" \
    "v_mfma_f32_16x16x32_bf16 v[32:35], v[112:115], a[232:235], v[32:35]\nv_mfma_f32_16x16x32_bf16 v[36:39], v[112:115], a[248:251], v[36:39]\nv_cvt_pk_bf16_f32 v180, v28, v29\n" \
    "v_cvt_pk_bf16_f32 v182, v30, v31\ns_waitcnt vmcnt(0)\nv_mfma_f32_16x16x32_bf16 v[32:35], v[116:119], a[236:239], v[32:35]\n" \
    "v_mfma_f32_16x16x32_bf16 v[36:39], v[116:119], a[252:255], v[36:39]\ns_nop 6\nv_cvt_pk_bf16_f32 v149, v32, v33\nv_cvt_pk_bf16_f32 v151, v34, v35\n" \
    "v_cvt_pk_bf16_f32 v181, v36, v37\nv_cvt_pk_bf16_f32 v183, v38, v39\ns_sub_u32 s69, s50, s42\ns_lshr_b32 s69, s69, 1\ns_cmp_lg_u32 s69, 4\ns_cbranch_scc1 .Lr14_%=\n" \
    "buffer_load_dword v20, off, s[64:67], s40 sc1\ns_waitcnt vmcnt(0)\nv_readfirstlane_b32 s69, v20\ns_add_u32 s69, s69, s58\ns_lshr_b32 s69, s69, 3\ns_add_u32 s71, s94, 8\n" \
    "s_min_u32 s69, s69, s71\ns_sub_u32 s71, s94, 12\ns_max_u32 s69, s69, s71\ns_sub_u32 s69, s69, s94\ns_sub_u32 s69, s69, s59\ns_add_u32 s55, s55, s69\n" \
    "s_sub_u32 s57, s57, s59\n.Lr14_%=:\ns_add_u32 s50, s50, 2\ns_add_u32 s51, s51, s55\ns_add_u32 s51, s51, s55\ns_memrealtime s[72:73]\ns_waitcnt lgkmcnt(0)\n" \
    "s_mov_b32 s51, s72\ns_add_u32 s69, s50, 0\ns_and_b32 s69, s69, s41\ns_lshl_b32 s69, s69, 20\ns_add_u32 s69, s69, s32\ns_add_u32 s74, s48, s69\ns_lshr_b32 s74, s74, 8\n" \
    "s_xor_b32 s74, s74, s49\ns_and_b32 s74, s74, 0x2000\ns_add_u32 s74, s74, s69\ns_add_u32 s69, s50, 0\ns_and_b32 s69, s69, s41\ns_lshl_b32 s69, s69, 19\n" \
    "s_add_u32 s69, s69, s33\ns_add_u32 s87, s48, s69\ns_lshr_b32 s87, s87, 8\ns_xor_b32 s87, s87, s49\ns_and_b32 s87, s87, 0x2000\ns_add_u32 s87, s87, s69\n" \
    "s_add_u32 s69, s50, 1\ns_and_b32 s69, s69, s41\ns_lshl_b32 s69, s69, 18\ns_add_u32 s69, s69, s34\ns_add_u32 s88, s48, s69\ns_lshr_b32 s88, s88, 8\ns_xor_b32 s88, s88, s49\n" \
    "s_and_b32 s88, s88, 0x2000\ns_add_u32 s88, s88, s69\ns_mov_b32 s68, 0\ns_add_u32 s78, s87, s38\n.Lr15_%=:\ns_sub_u32 s70, s51, 52\n.Lr24_%=:\ns_sleep 1\n" \
    "s_memrealtime s[72:73]\ns_waitcnt lgkmcnt(0)\ns_sub_u32 s72, s72, s70\ns_cmp_lt_i32 s72, 0\ns_cbranch_scc1 .Lr24_%=\nbuffer_load_dword v20, off, s[64:67], s78 sc1\n" \
    "buffer_load_dword v17, v8, s[64:67], 0 offen sc1\ns_waitcnt vmcnt(1)\nv_readfirstlane_b32 s70, v20\ns_memrealtime s[72:73]\ns_cmp_eq_u32 s70, -1\ns_cbranch_scc1 .Lr16_%=\n" \
    "s_lshr_b32 s71, s70, 8\ns_waitcnt lgkmcnt(0)\ns_sub_u32 s71, s72, s71\ns_and_b32 s71, s71, 0xffffff\ns_sub_u32 s51, s72, s71\ns_add_u32 s51, s51, 176\n" \
    "s_and_b32 s70, s70, 128\ns_lshl_b32 s52, s70, 5\ns_xor_b32 s87, s87, s52\ns_mov_b32 s68, 0\ns_sub_u32 s70, s51, s54\n.Lr25_%=:\ns_memrealtime s[72:73]\n" \
    "s_waitcnt lgkmcnt(0)\ns_sub_u32 s72, s72, s70\ns_cmp_lt_i32 s72, 0\ns_cbranch_scc1 .Lr25_%=\nbuffer_load_dwordx4 v[40:43], v0, s[64:67], s74 offen offset:0 sc1\n" \
    "buffer_load_dwordx4 v[44:47], v0, s[64:67], s74 offen offset:1024 sc1\ns_add_u32 s69, s74, 0x4000\nbuffer_load_dwordx4 v[48:51], v0, s[64:67], s69 offen offset:0 sc1\n" \
    "buffer_load_dwordx4 v[52:55], v0, s[64:67], s69 offen offset:1024 sc1\n.Lr26_%=:\ns_memrealtime s[72:73]\ns_waitcnt lgkmcnt(0)\ns_sub_u32 s72, s72, s51\n" \
    "s_cmp_lt_i32 s72, -3\ns_cbranch_scc1 .Lr26_%=\ns_cmp_ge_i32 s72, 0\ns_cbranch_scc1 .Lr27_%=\ns_nop 2\ns_cmp_ge_i32 s72, -1\ns_cbranch_scc1 .Lr27_%=\ns_nop 2\n" \
    "s_cmp_ge_i32 s72, -2\ns_cbranch_scc1 .Lr27_%=\ns_nop 2\n.Lr27_%=:\nbuffer_load_dwordx2 v[116:117], v5, s[64:67], s87 offen offset:0 sc1\ns_nop 5\n" \
    "buffer_load_dwordx2 v[118:119], v5, s[64:67], s87 offen offset:128 sc1\ns_nop 5\nbuffer_load_dwordx4 v[100:103], v0, s[64:67], s87 offen offset:0 sc1\ns_nop 10\n" \
    "buffer_load_dwordx4 v[104:107], v0, s[64:67], s87 offen offset:1024 sc1\ns_nop 10\nbuffer_load_dwordx4 v[108:111], v0, s[64:67], s87 offen offset:2048 sc1\ns_nop 10\n" \
    "buffer_load_dwordx4 v[112:115], v0, s[64:67], s87 offen offset:3072 sc1\n.Lr19_%=:\nv_mov_b64 v[24:25], 0\nv_mov_b64 v[26:27], 0\nv_mov_b64 v[28:29], 0\n" \
    "v_mov_b64 v[30:31], 0\ns_waitcnt vmcnt(6)\nv_max3_u32 v22, v40, v41, v42\nv_max3_u32 v22, v22, v43, v44\nv_max3_u32 v22, v22, v45, v46\nv_max3_u32 v22, v22, v47, v48\n" \
    "v_max3_u32 v22, v22, v49, v50\nv_max3_u32 v22, v22, v51, v52\nv_max3_u32 v22, v22, v53, v54\nv_max_u32 v22, v22, v55\ns_waitcnt vmcnt(4)\n" \
    "v_smfmac_f32_16x16x64_bf16 v[24:27], v[116:119], v[40:47], v4\nv_max3_u32 v22, v22, v116, v117\nv_smfmac_f32_16x16x64_bf16 v[28:31], v[116:119], v[48:55], v4\n" \
    "v_max3_u32 v22, v22, v118, v119\ns_waitcnt vmcnt(3)\nv_smfmac_f32_16x16x64_bf16 v[24:27], v[100:103], v[120:127], v4\nv_max3_u32 v22, v22, v100, v101\n" \
    "v_smfmac_f32_16x16x64_bf16 v[28:31], v[100:103], v[152:159], v4\nv_max3_u32 v22, v22, v102, v103\ns_waitcnt vmcnt(2)\n" \
    "v_smfmac_f32_16x16x64_bf16 v[24:27], v[104:107], v[128:135], v4\nv_max3_u32 v22, v22, v104, v105\nv_smfmac_f32_16x16x64_bf16 v[28:31], v[104:107], v[160:167], v4\n" \
    "v_max3_u32 v22, v22, v106, v107\ns_waitcnt vmcnt(1)\nv_smfmac_f32_16x16x64_bf16 v[24:27], v[108:111], v[136:143], v4\nv_max3_u32 v22, v22, v108, v109\n" \
    "v_smfmac_f32_16x16x64_bf16 v[28:31], v[108:111], v[168:175], v4\nv_max3_u32 v22, v22, v110, v111\ns_waitcnt vmcnt(0)\n" \
    "v_smfmac_f32_16x16x64_bf16 v[24:27], v[112:115], v[144:151], v4\nv_max3_u32 v22, v22, v112, v113\nv_smfmac_f32_16x16x64_bf16 v[28:31], v[112:115], v[176:183], v4\n" \
    "v_max3_u32 v22, v22, v114, v115\nv_cmp_eq_u32 vcc, -1, v22\ns_nop 5\nv_permlane32_swap_b32 v24, v28\nv_permlane32_swap_b32 v25, v29\nv_permlane32_swap_b32 v26, v30\n" \
    "v_permlane32_swap_b32 v27, v31\nv_pk_add_f32 v[24:25], v[24:25], v[28:29]\nv_pk_add_f32 v[26:27], v[26:27], v[30:31]\nds_write2_b32 v6, v24, v25 offset0:0 offset1:36\n" \
    "ds_write2_b32 v6, v26, v27 offset0:72 offset1:108\ns_cbranch_vccnz .Lr20_%=\ns_memrealtime s[72:73]\ns_waitcnt lgkmcnt(0)\ns_barrier\nds_read_b128 v[24:27], v7 offset:0\n" \
    "ds_read_b128 v[28:31], v7 offset:1152\nds_read_b128 v[32:35], v7 offset:2304\nds_read_b128 v[36:39], v7 offset:3456\ns_and_b32 s89, s87, 0x1000\ns_xor_b32 s90, s88, s89\n" \
    "s_sub_u32 s71, s72, s51\ns_sub_u32 s71, s71, s57\ns_cmp_ge_i32 s71, 0\ns_cbranch_scc1 .Lr28_%=\n.Lr29_%=:\ns_xor_b32 s89, s90, 0x1000\ns_waitcnt lgkmcnt(2)\n" \
    "v_pk_add_f32 v[24:25], v[24:25], v[28:29]\nv_pk_add_f32 v[26:27], v[26:27], v[30:31]\ns_waitcnt lgkmcnt(0)\nv_pk_add_f32 v[32:33], v[32:33], v[36:37]\n" \
    "v_pk_add_f32 v[34:35], v[34:35], v[38:39]\nv_pk_add_f32 v[24:25], v[24:25], v[32:33]\nv_pk_add_f32 v[26:27], v[26:27], v[34:35]\nv_cvt_pk_bf16_f32 v248, v24, v25\n" \
    "v_cvt_pk_bf16_f32 v249, v26, v27\ns_nop 0\nv_permlane32_swap_b32 v248, v250\nv_permlane32_swap_b32 v249, v251\ns_mov_b32 exec_hi, 0\n" \
    "buffer_store_dwordx4 v[248:251], v0, s[64:67], s90 offen sc1\ns_cmp_ge_i32 s71, 0\ns_cbranch_scc1 .Lr31_%=\n.Lr30_%=:\n" \
    "buffer_store_dwordx4 v[248:251], v0, s[64:67], s89 offen sc1\ns_mov_b32 exec_hi, -1\ns_add_u32 s70, s50, 2\ns_min_i32 s70, s70, 63\ns_add_u32 s71, s50, -2\n" \
    "s_mov_b32 s68, 0\ns_branch .Lr37_%=\n.Lr36_%=:\nbuffer_load_dword v17, v8, s[64:67], 0 offen sc1\ns_waitcnt vmcnt(0)\n.Lr37_%=:\nv_cmp_lt_i32 vcc, s70, v17\ns_nop 3\n" \
    "s_and_b32 s69, vcc_lo, 0xff\ns_cmp_eq_u32 s69, 0xff\ns_cbranch_scc0 .Lr38_%=\ns_mov_b32 s68, 0\ns_add_u32 s69, s50, 2\ns_and_b32 s69, s69, s41\ns_lshl_b32 s69, s69, 19\n" \
    "s_add_u32 s69, s69, s35\ns_add_u32 s84, s48, s69\ns_lshr_b32 s84, s84, 8\ns_xor_b32 s84, s84, s49\ns_and_b32 s84, s84, 0x2000\ns_add_u32 s84, s84, s69\n.Lr41_%=:\n" \
    "s_add_u32 s85, s84, 0\nbuffer_load_dwordx4 v[104:107], v2, s[64:67], s85 offen offset:0 sc1\nbuffer_load_dwordx4 v[108:111], v2, s[64:67], s85 offen offset:1024 sc1\n" \
    "buffer_load_dwordx4 v[112:115], v2, s[64:67], s85 offen offset:2048 sc1\nbuffer_load_dwordx4 v[116:119], v2, s[64:67], s85 offen offset:3072 sc1\n" \
    "buffer_load_dwordx4 v[72:75], v3, s[64:67], s85 offen offset:0 sc1\nbuffer_load_dwordx4 v[76:79], v3, s[64:67], s85 offen offset:1024 sc1\n" \
    "buffer_load_dwordx4 v[80:83], v3, s[64:67], s85 offen offset:2048 sc1\nbuffer_load_dwordx4 v[84:87], v3, s[64:67], s85 offen offset:3072 sc1\ns_add_u32 s85, s84, 16384\n" \
    "buffer_load_dwordx4 v[88:91], v2, s[64:67], s85 offen offset:0 sc1\nbuffer_load_dwordx4 v[92:95], v2, s[64:67], s85 offen offset:1024 sc1\n" \
    "buffer_load_dwordx4 v[96:99], v2, s[64:67], s85 offen offset:2048 sc1\n.Lr39_%=:\nv_mov_b32 v22, 0\ns_waitcnt vmcnt(10)\n" \
    "v_mfma_f32_16x16x32_bf16 v[24:27], v[104:107], a[0:3], 0\nbuffer_load_dwordx4 v[100:103], v2, s[64:67], s85 offen offset:3072 sc1\n" \
    "v_mfma_f32_16x16x32_bf16 v[28:31], v[104:107], a[16:19], 0\ns_add_u32 s69, s50, 2\ns_and_b32 s69, s69, s41\ns_lshl_b32 s69, s69, 20\ns_add_u32 s69, s69, s32\n" \
    "s_add_u32 s74, s48, s69\ns_lshr_b32 s74, s74, 8\ns_xor_b32 s74, s74, s49\ns_waitcnt vmcnt(10)\nv_mfma_f32_16x16x32_bf16 v[24:27], v[108:111], a[4:7], v[24:27]\n" \
    "buffer_load_dwordx4 v[104:107], v3, s[64:67], s85 offen offset:0 sc1\nv_mfma_f32_16x16x32_bf16 v[28:31], v[108:111], a[20:23], v[28:31]\ns_and_b32 s74, s74, 0x2000\n" \
    "s_add_u32 s74, s74, s69\ns_add_u32 s69, s50, 2\ns_and_b32 s69, s69, s41\ns_lshl_b32 s69, s69, 19\ns_add_u32 s69, s69, s33\ns_add_u32 s87, s48, s69\ns_waitcnt vmcnt(10)\n" \
    "v_mfma_f32_16x16x32_bf16 v[24:27], v[112:115], a[8:11], v[24:27]\nbuffer_load_dwordx4 v[108:111], v3, s[64:67], s85 offen offset:1024 sc1\n" \
    "v_mfma_f32_16x16x32_bf16 v[28:31], v[112:115], a[24:27], v[28:31]\ns_lshr_b32 s87, s87, 8\ns_xor_b32 s87, s87, s49\ns_and_b32 s87, s87, 0x2000\ns_add_u32 s87, s87, s69\n" \
    "s_add_u32 s69, s50, 3\ns_and_b32 s69, s69, s41\ns_lshl_b32 s69, s69, 18\ns_waitcnt vmcnt(10)\nv_mfma_f32_16x16x32_bf16 v[24:27], v[116:119], a[12:15], v[24:27]\n" \
    "buffer_load_dwordx4 v[112:115], v3, s[64:67], s85 offen offset:2048 sc1\nv_mfma_f32_16x16x32_bf16 v[28:31], v[116:119], a[28:31], v[28:31]\ns_add_u32 s69, s69, s34\n" \
    "s_add_u32 s88, s48, s69\ns_lshr_b32 s88, s88, 8\ns_xor_b32 s88, s88, s49\ns_and_b32 s88, s88, 0x2000\ns_add_u32 s88, s88, s69\ns_add_u32 s69, s50, 0\ns_waitcnt vmcnt(10)\n" \
    "v_mfma_f32_16x16x32_bf16 v[32:35], v[72:75], a[0:3], 0\nbuffer_load_dwordx4 v[116:119], v3, s[64:67], s85 offen offset:3072 sc1\n" \
    "v_mfma_f32_16x16x32_bf16 v[36:39], v[72:75], a[16:19], 0\ns_lshl_b32 s69, s69, 20\ns_add_u32 s69, s69, s36\ns_add_u32 s75, s48, s69\ns_lshr_b32 s75, s75, 8\n" \
    "s_xor_b32 s75, s75, s49\ns_and_b32 s75, s75, 0x2000\ns_add_u32 s75, s75, s69\ns_waitcnt vmcnt(10)\nv_mfma_f32_16x16x32_bf16 v[32:35], v[76:79], a[4:7], v[32:35]\n" \
    "s_add_u32 s85, s84, 32768\nbuffer_load_dwordx4 v[72:75], v2, s[64:67], s85 offen offset:0 sc1\nv_mfma_f32_16x16x32_bf16 v[36:39], v[76:79], a[20:23], v[36:39]\n" \
    "v_cvt_pk_bf16_f32 v184, v24, v25\nv_cvt_pk_bf16_f32 v186, v26, v27\ns_add_u32 s76, s75, 0x4000\ns_add_u32 s77, s75, s37\ns_xor_b32 s79, s75, 0x2000\ns_add_u32 s82, s50, 0\n" \
    "s_lshl_b32 s80, s82, 16\ns_waitcnt vmcnt(10)\nv_mfma_f32_16x16x32_bf16 v[32:35], v[80:83], a[8:11], v[32:35]\n" \
    "buffer_load_dwordx4 v[76:79], v2, s[64:67], s85 offen offset:1024 sc1\nv_mfma_f32_16x16x32_bf16 v[36:39], v[80:83], a[24:27], v[36:39]\nv_cvt_pk_bf16_f32 v216, v28, v29\n" \
    "v_cvt_pk_bf16_f32 v218, v30, v31\ns_waitcnt vmcnt(10)\nv_mfma_f32_16x16x32_bf16 v[32:35], v[84:87], a[12:15], v[32:35]\n" \
    "buffer_load_dwordx4 v[80:83], v2, s[64:67], s85 offen offset:2048 sc1\nv_mfma_f32_16x16x32_bf16 v[36:39], v[84:87], a[28:31], v[36:39]\ns_waitcnt vmcnt(10)\n" \
    "v_mfma_f32_16x16x32_bf16 v[24:27], v[88:91], a[32:35], 0\nbuffer_load_dwordx4 v[84:87], v2, s[64:67], s85 offen offset:3072 sc1\n" \
    "v_mfma_f32_16x16x32_bf16 v[28:31], v[88:91], a[48:51], 0\ns_waitcnt vmcnt(10)\nv_mfma_f32_16x16x32_bf16 v[24:27], v[92:95], a[36:39], v[24:27]\n" \
    "buffer_load_dwordx4 v[88:91], v3, s[64:67], s85 offen offset:0 sc1\nv_mfma_f32_16x16x32_bf16 v[28:31], v[92:95], a[52:55], v[28:31]\nv_cvt_pk_bf16_f32 v185, v32, v33\n" \
    "v_cvt_pk_bf16_f32 v187, v34, v35\ns_waitcnt vmcnt(10)\nv_mfma_f32_16x16x32_bf16 v[24:27], v[96:99], a[40:43], v[24:27]\n" \
    "buffer_load_dwordx4 v[92:95], v3, s[64:67], s85 offen offset:1024 sc1\nv_mfma_f32_16x16x32_bf16 v[28:31], v[96:99], a[56:59], v[28:31]\nv_cvt_pk_bf16_f32 v217, v36, v37\n" \
    "v_cvt_pk_bf16_f32 v219, v38, v39\ns_waitcnt vmcnt(10)\nv_mfma_f32_16x16x32_bf16 v[24:27], v[100:103], a[44:47], v[24:27]\n" \
    "buffer_load_dwordx4 v[96:99], v3, s[64:67], s85 offen offset:2048 sc1\nv_mfma_f32_16x16x32_bf16 v[28:31], v[100:103], a[60:63], v[28:31]\ns_waitcnt vmcnt(10)\n" \
    "v_mfma_f32_16x16x32_bf16 v[32:35], v[104:107], a[32:35], 0\nbuffer_load_dwordx4 v[100:103], v3, s[64:67], s85 offen offset:3072 sc1\n" \
    "v_mfma_f32_16x16x32_bf16 v[36:39], v[104:107], a[48:51], 0\ns_waitcnt vmcnt(10)\nv_mfma_f32_16x16x32_bf16 v[32:35], v[108:111], a[36:39], v[32:35]\n" \
    "s_add_u32 s85, s84, 49152\nbuffer_load_dwordx4 v[104:107], v2, s[64:67], s85 offen offset:0 sc1\nv_mfma_f32_16x16x32_bf16 v[36:39], v[108:111], a[52:55], v[36:39]\n" \
    "v_cvt_pk_bf16_f32 v188, v24, v25\nv_cvt_pk_bf16_f32 v190, v26, v27\ns_waitcnt vmcnt(10)\nv_mfma_f32_16x16x32_bf16 v[32:35], v[112:115], a[40:43], v[32:35]\n" \
    "buffer_load_dwordx4 v[108:111], v2, s[64:67], s85 offen offset:1024 sc1\nv_mfma_f32_16x16x32_bf16 v[36:39], v[112:115], a[56:59], v[36:39]\n" \
    "v_cvt_pk_bf16_f32 v220, v28, v29\nv_cvt_pk_bf16_f32 v222, v30, v31\ns_waitcnt vmcnt(10)\nv_mfma_f32_16x16x32_bf16 v[32:35], v[116:119], a[44:47], v[32:35]\n" \
    "buffer_load_dwordx4 v[112:115], v2, s[64:67], s85 offen offset:2048 sc1\nv_mfma_f32_16x16x32_bf16 v[36:39], v[116:119], a[60:63], v[36:39]\ns_waitcnt vmcnt(10)\n" \
    "v_mfma_f32_16x16x32_bf16 v[24:27], v[72:75], a[64:67], 0\nbuffer_load_dwordx4 v[116:119], v2, s[64:67], s85 offen offset:3072 sc1\n" \
    "v_mfma_f32_16x16x32_bf16 v[28:31], v[72:75], a[80:83], 0\ns_waitcnt vmcnt(10)\nv_mfma_f32_16x16x32_bf16 v[24:27], v[76:79], a[68:71], v[24:27]\n" \
    "buffer_load_dwordx4 v[72:75], v3, s[64:67], s85 offen offset:0 sc1\nv_mfma_f32_16x16x32_bf16 v[28:31], v[76:79], a[84:87], v[28:31]\nv_cvt_pk_bf16_f32 v189, v32, v33\n" \
    "v_cvt_pk_bf16_f32 v191, v34, v35\ns_waitcnt vmcnt(10)\nv_mfma_f32_16x16x32_bf16 v[24:27], v[80:83], a[72:75], v[24:27]\n" \
    "buffer_load_dwordx4 v[76:79], v3, s[64:67], s85 offen offset:1024 sc1\nv_mfma_f32_16x16x32_bf16 v[28:31], v[80:83], a[88:91], v[28:31]\nv_cvt_pk_bf16_f32 v221, v36, v37\n" \
    "v_cvt_pk_bf16_f32 v223, v38, v39\ns_waitcnt vmcnt(10)\nv_mfma_f32_16x16x32_bf16 v[24:27], v[84:87], a[76:79], v[24:27]\n" \
    "buffer_load_dwordx4 v[80:83], v3, s[64:67], s85 offen offset:2048 sc1\nv_mfma_f32_16x16x32_bf16 v[28:31], v[84:87], a[92:95], v[28:31]\ns_waitcnt vmcnt(10)\n" \
    "v_mfma_f32_16x16x32_bf16 v[32:35], v[88:91], a[64:67], 0\nbuffer_load_dwordx4 v[84:87], v3, s[64:67], s85 offen offset:3072 sc1\n" \
    "v_mfma_f32_16x16x32_bf16 v[36:39], v[88:91], a[80:83], 0\ns_waitcnt vmcnt(10)\nv_mfma_f32_16x16x32_bf16 v[32:35], v[92:95], a[68:71], v[32:35]\ns_add_u32 s85, s84, 65536\n" \
    "buffer_load_dwordx4 v[88:91], v2, s[64:67], s85 offen offset:0 sc1\nv_mfma_f32_16x16x32_bf16 v[36:39], v[92:95], a[84:87], v[36:39]\nv_cvt_pk_bf16_f32 v192, v24, v25\n" \
    "v_cvt_pk_bf16_f32 v194, v26, v27\ns_waitcnt vmcnt(10)\nv_mfma_f32_16x16x32_bf16 v[32:35], v[96:99], a[72:75], v[32:35]\n" \
    "buffer_load_dwordx4 v[92:95], v2, s[64:67], s85 offen offset:1024 sc1\nv_mfma_f32_16x16x32_bf16 v[36:39], v[96:99], a[88:91], v[36:39]\nv_cvt_pk_bf16_f32 v224, v28, v29\n" \
    "v_cvt_pk_bf16_f32 v226, v30, v31\ns_waitcnt vmcnt(10)\nv_mfma_f32_16x16x32_bf16 v[32:35], v[100:103], a[76:79], v[32:35]\n" \
    "buffer_load_dwordx4 v[96:99], v2, s[64:67], s85 offen offset:2048 sc1\nv_mfma_f32_16x16x32_bf16 v[36:39], v[100:103], a[92:95], v[36:39]\ns_waitcnt vmcnt(10)\n" \
    "v_mfma_f32_16x16x32_bf16 v[24:27], v[104:107], a[96:99], 0\nbuffer_load_dwordx4 v[100:103], v2, s[64:67], s85 offen offset:3072 sc1\n" \
    "v_mfma_f32_16x16x32_bf16 v[28:31], v[104:107], a[112:115], 0\ns_waitcnt vmcnt(10)\nv_mfma_f32_16x16x32_bf16 v[24:27], v[108:111], a[100:103], v[24:27]\n" \
    "buffer_load_dwordx4 v[104:107], v3, s[64:67], s85 offen offset:0 sc1\nv_mfma_f32_16x16x32_bf16 v[28:31], v[108:111], a[116:119], v[28:31]\n" \
    "v_cvt_pk_bf16_f32 v193, v32, v33\nv_cvt_pk_bf16_f32 v195, v34, v35\ns_waitcnt vmcnt(10)\nv_mfma_f32_16x16x32_bf16 v[24:27], v[112:115], a[104:107], v[24:27]\n" \
    "buffer_load_dwordx4 v[108:111], v3, s[64:67], s85 offen offset:1024 sc1\nv_mfma_f32_16x16x32_bf16 v[28:31], v[112:115], a[120:123], v[28:31]\n" \
    "v_cvt_pk_bf16_f32 v225, v36, v37\nv_cvt_pk_bf16_f32 v227, v38, v39\ns_waitcnt vmcnt(10)\nv_mfma_f32_16x16x32_bf16 v[24:27], v[116:119], a[108:111], v[24:27]\n" \
    "buffer_load_dwordx4 v[112:115], v3, s[64:67], s85 offen offset:2048 sc1\nv_mfma_f32_16x16x32_bf16 v[28:31], v[116:119], a[124:127], v[28:31]\ns_waitcnt vmcnt(10)\n" \
    "v_mfma_f32_16x16x32_bf16 v[32:35], v[72:75], a[96:99], 0\nbuffer_load_dwordx4 v[116:119], v3, s[64:67], s85 offen offset:3072 sc1\n" \
    "v_mfma_f32_16x16x32_bf16 v[36:39], v[72:75], a[112:115], 0\ns_waitcnt vmcnt(10)\nv_mfma_f32_16x16x32_bf16 v[32:35], v[76:79], a[100:103], v[32:35]\n" \
    "s_add_u32 s85, s84, 81920\nbuffer_load_dwordx4 v[72:75], v2, s[64:67], s85 offen offset:0 sc1\nv_mfma_f32_16x16x32_bf16 v[36:39], v[76:79], a[116:119], v[36:39]\n" \
    "v_cvt_pk_bf16_f32 v196, v24, v25\nv_cvt_pk_bf16_f32 v198, v26, v27\ns_waitcnt vmcnt(10)\nv_mfma_f32_16x16x32_bf16 v[32:35], v[80:83], a[104:107], v[32:35]\n" \
    "buffer_load_dwordx4 v[76:79], v2, s[64:67], s85 offen offset:1024 sc1\nv_mfma_f32_16x16x32_bf16 v[36:39], v[80:83], a[120:123], v[36:39]\nv_cvt_pk_bf16_f32 v228, v28, v29\n" \
    "v_cvt_pk_bf16_f32 v230, v30, v31\ns_waitcnt vmcnt(10)\nv_mfma_f32_16x16x32_bf16 v[32:35], v[84:87], a[108:111], v[32:35]\n" \
    "buffer_load_dwordx4 v[80:83], v2, s[64:67], s85 offen offset:2048 sc1\nv_mfma_f32_16x16x32_bf16 v[36:39], v[84:87], a[124:127], v[36:39]\ns_waitcnt vmcnt(10)\n" \
    "v_mfma_f32_16x16x32_bf16 v[24:27], v[88:91], a[128:131], 0\nbuffer_load_dwordx4 v[84:87], v2, s[64:67], s85 offen offset:3072 sc1\n" \
    "v_mfma_f32_16x16x32_bf16 v[28:31], v[88:91], a[144:147], 0\ns_waitcnt vmcnt(10)\nv_mfma_f32_16x16x32_bf16 v[24:27], v[92:95], a[132:135], v[24:27]\n" \
    "buffer_load_dwordx4 v[88:91], v3, s[64:67], s85 offen offset:0 sc1\nv_mfma_f32_16x16x32_bf16 v[28:31], v[92:95], a[148:151], v[28:31]\nv_cvt_pk_bf16_f32 v197, v32, v33\n" \
    "v_cvt_pk_bf16_f32 v199, v34, v35\ns_waitcnt vmcnt(10)\nv_mfma_f32_16x16x32_bf16 v[24:27], v[96:99], a[136:139], v[24:27]\n" \
    "buffer_load_dwordx4 v[92:95], v3, s[64:67], s85 offen offset:1024 sc1\nv_mfma_f32_16x16x32_bf16 v[28:31], v[96:99], a[152:155], v[28:31]\nv_cvt_pk_bf16_f32 v229, v36, v37\n" \
    "v_cvt_pk_bf16_f32 v231, v38, v39\ns_waitcnt vmcnt(10)\nv_mfma_f32_16x16x32_bf16 v[24:27], v[100:103], a[140:143], v[24:27]\n" \
    "buffer_load_dwordx4 v[96:99], v3, s[64:67], s85 offen offset:2048 sc1\nv_mfma_f32_16x16x32_bf16 v[28:31], v[100:103], a[156:159], v[28:31]\ns_waitcnt vmcnt(10)\n" \
    "v_mfma_f32_16x16x32_bf16 v[32:35], v[104:107], a[128:131], 0\nbuffer_load_dwordx4 v[100:103], v3, s[64:67], s85 offen offset:3072 sc1\n" \
    "v_mfma_f32_16x16x32_bf16 v[36:39], v[104:107], a[144:147], 0\ns_waitcnt vmcnt(10)\nv_mfma_f32_16x16x32_bf16 v[32:35], v[108:111], a[132:135], v[32:35]\n" \
    "s_add_u32 s85, s84, 98304\nbuffer_load_dwordx4 v[104:107], v2, s[64:67], s85 offen offset:0 sc1\nv_mfma_f32_16x16x32_bf16 v[36:39], v[108:111], a[148:151], v[36:39]\n" \
    "v_cvt_pk_bf16_f32 v200, v24, v25\nv_cvt_pk_bf16_f32 v202, v26, v27\ns_waitcnt vmcnt(10)\nv_mfma_f32_16x16x32_bf16 v[32:35], v[112:115], a[136:139], v[32:35]\n" \
    "buffer_load_dwordx4 v[108:111], v2, s[64:67], s85 offen offset:1024 sc1\nv_mfma_f32_16x16x32_bf16 v[36:39], v[112:115], a[152:155], v[36:39]\n" \
    "v_cvt_pk_bf16_f32 v232, v28, v29\nv_cvt_pk_bf16_f32 v234, v30, v31\ns_waitcnt vmcnt(10)\nv_mfma_f32_16x16x32_bf16 v[32:35], v[116:119], a[140:143], v[32:35]\n" \
    "buffer_load_dwordx4 v[112:115], v2, s[64:67], s85 offen offset:2048 sc1\nv_mfma_f32_16x16x32_bf16 v[36:39], v[116:119], a[156:159], v[36:39]\ns_waitcnt vmcnt(10)\n" \
    "v_mfma_f32_16x16x32_bf16 v[24:27], v[72:75], a[160:163], 0\nbuffer_load_dwordx4 v[116:119], v2, s[64:67], s85 offen offset:3072 sc1\n" \
    "v_mfma_f32_16x16x32_bf16 v[28:31], v[72:75], a[176:179], 0\nbuffer_load_dword v17, v8, s[64:67], 0 offen sc1\ns_waitcnt vmcnt(11)\n" \
    "v_mfma_f32_16x16x32_bf16 v[24:27], v[76:79], a[164:167], v[24:27]\nbuffer_load_dwordx4 v[72:75], v3, s[64:67], s85 offen offset:0 sc1\n" \
    "v_mfma_f32_16x16x32_bf16 v[28:31], v[76:79], a[180:183], v[28:31]\nv_cvt_pk_bf16_f32 v201, v32, v33\nv_cvt_pk_bf16_f32 v203, v34, v35\ns_waitcnt vmcnt(11)\n" \
    "v_mfma_f32_16x16x32_bf16 v[24:27], v[80:83], a[168:171], v[24:27]\nbuffer_load_dwordx4 v[76:79], v3, s[64:67], s85 offen offset:1024 sc1\n" \
    "v_mfma_f32_16x16x32_bf16 v[28:31], v[80:83], a[184:187], v[28:31]\nv_cvt_pk_bf16_f32 v233, v36, v37\nv_cvt_pk_bf16_f32 v235, v38, v39\ns_waitcnt vmcnt(11)\n" \
    "v_mfma_f32_16x16x32_bf16 v[24:27], v[84:87], a[172:175], v[24:27]\nbuffer_load_dwordx4 v[80:83], v3, s[64:67], s85 offen offset:2048 sc1\n" \
    "v_mfma_f32_16x16x32_bf16 v[28:31], v[84:87], a[188:191], v[28:31]\ns_waitcnt vmcnt(11)\nv_mfma_f32_16x16x32_bf16 v[32:35], v[88:91], a[160:163], 0\n" \
    "buffer_load_dwordx4 v[84:87], v3, s[64:67], s85 offen offset:3072 sc1\nv_mfma_f32_16x16x32_bf16 v[36:39], v[88:91], a[176:179], 0\ns_waitcnt vmcnt(11)\n" \
    "v_mfma_f32_16x16x32_bf16 v[32:35], v[92:95], a[164:167], v[32:35]\ns_add_u32 s85, s84, 114688\nbuffer_load_dwordx4 v[88:91], v2, s[64:67], s85 offen offset:0 sc1\n" \
    "v_mfma_f32_16x16x32_bf16 v[36:39], v[92:95], a[180:183], v[36:39]\nv_cvt_pk_bf16_f32 v204, v24, v25\nv_cvt_pk_bf16_f32 v206, v26, v27\ns_waitcnt vmcnt(11)\n" \
    "v_mfma_f32_16x16x32_bf16 v[32:35], v[96:99], a[168:171], v[32:35]\nbuffer_load_dwordx4 v[92:95], v2, s[64:67], s85 offen offset:1024 sc1\n" \
    "v_mfma_f32_16x16x32_bf16 v[36:39], v[96:99], a[184:187], v[36:39]\nv_cvt_pk_bf16_f32 v236, v28, v29\nv_cvt_pk_bf16_f32 v238, v30, v31\ns_add_u32 s69, s50, 4\n" \
    "s_and_b32 s69, s69, s41\ns_lshl_b32 s69, s69, 19\ns_add_u32 s69, s69, s35\ns_add_u32 s84, s48, s69\ns_waitcnt vmcnt(11)\n" \
    "v_mfma_f32_16x16x32_bf16 v[32:35], v[100:103], a[172:175], v[32:35]\nbuffer_load_dwordx4 v[96:99], v2, s[64:67], s85 offen offset:2048 sc1\n" \
    "v_mfma_f32_16x16x32_bf16 v[36:39], v[100:103], a[188:191], v[36:39]\ns_lshr_b32 s84, s84, 8\ns_xor_b32 s84, s84, s49\ns_and_b32 s84, s84, 0x2000\ns_add_u32 s84, s84, s69\n" \
    "s_waitcnt vmcnt(11)\nv_mfma_f32_16x16x32_bf16 v[24:27], v[104:107], a[192:195], 0\nbuffer_load_dwordx4 v[100:103], v2, s[64:67], s85 offen offset:3072 sc1\n" \
    "v_mfma_f32_16x16x32_bf16 v[28:31], v[104:107], a[208:211], 0\ns_waitcnt vmcnt(11)\nv_mfma_f32_16x16x32_bf16 v[24:27], v[108:111], a[196:199], v[24:27]\n" \
    "buffer_load_dwordx4 v[104:107], v3, s[64:67], s85 offen offset:0 sc1\nv_mfma_f32_16x16x32_bf16 v[28:31], v[108:111], a[212:215], v[28:31]\n" \
    "v_cvt_pk_bf16_f32 v205, v32, v33\nv_cvt_pk_bf16_f32 v207, v34, v35\ns_waitcnt vmcnt(11)\nv_mfma_f32_16x16x32_bf16 v[24:27], v[112:115], a[200:203], v[24:27]\n" \
    "buffer_load_dwordx4 v[108:111], v3, s[64:67], s85 offen offset:1024 sc1\nv_mfma_f32_16x16x32_bf16 v[28:31], v[112:115], a[216:219], v[28:31]\n" \
    "v_cvt_pk_bf16_f32 v237, v36, v37\nv_cvt_pk_bf16_f32 v239, v38, v39\ns_waitcnt vmcnt(11)\nv_mfma_f32_16x16x32_bf16 v[24:27], v[116:119], a[204:207], v[24:27]\n" \
    "buffer_load_dwordx4 v[112:115], v3, s[64:67], s85 offen offset:2048 sc1\nv_mfma_f32_16x16x32_bf16 v[28:31], v[116:119], a[220:223], v[28:31]\ns_waitcnt vmcnt(10)\n" \
    "v_mfma_f32_16x16x32_bf16 v[32:35], v[72:75], a[192:195], 0\nbuffer_load_dwordx4 v[116:119], v3, s[64:67], s85 offen offset:3072 sc1\n" \
    "v_mfma_f32_16x16x32_bf16 v[36:39], v[72:75], a[208:211], 0\ns_mov_b32 s86, 0\nv_mov_b32 v18, 0x7f000000\ns_add_u32 s70, s50, 4\ns_min_i32 s70, s70, 63\n" \
    "s_add_u32 s71, s50, 0\nv_readlane_b32 s69, v17, 9\ns_cmp_gt_i32 s69, s71\ns_cbranch_scc0 .Lr42_%=\ns_mov_b32 s86, 1\nv_mov_b32 v18, v0\n.Lr42_%=:\n" \
    "v_add_u32 v19, 0x1000, v18\ns_waitcnt vmcnt(10)\nv_mfma_f32_16x16x32_bf16 v[32:35], v[76:79], a[196:199], v[32:35]\n" \
    "v_mfma_f32_16x16x32_bf16 v[36:39], v[76:79], a[212:215], v[36:39]\nv_cvt_pk_bf16_f32 v208, v24, v25\nv_cvt_pk_bf16_f32 v210, v26, v27\n" \
    "buffer_load_dwordx4 v[72:75], v18, s[64:67], s75 offen offset:0 sc1\ns_waitcnt vmcnt(10)\nv_mfma_f32_16x16x32_bf16 v[32:35], v[80:83], a[200:203], v[32:35]\n" \
    "v_mfma_f32_16x16x32_bf16 v[36:39], v[80:83], a[216:219], v[36:39]\nv_cvt_pk_bf16_f32 v240, v28, v29\nv_cvt_pk_bf16_f32 v242, v30, v31\n" \
    "buffer_load_dwordx4 v[76:79], v18, s[64:67], s75 offen offset:1024 sc1\ns_waitcnt vmcnt(10)\nv_mfma_f32_16x16x32_bf16 v[32:35], v[84:87], a[204:207], v[32:35]\n" \
    "v_mfma_f32_16x16x32_bf16 v[36:39], v[84:87], a[220:223], v[36:39]\nbuffer_load_dwordx4 v[80:83], v18, s[64:67], s75 offen offset:2048 sc1\ns_waitcnt vmcnt(10)\n" \
    "v_mfma_f32_16x16x32_bf16 v[24:27], v[88:91], a[224:227], 0\nv_mfma_f32_16x16x32_bf16 v[28:31], v[88:91], a[240:243], 0\n" \
    "buffer_load_dwordx4 v[84:87], v18, s[64:67], s75 offen offset:3072 sc1\ns_waitcnt vmcnt(10)\nv_mfma_f32_16x16x32_bf16 v[24:27], v[92:95], a[228:231], v[24:27]\n" \
    "v_mfma_f32_16x16x32_bf16 v[28:31], v[92:95], a[244:247], v[28:31]\nv_cvt_pk_bf16_f32 v209, v32, v33\nv_cvt_pk_bf16_f32 v211, v34, v35\n" \
    "buffer_load_dwordx4 v[88:91], v19, s[64:67], s75 offen offset:0 sc1\ns_waitcnt vmcnt(10)\nv_mfma_f32_16x16x32_bf16 v[24:27], v[96:99], a[232:235], v[24:27]\n" \
    "v_mfma_f32_16x16x32_bf16 v[28:31], v[96:99], a[248:251], v[28:31]\nv_cvt_pk_bf16_f32 v241, v36, v37\nv_cvt_pk_bf16_f32 v243, v38, v39\n" \
    "buffer_load_dwordx4 v[92:95], v19, s[64:67], s75 offen offset:1024 sc1\ns_waitcnt vmcnt(10)\nv_mfma_f32_16x16x32_bf16 v[24:27], v[100:103], a[236:239], v[24:27]\n" \
    "v_mfma_f32_16x16x32_bf16 v[28:31], v[100:103], a[252:255], v[28:31]\nbuffer_load_dwordx4 v[96:99], v19, s[64:67], s75 offen offset:2048 sc1\n" \
    "buffer_load_dwordx2 v[252:253], v11, s[60:63], s80 offen\ns_waitcnt vmcnt(11)\nv_mfma_f32_16x16x32_bf16 v[32:35], v[104:107], a[224:227], 0\n" \
    "v_mfma_f32_16x16x32_bf16 v[36:39], v[104:107], a[240:243], 0\ns_waitcnt vmcnt(10)\nv_mfma_f32_16x16x32_bf16 v[32:35], v[108:111], a[228:231], v[32:35]\n" \
    "v_mfma_f32_16x16x32_bf16 v[36:39], v[108:111], a[244:247], v[36:39]\nv_cvt_pk_bf16_f32 v212, v24, v25\nv_cvt_pk_bf16_f32 v214, v26, v27\ns_waitcnt vmcnt(9)\n" \
    "v_mfma_f32_16x16x32_bf16 v[32:35], v[112:115], a[232:235], v[32:35]\nv_mfma_f32_16x16x32_bf16 v[36:39], v[112:115], a[248:251], v[36:39]\nv_cvt_pk_bf16_f32 v244, v28, v29\n" \
    "v_cvt_pk_bf16_f32 v246, v30, v31\ns_waitcnt vmcnt(8)\nv_mfma_f32_16x16x32_bf16 v[32:35], v[116:119], a[236:239], v[32:35]\n" \
    "v_mfma_f32_16x16x32_bf16 v[36:39], v[116:119], a[252:255], v[36:39]\ns_nop 6\nv_cvt_pk_bf16_f32 v213, v32, v33\nv_cvt_pk_bf16_f32 v215, v34, v35\n" \
    "v_cvt_pk_bf16_f32 v245, v36, v37\nv_cvt_pk_bf16_f32 v247, v38, v39\ns_sub_u32 s69, s50, s42\ns_lshr_b32 s69, s69, 1\ns_cmp_lg_u32 s69, 4\ns_cbranch_scc1 .Lr45_%=\n" \
    "buffer_load_dword v20, off, s[64:67], s40 sc1\ns_waitcnt vmcnt(0)\nv_readfirstlane_b32 s69, v20\ns_add_u32 s69, s69, s58\ns_lshr_b32 s69, s69, 3\ns_add_u32 s71, s94, 8\n" \
    "s_min_u32 s69, s69, s71\ns_sub_u32 s71, s94, 12\ns_max_u32 s69, s69, s71\ns_sub_u32 s69, s69, s94\ns_sub_u32 s69, s69, s59\ns_add_u32 s55, s55, s69\n" \
    "s_sub_u32 s57, s57, s59\n.Lr45_%=:\ns_add_u32 s50, s50, 2\ns_add_u32 s51, s51, s55\ns_add_u32 s51, s51, s55\n.Lr3_%=:\ns_mov_b32 s68, 0\ns_add_u32 s78, s87, s38\n" \
    ".Lr51_%=:\ns_sub_u32 s70, s51, 52\n.Lr60_%=:\ns_sleep 1\ns_memrealtime s[72:73]\ns_waitcnt lgkmcnt(0)\ns_sub_u32 s72, s72, s70\ns_cmp_lt_i32 s72, 0\n" \
    "s_cbranch_scc1 .Lr60_%=\nbuffer_load_dword v20, off, s[64:67], s78 sc1\nbuffer_load_dword v17, v8, s[64:67], 0 offen sc1\ns_waitcnt vmcnt(1)\nv_readfirstlane_b32 s70, v20\n" \
    "s_memrealtime s[72:73]\ns_cmp_eq_u32 s70, -1\ns_cbranch_scc1 .Lr52_%=\ns_lshr_b32 s71, s70, 8\ns_waitcnt lgkmcnt(0)\ns_sub_u32 s71, s72, s71\ns_and_b32 s71, s71, 0xffffff\n" \
    "s_sub_u32 s51, s72, s71\ns_add_u32 s51, s51, 176\ns_and_b32 s70, s70, 128\ns_lshl_b32 s52, s70, 5\ns_xor_b32 s87, s87, s52\ns_mov_b32 s68, 0\ns_sub_u32 s70, s51, s54\n" \
    ".Lr61_%=:\ns_memrealtime s[72:73]\ns_waitcnt lgkmcnt(0)\ns_sub_u32 s72, s72, s70\ns_cmp_lt_i32 s72, 0\ns_cbranch_scc1 .Lr61_%=\n" \
    "buffer_load_dwordx4 v[56:59], v0, s[64:67], s74 offen offset:0 sc1\nbuffer_load_dwordx4 v[60:63], v0, s[64:67], s74 offen offset:1024 sc1\ns_add_u32 s69, s74, 0x4000\n" \
    "buffer_load_dwordx4 v[64:67], v0, s[64:67], s69 offen offset:0 sc1\nbuffer_load_dwordx4 v[68:71], v0, s[64:67], s69 offen offset:1024 sc1\n.Lr62_%=:\n" \
    "s_memrealtime s[72:73]\ns_waitcnt lgkmcnt(0)\ns_sub_u32 s72, s72, s51\ns_cmp_lt_i32 s72, -3\ns_cbranch_scc1 .Lr62_%=\ns_cmp_ge_i32 s72, 0\ns_cbranch_scc1 .Lr63_%=\n" \
    "s_nop 2\ns_cmp_ge_i32 s72, -1\ns_cbranch_scc1 .Lr63_%=\ns_nop 2\ns_cmp_ge_i32 s72, -2\ns_cbranch_scc1 .Lr63_%=\ns_nop 2\n.Lr63_%=:\n" \
    "buffer_load_dwordx2 v[116:117], v5, s[64:67], s87 offen offset:0 sc1\ns_nop 5\nbuffer_load_dwordx2 v[118:119], v5, s[64:67], s87 offen offset:128 sc1\ns_nop 5\n" \
    "buffer_load_dwordx4 v[100:103], v0, s[64:67], s87 offen offset:0 sc1\ns_nop 10\nbuffer_load_dwordx4 v[104:107], v0, s[64:67], s87 offen offset:1024 sc1\ns_nop 10\n" \
    "buffer_load_dwordx4 v[108:111], v0, s[64:67], s87 offen offset:2048 sc1\ns_nop 10\nbuffer_load_dwordx4 v[112:115], v0, s[64:67], s87 offen offset:3072 sc1\n.Lr55_%=:\n" \
    "v_mov_b64 v[24:25], 0\nv_mov_b64 v[26:27], 0\nv_mov_b64 v[28:29], 0\nv_mov_b64 v[30:31], 0\ns_waitcnt vmcnt(6)\nv_max3_u32 v22, v56, v57, v58\n" \
    "v_max3_u32 v22, v22, v59, v60\nv_max3_u32 v22, v22, v61, v62\nv_max3_u32 v22, v22, v63, v64\nv_max3_u32 v22, v22, v65, v66\nv_max3_u32 v22, v22, v67, v68\n" \
    "v_max3_u32 v22, v22, v69, v70\nv_max_u32 v22, v22, v71\ns_waitcnt vmcnt(4)\nv_smfmac_f32_16x16x64_bf16 v[24:27], v[116:119], v[56:63], v4\nv_max3_u32 v22, v22, v116, v117\n" \
    "v_smfmac_f32_16x16x64_bf16 v[28:31], v[116:119], v[64:71], v4\nv_max3_u32 v22, v22, v118, v119\ns_waitcnt vmcnt(3)\n" \
    "v_smfmac_f32_16x16x64_bf16 v[24:27], v[100:103], v[184:191], v4\nv_max3_u32 v22, v22, v100, v101\nv_smfmac_f32_16x16x64_bf16 v[28:31], v[100:103], v[216:223], v4\n" \
    "v_max3_u32 v22, v22, v102, v103\nbuffer_load_dwordx4 v[100:103], v19, s[64:67], s75 offen offset:3072 sc1\ns_waitcnt vmcnt(3)\n" \
    "v_smfmac_f32_16x16x64_bf16 v[24:27], v[104:107], v[192:199], v4\nv_max3_u32 v22, v22, v104, v105\nv_smfmac_f32_16x16x64_bf16 v[28:31], v[104:107], v[224:231], v4\n" \
    "v_max3_u32 v22, v22, v106, v107\nbuffer_load_dwordx4 v[104:107], v18, s[64:67], s76 offen offset:0 sc1\ns_waitcnt vmcnt(3)\n" \
    "v_smfmac_f32_16x16x64_bf16 v[24:27], v[108:111], v[200:207], v4\nv_max3_u32 v22, v22, v108, v109\nv_smfmac_f32_16x16x64_bf16 v[28:31], v[108:111], v[232:239], v4\n" \
    "v_max3_u32 v22, v22, v110, v111\nbuffer_load_dwordx4 v[108:111], v18, s[64:67], s76 offen offset:1024 sc1\ns_waitcnt vmcnt(3)\n" \
    "v_smfmac_f32_16x16x64_bf16 v[24:27], v[112:115], v[208:215], v4\nv_max3_u32 v22, v22, v112, v113\nv_smfmac_f32_16x16x64_bf16 v[28:31], v[112:115], v[240:247], v4\n" \
    "v_max3_u32 v22, v22, v114, v115\nv_cmp_eq_u32 vcc, -1, v22\ns_nop 5\nv_permlane32_swap_b32 v24, v28\nv_permlane32_swap_b32 v25, v29\nv_permlane32_swap_b32 v26, v30\n" \
    "v_permlane32_swap_b32 v27, v31\nv_pk_add_f32 v[24:25], v[24:25], v[28:29]\nv_pk_add_f32 v[26:27], v[26:27], v[30:31]\nds_write2_b32 v6, v24, v25 offset0:0 offset1:36\n" \
    "ds_write2_b32 v6, v26, v27 offset0:72 offset1:108\ns_cbranch_vccnz .Lr56_%=\ns_memrealtime s[72:73]\n" \
    "buffer_load_dwordx4 v[112:115], v18, s[64:67], s76 offen offset:2048 sc1\ns_waitcnt lgkmcnt(0)\ns_barrier\nds_read_b128 v[24:27], v7 offset:0\n" \
    "ds_read_b128 v[28:31], v7 offset:1152\nds_read_b128 v[32:35], v7 offset:2304\nds_read_b128 v[36:39], v7 offset:3456\ns_and_b32 s89, s87, 0x1000\ns_xor_b32 s90, s88, s89\n" \
    "s_sub_u32 s71, s72, s51\ns_sub_u32 s71, s71, s57\ns_cmp_ge_i32 s71, 0\ns_cbranch_scc1 .Lr64_%=\n.Lr65_%=:\ns_xor_b32 s89, s90, 0x1000\ns_waitcnt lgkmcnt(2)\n" \
    "v_pk_add_f32 v[24:25], v[24:25], v[28:29]\nv_pk_add_f32 v[26:27], v[26:27], v[30:31]\ns_waitcnt lgkmcnt(0)\nv_pk_add_f32 v[32:33], v[32:33], v[36:37]\n" \
    "v_pk_add_f32 v[34:35], v[34:35], v[38:39]\nv_pk_add_f32 v[24:25], v[24:25], v[32:33]\nv_pk_add_f32 v[26:27], v[26:27], v[34:35]\nv_cvt_pk_bf16_f32 v248, v24, v25\n" \
    "v_cvt_pk_bf16_f32 v249, v26, v27\ns_nop 0\nv_permlane32_swap_b32 v248, v250\nv_permlane32_swap_b32 v249, v251\ns_mov_b32 exec_hi, 0\n" \
    "buffer_store_dwordx4 v[248:251], v0, s[64:67], s90 offen sc1\ns_cmp_ge_i32 s71, 0\ns_cbranch_scc1 .Lr67_%=\n.Lr66_%=:\n" \
    "buffer_store_dwordx4 v[248:251], v0, s[64:67], s89 offen sc1\ns_mov_b32 exec_hi, -1\n.Lr7_%=:\ns_mov_b32 s68, 0\ns_add_u32 s70, s50, 2\ns_min_i32 s70, s70, 63\n" \
    "s_add_u32 s71, s50, -2\ns_mov_b32 s68, 0\ns_branch .Lr76_%=\n.Lr75_%=:\nbuffer_load_dword v17, v8, s[64:67], 0 offen sc1\ns_waitcnt vmcnt(0)\n.Lr76_%=:\n" \
    "v_cmp_lt_i32 vcc, s70, v17\ns_nop 3\ns_and_b32 s69, vcc_lo, 0xff\ns_cmp_eq_u32 s69, 0xff\ns_cbranch_scc0 .Lr77_%=\ns_mov_b32 s68, 0\ns_cmp_eq_u32 s86, 1\n" \
    "s_cbranch_scc1 .Lr72_%=\ns_add_u32 s70, s50, 2\ns_min_i32 s70, s70, 63\ns_add_u32 s71, s50, -2\ns_mov_b32 s68, 0\ns_branch .Lr79_%=\n.Lr78_%=:\n" \
    "buffer_load_dword v17, v8, s[64:67], 0 offen sc1\ns_waitcnt vmcnt(0)\n.Lr79_%=:\nv_readlane_b32 s69, v17, 9\ns_cmp_gt_i32 s69, s71\ns_cbranch_scc0 .Lr80_%=\n" \
    "s_mov_b32 s68, 0\n.Lr74_%=:\nbuffer_load_dwordx2 v[252:253], v11, s[60:63], s80 offen\nbuffer_load_dwordx4 v[72:75], v0, s[64:67], s75 offen offset:0 sc1\n" \
    "buffer_load_dwordx4 v[76:79], v0, s[64:67], s75 offen offset:1024 sc1\nbuffer_load_dwordx4 v[80:83], v0, s[64:67], s75 offen offset:2048 sc1\n" \
    "buffer_load_dwordx4 v[84:87], v0, s[64:67], s75 offen offset:3072 sc1\nbuffer_load_dwordx4 v[88:91], v1, s[64:67], s75 offen offset:0 sc1\n" \
    "buffer_load_dwordx4 v[92:95], v1, s[64:67], s75 offen offset:1024 sc1\nbuffer_load_dwordx4 v[96:99], v1, s[64:67], s75 offen offset:2048 sc1\n" \
    "buffer_load_dwordx4 v[100:103], v1, s[64:67], s75 offen offset:3072 sc1\nbuffer_load_dwordx4 v[104:107], v0, s[64:67], s76 offen offset:0 sc1\n" \
    "buffer_load_dwordx4 v[108:111], v0, s[64:67], s76 offen offset:1024 sc1\nbuffer_load_dwordx4 v[112:115], v0, s[64:67], s76 offen offset:2048 sc1\n.Lr72_%=:\n" \
    "v_mov_b32 v22, 0\ns_waitcnt vmcnt(10)\nv_mfma_f32_16x16x32_bf16 v[24:27], v[120:123], v[72:75], 0\nbuffer_load_dwordx4 v[116:119], v0, s[64:67], s76 offen offset:3072 sc1\n" \
    "v_mfma_f32_16x16x32_bf16 v[28:31], v[152:155], v[72:75], 0\nv_max3_u32 v22, v22, v72, v73\nv_max3_u32 v22, v22, v74, v75\ns_waitcnt vmcnt(10)\n" \
    "v_mfma_f32_16x16x32_bf16 v[32:35], v[120:123], v[76:79], 0\nbuffer_load_dwordx4 v[72:75], v1, s[64:67], s76 offen offset:0 sc1\n" \
    "v_mfma_f32_16x16x32_bf16 v[36:39], v[152:155], v[76:79], 0\nv_max3_u32 v22, v22, v76, v77\nv_max3_u32 v22, v22, v78, v79\ns_waitcnt vmcnt(10)\n" \
    "v_mfma_f32_16x16x32_bf16 v[24:27], v[124:127], v[80:83], v[24:27]\nbuffer_load_dwordx4 v[76:79], v1, s[64:67], s76 offen offset:1024 sc1\n" \
    "v_mfma_f32_16x16x32_bf16 v[28:31], v[156:159], v[80:83], v[28:31]\nv_max3_u32 v22, v22, v80, v81\nv_max3_u32 v22, v22, v82, v83\ns_waitcnt vmcnt(10)\n" \
    "v_mfma_f32_16x16x32_bf16 v[32:35], v[124:127], v[84:87], v[32:35]\nbuffer_load_dwordx4 v[80:83], v1, s[64:67], s76 offen offset:2048 sc1\n" \
    "v_mfma_f32_16x16x32_bf16 v[36:39], v[156:159], v[84:87], v[36:39]\nv_max3_u32 v22, v22, v84, v85\nv_max3_u32 v22, v22, v86, v87\ns_waitcnt vmcnt(10)\n" \
    "v_mfma_f32_16x16x32_bf16 v[24:27], v[128:131], v[88:91], v[24:27]\nbuffer_load_dwordx4 v[84:87], v1, s[64:67], s76 offen offset:3072 sc1\n" \
    "v_mfma_f32_16x16x32_bf16 v[28:31], v[160:163], v[88:91], v[28:31]\nv_max3_u32 v22, v22, v88, v89\nv_max3_u32 v22, v22, v90, v91\ns_waitcnt vmcnt(10)\n" \
    "v_mfma_f32_16x16x32_bf16 v[32:35], v[128:131], v[92:95], v[32:35]\nbuffer_load_dwordx4 v[88:91], v0, s[64:67], s77 offen offset:0 sc1\n" \
    "v_mfma_f32_16x16x32_bf16 v[36:39], v[160:163], v[92:95], v[36:39]\nv_max3_u32 v22, v22, v92, v93\nv_max3_u32 v22, v22, v94, v95\ns_waitcnt vmcnt(10)\n" \
    "v_mfma_f32_16x16x32_bf16 v[24:27], v[132:135], v[96:99], v[24:27]\nbuffer_load_dwordx4 v[92:95], v0, s[64:67], s77 offen offset:1024 sc1\n" \
    "v_mfma_f32_16x16x32_bf16 v[28:31], v[164:167], v[96:99], v[28:31]\nv_max3_u32 v22, v22, v96, v97\nv_max3_u32 v22, v22, v98, v99\ns_waitcnt vmcnt(10)\n" \
    "v_mfma_f32_16x16x32_bf16 v[32:35], v[132:135], v[100:103], v[32:35]\nbuffer_load_dwordx4 v[96:99], v0, s[64:67], s77 offen offset:2048 sc1\n" \
    "v_mfma_f32_16x16x32_bf16 v[36:39], v[164:167], v[100:103], v[36:39]\nv_max3_u32 v22, v22, v100, v101\nv_max3_u32 v22, v22, v102, v103\ns_waitcnt vmcnt(10)\n" \
    "v_mfma_f32_16x16x32_bf16 v[24:27], v[136:139], v[104:107], v[24:27]\nbuffer_load_dwordx4 v[100:103], v0, s[64:67], s77 offen offset:3072 sc1\n" \
    "v_mfma_f32_16x16x32_bf16 v[28:31], v[168:171], v[104:107], v[28:31]\nv_max3_u32 v22, v22, v104, v105\nv_max3_u32 v22, v22, v106, v107\ns_waitcnt vmcnt(10)\n" \
    "v_mfma_f32_16x16x32_bf16 v[32:35], v[136:139], v[108:111], v[32:35]\ns_add_u32 s85, s84, 0\nbuffer_load_dwordx4 v[104:107], v2, s[64:67], s85 offen offset:0 sc1\n" \
    "v_mfma_f32_16x16x32_bf16 v[36:39], v[168:171], v[108:111], v[36:39]\nv_max3_u32 v22, v22, v108, v109\nv_max3_u32 v22, v22, v110, v111\ns_waitcnt vmcnt(10)\n" \
    "v_mfma_f32_16x16x32_bf16 v[24:27], v[140:143], v[112:115], v[24:27]\nbuffer_load_dwordx4 v[108:111], v2, s[64:67], s85 offen offset:1024 sc1\n" \
    "v_mfma_f32_16x16x32_bf16 v[28:31], v[172:175], v[112:115], v[28:31]\nv_max3_u32 v22, v22, v112, v113\nv_max3_u32 v22, v22, v114, v115\ns_waitcnt vmcnt(10)\n" \
    "v_mfma_f32_16x16x32_bf16 v[32:35], v[140:143], v[116:119], v[32:35]\nbuffer_load_dwordx4 v[112:115], v2, s[64:67], s85 offen offset:2048 sc1\n" \
    "v_mfma_f32_16x16x32_bf16 v[36:39], v[172:175], v[116:119], v[36:39]\nv_max3_u32 v22, v22, v116, v117\nv_max3_u32 v22, v22, v118, v119\ns_waitcnt vmcnt(10)\n" \
    "v_mfma_f32_16x16x32_bf16 v[24:27], v[144:147], v[72:75], v[24:27]\nbuffer_load_dwordx4 v[116:119], v2, s[64:67], s85 offen offset:3072 sc1\n" \
    "v_mfma_f32_16x16x32_bf16 v[28:31], v[176:179], v[72:75], v[28:31]\nv_max3_u32 v22, v22, v72, v73\nv_max3_u32 v22, v22, v74, v75\ns_waitcnt vmcnt(10)\n" \
    "v_mfma_f32_16x16x32_bf16 v[32:35], v[144:147], v[76:79], v[32:35]\nbuffer_load_dwordx4 v[72:75], v3, s[64:67], s85 offen offset:0 sc1\n" \
    "v_mfma_f32_16x16x32_bf16 v[36:39], v[176:179], v[76:79], v[36:39]\nv_max3_u32 v22, v22, v76, v77\nv_max3_u32 v22, v22, v78, v79\ns_waitcnt vmcnt(10)\n" \
    "v_mfma_f32_16x16x32_bf16 v[24:27], v[148:151], v[80:83], v[24:27]\nbuffer_load_dwordx4 v[76:79], v3, s[64:67], s85 offen offset:1024 sc1\n" \
    "v_mfma_f32_16x16x32_bf16 v[28:31], v[180:183], v[80:83], v[28:31]\nv_max3_u32 v22, v22, v80, v81\nv_max3_u32 v22, v22, v82, v83\ns_waitcnt vmcnt(10)\n" \
    "v_mfma_f32_16x16x32_bf16 v[32:35], v[148:151], v[84:87], v[32:35]\nbuffer_load_dwordx4 v[80:83], v3, s[64:67], s85 offen offset:2048 sc1\n" \
    "v_mfma_f32_16x16x32_bf16 v[36:39], v[180:183], v[84:87], v[36:39]\nv_max3_u32 v22, v22, v84, v85\nv_max3_u32 v22, v22, v86, v87\ns_waitcnt vmcnt(10)\n" \
    "v_mfma_f32_16x16x32_bf16 v[24:27], v[40:43], v[88:91], v[24:27]\nbuffer_load_dwordx4 v[84:87], v3, s[64:67], s85 offen offset:3072 sc1\n" \
    "v_mfma_f32_16x16x32_bf16 v[28:31], v[48:51], v[88:91], v[28:31]\nv_max3_u32 v22, v22, v88, v89\nv_max3_u32 v22, v22, v90, v91\ns_waitcnt vmcnt(10)\n" \
    "v_mfma_f32_16x16x32_bf16 v[32:35], v[40:43], v[92:95], v[32:35]\ns_add_u32 s85, s84, 16384\nbuffer_load_dwordx4 v[88:91], v2, s[64:67], s85 offen offset:0 sc1\n" \
    "v_mfma_f32_16x16x32_bf16 v[36:39], v[48:51], v[92:95], v[36:39]\nv_max3_u32 v22, v22, v92, v93\nv_max3_u32 v22, v22, v94, v95\ns_waitcnt vmcnt(10)\n" \
    "v_mfma_f32_16x16x32_bf16 v[24:27], v[44:47], v[96:99], v[24:27]\nbuffer_load_dwordx4 v[92:95], v2, s[64:67], s85 offen offset:1024 sc1\n" \
    "v_mfma_f32_16x16x32_bf16 v[28:31], v[52:55], v[96:99], v[28:31]\nv_max3_u32 v22, v22, v96, v97\nv_max3_u32 v22, v22, v98, v99\ns_waitcnt vmcnt(10)\n" \
    "v_mfma_f32_16x16x32_bf16 v[32:35], v[44:47], v[100:103], v[32:35]\nbuffer_load_dwordx4 v[96:99], v2, s[64:67], s85 offen offset:2048 sc1\n" \
    "v_mfma_f32_16x16x32_bf16 v[36:39], v[52:55], v[100:103], v[36:39]\nv_max3_u32 v22, v22, v100, v101\nv_max3_u32 v22, v22, v102, v103\nv_cmp_eq_u32 vcc, -1, v22\n" \
    "s_cbranch_vccnz .Lr73_%=\nds_write_b128 v9, v[24:27] offset:0\nds_write_b128 v9, v[28:31] offset:64\ns_waitcnt vmcnt(10)\n" \
    "v_mfma_f32_16x16x32_bf16 v[24:27], v[104:107], a[0:3], 0\nbuffer_load_dwordx4 v[100:103], v2, s[64:67], s85 offen offset:3072 sc1\n" \
    "v_mfma_f32_16x16x32_bf16 v[28:31], v[104:107], a[16:19], 0\nds_write_b128 v9, v[32:35] offset:2304\nds_write_b128 v9, v[36:39] offset:2368\ns_waitcnt vmcnt(10)\n" \
    "v_mfma_f32_16x16x32_bf16 v[24:27], v[108:111], a[4:7], v[24:27]\nbuffer_load_dwordx4 v[104:107], v3, s[64:67], s85 offen offset:0 sc1\n" \
    "v_mfma_f32_16x16x32_bf16 v[28:31], v[108:111], a[20:23], v[28:31]\nds_read_b128 v[18:21], v16\ns_waitcnt vmcnt(10)\n" \
    "v_mfma_f32_16x16x32_bf16 v[24:27], v[112:115], a[8:11], v[24:27]\nbuffer_load_dwordx4 v[108:111], v3, s[64:67], s85 offen offset:1024 sc1\n" \
    "v_mfma_f32_16x16x32_bf16 v[28:31], v[112:115], a[24:27], v[28:31]\ns_waitcnt lgkmcnt(0)\ns_barrier\nds_read_b128 v[40:43], v10 offset:0\n" \
    "ds_read_b128 v[44:47], v10 offset:4608\ns_waitcnt vmcnt(10)\nv_mfma_f32_16x16x32_bf16 v[24:27], v[116:119], a[12:15], v[24:27]\n" \
    "buffer_load_dwordx4 v[112:115], v3, s[64:67], s85 offen offset:2048 sc1\nv_mfma_f32_16x16x32_bf16 v[28:31], v[116:119], a[28:31], v[28:31]\n" \
    "ds_read_b128 v[48:51], v10 offset:9216\nds_read_b128 v[52:55], v10 offset:13824\nv_lshlrev_b32 v248, 16, v252\nv_and_b32 v249, 0xffff0000, v252\n" \
    "v_lshlrev_b32 v250, 16, v253\ns_waitcnt vmcnt(10)\nv_mfma_f32_16x16x32_bf16 v[32:35], v[72:75], a[0:3], 0\n" \
    "buffer_load_dwordx4 v[116:119], v3, s[64:67], s85 offen offset:3072 sc1\nv_mfma_f32_16x16x32_bf16 v[36:39], v[72:75], a[16:19], 0\nv_and_b32 v251, 0xffff0000, v253\n" \
    "s_waitcnt vmcnt(10)\nv_mfma_f32_16x16x32_bf16 v[32:35], v[76:79], a[4:7], v[32:35]\ns_add_u32 s85, s84, 32768\n" \
    "buffer_load_dwordx4 v[72:75], v2, s[64:67], s85 offen offset:0 sc1\nv_mfma_f32_16x16x32_bf16 v[36:39], v[76:79], a[20:23], v[36:39]\nv_cvt_pk_bf16_f32 v120, v24, v25\n" \
    "v_cvt_pk_bf16_f32 v122, v26, v27\ns_waitcnt lgkmcnt(0)\nv_add_f32 v40, v40, v44\nv_add_f32 v41, v41, v45\nv_add_f32 v42, v42, v46\nv_add_f32 v43, v43, v47\n" \
    "s_waitcnt vmcnt(10)\nv_mfma_f32_16x16x32_bf16 v[32:35], v[80:83], a[8:11], v[32:35]\nbuffer_load_dwordx4 v[76:79], v2, s[64:67], s85 offen offset:1024 sc1\n" \
    "v_mfma_f32_16x16x32_bf16 v[36:39], v[80:83], a[24:27], v[36:39]\nv_cvt_pk_bf16_f32 v152, v28, v29\nv_cvt_pk_bf16_f32 v154, v30, v31\nv_add_f32 v48, v48, v52\n" \
    "v_add_f32 v49, v49, v53\nv_add_f32 v50, v50, v54\nv_add_f32 v51, v51, v55\nv_add_f32 v40, v40, v48\ns_waitcnt vmcnt(10)\n" \
    "v_mfma_f32_16x16x32_bf16 v[32:35], v[84:87], a[12:15], v[32:35]\nbuffer_load_dwordx4 v[80:83], v2, s[64:67], s85 offen offset:2048 sc1\n" \
    "v_mfma_f32_16x16x32_bf16 v[36:39], v[84:87], a[28:31], v[36:39]\nv_add_f32 v41, v41, v49\nv_add_f32 v42, v42, v50\nv_add_f32 v43, v43, v51\nv_fma_f32 v248, v40, v18, v248\n" \
    "v_fma_f32 v249, v41, v19, v249\nv_fma_f32 v250, v42, v20, v250\nv_fma_f32 v251, v43, v21, v251\ns_waitcnt vmcnt(10)\n" \
    "v_mfma_f32_16x16x32_bf16 v[24:27], v[88:91], a[32:35], 0\nbuffer_load_dwordx4 v[84:87], v2, s[64:67], s85 offen offset:3072 sc1\n" \
    "v_mfma_f32_16x16x32_bf16 v[28:31], v[88:91], a[48:51], 0\nv_cvt_pk_bf16_f32 v40, v248, v249\ns_add_u32 s69, s50, -1\nv_mov_b32 v22, s69\n" \
    "buffer_store_dword v22, v15, s[64:67], 0 offen sc1\nv_cvt_pk_bf16_f32 v41, v250, v251\ns_waitcnt vmcnt(11)\n" \
    "v_mfma_f32_16x16x32_bf16 v[24:27], v[92:95], a[36:39], v[24:27]\nbuffer_load_dwordx4 v[88:91], v3, s[64:67], s85 offen offset:0 sc1\n" \
    "v_mfma_f32_16x16x32_bf16 v[28:31], v[92:95], a[52:55], v[28:31]\nv_cvt_pk_bf16_f32 v121, v32, v33\nv_cvt_pk_bf16_f32 v123, v34, v35\nv_lshlrev_b32 v248, 16, v40\n" \
    "v_and_b32 v249, 0xffff0000, v40\nv_lshlrev_b32 v250, 16, v41\nv_and_b32 v251, 0xffff0000, v41\nv_add_f32 v18, v248, v249\ns_waitcnt vmcnt(11)\n" \
    "v_mfma_f32_16x16x32_bf16 v[24:27], v[96:99], a[40:43], v[24:27]\nbuffer_load_dwordx4 v[92:95], v3, s[64:67], s85 offen offset:1024 sc1\n" \
    "v_mfma_f32_16x16x32_bf16 v[28:31], v[96:99], a[56:59], v[28:31]\nv_cvt_pk_bf16_f32 v153, v36, v37\nv_cvt_pk_bf16_f32 v155, v38, v39\nv_add_f32 v19, v250, v251\n" \
    "v_min_u32 v40, -2, v40\nv_min_u32 v41, -2, v41\nv_add_f32 v18, v18, v19\nv_permlane32_swap_b32 v40, v42\ns_waitcnt vmcnt(11)\n" \
    "v_mfma_f32_16x16x32_bf16 v[24:27], v[100:103], a[44:47], v[24:27]\nbuffer_load_dwordx4 v[96:99], v3, s[64:67], s85 offen offset:2048 sc1\n" \
    "v_mfma_f32_16x16x32_bf16 v[28:31], v[100:103], a[60:63], v[28:31]\nv_permlane32_swap_b32 v41, v43\nbuffer_store_dwordx4 v[40:43], v12, s[64:67], s79 offen sc1\n" \
    "v_add_f32_dpp v19, v18, v18 row_ror:8 row_mask:0xf bank_mask:0xf\ns_waitcnt vmcnt(12)\nv_mfma_f32_16x16x32_bf16 v[32:35], v[104:107], a[32:35], 0\n" \
    "buffer_load_dwordx4 v[100:103], v3, s[64:67], s85 offen offset:3072 sc1\nv_mfma_f32_16x16x32_bf16 v[36:39], v[104:107], a[48:51], 0\n" \
    "v_add_f32_dpp v20, v18, v18 row_ror:8 row_mask:0xf bank_mask:0xf\ns_nop 1\nv_permlane16_swap_b32 v19, v20\nv_add_f32 v18, v19, v20\nv_add_f32 v19, v19, v20\ns_nop 1\n" \
    "v_permlane32_swap_b32 v18, v19\ns_waitcnt vmcnt(12)\nv_mfma_f32_16x16x32_bf16 v[32:35], v[108:111], a[36:39], v[32:35]\ns_add_u32 s85, s84, 49152\n" \
    "buffer_load_dwordx4 v[104:107], v2, s[64:67], s85 offen offset:0 sc1\nv_mfma_f32_16x16x32_bf16 v[36:39], v[108:111], a[52:55], v[36:39]\nv_cvt_pk_bf16_f32 v124, v24, v25\n" \
    "v_cvt_pk_bf16_f32 v126, v26, v27\nv_add_f32 v18, v18, v19\nv_mul_f32 v18, 0x3d000000, v18\nv_sub_f32 v44, v248, v18\nv_sub_f32 v45, v249, v18\nv_sub_f32 v46, v250, v18\n" \
    "s_waitcnt vmcnt(12)\nv_mfma_f32_16x16x32_bf16 v[32:35], v[112:115], a[40:43], v[32:35]\nbuffer_load_dwordx4 v[108:111], v2, s[64:67], s85 offen offset:1024 sc1\n" \
    "v_mfma_f32_16x16x32_bf16 v[36:39], v[112:115], a[56:59], v[36:39]\nv_cvt_pk_bf16_f32 v156, v28, v29\nv_cvt_pk_bf16_f32 v158, v30, v31\nv_sub_f32 v47, v251, v18\n" \
    "v_mul_f32 v19, v44, v44\nv_fmac_f32 v19, v45, v45\nv_fmac_f32 v19, v46, v46\nv_fmac_f32 v19, v47, v47\ns_waitcnt vmcnt(12)\n" \
    "v_mfma_f32_16x16x32_bf16 v[32:35], v[116:119], a[44:47], v[32:35]\nbuffer_load_dwordx4 v[112:115], v2, s[64:67], s85 offen offset:2048 sc1\n" \
    "v_mfma_f32_16x16x32_bf16 v[36:39], v[116:119], a[60:63], v[36:39]\nv_min_u32 v18, -2, v18\nv_add_f32_dpp v20, v19, v19 row_ror:8 row_mask:0xf bank_mask:0xf\n" \
    "v_add_f32_dpp v21, v19, v19 row_ror:8 row_mask:0xf bank_mask:0xf\ns_nop 1\nv_permlane16_swap_b32 v20, v21\nv_add_f32 v19, v20, v21\nv_add_f32 v20, v20, v21\n" \
    "s_waitcnt vmcnt(12)\nv_mfma_f32_16x16x32_bf16 v[24:27], v[72:75], a[64:67], 0\nbuffer_load_dwordx4 v[116:119], v2, s[64:67], s85 offen offset:3072 sc1\n" \
    "v_mfma_f32_16x16x32_bf16 v[28:31], v[72:75], a[80:83], 0\nv_permlane32_swap_b32 v19, v20\nv_add_f32 v19, v19, v20\nv_min_u32 v19, -2, v19\n" \
    "buffer_store_dwordx2 v[18:19], v13, s[64:67], s79 offen sc1\ns_waitcnt vmcnt(13)\nv_mfma_f32_16x16x32_bf16 v[24:27], v[76:79], a[68:71], v[24:27]\n" \
    "buffer_load_dwordx4 v[72:75], v3, s[64:67], s85 offen offset:0 sc1\nv_mfma_f32_16x16x32_bf16 v[28:31], v[76:79], a[84:87], v[28:31]\nv_cvt_pk_bf16_f32 v125, v32, v33\n" \
    "v_cvt_pk_bf16_f32 v127, v34, v35\nbuffer_store_dword v22, v14, s[64:67], 0 offen sc1\ns_cmp_ge_u32 s50, 62\ns_cbranch_scc1 .Lr46_%=\ns_waitcnt vmcnt(14)\n" \
    "v_mfma_f32_16x16x32_bf16 v[24:27], v[80:83], a[72:75], v[24:27]\nbuffer_load_dwordx4 v[76:79], v3, s[64:67], s85 offen offset:1024 sc1\n" \
    "v_mfma_f32_16x16x32_bf16 v[28:31], v[80:83], a[88:91], v[28:31]\nv_cvt_pk_bf16_f32 v157, v36, v37\nv_cvt_pk_bf16_f32 v159, v38, v39\ns_add_u32 s69, s50, 2\n" \
    "s_and_b32 s69, s69, s41\ns_lshl_b32 s69, s69, 20\ns_add_u32 s69, s69, s32\ns_add_u32 s74, s48, s69\ns_waitcnt vmcnt(14)\n" \
    "v_mfma_f32_16x16x32_bf16 v[24:27], v[84:87], a[76:79], v[24:27]\nbuffer_load_dwordx4 v[80:83], v3, s[64:67], s85 offen offset:2048 sc1\n" \
    "v_mfma_f32_16x16x32_bf16 v[28:31], v[84:87], a[92:95], v[28:31]\ns_lshr_b32 s74, s74, 8\ns_xor_b32 s74, s74, s49\ns_and_b32 s74, s74, 0x2000\ns_add_u32 s74, s74, s69\n" \
    "s_add_u32 s69, s50, 2\ns_and_b32 s69, s69, s41\ns_lshl_b32 s69, s69, 19\ns_waitcnt vmcnt(13)\nv_mfma_f32_16x16x32_bf16 v[32:35], v[88:91], a[64:67], 0\n" \
    "buffer_load_dwordx4 v[84:87], v3, s[64:67], s85 offen offset:3072 sc1\nv_mfma_f32_16x16x32_bf16 v[36:39], v[88:91], a[80:83], 0\ns_add_u32 s69, s69, s33\n" \
    "s_add_u32 s87, s48, s69\ns_lshr_b32 s87, s87, 8\ns_xor_b32 s87, s87, s49\ns_and_b32 s87, s87, 0x2000\ns_add_u32 s87, s87, s69\ns_add_u32 s69, s50, 3\ns_waitcnt vmcnt(13)\n" \
    "v_mfma_f32_16x16x32_bf16 v[32:35], v[92:95], a[68:71], v[32:35]\ns_add_u32 s85, s84, 65536\nbuffer_load_dwordx4 v[88:91], v2, s[64:67], s85 offen offset:0 sc1\n" \
    "v_mfma_f32_16x16x32_bf16 v[36:39], v[92:95], a[84:87], v[36:39]\nv_cvt_pk_bf16_f32 v128, v24, v25\nv_cvt_pk_bf16_f32 v130, v26, v27\ns_and_b32 s69, s69, s41\n" \
    "s_lshl_b32 s69, s69, 18\ns_add_u32 s69, s69, s34\ns_add_u32 s88, s48, s69\ns_lshr_b32 s88, s88, 8\ns_waitcnt vmcnt(13)\n" \
    "v_mfma_f32_16x16x32_bf16 v[32:35], v[96:99], a[72:75], v[32:35]\nbuffer_load_dwordx4 v[92:95], v2, s[64:67], s85 offen offset:1024 sc1\n" \
    "v_mfma_f32_16x16x32_bf16 v[36:39], v[96:99], a[88:91], v[36:39]\nv_cvt_pk_bf16_f32 v160, v28, v29\nv_cvt_pk_bf16_f32 v162, v30, v31\ns_xor_b32 s88, s88, s49\n" \
    "s_and_b32 s88, s88, 0x2000\ns_add_u32 s88, s88, s69\ns_add_u32 s69, s50, 0\ns_lshl_b32 s69, s69, 20\ns_waitcnt vmcnt(12)\n" \
    "v_mfma_f32_16x16x32_bf16 v[32:35], v[100:103], a[76:79], v[32:35]\nbuffer_load_dwordx4 v[96:99], v2, s[64:67], s85 offen offset:2048 sc1\n" \
    "v_mfma_f32_16x16x32_bf16 v[36:39], v[100:103], a[92:95], v[36:39]\ns_add_u32 s69, s69, s36\ns_add_u32 s75, s48, s69\ns_lshr_b32 s75, s75, 8\ns_xor_b32 s75, s75, s49\n" \
    "s_and_b32 s75, s75, 0x2000\ns_add_u32 s75, s75, s69\ns_add_u32 s76, s75, 0x4000\ns_waitcnt vmcnt(12)\nv_mfma_f32_16x16x32_bf16 v[24:27], v[104:107], a[96:99], 0\n" \
    "buffer_load_dwordx4 v[100:103], v2, s[64:67], s85 offen offset:3072 sc1\nv_mfma_f32_16x16x32_bf16 v[28:31], v[104:107], a[112:115], 0\ns_add_u32 s77, s75, s37\n" \
    "s_xor_b32 s79, s75, 0x2000\ns_add_u32 s82, s50, 0\ns_lshl_b32 s80, s82, 16\ns_waitcnt vmcnt(12)\nv_mfma_f32_16x16x32_bf16 v[24:27], v[108:111], a[100:103], v[24:27]\n" \
    "buffer_load_dwordx4 v[104:107], v3, s[64:67], s85 offen offset:0 sc1\nv_mfma_f32_16x16x32_bf16 v[28:31], v[108:111], a[116:119], v[28:31]\n" \
    "v_cvt_pk_bf16_f32 v129, v32, v33\nv_cvt_pk_bf16_f32 v131, v34, v35\ns_waitcnt vmcnt(12)\nv_mfma_f32_16x16x32_bf16 v[24:27], v[112:115], a[104:107], v[24:27]\n" \
    "buffer_load_dwordx4 v[108:111], v3, s[64:67], s85 offen offset:1024 sc1\nv_mfma_f32_16x16x32_bf16 v[28:31], v[112:115], a[120:123], v[28:31]\n" \
    "v_cvt_pk_bf16_f32 v161, v36, v37\nv_cvt_pk_bf16_f32 v163, v38, v39\ns_waitcnt vmcnt(12)\nv_mfma_f32_16x16x32_bf16 v[24:27], v[116:119], a[108:111], v[24:27]\n" \
    "buffer_load_dwordx4 v[112:115], v3, s[64:67], s85 offen offset:2048 sc1\nv_mfma_f32_16x16x32_bf16 v[28:31], v[116:119], a[124:127], v[28:31]\ns_waitcnt vmcnt(11)\n" \
    "v_mfma_f32_16x16x32_bf16 v[32:35], v[72:75], a[96:99], 0\nbuffer_load_dwordx4 v[116:119], v3, s[64:67], s85 offen offset:3072 sc1\n" \
    "v_mfma_f32_16x16x32_bf16 v[36:39], v[72:75], a[112:115], 0\ns_waitcnt vmcnt(10)\nv_mfma_f32_16x16x32_bf16 v[32:35], v[76:79], a[100:103], v[32:35]\n" \
    "s_add_u32 s85, s84, 81920\nbuffer_load_dwordx4 v[72:75], v2, s[64:67], s85 offen offset:0 sc1\nv_mfma_f32_16x16x32_bf16 v[36:39], v[76:79], a[116:119], v[36:39]\n" \
    "v_cvt_pk_bf16_f32 v132, v24, v25\nv_cvt_pk_bf16_f32 v134, v26, v27\ns_waitcnt vmcnt(10)\nv_mfma_f32_16x16x32_bf16 v[32:35], v[80:83], a[104:107], v[32:35]\n" \
    "buffer_load_dwordx4 v[76:79], v2, s[64:67], s85 offen offset:1024 sc1\nv_mfma_f32_16x16x32_bf16 v[36:39], v[80:83], a[120:123], v[36:39]\nv_cvt_pk_bf16_f32 v164, v28, v29\n" \
    "v_cvt_pk_bf16_f32 v166, v30, v31\ns_waitcnt vmcnt(10)\nv_mfma_f32_16x16x32_bf16 v[32:35], v[84:87], a[108:111], v[32:35]\n" \
    "buffer_load_dwordx4 v[80:83], v2, s[64:67], s85 offen offset:2048 sc1\nv_mfma_f32_16x16x32_bf16 v[36:39], v[84:87], a[124:127], v[36:39]\ns_waitcnt vmcnt(10)\n" \
    "v_mfma_f32_16x16x32_bf16 v[24:27], v[88:91], a[128:131], 0\nbuffer_load_dwordx4 v[84:87], v2, s[64:67], s85 offen offset:3072 sc1\n" \
    "v_mfma_f32_16x16x32_bf16 v[28:31], v[88:91], a[144:147], 0\ns_waitcnt vmcnt(10)\nv_mfma_f32_16x16x32_bf16 v[24:27], v[92:95], a[132:135], v[24:27]\n" \
    "buffer_load_dwordx4 v[88:91], v3, s[64:67], s85 offen offset:0 sc1\nv_mfma_f32_16x16x32_bf16 v[28:31], v[92:95], a[148:151], v[28:31]\nv_cvt_pk_bf16_f32 v133, v32, v33\n" \
    "v_cvt_pk_bf16_f32 v135, v34, v35\ns_waitcnt vmcnt(10)\nv_mfma_f32_16x16x32_bf16 v[24:27], v[96:99], a[136:139], v[24:27]\n" \
    "buffer_load_dwordx4 v[92:95], v3, s[64:67], s85 offen offset:1024 sc1\nv_mfma_f32_16x16x32_bf16 v[28:31], v[96:99], a[152:155], v[28:31]\nv_cvt_pk_bf16_f32 v165, v36, v37\n" \
    "v_cvt_pk_bf16_f32 v167, v38, v39\ns_waitcnt vmcnt(10)\nv_mfma_f32_16x16x32_bf16 v[24:27], v[100:103], a[140:143], v[24:27]\n" \
    "buffer_load_dwordx4 v[96:99], v3, s[64:67], s85 offen offset:2048 sc1\nv_mfma_f32_16x16x32_bf16 v[28:31], v[100:103], a[156:159], v[28:31]\ns_waitcnt vmcnt(10)\n" \
    "v_mfma_f32_16x16x32_bf16 v[32:35], v[104:107], a[128:131], 0\nbuffer_load_dwordx4 v[100:103], v3, s[64:67], s85 offen offset:3072 sc1\n" \
    "v_mfma_f32_16x16x32_bf16 v[36:39], v[104:107], a[144:147], 0\ns_waitcnt vmcnt(10)\nv_mfma_f32_16x16x32_bf16 v[32:35], v[108:111], a[132:135], v[32:35]\n" \
    "s_add_u32 s85, s84, 98304\nbuffer_load_dwordx4 v[104:107], v2, s[64:67], s85 offen offset:0 sc1\nv_mfma_f32_16x16x32_bf16 v[36:39], v[108:111], a[148:151], v[36:39]\n" \
    "v_cvt_pk_bf16_f32 v136, v24, v25\nv_cvt_pk_bf16_f32 v138, v26, v27\ns_waitcnt vmcnt(10)\nv_mfma_f32_16x16x32_bf16 v[32:35], v[112:115], a[136:139], v[32:35]\n" \
    "buffer_load_dwordx4 v[108:111], v2, s[64:67], s85 offen offset:1024 sc1\nv_mfma_f32_16x16x32_bf16 v[36:39], v[112:115], a[152:155], v[36:39]\n" \
    "v_cvt_pk_bf16_f32 v168, v28, v29\nv_cvt_pk_bf16_f32 v170, v30, v31\ns_waitcnt vmcnt(10)\nv_mfma_f32_16x16x32_bf16 v[32:35], v[116:119], a[140:143], v[32:35]\n" \
    "buffer_load_dwordx4 v[112:115], v2, s[64:67], s85 offen offset:2048 sc1\nv_mfma_f32_16x16x32_bf16 v[36:39], v[116:119], a[156:159], v[36:39]\ns_waitcnt vmcnt(10)\n" \
    "v_mfma_f32_16x16x32_bf16 v[24:27], v[72:75], a[160:163], 0\nbuffer_load_dwordx4 v[116:119], v2, s[64:67], s85 offen offset:3072 sc1\n" \
    "v_mfma_f32_16x16x32_bf16 v[28:31], v[72:75], a[176:179], 0\nbuffer_load_dword v17, v8, s[64:67], 0 offen sc1\ns_waitcnt vmcnt(11)\n" \
    "v_mfma_f32_16x16x32_bf16 v[24:27], v[76:79], a[164:167], v[24:27]\nbuffer_load_dwordx4 v[72:75], v3, s[64:67], s85 offen offset:0 sc1\n" \
    "v_mfma_f32_16x16x32_bf16 v[28:31], v[76:79], a[180:183], v[28:31]\nv_cvt_pk_bf16_f32 v137, v32, v33\nv_cvt_pk_bf16_f32 v139, v34, v35\ns_waitcnt vmcnt(11)\n" \
    "v_mfma_f32_16x16x32_bf16 v[24:27], v[80:83], a[168:171], v[24:27]\nbuffer_load_dwordx4 v[76:79], v3, s[64:67], s85 offen offset:1024 sc1\n" \
    "v_mfma_f32_16x16x32_bf16 v[28:31], v[80:83], a[184:187], v[28:31]\nv_cvt_pk_bf16_f32 v169, v36, v37\nv_cvt_pk_bf16_f32 v171, v38, v39\ns_waitcnt vmcnt(11)\n" \
    "v_mfma_f32_16x16x32_bf16 v[24:27], v[84:87], a[172:175], v[24:27]\nbuffer_load_dwordx4 v[80:83], v3, s[64:67], s85 offen offset:2048 sc1\n" \
    "v_mfma_f32_16x16x32_bf16 v[28:31], v[84:87], a[188:191], v[28:31]\ns_waitcnt vmcnt(11)\nv_mfma_f32_16x16x32_bf16 v[32:35], v[88:91], a[160:163], 0\n" \
    "buffer_load_dwordx4 v[84:87], v3, s[64:67], s85 offen offset:3072 sc1\nv_mfma_f32_16x16x32_bf16 v[36:39], v[88:91], a[176:179], 0\ns_waitcnt vmcnt(11)\n" \
    "v_mfma_f32_16x16x32_bf16 v[32:35], v[92:95], a[164:167], v[32:35]\ns_add_u32 s85, s84, 114688\nbuffer_load_dwordx4 v[88:91], v2, s[64:67], s85 offen offset:0 sc1\n" \
    "v_mfma_f32_16x16x32_bf16 v[36:39], v[92:95], a[180:183], v[36:39]\nv_cvt_pk_bf16_f32 v140, v24, v25\nv_cvt_pk_bf16_f32 v142, v26, v27\ns_waitcnt vmcnt(11)\n" \
    "v_mfma_f32_16x16x32_bf16 v[32:35], v[96:99], a[168:171], v[32:35]\nbuffer_load_dwordx4 v[92:95], v2, s[64:67], s85 offen offset:1024 sc1\n" \
    "v_mfma_f32_16x16x32_bf16 v[36:39], v[96:99], a[184:187], v[36:39]\nv_cvt_pk_bf16_f32 v172, v28, v29\nv_cvt_pk_bf16_f32 v174, v30, v31\ns_add_u32 s69, s50, 4\n" \
    "s_and_b32 s69, s69, s41\ns_lshl_b32 s69, s69, 19\ns_add_u32 s69, s69, s35\ns_add_u32 s84, s48, s69\ns_waitcnt vmcnt(11)\n" \
    "v_mfma_f32_16x16x32_bf16 v[32:35], v[100:103], a[172:175], v[32:35]\nbuffer_load_dwordx4 v[96:99], v2, s[64:67], s85 offen offset:2048 sc1\n" \
    "v_mfma_f32_16x16x32_bf16 v[36:39], v[100:103], a[188:191], v[36:39]\ns_lshr_b32 s84, s84, 8\ns_xor_b32 s84, s84, s49\ns_and_b32 s84, s84, 0x2000\ns_add_u32 s84, s84, s69\n" \
    "s_waitcnt vmcnt(11)\nv_mfma_f32_16x16x32_bf16 v[24:27], v[104:107], a[192:195], 0\nbuffer_load_dwordx4 v[100:103], v2, s[64:67], s85 offen offset:3072 sc1\n" \
    "v_mfma_f32_16x16x32_bf16 v[28:31], v[104:107], a[208:211], 0\ns_waitcnt vmcnt(11)\nv_mfma_f32_16x16x32_bf16 v[24:27], v[108:111], a[196:199], v[24:27]\n" \
    "buffer_load_dwordx4 v[104:107], v3, s[64:67], s85 offen offset:0 sc1\nv_mfma_f32_16x16x32_bf16 v[28:31], v[108:111], a[212:215], v[28:31]\n" \
    "v_cvt_pk_bf16_f32 v141, v32, v33\nv_cvt_pk_bf16_f32 v143, v34, v35\ns_waitcnt vmcnt(11)\nv_mfma_f32_16x16x32_bf16 v[24:27], v[112:115], a[200:203], v[24:27]\n" \
    "buffer_load_dwordx4 v[108:111], v3, s[64:67], s85 offen offset:1024 sc1\nv_mfma_f32_16x16x32_bf16 v[28:31], v[112:115], a[216:219], v[28:31]\n" \
    "v_cvt_pk_bf16_f32 v173, v36, v37\nv_cvt_pk_bf16_f32 v175, v38, v39\ns_waitcnt vmcnt(11)\nv_mfma_f32_16x16x32_bf16 v[24:27], v[116:119], a[204:207], v[24:27]\n" \
    "buffer_load_dwordx4 v[112:115], v3, s[64:67], s85 offen offset:2048 sc1\nv_mfma_f32_16x16x32_bf16 v[28:31], v[116:119], a[220:223], v[28:31]\ns_waitcnt vmcnt(10)\n" \
    "v_mfma_f32_16x16x32_bf16 v[32:35], v[72:75], a[192:195], 0\nbuffer_load_dwordx4 v[116:119], v3, s[64:67], s85 offen offset:3072 sc1\n" \
    "v_mfma_f32_16x16x32_bf16 v[36:39], v[72:75], a[208:211], 0\ns_mov_b32 s86, 0\nv_mov_b32 v18, 0x7f000000\ns_add_u32 s70, s50, 4\ns_min_i32 s70, s70, 63\n" \
    "s_add_u32 s71, s50, 0\nv_readlane_b32 s69, v17, 10\ns_cmp_ge_i32 s69, s71\ns_cbranch_scc0 .Lr81_%=\ns_mov_b32 s86, 1\nv_mov_b32 v18, v0\n.Lr81_%=:\n" \
    "v_add_u32 v19, 0x1000, v18\ns_waitcnt vmcnt(10)\nv_mfma_f32_16x16x32_bf16 v[32:35], v[76:79], a[196:199], v[32:35]\n" \
    "v_mfma_f32_16x16x32_bf16 v[36:39], v[76:79], a[212:215], v[36:39]\nv_cvt_pk_bf16_f32 v144, v24, v25\nv_cvt_pk_bf16_f32 v146, v26, v27\n" \
    "buffer_load_dwordx4 v[72:75], v18, s[64:67], s75 offen offset:0 sc1\ns_waitcnt vmcnt(10)\nv_mfma_f32_16x16x32_bf16 v[32:35], v[80:83], a[200:203], v[32:35]\n" \
    "v_mfma_f32_16x16x32_bf16 v[36:39], v[80:83], a[216:219], v[36:39]\nv_cvt_pk_bf16_f32 v176, v28, v29\nv_cvt_pk_bf16_f32 v178, v30, v31\n" \
    "buffer_load_dwordx4 v[76:79], v18, s[64:67], s75 offen offset:1024 sc1\ns_waitcnt vmcnt(10)\nv_mfma_f32_16x16x32_bf16 v[32:35], v[84:87], a[204:207], v[32:35]\n" \
    "v_mfma_f32_16x16x32_bf16 v[36:39], v[84:87], a[220:223], v[36:39]\nbuffer_load_dwordx4 v[80:83], v18, s[64:67], s75 offen offset:2048 sc1\ns_waitcnt vmcnt(10)\n" \
    "v_mfma_f32_16x16x32_bf16 v[24:27], v[88:91], a[224:227], 0\nv_mfma_f32_16x16x32_bf16 v[28:31], v[88:91], a[240:243], 0\n" \
    "buffer_load_dwordx4 v[84:87], v18, s[64:67], s75 offen offset:3072 sc1\ns_waitcnt vmcnt(10)\nv_mfma_f32_16x16x32_bf16 v[24:27], v[92:95], a[228:231], v[24:27]\n" \
    "v_mfma_f32_16x16x32_bf16 v[28:31], v[92:95], a[244:247], v[28:31]\nv_cvt_pk_bf16_f32 v145, v32, v33\nv_cvt_pk_bf16_f32 v147, v34, v35\n" \
    "buffer_load_dwordx4 v[88:91], v19, s[64:67], s75 offen offset:0 sc1\ns_waitcnt vmcnt(10)\nv_mfma_f32_16x16x32_bf16 v[24:27], v[96:99], a[232:235], v[24:27]\n" \
    "v_mfma_f32_16x16x32_bf16 v[28:31], v[96:99], a[248:251], v[28:31]\nv_cvt_pk_bf16_f32 v177, v36, v37\nv_cvt_pk_bf16_f32 v179, v38, v39\n" \
    "buffer_load_dwordx4 v[92:95], v19, s[64:67], s75 offen offset:1024 sc1\ns_waitcnt vmcnt(10)\nv_mfma_f32_16x16x32_bf16 v[24:27], v[100:103], a[236:239], v[24:27]\n" \
    "v_mfma_f32_16x16x32_bf16 v[28:31], v[100:103], a[252:255], v[28:31]\nbuffer_load_dwordx4 v[96:99], v19, s[64:67], s75 offen offset:2048 sc1\n" \
    "buffer_load_dwordx2 v[252:253], v11, s[60:63], s80 offen\ns_waitcnt vmcnt(11)\nv_mfma_f32_16x16x32_bf16 v[32:35], v[104:107], a[224:227], 0\n" \
    "v_mfma_f32_16x16x32_bf16 v[36:39], v[104:107], a[240:243], 0\ns_waitcnt vmcnt(10)\nv_mfma_f32_16x16x32_bf16 v[32:35], v[108:111], a[228:231], v[32:35]\n" \
    "v_mfma_f32_16x16x32_bf16 v[36:39], v[108:111], a[244:247], v[36:39]\nv_cvt_pk_bf16_f32 v148, v24, v25\nv_cvt_pk_bf16_f32 v150, v26, v27\ns_waitcnt vmcnt(9)\n" \
    "v_mfma_f32_16x16x32_bf16 v[32:35], v[112:115], a[232:235], v[32:35]\nv_mfma_f32_16x16x32_bf16 v[36:39], v[112:115], a[248:251], v[36:39]\nv_cvt_pk_bf16_f32 v180, v28, v29\n" \
    "v_cvt_pk_bf16_f32 v182, v30, v31\ns_waitcnt vmcnt(8)\nv_mfma_f32_16x16x32_bf16 v[32:35], v[116:119], a[236:239], v[32:35]\n" \
    "v_mfma_f32_16x16x32_bf16 v[36:39], v[116:119], a[252:255], v[36:39]\ns_nop 6\nv_cvt_pk_bf16_f32 v149, v32, v33\nv_cvt_pk_bf16_f32 v151, v34, v35\n" \
    "v_cvt_pk_bf16_f32 v181, v36, v37\nv_cvt_pk_bf16_f32 v183, v38, v39\ns_sub_u32 s69, s50, s42\ns_lshr_b32 s69, s69, 1\ns_cmp_lg_u32 s69, 4\ns_cbranch_scc1 .Lr84_%=\n" \
    "buffer_load_dword v20, off, s[64:67], s40 sc1\ns_waitcnt vmcnt(0)\nv_readfirstlane_b32 s69, v20\ns_add_u32 s69, s69, s58\ns_lshr_b32 s69, s69, 3\ns_add_u32 s71, s94, 8\n" \
    "s_min_u32 s69, s69, s71\ns_sub_u32 s71, s94, 12\ns_max_u32 s69, s69, s71\ns_sub_u32 s69, s69, s94\ns_sub_u32 s69, s69, s59\ns_add_u32 s55, s55, s69\n" \
    "s_sub_u32 s57, s57, s59\n.Lr84_%=:\ns_add_u32 s50, s50, 2\ns_add_u32 s51, s51, s55\ns_add_u32 s51, s51, s55\ns_mov_b32 s68, 0\ns_add_u32 s78, s87, s38\n.Lr85_%=:\n" \
    "s_sub_u32 s70, s51, 52\n.Lr94_%=:\ns_sleep 1\ns_memrealtime s[72:73]\ns_waitcnt lgkmcnt(0)\ns_sub_u32 s72, s72, s70\ns_cmp_lt_i32 s72, 0\ns_cbranch_scc1 .Lr94_%=\n" \
    "buffer_load_dword v20, off, s[64:67], s78 sc1\nbuffer_load_dword v17, v8, s[64:67], 0 offen sc1\ns_waitcnt vmcnt(1)\nv_readfirstlane_b32 s70, v20\ns_memrealtime s[72:73]\n" \
    "s_cmp_eq_u32 s70, -1\ns_cbranch_scc1 .Lr86_%=\ns_lshr_b32 s71, s70, 8\ns_waitcnt lgkmcnt(0)\ns_sub_u32 s71, s72, s71\ns_and_b32 s71, s71, 0xffffff\n" \
    "s_sub_u32 s51, s72, s71\ns_add_u32 s51, s51, 176\ns_and_b32 s70, s70, 128\ns_lshl_b32 s52, s70, 5\ns_xor_b32 s87, s87, s52\ns_mov_b32 s68, 0\ns_sub_u32 s70, s51, s54\n" \
    ".Lr95_%=:\ns_memrealtime s[72:73]\ns_waitcnt lgkmcnt(0)\ns_sub_u32 s72, s72, s70\ns_cmp_lt_i32 s72, 0\ns_cbranch_scc1 .Lr95_%=\n" \
    "buffer_load_dwordx4 v[40:43], v0, s[64:67], s74 offen offset:0 sc1\nbuffer_load_dwordx4 v[44:47], v0, s[64:67], s74 offen offset:1024 sc1\ns_add_u32 s69, s74, 0x4000\n" \
    "buffer_load_dwordx4 v[48:51], v0, s[64:67], s69 offen offset:0 sc1\nbuffer_load_dwordx4 v[52:55], v0, s[64:67], s69 offen offset:1024 sc1\n.Lr96_%=:\n" \
    "s_memrealtime s[72:73]\ns_waitcnt lgkmcnt(0)\ns_sub_u32 s72, s72, s51\ns_cmp_lt_i32 s72, -3\ns_cbranch_scc1 .Lr96_%=\ns_cmp_ge_i32 s72, 0\ns_cbranch_scc1 .Lr97_%=\n" \
    "s_nop 2\ns_cmp_ge_i32 s72, -1\ns_cbranch_scc1 .Lr97_%=\ns_nop 2\ns_cmp_ge_i32 s72, -2\ns_cbranch_scc1 .Lr97_%=\ns_nop 2\n.Lr97_%=:\n" \
    "buffer_load_dwordx2 v[116:117], v5, s[64:67], s87 offen offset:0 sc1\ns_nop 5\nbuffer_load_dwordx2 v[118:119], v5, s[64:67], s87 offen offset:128 sc1\ns_nop 5\n" \
    "buffer_load_dwordx4 v[100:103], v0, s[64:67], s87 offen offset:0 sc1\ns_nop 10\nbuffer_load_dwordx4 v[104:107], v0, s[64:67], s87 offen offset:1024 sc1\ns_nop 10\n" \
    "buffer_load_dwordx4 v[108:111], v0, s[64:67], s87 offen offset:2048 sc1\ns_nop 10\nbuffer_load_dwordx4 v[112:115], v0, s[64:67], s87 offen offset:3072 sc1\n.Lr89_%=:\n" \
    "v_mov_b64 v[24:25], 0\nv_mov_b64 v[26:27], 0\nv_mov_b64 v[28:29], 0\nv_mov_b64 v[30:31], 0\ns_waitcnt vmcnt(6)\nv_max3_u32 v22, v40, v41, v42\n" \
    "v_max3_u32 v22, v22, v43, v44\nv_max3_u32 v22, v22, v45, v46\nv_max3_u32 v22, v22, v47, v48\nv_max3_u32 v22, v22, v49, v50\nv_max3_u32 v22, v22, v51, v52\n" \
    "v_max3_u32 v22, v22, v53, v54\nv_max_u32 v22, v22, v55\ns_waitcnt vmcnt(4)\nv_smfmac_f32_16x16x64_bf16 v[24:27], v[116:119], v[40:47], v4\nv_max3_u32 v22, v22, v116, v117\n" \
    "v_smfmac_f32_16x16x64_bf16 v[28:31], v[116:119], v[48:55], v4\nv_max3_u32 v22, v22, v118, v119\ns_waitcnt vmcnt(3)\n" \
    "v_smfmac_f32_16x16x64_bf16 v[24:27], v[100:103], v[120:127], v4\nv_max3_u32 v22, v22, v100, v101\nv_smfmac_f32_16x16x64_bf16 v[28:31], v[100:103], v[152:159], v4\n" \
    "v_max3_u32 v22, v22, v102, v103\nbuffer_load_dwordx4 v[100:103], v19, s[64:67], s75 offen offset:3072 sc1\ns_waitcnt vmcnt(3)\n" \
    "v_smfmac_f32_16x16x64_bf16 v[24:27], v[104:107], v[128:135], v4\nv_max3_u32 v22, v22, v104, v105\nv_smfmac_f32_16x16x64_bf16 v[28:31], v[104:107], v[160:167], v4\n" \
    "v_max3_u32 v22, v22, v106, v107\nbuffer_load_dwordx4 v[104:107], v18, s[64:67], s76 offen offset:0 sc1\ns_waitcnt vmcnt(3)\n" \
    "v_smfmac_f32_16x16x64_bf16 v[24:27], v[108:111], v[136:143], v4\nv_max3_u32 v22, v22, v108, v109\nv_smfmac_f32_16x16x64_bf16 v[28:31], v[108:111], v[168:175], v4\n" \
    "v_max3_u32 v22, v22, v110, v111\nbuffer_load_dwordx4 v[108:111], v18, s[64:67], s76 offen offset:1024 sc1\ns_waitcnt vmcnt(3)\n" \
    "v_smfmac_f32_16x16x64_bf16 v[24:27], v[112:115], v[144:151], v4\nv_max3_u32 v22, v22, v112, v113\nv_smfmac_f32_16x16x64_bf16 v[28:31], v[112:115], v[176:183], v4\n" \
    "v_max3_u32 v22, v22, v114, v115\nv_cmp_eq_u32 vcc, -1, v22\ns_nop 5\nv_permlane32_swap_b32 v24, v28\nv_permlane32_swap_b32 v25, v29\nv_permlane32_swap_b32 v26, v30\n" \
    "v_permlane32_swap_b32 v27, v31\nv_pk_add_f32 v[24:25], v[24:25], v[28:29]\nv_pk_add_f32 v[26:27], v[26:27], v[30:31]\nds_write2_b32 v6, v24, v25 offset0:0 offset1:36\n" \
    "ds_write2_b32 v6, v26, v27 offset0:72 offset1:108\ns_cbranch_vccnz .Lr90_%=\ns_memrealtime s[72:73]\n" \
    "buffer_load_dwordx4 v[112:115], v18, s[64:67], s76 offen offset:2048 sc1\ns_waitcnt lgkmcnt(0)\ns_barrier\nds_read_b128 v[24:27], v7 offset:0\n" \
    "ds_read_b128 v[28:31], v7 offset:1152\nds_read_b128 v[32:35], v7 offset:2304\nds_read_b128 v[36:39], v7 offset:3456\ns_and_b32 s89, s87, 0x1000\ns_xor_b32 s90, s88, s89\n" \
    "s_sub_u32 s71, s72, s51\ns_sub_u32 s71, s71, s57\ns_cmp_ge_i32 s71, 0\ns_cbranch_scc1 .Lr98_%=\n.Lr99_%=:\ns_xor_b32 s89, s90, 0x1000\ns_waitcnt lgkmcnt(2)\n" \
    "v_pk_add_f32 v[24:25], v[24:25], v[28:29]\nv_pk_add_f32 v[26:27], v[26:27], v[30:31]\ns_waitcnt lgkmcnt(0)\nv_pk_add_f32 v[32:33], v[32:33], v[36:37]\n" \
    "v_pk_add_f32 v[34:35], v[34:35], v[38:39]\nv_pk_add_f32 v[24:25], v[24:25], v[32:33]\nv_pk_add_f32 v[26:27], v[26:27], v[34:35]\nv_cvt_pk_bf16_f32 v248, v24, v25\n" \
    "v_cvt_pk_bf16_f32 v249, v26, v27\ns_nop 0\nv_permlane32_swap_b32 v248, v250\nv_permlane32_swap_b32 v249, v251\ns_mov_b32 exec_hi, 0\n" \
    "buffer_store_dwordx4 v[248:251], v0, s[64:67], s90 offen sc1\ns_cmp_ge_i32 s71, 0\ns_cbranch_scc1 .Lr101_%=\n.Lr100_%=:\n" \
    "buffer_store_dwordx4 v[248:251], v0, s[64:67], s89 offen sc1\ns_mov_b32 exec_hi, -1\ns_mov_b32 s68, 0\ns_add_u32 s70, s50, 2\ns_min_i32 s70, s70, 63\n" \
    "s_add_u32 s71, s50, -2\ns_mov_b32 s68, 0\ns_branch .Lr109_%=\n.Lr108_%=:\nbuffer_load_dword v17, v8, s[64:67], 0 offen sc1\ns_waitcnt vmcnt(0)\n.Lr109_%=:\n" \
    "v_cmp_lt_i32 vcc, s70, v17\ns_nop 3\ns_and_b32 s69, vcc_lo, 0xff\ns_cmp_eq_u32 s69, 0xff\ns_cbranch_scc0 .Lr110_%=\ns_mov_b32 s68, 0\ns_cmp_eq_u32 s86, 1\n" \
    "s_cbranch_scc1 .Lr106_%=\ns_add_u32 s70, s50, 2\ns_min_i32 s70, s70, 63\ns_add_u32 s71, s50, -2\ns_mov_b32 s68, 0\ns_branch .Lr112_%=\n.Lr111_%=:\n" \
    "buffer_load_dword v17, v8, s[64:67], 0 offen sc1\ns_waitcnt vmcnt(0)\n.Lr112_%=:\nv_readlane_b32 s69, v17, 9\ns_cmp_gt_i32 s69, s71\ns_cbranch_scc0 .Lr113_%=\n" \
    "s_mov_b32 s68, 0\n.Lr48_%=:\nbuffer_load_dwordx2 v[252:253], v11, s[60:63], s80 offen\nbuffer_load_dwordx4 v[72:75], v0, s[64:67], s75 offen offset:0 sc1\n" \
    "buffer_load_dwordx4 v[76:79], v0, s[64:67], s75 offen offset:1024 sc1\nbuffer_load_dwordx4 v[80:83], v0, s[64:67], s75 offen offset:2048 sc1\n" \
    "buffer_load_dwordx4 v[84:87], v0, s[64:67], s75 offen offset:3072 sc1\nbuffer_load_dwordx4 v[88:91], v1, s[64:67], s75 offen offset:0 sc1\n" \
    "buffer_load_dwordx4 v[92:95], v1, s[64:67], s75 offen offset:1024 sc1\nbuffer_load_dwordx4 v[96:99], v1, s[64:67], s75 offen offset:2048 sc1\n" \
    "buffer_load_dwordx4 v[100:103], v1, s[64:67], s75 offen offset:3072 sc1\nbuffer_load_dwordx4 v[104:107], v0, s[64:67], s76 offen offset:0 sc1\n" \
    "buffer_load_dwordx4 v[108:111], v0, s[64:67], s76 offen offset:1024 sc1\nbuffer_load_dwordx4 v[112:115], v0, s[64:67], s76 offen offset:2048 sc1\n.Lr106_%=:\n" \
    "v_mov_b32 v22, 0\ns_waitcnt vmcnt(10)\nv_mfma_f32_16x16x32_bf16 v[24:27], v[184:187], v[72:75], 0\nbuffer_load_dwordx4 v[116:119], v0, s[64:67], s76 offen offset:3072 sc1\n" \
    "v_mfma_f32_16x16x32_bf16 v[28:31], v[216:219], v[72:75], 0\nv_max3_u32 v22, v22, v72, v73\nv_max3_u32 v22, v22, v74, v75\ns_waitcnt vmcnt(10)\n" \
    "v_mfma_f32_16x16x32_bf16 v[32:35], v[184:187], v[76:79], 0\nbuffer_load_dwordx4 v[72:75], v1, s[64:67], s76 offen offset:0 sc1\n" \
    "v_mfma_f32_16x16x32_bf16 v[36:39], v[216:219], v[76:79], 0\nv_max3_u32 v22, v22, v76, v77\nv_max3_u32 v22, v22, v78, v79\ns_waitcnt vmcnt(10)\n" \
    "v_mfma_f32_16x16x32_bf16 v[24:27], v[188:191], v[80:83], v[24:27]\nbuffer_load_dwordx4 v[76:79], v1, s[64:67], s76 offen offset:1024 sc1\n" \
    "v_mfma_f32_16x16x32_bf16 v[28:31], v[220:223], v[80:83], v[28:31]\nv_max3_u32 v22, v22, v80, v81\nv_max3_u32 v22, v22, v82, v83\ns_waitcnt vmcnt(10)\n" \
    "v_mfma_f32_16x16x32_bf16 v[32:35], v[188:191], v[84:87], v[32:35]\nbuffer_load_dwordx4 v[80:83], v1, s[64:67], s76 offen offset:2048 sc1\n" \
    "v_mfma_f32_16x16x32_bf16 v[36:39], v[220:223], v[84:87], v[36:39]\nv_max3_u32 v22, v22, v84, v85\nv_max3_u32 v22, v22, v86, v87\ns_waitcnt vmcnt(10)\n" \
    "v_mfma_f32_16x16x32_bf16 v[24:27], v[192:195], v[88:91], v[24:27]\nbuffer_load_dwordx4 v[84:87], v1, s[64:67], s76 offen offset:3072 sc1\n" \
    "v_mfma_f32_16x16x32_bf16 v[28:31], v[224:227], v[88:91], v[28:31]\nv_max3_u32 v22, v22, v88, v89\nv_max3_u32 v22, v22, v90, v91\ns_waitcnt vmcnt(10)\n" \
    "v_mfma_f32_16x16x32_bf16 v[32:35], v[192:195], v[92:95], v[32:35]\nbuffer_load_dwordx4 v[88:91], v0, s[64:67], s77 offen offset:0 sc1\n" \
    "v_mfma_f32_16x16x32_bf16 v[36:39], v[224:227], v[92:95], v[36:39]\nv_max3_u32 v22, v22, v92, v93\nv_max3_u32 v22, v22, v94, v95\ns_waitcnt vmcnt(10)\n" \
    "v_mfma_f32_16x16x32_bf16 v[24:27], v[196:199], v[96:99], v[24:27]\nbuffer_load_dwordx4 v[92:95], v0, s[64:67], s77 offen offset:1024 sc1\n" \
    "v_mfma_f32_16x16x32_bf16 v[28:31], v[228:231], v[96:99], v[28:31]\nv_max3_u32 v22, v22, v96, v97\nv_max3_u32 v22, v22, v98, v99\ns_waitcnt vmcnt(10)\n" \
    "v_mfma_f32_16x16x32_bf16 v[32:35], v[196:199], v[100:103], v[32:35]\nbuffer_load_dwordx4 v[96:99], v0, s[64:67], s77 offen offset:2048 sc1\n" \
    "v_mfma_f32_16x16x32_bf16 v[36:39], v[228:231], v[100:103], v[36:39]\nv_max3_u32 v22, v22, v100, v101\nv_max3_u32 v22, v22, v102, v103\ns_waitcnt vmcnt(10)\n" \
    "v_mfma_f32_16x16x32_bf16 v[24:27], v[200:203], v[104:107], v[24:27]\nbuffer_load_dwordx4 v[100:103], v0, s[64:67], s77 offen offset:3072 sc1\n" \
    "v_mfma_f32_16x16x32_bf16 v[28:31], v[232:235], v[104:107], v[28:31]\nv_max3_u32 v22, v22, v104, v105\nv_max3_u32 v22, v22, v106, v107\ns_waitcnt vmcnt(10)\n" \
    "v_mfma_f32_16x16x32_bf16 v[32:35], v[200:203], v[108:111], v[32:35]\ns_add_u32 s85, s84, 0\nbuffer_load_dwordx4 v[104:107], v2, s[64:67], s85 offen offset:0 sc1\n" \
    "v_mfma_f32_16x16x32_bf16 v[36:39], v[232:235], v[108:111], v[36:39]\nv_max3_u32 v22, v22, v108, v109\nv_max3_u32 v22, v22, v110, v111\ns_waitcnt vmcnt(10)\n" \
    "v_mfma_f32_16x16x32_bf16 v[24:27], v[204:207], v[112:115], v[24:27]\nbuffer_load_dwordx4 v[108:111], v2, s[64:67], s85 offen offset:1024 sc1\n" \
    "v_mfma_f32_16x16x32_bf16 v[28:31], v[236:239], v[112:115], v[28:31]\nv_max3_u32 v22, v22, v112, v113\nv_max3_u32 v22, v22, v114, v115\ns_waitcnt vmcnt(10)\n" \
    "v_mfma_f32_16x16x32_bf16 v[32:35], v[204:207], v[116:119], v[32:35]\nbuffer_load_dwordx4 v[112:115], v2, s[64:67], s85 offen offset:2048 sc1\n" \
    "v_mfma_f32_16x16x32_bf16 v[36:39], v[236:239], v[116:119], v[36:39]\nv_max3_u32 v22, v22, v116, v117\nv_max3_u32 v22, v22, v118, v119\ns_waitcnt vmcnt(10)\n" \
    "v_mfma_f32_16x16x32_bf16 v[24:27], v[208:211], v[72:75], v[24:27]\nbuffer_load_dwordx4 v[116:119], v2, s[64:67], s85 offen offset:3072 sc1\n" \
    "v_mfma_f32_16x16x32_bf16 v[28:31], v[240:243], v[72:75], v[28:31]\nv_max3_u32 v22, v22, v72, v73\nv_max3_u32 v22, v22, v74, v75\ns_waitcnt vmcnt(10)\n" \
    "v_mfma_f32_16x16x32_bf16 v[32:35], v[208:211], v[76:79], v[32:35]\nbuffer_load_dwordx4 v[72:75], v3, s[64:67], s85 offen offset:0 sc1\n" \
    "v_mfma_f32_16x16x32_bf16 v[36:39], v[240:243], v[76:79], v[36:39]\nv_max3_u32 v22, v22, v76, v77\nv_max3_u32 v22, v22, v78, v79\ns_waitcnt vmcnt(10)\n" \
    "v_mfma_f32_16x16x32_bf16 v[24:27], v[212:215], v[80:83], v[24:27]\nbuffer_load_dwordx4 v[76:79], v3, s[64:67], s85 offen offset:1024 sc1\n" \
    "v_mfma_f32_16x16x32_bf16 v[28:31], v[244:247], v[80:83], v[28:31]\nv_max3_u32 v22, v22, v80, v81\nv_max3_u32 v22, v22, v82, v83\ns_waitcnt vmcnt(10)\n" \
    "v_mfma_f32_16x16x32_bf16 v[32:35], v[212:215], v[84:87], v[32:35]\nbuffer_load_dwordx4 v[80:83], v3, s[64:67], s85 offen offset:2048 sc1\n" \
    "v_mfma_f32_16x16x32_bf16 v[36:39], v[244:247], v[84:87], v[36:39]\nv_max3_u32 v22, v22, v84, v85\nv_max3_u32 v22, v22, v86, v87\ns_waitcnt vmcnt(10)\n" \
    "v_mfma_f32_16x16x32_bf16 v[24:27], v[56:59], v[88:91], v[24:27]\nbuffer_load_dwordx4 v[84:87], v3, s[64:67], s85 offen offset:3072 sc1\n" \
    "v_mfma_f32_16x16x32_bf16 v[28:31], v[64:67], v[88:91], v[28:31]\nv_max3_u32 v22, v22, v88, v89\nv_max3_u32 v22, v22, v90, v91\ns_waitcnt vmcnt(10)\n" \
    "v_mfma_f32_16x16x32_bf16 v[32:35], v[56:59], v[92:95], v[32:35]\ns_add_u32 s85, s84, 16384\nbuffer_load_dwordx4 v[88:91], v2, s[64:67], s85 offen offset:0 sc1\n" \
    "v_mfma_f32_16x16x32_bf16 v[36:39], v[64:67], v[92:95], v[36:39]\nv_max3_u32 v22, v22, v92, v93\nv_max3_u32 v22, v22, v94, v95\ns_waitcnt vmcnt(10)\n" \
    "v_mfma_f32_16x16x32_bf16 v[24:27], v[60:63], v[96:99], v[24:27]\nbuffer_load_dwordx4 v[92:95], v2, s[64:67], s85 offen offset:1024 sc1\n" \
    "v_mfma_f32_16x16x32_bf16 v[28:31], v[68:71], v[96:99], v[28:31]\nv_max3_u32 v22, v22, v96, v97\nv_max3_u32 v22, v22, v98, v99\ns_waitcnt vmcnt(10)\n" \
    "v_mfma_f32_16x16x32_bf16 v[32:35], v[60:63], v[100:103], v[32:35]\nbuffer_load_dwordx4 v[96:99], v2, s[64:67], s85 offen offset:2048 sc1\n" \
    "v_mfma_f32_16x16x32_bf16 v[36:39], v[68:71], v[100:103], v[36:39]\nv_max3_u32 v22, v22, v100, v101\nv_max3_u32 v22, v22, v102, v103\nv_cmp_eq_u32 vcc, -1, v22\n" \
    "s_cbranch_vccnz .Lr107_%=\nds_write_b128 v9, v[24:27] offset:0\nds_write_b128 v9, v[28:31] offset:64\ns_waitcnt vmcnt(10)\n" \
    "v_mfma_f32_16x16x32_bf16 v[24:27], v[104:107], a[0:3], 0\nbuffer_load_dwordx4 v[100:103], v2, s[64:67], s85 offen offset:3072 sc1\n" \
    "v_mfma_f32_16x16x32_bf16 v[28:31], v[104:107], a[16:19], 0\nds_write_b128 v9, v[32:35] offset:2304\nds_write_b128 v9, v[36:39] offset:2368\ns_waitcnt vmcnt(10)\n" \
    "v_mfma_f32_16x16x32_bf16 v[24:27], v[108:111], a[4:7], v[24:27]\nbuffer_load_dwordx4 v[104:107], v3, s[64:67], s85 offen offset:0 sc1\n" \
    "v_mfma_f32_16x16x32_bf16 v[28:31], v[108:111], a[20:23], v[28:31]\nds_read_b128 v[18:21], v16\ns_waitcnt vmcnt(10)\n" \
    "v_mfma_f32_16x16x32_bf16 v[24:27], v[112:115], a[8:11], v[24:27]\nbuffer_load_dwordx4 v[108:111], v3, s[64:67], s85 offen offset:1024 sc1\n" \
    "v_mfma_f32_16x16x32_bf16 v[28:31], v[112:115], a[24:27], v[28:31]\ns_waitcnt lgkmcnt(0)\ns_barrier\nds_read_b128 v[56:59], v10 offset:0\n" \
    "ds_read_b128 v[60:63], v10 offset:4608\ns_waitcnt vmcnt(10)\nv_mfma_f32_16x16x32_bf16 v[24:27], v[116:119], a[12:15], v[24:27]\n" \
    "buffer_load_dwordx4 v[112:115], v3, s[64:67], s85 offen offset:2048 sc1\nv_mfma_f32_16x16x32_bf16 v[28:31], v[116:119], a[28:31], v[28:31]\n" \
    "ds_read_b128 v[64:67], v10 offset:9216\nds_read_b128 v[68:71], v10 offset:13824\nv_lshlrev_b32 v248, 16, v252\nv_and_b32 v249, 0xffff0000, v252\n" \
    "v_lshlrev_b32 v250, 16, v253\ns_waitcnt vmcnt(10)\nv_mfma_f32_16x16x32_bf16 v[32:35], v[72:75], a[0:3], 0\n" \
    "buffer_load_dwordx4 v[116:119], v3, s[64:67], s85 offen offset:3072 sc1\nv_mfma_f32_16x16x32_bf16 v[36:39], v[72:75], a[16:19], 0\nv_and_b32 v251, 0xffff0000, v253\n" \
    "s_waitcnt vmcnt(10)\nv_mfma_f32_16x16x32_bf16 v[32:35], v[76:79], a[4:7], v[32:35]\ns_add_u32 s85, s84, 32768\n" \
    "buffer_load_dwordx4 v[72:75], v2, s[64:67], s85 offen offset:0 sc1\nv_mfma_f32_16x16x32_bf16 v[36:39], v[76:79], a[20:23], v[36:39]\nv_cvt_pk_bf16_f32 v184, v24, v25\n" \
    "v_cvt_pk_bf16_f32 v186, v26, v27\ns_waitcnt lgkmcnt(0)\nv_add_f32 v56, v56, v60\nv_add_f32 v57, v57, v61\nv_add_f32 v58, v58, v62\nv_add_f32 v59, v59, v63\n" \
    "s_waitcnt vmcnt(10)\nv_mfma_f32_16x16x32_bf16 v[32:35], v[80:83], a[8:11], v[32:35]\nbuffer_load_dwordx4 v[76:79], v2, s[64:67], s85 offen offset:1024 sc1\n" \
    "v_mfma_f32_16x16x32_bf16 v[36:39], v[80:83], a[24:27], v[36:39]\nv_cvt_pk_bf16_f32 v216, v28, v29\nv_cvt_pk_bf16_f32 v218, v30, v31\nv_add_f32 v64, v64, v68\n" \
    "v_add_f32 v65, v65, v69\nv_add_f32 v66, v66, v70\nv_add_f32 v67, v67, v71\nv_add_f32 v56, v56, v64\ns_waitcnt vmcnt(10)\n" \
    "v_mfma_f32_16x16x32_bf16 v[32:35], v[84:87], a[12:15], v[32:35]\nbuffer_load_dwordx4 v[80:83], v2, s[64:67], s85 offen offset:2048 sc1\n" \
    "v_mfma_f32_16x16x32_bf16 v[36:39], v[84:87], a[28:31], v[36:39]\nv_add_f32 v57, v57, v65\nv_add_f32 v58, v58, v66\nv_add_f32 v59, v59, v67\nv_fma_f32 v248, v56, v18, v248\n" \
    "v_fma_f32 v249, v57, v19, v249\nv_fma_f32 v250, v58, v20, v250\nv_fma_f32 v251, v59, v21, v251\ns_waitcnt vmcnt(10)\n" \
    "v_mfma_f32_16x16x32_bf16 v[24:27], v[88:91], a[32:35], 0\nbuffer_load_dwordx4 v[84:87], v2, s[64:67], s85 offen offset:3072 sc1\n" \
    "v_mfma_f32_16x16x32_bf16 v[28:31], v[88:91], a[48:51], 0\nv_cvt_pk_bf16_f32 v56, v248, v249\ns_add_u32 s69, s50, -1\nv_mov_b32 v22, s69\n" \
    "buffer_store_dword v22, v15, s[64:67], 0 offen sc1\nv_cvt_pk_bf16_f32 v57, v250, v251\ns_waitcnt vmcnt(11)\n" \
    "v_mfma_f32_16x16x32_bf16 v[24:27], v[92:95], a[36:39], v[24:27]\nbuffer_load_dwordx4 v[88:91], v3, s[64:67], s85 offen offset:0 sc1\n" \
    "v_mfma_f32_16x16x32_bf16 v[28:31], v[92:95], a[52:55], v[28:31]\nv_cvt_pk_bf16_f32 v185, v32, v33\nv_cvt_pk_bf16_f32 v187, v34, v35\nv_lshlrev_b32 v248, 16, v56\n" \
    "v_and_b32 v249, 0xffff0000, v56\nv_lshlrev_b32 v250, 16, v57\nv_and_b32 v251, 0xffff0000, v57\nv_add_f32 v18, v248, v249\ns_waitcnt vmcnt(11)\n" \
    "v_mfma_f32_16x16x32_bf16 v[24:27], v[96:99], a[40:43], v[24:27]\nbuffer_load_dwordx4 v[92:95], v3, s[64:67], s85 offen offset:1024 sc1\n" \
    "v_mfma_f32_16x16x32_bf16 v[28:31], v[96:99], a[56:59], v[28:31]\nv_cvt_pk_bf16_f32 v217, v36, v37\nv_cvt_pk_bf16_f32 v219, v38, v39\nv_add_f32 v19, v250, v251\n" \
    "v_min_u32 v56, -2, v56\nv_min_u32 v57, -2, v57\nv_add_f32 v18, v18, v19\nv_permlane32_swap_b32 v56, v58\ns_waitcnt vmcnt(11)\n" \
    "v_mfma_f32_16x16x32_bf16 v[24:27], v[100:103], a[44:47], v[24:27]\nbuffer_load_dwordx4 v[96:99], v3, s[64:67], s85 offen offset:2048 sc1\n" \
    "v_mfma_f32_16x16x32_bf16 v[28:31], v[100:103], a[60:63], v[28:31]\nv_permlane32_swap_b32 v57, v59\nbuffer_store_dwordx4 v[56:59], v12, s[64:67], s79 offen sc1\n" \
    "v_add_f32_dpp v19, v18, v18 row_ror:8 row_mask:0xf bank_mask:0xf\ns_waitcnt vmcnt(12)\nv_mfma_f32_16x16x32_bf16 v[32:35], v[104:107], a[32:35], 0\n" \
    "buffer_load_dwordx4 v[100:103], v3, s[64:67], s85 offen offset:3072 sc1\nv_mfma_f32_16x16x32_bf16 v[36:39], v[104:107], a[48:51], 0\n" \
    "v_add_f32_dpp v20, v18, v18 row_ror:8 row_mask:0xf bank_mask:0xf\ns_nop 1\nv_permlane16_swap_b32 v19, v20\nv_add_f32 v18, v19, v20\nv_add_f32 v19, v19, v20\ns_nop 1\n" \
    "v_permlane32_swap_b32 v18, v19\ns_waitcnt vmcnt(12)\nv_mfma_f32_16x16x32_bf16 v[32:35], v[108:111], a[36:39], v[32:35]\ns_add_u32 s85, s84, 49152\n" \
    "buffer_load_dwordx4 v[104:107], v2, s[64:67], s85 offen offset:0 sc1\nv_mfma_f32_16x16x32_bf16 v[36:39], v[108:111], a[52:55], v[36:39]\nv_cvt_pk_bf16_f32 v188, v24, v25\n" \
    "v_cvt_pk_bf16_f32 v190, v26, v27\nv_add_f32 v18, v18, v19\nv_mul_f32 v18, 0x3d000000, v18\nv_sub_f32 v60, v248, v18\nv_sub_f32 v61, v249, v18\nv_sub_f32 v62, v250, v18\n" \
    "s_waitcnt vmcnt(12)\nv_mfma_f32_16x16x32_bf16 v[32:35], v[112:115], a[40:43], v[32:35]\nbuffer_load_dwordx4 v[108:111], v2, s[64:67], s85 offen offset:1024 sc1\n" \
    "v_mfma_f32_16x16x32_bf16 v[36:39], v[112:115], a[56:59], v[36:39]\nv_cvt_pk_bf16_f32 v220, v28, v29\nv_cvt_pk_bf16_f32 v222, v30, v31\nv_sub_f32 v63, v251, v18\n" \
    "v_mul_f32 v19, v60, v60\nv_fmac_f32 v19, v61, v61\nv_fmac_f32 v19, v62, v62\nv_fmac_f32 v19, v63, v63\ns_waitcnt vmcnt(12)\n" \
    "v_mfma_f32_16x16x32_bf16 v[32:35], v[116:119], a[44:47], v[32:35]\nbuffer_load_dwordx4 v[112:115], v2, s[64:67], s85 offen offset:2048 sc1\n" \
    "v_mfma_f32_16x16x32_bf16 v[36:39], v[116:119], a[60:63], v[36:39]\nv_min_u32 v18, -2, v18\nv_add_f32_dpp v20, v19, v19 row_ror:8 row_mask:0xf bank_mask:0xf\n" \
    "v_add_f32_dpp v21, v19, v19 row_ror:8 row_mask:0xf bank_mask:0xf\ns_nop 1\nv_permlane16_swap_b32 v20, v21\nv_add_f32 v19, v20, v21\nv_add_f32 v20, v20, v21\n" \
    "s_waitcnt vmcnt(12)\nv_mfma_f32_16x16x32_bf16 v[24:27], v[72:75], a[64:67], 0\nbuffer_load_dwordx4 v[116:119], v2, s[64:67], s85 offen offset:3072 sc1\n" \
    "v_mfma_f32_16x16x32_bf16 v[28:31], v[72:75], a[80:83], 0\nv_permlane32_swap_b32 v19, v20\nv_add_f32 v19, v19, v20\nv_min_u32 v19, -2, v19\n" \
    "buffer_store_dwordx2 v[18:19], v13, s[64:67], s79 offen sc1\ns_waitcnt vmcnt(13)\nv_mfma_f32_16x16x32_bf16 v[24:27], v[76:79], a[68:71], v[24:27]\n" \
    "buffer_load_dwordx4 v[72:75], v3, s[64:67], s85 offen offset:0 sc1\nv_mfma_f32_16x16x32_bf16 v[28:31], v[76:79], a[84:87], v[28:31]\nv_cvt_pk_bf16_f32 v189, v32, v33\n" \
    "v_cvt_pk_bf16_f32 v191, v34, v35\nbuffer_store_dword v22, v14, s[64:67], 0 offen sc1\ns_cmp_ge_u32 s50, 62\ns_cbranch_scc1 .Lr47_%=\ns_waitcnt vmcnt(14)\n" \
    "v_mfma_f32_16x16x32_bf16 v[24:27], v[80:83], a[72:75], v[24:27]\nbuffer_load_dwordx4 v[76:79], v3, s[64:67], s85 offen offset:1024 sc1\n" \
    "v_mfma_f32_16x16x32_bf16 v[28:31], v[80:83], a[88:91], v[28:31]\nv_cvt_pk_bf16_f32 v221, v36, v37\nv_cvt_pk_bf16_f32 v223, v38, v39\ns_add_u32 s69, s50, 2\n" \
    "s_and_b32 s69, s69, s41\ns_lshl_b32 s69, s69, 20\ns_add_u32 s69, s69, s32\ns_add_u32 s74, s48, s69\ns_waitcnt vmcnt(14)\n" \
    "v_mfma_f32_16x16x32_bf16 v[24:27], v[84:87], a[76:79], v[24:27]\nbuffer_load_dwordx4 v[80:83], v3, s[64:67], s85 offen offset:2048 sc1\n" \
    "v_mfma_f32_16x16x32_bf16 v[28:31], v[84:87], a[92:95], v[28:31]\ns_lshr_b32 s74, s74, 8\ns_xor_b32 s74, s74, s49\ns_and_b32 s74, s74, 0x2000\ns_add_u32 s74, s74, s69\n" \
    "s_add_u32 s69, s50, 2\ns_and_b32 s69, s69, s41\ns_lshl_b32 s69, s69, 19\ns_waitcnt vmcnt(13)\nv_mfma_f32_16x16x32_bf16 v[32:35], v[88:91], a[64:67], 0\n" \
    "buffer_load_dwordx4 v[84:87], v3, s[64:67], s85 offen offset:3072 sc1\nv_mfma_f32_16x16x32_bf16 v[36:39], v[88:91], a[80:83], 0\ns_add_u32 s69, s69, s33\n" \
    "s_add_u32 s87, s48, s69\ns_lshr_b32 s87, s87, 8\ns_xor_b32 s87, s87, s49\ns_and_b32 s87, s87, 0x2000\ns_add_u32 s87, s87, s69\ns_add_u32 s69, s50, 3\ns_waitcnt vmcnt(13)\n" \
    "v_mfma_f32_16x16x32_bf16 v[32:35], v[92:95], a[68:71], v[32:35]\ns_add_u32 s85, s84, 65536\nbuffer_load_dwordx4 v[88:91], v2, s[64:67], s85 offen offset:0 sc1\n" \
    "v_mfma_f32_16x16x32_bf16 v[36:39], v[92:95], a[84:87], v[36:39]\nv_cvt_pk_bf16_f32 v192, v24, v25\nv_cvt_pk_bf16_f32 v194, v26, v27\ns_and_b32 s69, s69, s41\n" \
    "s_lshl_b32 s69, s69, 18\ns_add_u32 s69, s69, s34\ns_add_u32 s88, s48, s69\ns_lshr_b32 s88, s88, 8\ns_waitcnt vmcnt(13)\n" \
    "v_mfma_f32_16x16x32_bf16 v[32:35], v[96:99], a[72:75], v[32:35]\nbuffer_load_dwordx4 v[92:95], v2, s[64:67], s85 offen offset:1024 sc1\n" \
    "v_mfma_f32_16x16x32_bf16 v[36:39], v[96:99], a[88:91], v[36:39]\nv_cvt_pk_bf16_f32 v224, v28, v29\nv_cvt_pk_bf16_f32 v226, v30, v31\ns_xor_b32 s88, s88, s49\n" \
    "s_and_b32 s88, s88, 0x2000\ns_add_u32 s88, s88, s69\ns_add_u32 s69, s50, 0\ns_lshl_b32 s69, s69, 20\ns_waitcnt vmcnt(12)\n" \
    "v_mfma_f32_16x16x32_bf16 v[32:35], v[100:103], a[76:79], v[32:35]\nbuffer_load_dwordx4 v[96:99], v2, s[64:67], s85 offen offset:2048 sc1\n" \
    "v_mfma_f32_16x16x32_bf16 v[36:39], v[100:103], a[92:95], v[36:39]\ns_add_u32 s69, s69, s36\ns_add_u32 s75, s48, s69\ns_lshr_b32 s75, s75, 8\ns_xor_b32 s75, s75, s49\n" \
    "s_and_b32 s75, s75, 0x2000\ns_add_u32 s75, s75, s69\ns_add_u32 s76, s75, 0x4000\ns_waitcnt vmcnt(12)\nv_mfma_f32_16x16x32_bf16 v[24:27], v[104:107], a[96:99], 0\n" \
    "buffer_load_dwordx4 v[100:103], v2, s[64:67], s85 offen offset:3072 sc1\nv_mfma_f32_16x16x32_bf16 v[28:31], v[104:107], a[112:115], 0\ns_add_u32 s77, s75, s37\n" \
    "s_xor_b32 s79, s75, 0x2000\ns_add_u32 s82, s50, 0\ns_lshl_b32 s80, s82, 16\ns_waitcnt vmcnt(12)\nv_mfma_f32_16x16x32_bf16 v[24:27], v[108:111], a[100:103], v[24:27]\n" \
    "buffer_load_dwordx4 v[104:107], v3, s[64:67], s85 offen offset:0 sc1\nv_mfma_f32_16x16x32_bf16 v[28:31], v[108:111], a[116:119], v[28:31]\n" \
    "v_cvt_pk_bf16_f32 v193, v32, v33\nv_cvt_pk_bf16_f32 v195, v34, v35\ns_waitcnt vmcnt(12)\nv_mfma_f32_16x16x32_bf16 v[24:27], v[112:115], a[104:107], v[24:27]\n" \
    "buffer_load_dwordx4 v[108:111], v3, s[64:67], s85 offen offset:1024 sc1\nv_mfma_f32_16x16x32_bf16 v[28:31], v[112:115], a[120:123], v[28:31]\n" \
    "v_cvt_pk_bf16_f32 v225, v36, v37\nv_cvt_pk_bf16_f32 v227, v38, v39\ns_waitcnt vmcnt(12)\nv_mfma_f32_16x16x32_bf16 v[24:27], v[116:119], a[108:111], v[24:27]\n" \
    "buffer_load_dwordx4 v[112:115], v3, s[64:67], s85 offen offset:2048 sc1\nv_mfma_f32_16x16x32_bf16 v[28:31], v[116:119], a[124:127], v[28:31]\ns_waitcnt vmcnt(11)\n" \
    "v_mfma_f32_16x16x32_bf16 v[32:35], v[72:75], a[96:99], 0\nbuffer_load_dwordx4 v[116:119], v3, s[64:67], s85 offen offset:3072 sc1\n" \
    "v_mfma_f32_16x16x32_bf16 v[36:39], v[72:75], a[112:115], 0\ns_waitcnt vmcnt(10)\nv_mfma_f32_16x16x32_bf16 v[32:35], v[76:79], a[100:103], v[32:35]\n" \
    "s_add_u32 s85, s84, 81920\nbuffer_load_dwordx4 v[72:75], v2, s[64:67], s85 offen offset:0 sc1\nv_mfma_f32_16x16x32_bf16 v[36:39], v[76:79], a[116:119], v[36:39]\n" \
    "v_cvt_pk_bf16_f32 v196, v24, v25\nv_cvt_pk_bf16_f32 v198, v26, v27\ns_waitcnt vmcnt(10)\nv_mfma_f32_16x16x32_bf16 v[32:35], v[80:83], a[104:107], v[32:35]\n" \
    "buffer_load_dwordx4 v[76:79], v2, s[64:67], s85 offen offset:1024 sc1\nv_mfma_f32_16x16x32_bf16 v[36:39], v[80:83], a[120:123], v[36:39]\nv_cvt_pk_bf16_f32 v228, v28, v29\n" \
    "v_cvt_pk_bf16_f32 v230, v30, v31\ns_waitcnt vmcnt(10)\nv_mfma_f32_16x16x32_bf16 v[32:35], v[84:87], a[108:111], v[32:35]\n" \
    "buffer_load_dwordx4 v[80:83], v2, s[64:67], s85 offen offset:2048 sc1\nv_mfma_f32_16x16x32_bf16 v[36:39], v[84:87], a[124:127], v[36:39]\ns_waitcnt vmcnt(10)\n" \
    "v_mfma_f32_16x16x32_bf16 v[24:27], v[88:91], a[128:131], 0\nbuffer_load_dwordx4 v[84:87], v2, s[64:67], s85 offen offset:3072 sc1\n" \
    "v_mfma_f32_16x16x32_bf16 v[28:31], v[88:91], a[144:147], 0\ns_waitcnt vmcnt(10)\nv_mfma_f32_16x16x32_bf16 v[24:27], v[92:95], a[132:135], v[24:27]\n" \
    "buffer_load_dwordx4 v[88:91], v3, s[64:67], s85 offen offset:0 sc1\nv_mfma_f32_16x16x32_bf16 v[28:31], v[92:95], a[148:151], v[28:31]\nv_cvt_pk_bf16_f32 v197, v32, v33\n" \
    "v_cvt_pk_bf16_f32 v199, v34, v35\ns_waitcnt vmcnt(10)\nv_mfma_f32_16x16x32_bf16 v[24:27], v[96:99], a[136:139], v[24:27]\n" \
    "buffer_load_dwordx4 v[92:95], v3, s[64:67], s85 offen offset:1024 sc1\nv_mfma_f32_16x16x32_bf16 v[28:31], v[96:99], a[152:155], v[28:31]\nv_cvt_pk_bf16_f32 v229, v36, v37\n" \
    "v_cvt_pk_bf16_f32 v231, v38, v39\ns_waitcnt vmcnt(10)\nv_mfma_f32_16x16x32_bf16 v[24:27], v[100:103], a[140:143], v[24:27]\n" \
    "buffer_load_dwordx4 v[96:99], v3, s[64:67], s85 offen offset:2048 sc1\nv_mfma_f32_16x16x32_bf16 v[28:31], v[100:103], a[156:159], v[28:31]\ns_waitcnt vmcnt(10)\n" \
    "v_mfma_f32_16x16x32_bf16 v[32:35], v[104:107], a[128:131], 0\nbuffer_load_dwordx4 v[100:103], v3, s[64:67], s85 offen offset:3072 sc1\n" \
    "v_mfma_f32_16x16x32_bf16 v[36:39], v[104:107], a[144:147], 0\ns_waitcnt vmcnt(10)\nv_mfma_f32_16x16x32_bf16 v[32:35], v[108:111], a[132:135], v[32:35]\n" \
    "s_add_u32 s85, s84, 98304\nbuffer_load_dwordx4 v[104:107], v2, s[64:67], s85 offen offset:0 sc1\nv_mfma_f32_16x16x32_bf16 v[36:39], v[108:111], a[148:151], v[36:39]\n" \
    "v_cvt_pk_bf16_f32 v200, v24, v25\nv_cvt_pk_bf16_f32 v202, v26, v27\ns_waitcnt vmcnt(10)\nv_mfma_f32_16x16x32_bf16 v[32:35], v[112:115], a[136:139], v[32:35]\n" \
    "buffer_load_dwordx4 v[108:111], v2, s[64:67], s85 offen offset:1024 sc1\nv_mfma_f32_16x16x32_bf16 v[36:39], v[112:115], a[152:155], v[36:39]\n" \
    "v_cvt_pk_bf16_f32 v232, v28, v29\nv_cvt_pk_bf16_f32 v234, v30, v31\ns_waitcnt vmcnt(10)\nv_mfma_f32_16x16x32_bf16 v[32:35], v[116:119], a[140:143], v[32:35]\n" \
    "buffer_load_dwordx4 v[112:115], v2, s[64:67], s85 offen offset:2048 sc1\nv_mfma_f32_16x16x32_bf16 v[36:39], v[116:119], a[156:159], v[36:39]\ns_waitcnt vmcnt(10)\n" \
    "v_mfma_f32_16x16x32_bf16 v[24:27], v[72:75], a[160:163], 0\nbuffer_load_dwordx4 v[116:119], v2, s[64:67], s85 offen offset:3072 sc1\n" \
    "v_mfma_f32_16x16x32_bf16 v[28:31], v[72:75], a[176:179], 0\nbuffer_load_dword v17, v8, s[64:67], 0 offen sc1\ns_waitcnt vmcnt(11)\n" \
    "v_mfma_f32_16x16x32_bf16 v[24:27], v[76:79], a[164:167], v[24:27]\nbuffer_load_dwordx4 v[72:75], v3, s[64:67], s85 offen offset:0 sc1\n" \
    "v_mfma_f32_16x16x32_bf16 v[28:31], v[76:79], a[180:183], v[28:31]\nv_cvt_pk_bf16_f32 v201, v32, v33\nv_cvt_pk_bf16_f32 v203, v34, v35\ns_waitcnt vmcnt(11)\n" \
    "v_mfma_f32_16x16x32_bf16 v[24:27], v[80:83], a[168:171], v[24:27]\nbuffer_load_dwordx4 v[76:79], v3, s[64:67], s85 offen offset:1024 sc1\n" \
    "v_mfma_f32_16x16x32_bf16 v[28:31], v[80:83], a[184:187], v[28:31]\nv_cvt_pk_bf16_f32 v233, v36, v37\nv_cvt_pk_bf16_f32 v235, v38, v39\ns_waitcnt vmcnt(11)\n" \
    "v_mfma_f32_16x16x32_bf16 v[24:27], v[84:87], a[172:175], v[24:27]\nbuffer_load_dwordx4 v[80:83], v3, s[64:67], s85 offen offset:2048 sc1\n" \
    "v_mfma_f32_16x16x32_bf16 v[28:31], v[84:87], a[188:191], v[28:31]\ns_waitcnt vmcnt(11)\nv_mfma_f32_16x16x32_bf16 v[32:35], v[88:91], a[160:163], 0\n" \
    "buffer_load_dwordx4 v[84:87], v3, s[64:67], s85 offen offset:3072 sc1\nv_mfma_f32_16x16x32_bf16 v[36:39], v[88:91], a[176:179], 0\ns_waitcnt vmcnt(11)\n" \
    "v_mfma_f32_16x16x32_bf16 v[32:35], v[92:95], a[164:167], v[32:35]\ns_add_u32 s85, s84, 114688\nbuffer_load_dwordx4 v[88:91], v2, s[64:67], s85 offen offset:0 sc1\n" \
    "v_mfma_f32_16x16x32_bf16 v[36:39], v[92:95], a[180:183], v[36:39]\nv_cvt_pk_bf16_f32 v204, v24, v25\nv_cvt_pk_bf16_f32 v206, v26, v27\ns_waitcnt vmcnt(11)\n" \
    "v_mfma_f32_16x16x32_bf16 v[32:35], v[96:99], a[168:171], v[32:35]\nbuffer_load_dwordx4 v[92:95], v2, s[64:67], s85 offen offset:1024 sc1\n" \
    "v_mfma_f32_16x16x32_bf16 v[36:39], v[96:99], a[184:187], v[36:39]\nv_cvt_pk_bf16_f32 v236, v28, v29\nv_cvt_pk_bf16_f32 v238, v30, v31\ns_add_u32 s69, s50, 4\n" \
    "s_and_b32 s69, s69, s41\ns_lshl_b32 s69, s69, 19\ns_add_u32 s69, s69, s35\ns_add_u32 s84, s48, s69\ns_waitcnt vmcnt(11)\n" \
    "v_mfma_f32_16x16x32_bf16 v[32:35], v[100:103], a[172:175], v[32:35]\nbuffer_load_dwordx4 v[96:99], v2, s[64:67], s85 offen offset:2048 sc1\n" \
    "v_mfma_f32_16x16x32_bf16 v[36:39], v[100:103], a[188:191], v[36:39]\ns_lshr_b32 s84, s84, 8\ns_xor_b32 s84, s84, s49\ns_and_b32 s84, s84, 0x2000\ns_add_u32 s84, s84, s69\n" \
    "s_waitcnt vmcnt(11)\nv_mfma_f32_16x16x32_bf16 v[24:27], v[104:107], a[192:195], 0\nbuffer_load_dwordx4 v[100:103], v2, s[64:67], s85 offen offset:3072 sc1\n" \
    "v_mfma_f32_16x16x32_bf16 v[28:31], v[104:107], a[208:211], 0\ns_waitcnt vmcnt(11)\nv_mfma_f32_16x16x32_bf16 v[24:27], v[108:111], a[196:199], v[24:27]\n" \
    "buffer_load_dwordx4 v[104:107], v3, s[64:67], s85 offen offset:0 sc1\nv_mfma_f32_16x16x32_bf16 v[28:31], v[108:111], a[212:215], v[28:31]\n" \
    "v_cvt_pk_bf16_f32 v205, v32, v33\nv_cvt_pk_bf16_f32 v207, v34, v35\ns_waitcnt vmcnt(11)\nv_mfma_f32_16x16x32_bf16 v[24:27], v[112:115], a[200:203], v[24:27]\n" \
    "buffer_load_dwordx4 v[108:111], v3, s[64:67], s85 offen offset:1024 sc1\nv_mfma_f32_16x16x32_bf16 v[28:31], v[112:115], a[216:219], v[28:31]\n" \
    "v_cvt_pk_bf16_f32 v237, v36, v37\nv_cvt_pk_bf16_f32 v239, v38, v39\ns_waitcnt vmcnt(11)\nv_mfma_f32_16x16x32_bf16 v[24:27], v[116:119], a[204:207], v[24:27]\n" \
    "buffer_load_dwordx4 v[112:115], v3, s[64:67], s85 offen offset:2048 sc1\nv_mfma_f32_16x16x32_bf16 v[28:31], v[116:119], a[220:223], v[28:31]\ns_waitcnt vmcnt(10)\n" \
    "v_mfma_f32_16x16x32_bf16 v[32:35], v[72:75], a[192:195], 0\nbuffer_load_dwordx4 v[116:119], v3, s[64:67], s85 offen offset:3072 sc1\n" \
    "v_mfma_f32_16x16x32_bf16 v[36:39], v[72:75], a[208:211], 0\ns_mov_b32 s86, 0\nv_mov_b32 v18, 0x7f000000\ns_add_u32 s70, s50, 4\ns_min_i32 s70, s70, 63\n" \
    "s_add_u32 s71, s50, 0\nv_readlane_b32 s69, v17, 10\ns_cmp_ge_i32 s69, s71\ns_cbranch_scc0 .Lr114_%=\ns_mov_b32 s86, 1\nv_mov_b32 v18, v0\n.Lr114_%=:\n" \
    "v_add_u32 v19, 0x1000, v18\ns_waitcnt vmcnt(10)\nv_mfma_f32_16x16x32_bf16 v[32:35], v[76:79], a[196:199], v[32:35]\n" \
    "v_mfma_f32_16x16x32_bf16 v[36:39], v[76:79], a[212:215], v[36:39]\nv_cvt_pk_bf16_f32 v208, v24, v25\nv_cvt_pk_bf16_f32 v210, v26, v27\n" \
    "buffer_load_dwordx4 v[72:75], v18, s[64:67], s75 offen offset:0 sc1\ns_waitcnt vmcnt(10)\nv_mfma_f32_16x16x32_bf16 v[32:35], v[80:83], a[200:203], v[32:35]\n" \
    "v_mfma_f32_16x16x32_bf16 v[36:39], v[80:83], a[216:219], v[36:39]\nv_cvt_pk_bf16_f32 v240, v28, v29\nv_cvt_pk_bf16_f32 v242, v30, v31\n" \
    "buffer_load_dwordx4 v[76:79], v18, s[64:67], s75 offen offset:1024 sc1\ns_waitcnt vmcnt(10)\nv_mfma_f32_16x16x32_bf16 v[32:35], v[84:87], a[204:207], v[32:35]\n" \
    "v_mfma_f32_16x16x32_bf16 v[36:39], v[84:87], a[220:223], v[36:39]\nbuffer_load_dwordx4 v[80:83], v18, s[64:67], s75 offen offset:2048 sc1\ns_waitcnt vmcnt(10)\n" \
    "v_mfma_f32_16x16x32_bf16 v[24:27], v[88:91], a[224:227], 0\nv_mfma_f32_16x16x32_bf16 v[28:31], v[88:91], a[240:243], 0\n" \
    "buffer_load_dwordx4 v[84:87], v18, s[64:67], s75 offen offset:3072 sc1\ns_waitcnt vmcnt(10)\nv_mfma_f32_16x16x32_bf16 v[24:27], v[92:95], a[228:231], v[24:27]\n" \
    "v_mfma_f32_16x16x32_bf16 v[28:31], v[92:95], a[244:247], v[28:31]\nv_cvt_pk_bf16_f32 v209, v32, v33\nv_cvt_pk_bf16_f32 v211, v34, v35\n" \
    "buffer_load_dwordx4 v[88:91], v19, s[64:67], s75 offen offset:0 sc1\ns_waitcnt vmcnt(10)\nv_mfma_f32_16x16x32_bf16 v[24:27], v[96:99], a[232:235], v[24:27]\n" \
    "v_mfma_f32_16x16x32_bf16 v[28:31], v[96:99], a[248:251], v[28:31]\nv_cvt_pk_bf16_f32 v241, v36, v37\nv_cvt_pk_bf16_f32 v243, v38, v39\n" \
    "buffer_load_dwordx4 v[92:95], v19, s[64:67], s75 offen offset:1024 sc1\ns_waitcnt vmcnt(10)\nv_mfma_f32_16x16x32_bf16 v[24:27], v[100:103], a[236:239], v[24:27]\n" \
    "v_mfma_f32_16x16x32_bf16 v[28:31], v[100:103], a[252:255], v[28:31]\nbuffer_load_dwordx4 v[96:99], v19, s[64:67], s75 offen offset:2048 sc1\n" \
    "buffer_load_dwordx2 v[252:253], v11, s[60:63], s80 offen\ns_waitcnt vmcnt(11)\nv_mfma_f32_16x16x32_bf16 v[32:35], v[104:107], a[224:227], 0\n" \
    "v_mfma_f32_16x16x32_bf16 v[36:39], v[104:107], a[240:243], 0\ns_waitcnt vmcnt(10)\nv_mfma_f32_16x16x32_bf16 v[32:35], v[108:111], a[228:231], v[32:35]\n" \
    "v_mfma_f32_16x16x32_bf16 v[36:39], v[108:111], a[244:247], v[36:39]\nv_cvt_pk_bf16_f32 v212, v24, v25\nv_cvt_pk_bf16_f32 v214, v26, v27\ns_waitcnt vmcnt(9)\n" \
    "v_mfma_f32_16x16x32_bf16 v[32:35], v[112:115], a[232:235], v[32:35]\nv_mfma_f32_16x16x32_bf16 v[36:39], v[112:115], a[248:251], v[36:39]\nv_cvt_pk_bf16_f32 v244, v28, v29\n" \
    "v_cvt_pk_bf16_f32 v246, v30, v31\ns_waitcnt vmcnt(8)\nv_mfma_f32_16x16x32_bf16 v[32:35], v[116:119], a[236:239], v[32:35]\n" \
    "v_mfma_f32_16x16x32_bf16 v[36:39], v[116:119], a[252:255], v[36:39]\ns_nop 6\nv_cvt_pk_bf16_f32 v213, v32, v33\nv_cvt_pk_bf16_f32 v215, v34, v35\n" \
    "v_cvt_pk_bf16_f32 v245, v36, v37\nv_cvt_pk_bf16_f32 v247, v38, v39\ns_sub_u32 s69, s50, s42\ns_lshr_b32 s69, s69, 1\ns_cmp_lg_u32 s69, 4\ns_cbranch_scc1 .Lr117_%=\n" \
    "buffer_load_dword v20, off, s[64:67], s40 sc1\ns_waitcnt vmcnt(0)\nv_readfirstlane_b32 s69, v20\ns_add_u32 s69, s69, s58\ns_lshr_b32 s69, s69, 3\ns_add_u32 s71, s94, 8\n" \
    "s_min_u32 s69, s69, s71\ns_sub_u32 s71, s94, 12\ns_max_u32 s69, s69, s71\ns_sub_u32 s69, s69, s94\ns_sub_u32 s69, s69, s59\ns_add_u32 s55, s55, s69\n" \
    "s_sub_u32 s57, s57, s59\n.Lr117_%=:\ns_add_u32 s50, s50, 2\ns_add_u32 s51, s51, s55\ns_add_u32 s51, s51, s55\ns_cmp_ge_u32 s50, 6\ns_cselect_b64 exec, 0xff, 0\n" \
    "v_mov_b32 v8, s39\ns_mov_b64 exec, -1\ns_cmp_lt_u32 s50, 62\ns_cbranch_scc1 .Lr3_%=\nv_mov_b32 v2, 0x7f000000\nv_mov_b32 v3, 0x7f000000\ns_cmp_eq_u32 s50, 62\n" \
    "s_cbranch_scc1 .Lr3_%=\nbuffer_load_dwordx4 v[100:103], v19, s[64:67], s75 offen offset:3072 sc1\nbuffer_load_dwordx4 v[104:107], v18, s[64:67], s76 offen offset:0 sc1\n" \
    "buffer_load_dwordx4 v[108:111], v18, s[64:67], s76 offen offset:1024 sc1\nbuffer_load_dwordx4 v[112:115], v18, s[64:67], s76 offen offset:2048 sc1\ns_branch .Lr7_%=\n" \
    ".Lr46_%=:\ns_add_u32 s69, s50, 0\ns_and_b32 s69, s69, s41\ns_lshl_b32 s69, s69, 20\ns_add_u32 s69, s69, s32\ns_add_u32 s74, s48, s69\ns_lshr_b32 s74, s74, 8\n" \
    "s_xor_b32 s74, s74, s49\ns_and_b32 s74, s74, 0x2000\ns_add_u32 s74, s74, s69\ns_add_u32 s69, s50, 0\ns_and_b32 s69, s69, s41\ns_lshl_b32 s69, s69, 19\n" \
    "s_add_u32 s69, s69, s33\ns_add_u32 s87, s48, s69\ns_lshr_b32 s87, s87, 8\ns_xor_b32 s87, s87, s49\ns_and_b32 s87, s87, 0x2000\ns_add_u32 s87, s87, s69\n" \
    "s_add_u32 s69, s50, 1\ns_and_b32 s69, s69, s41\ns_lshl_b32 s69, s69, 18\ns_add_u32 s69, s69, s34\ns_add_u32 s88, s48, s69\ns_lshr_b32 s88, s88, 8\ns_xor_b32 s88, s88, s49\n" \
    "s_and_b32 s88, s88, 0x2000\ns_add_u32 s88, s88, s69\ns_add_u32 s69, s50, 0\ns_lshl_b32 s69, s69, 20\ns_add_u32 s69, s69, s36\ns_add_u32 s75, s48, s69\n" \
    "s_lshr_b32 s75, s75, 8\ns_xor_b32 s75, s75, s49\ns_and_b32 s75, s75, 0x2000\ns_add_u32 s75, s75, s69\ns_add_u32 s76, s75, 0x4000\ns_add_u32 s77, s75, s37\n" \
    "s_xor_b32 s79, s75, 0x2000\ns_add_u32 s82, s50, 0\ns_lshl_b32 s80, s82, 16\ns_add_u32 s78, s87, s38\ns_mov_b32 s68, 0\n.Lr5_%=:\n" \
    "buffer_load_dword v20, off, s[64:67], s78 sc1\ns_waitcnt vmcnt(0)\nv_readfirstlane_b32 s70, v20\ns_memrealtime s[72:73]\ns_cmp_eq_u32 s70, -1\ns_cbranch_scc1 .Lr6_%=\n" \
    "s_lshr_b32 s71, s70, 8\ns_waitcnt lgkmcnt(0)\ns_sub_u32 s71, s72, s71\ns_and_b32 s71, s71, 0xffffff\ns_sub_u32 s71, s72, s71\ns_add_u32 s70, s71, 170\n.Lr118_%=:\n" \
    "s_sleep 1\ns_memrealtime s[72:73]\ns_waitcnt lgkmcnt(0)\ns_sub_u32 s72, s72, s70\ns_cmp_lt_i32 s72, 0\ns_cbranch_scc1 .Lr118_%=\n.Lr49_%=:\n" \
    "buffer_load_dwordx4 v[56:59], v0, s[64:67], s74 offen offset:0 sc1\nbuffer_load_dwordx4 v[60:63], v0, s[64:67], s74 offen offset:1024 sc1\ns_add_u32 s69, s74, 0x4000\n" \
    "buffer_load_dwordx4 v[64:67], v0, s[64:67], s69 offen offset:0 sc1\nbuffer_load_dwordx4 v[68:71], v0, s[64:67], s69 offen offset:1024 sc1\ns_waitcnt vmcnt(0)\n" \
    "v_max3_u32 v22, v56, v57, v57\nv_max3_u32 v22, v22, v58, v59\nv_max3_u32 v22, v22, v60, v61\nv_max3_u32 v22, v22, v62, v63\nv_max3_u32 v22, v22, v64, v65\n" \
    "v_max3_u32 v22, v22, v66, v67\nv_max3_u32 v22, v22, v68, v69\nv_max3_u32 v22, v22, v70, v71\nv_cmp_eq_u32 vcc, -1, v22\ns_cbranch_vccnz .Lr50_%=\ns_cmp_eq_u32 s50, 63\n" \
    "s_cselect_b32 s70, s44, s43\ns_add_u32 s70, s70, s71\ns_add_u32 s50, s50, 2\ns_barrier\ns_mov_b32 s68, 0\n.Lr119_%=:\ns_memrealtime s[72:73]\ns_waitcnt lgkmcnt(0)\n" \
    "s_sub_u32 s72, s72, s70\ns_cmp_lt_i32 s72, 0\ns_cbranch_scc1 .Lr119_%=\ns_branch .Lr48_%=\n.Lr47_%=:\ns_sub_u32 s50, s50, 2\ns_waitcnt vmcnt(0)\ns_branch .Lr2_%=\n" \
    ".Lr10_%=:\ns_sleep 3\ns_add_u32 s68, s68, 1\ns_and_b32 s69, s68, 63\ns_cbranch_scc1 .Lr8_%=\ns_cmp_gt_u32 s68, 0x200000\ns_cbranch_scc1 .Lr1_%=\n" \
    "buffer_load_dword v21, off, s[64:67], 0 sc1\ns_waitcnt vmcnt(0)\nv_readfirstlane_b32 s69, v21\ns_cmp_eq_u32 s69, 1\ns_cbranch_scc0 .Lr8_%=\ns_endpgm\n.Lr16_%=:\n" \
    "s_waitcnt lgkmcnt(0)\n.Lr17_%=:\ns_add_u32 s51, s51, 20\ns_sub_u32 s70, s51, s72\ns_cmp_lt_i32 s70, 52\ns_cbranch_scc1 .Lr17_%=\ns_add_u32 s68, s68, 1\n" \
    "s_and_b32 s69, s68, 63\ns_cbranch_scc1 .Lr15_%=\ns_cmp_gt_u32 s68, 0x200000\ns_cbranch_scc1 .Lr1_%=\nbuffer_load_dword v21, off, s[64:67], 0 sc1\ns_waitcnt vmcnt(0)\n" \
    "v_readfirstlane_b32 s69, v21\ns_cmp_eq_u32 s69, 1\ns_cbranch_scc0 .Lr15_%=\ns_endpgm\n.Lr28_%=:\ns_xor_b32 s90, s90, 0x1000\ns_sub_u32 s71, s71, s53\ns_cmp_ge_i32 s71, 0\n" \
    "s_cbranch_scc1 .Lr28_%=\ns_add_u32 s71, s71, s53\ns_cmp_lt_i32 s71, 36\ns_cselect_b32 s71, s71, -1\ns_branch .Lr29_%=\n.Lr31_%=:\ns_sub_u32 s70, s72, s71\n" \
    "s_add_u32 s70, s70, 36\n.Lr32_%=:\ns_memrealtime s[72:73]\ns_waitcnt lgkmcnt(0)\ns_sub_u32 s72, s72, s70\ns_cmp_lt_i32 s72, 0\ns_cbranch_scc1 .Lr32_%=\ns_branch .Lr30_%=\n" \
    ".Lr20_%=:\nv_max3_u32 v22, v40, v41, v42\nv_max3_u32 v22, v22, v43, v44\nv_max3_u32 v22, v22, v45, v46\nv_max3_u32 v22, v22, v47, v48\nv_max3_u32 v22, v22, v49, v50\n" \
    "v_max3_u32 v22, v22, v51, v52\nv_max3_u32 v22, v22, v53, v54\nv_max_u32 v22, v22, v55\nv_cmp_eq_u32 vcc, -1, v22\ns_add_u32 s51, s51, s53\ns_xor_b32 s87, s87, 0x1000\n" \
    "s_add_u32 s68, s68, 1\ns_and_b32 s69, s68, 63\ns_cbranch_scc1 .Lr21_%=\ns_cmp_gt_u32 s68, 0x200000\ns_cbranch_scc1 .Lr1_%=\nbuffer_load_dword v21, off, s[64:67], 0 sc1\n" \
    "s_waitcnt vmcnt(0)\nv_readfirstlane_b32 s69, v21\ns_cmp_eq_u32 s69, 1\ns_cbranch_scc0 .Lr21_%=\ns_endpgm\n.Lr21_%=:\ns_cbranch_vccz .Lr22_%=\ns_sub_u32 s70, s51, s54\n" \
    ".Lr33_%=:\ns_memrealtime s[72:73]\ns_waitcnt lgkmcnt(0)\ns_sub_u32 s72, s72, s70\ns_cmp_lt_i32 s72, 0\ns_cbranch_scc1 .Lr33_%=\n" \
    "buffer_load_dwordx4 v[40:43], v0, s[64:67], s74 offen offset:0 sc1\nbuffer_load_dwordx4 v[44:47], v0, s[64:67], s74 offen offset:1024 sc1\ns_add_u32 s69, s74, 0x4000\n" \
    "buffer_load_dwordx4 v[48:51], v0, s[64:67], s69 offen offset:0 sc1\nbuffer_load_dwordx4 v[52:55], v0, s[64:67], s69 offen offset:1024 sc1\n.Lr22_%=:\n.Lr34_%=:\n" \
    "s_memrealtime s[72:73]\ns_waitcnt lgkmcnt(0)\ns_sub_u32 s72, s72, s51\ns_cmp_lt_i32 s72, -3\ns_cbranch_scc1 .Lr34_%=\ns_cmp_ge_i32 s72, 0\ns_cbranch_scc1 .Lr35_%=\n" \
    "s_nop 2\ns_cmp_ge_i32 s72, -1\ns_cbranch_scc1 .Lr35_%=\ns_nop 2\ns_cmp_ge_i32 s72, -2\ns_cbranch_scc1 .Lr35_%=\ns_nop 2\n.Lr35_%=:\n" \
    "buffer_load_dwordx2 v[116:117], v5, s[64:67], s87 offen offset:0 sc1\ns_nop 5\nbuffer_load_dwordx2 v[118:119], v5, s[64:67], s87 offen offset:128 sc1\ns_nop 5\n" \
    "buffer_load_dwordx4 v[100:103], v0, s[64:67], s87 offen offset:0 sc1\ns_nop 10\nbuffer_load_dwordx4 v[104:107], v0, s[64:67], s87 offen offset:1024 sc1\ns_nop 10\n" \
    "buffer_load_dwordx4 v[108:111], v0, s[64:67], s87 offen offset:2048 sc1\ns_nop 10\nbuffer_load_dwordx4 v[112:115], v0, s[64:67], s87 offen offset:3072 sc1\n" \
    "s_branch .Lr19_%=\n.Lr38_%=:\ns_sleep 3\ns_add_u32 s68, s68, 1\ns_and_b32 s69, s68, 63\ns_cbranch_scc1 .Lr36_%=\ns_cmp_gt_u32 s68, 0x200000\ns_cbranch_scc1 .Lr1_%=\n" \
    "buffer_load_dword v21, off, s[64:67], 0 sc1\ns_waitcnt vmcnt(0)\nv_readfirstlane_b32 s69, v21\ns_cmp_eq_u32 s69, 1\ns_cbranch_scc0 .Lr36_%=\ns_endpgm\n.Lr52_%=:\n" \
    "s_waitcnt lgkmcnt(0)\n.Lr53_%=:\ns_add_u32 s51, s51, s53\ns_sub_u32 s70, s51, s72\ns_cmp_lt_i32 s70, 52\ns_cbranch_scc1 .Lr53_%=\ns_add_u32 s68, s68, 1\n" \
    "s_and_b32 s69, s68, 63\ns_cbranch_scc1 .Lr51_%=\ns_cmp_gt_u32 s68, 0x200000\ns_cbranch_scc1 .Lr1_%=\nbuffer_load_dword v21, off, s[64:67], 0 sc1\ns_waitcnt vmcnt(0)\n" \
    "v_readfirstlane_b32 s69, v21\ns_cmp_eq_u32 s69, 1\ns_cbranch_scc0 .Lr51_%=\ns_endpgm\n.Lr64_%=:\ns_xor_b32 s90, s90, 0x1000\ns_sub_u32 s71, s71, s53\ns_cmp_ge_i32 s71, 0\n" \
    "s_cbranch_scc1 .Lr64_%=\ns_add_u32 s71, s71, s53\ns_cmp_lt_i32 s71, 36\ns_cselect_b32 s71, s71, -1\ns_branch .Lr65_%=\n.Lr67_%=:\ns_sub_u32 s70, s72, s71\n" \
    "s_add_u32 s70, s70, 36\n.Lr68_%=:\ns_memrealtime s[72:73]\ns_waitcnt lgkmcnt(0)\ns_sub_u32 s72, s72, s70\ns_cmp_lt_i32 s72, 0\ns_cbranch_scc1 .Lr68_%=\ns_branch .Lr66_%=\n" \
    ".Lr56_%=:\nv_max3_u32 v22, v56, v57, v58\nv_max3_u32 v22, v22, v59, v60\nv_max3_u32 v22, v22, v61, v62\nv_max3_u32 v22, v22, v63, v64\nv_max3_u32 v22, v22, v65, v66\n" \
    "v_max3_u32 v22, v22, v67, v68\nv_max3_u32 v22, v22, v69, v70\nv_max_u32 v22, v22, v71\nv_cmp_eq_u32 vcc, -1, v22\ns_add_u32 s51, s51, s53\ns_xor_b32 s87, s87, 0x1000\n" \
    "s_add_u32 s68, s68, 1\ns_and_b32 s69, s68, 63\ns_cbranch_scc1 .Lr57_%=\ns_cmp_gt_u32 s68, 0x200000\ns_cbranch_scc1 .Lr1_%=\nbuffer_load_dword v21, off, s[64:67], 0 sc1\n" \
    "s_waitcnt vmcnt(0)\nv_readfirstlane_b32 s69, v21\ns_cmp_eq_u32 s69, 1\ns_cbranch_scc0 .Lr57_%=\ns_endpgm\n.Lr57_%=:\ns_cbranch_vccz .Lr58_%=\ns_sub_u32 s70, s51, s54\n" \
    ".Lr69_%=:\ns_memrealtime s[72:73]\ns_waitcnt lgkmcnt(0)\ns_sub_u32 s72, s72, s70\ns_cmp_lt_i32 s72, 0\ns_cbranch_scc1 .Lr69_%=\n" \
    "buffer_load_dwordx4 v[56:59], v0, s[64:67], s74 offen offset:0 sc1\nbuffer_load_dwordx4 v[60:63], v0, s[64:67], s74 offen offset:1024 sc1\ns_add_u32 s69, s74, 0x4000\n" \
    "buffer_load_dwordx4 v[64:67], v0, s[64:67], s69 offen offset:0 sc1\nbuffer_load_dwordx4 v[68:71], v0, s[64:67], s69 offen offset:1024 sc1\n.Lr58_%=:\n.Lr70_%=:\n" \
    "s_memrealtime s[72:73]\ns_waitcnt lgkmcnt(0)\ns_sub_u32 s72, s72, s51\ns_cmp_lt_i32 s72, -3\ns_cbranch_scc1 .Lr70_%=\ns_cmp_ge_i32 s72, 0\ns_cbranch_scc1 .Lr71_%=\n" \
    "s_nop 2\ns_cmp_ge_i32 s72, -1\ns_cbranch_scc1 .Lr71_%=\ns_nop 2\ns_cmp_ge_i32 s72, -2\ns_cbranch_scc1 .Lr71_%=\ns_nop 2\n.Lr71_%=:\n" \
    "buffer_load_dwordx2 v[116:117], v5, s[64:67], s87 offen offset:0 sc1\ns_nop 5\nbuffer_load_dwordx2 v[118:119], v5, s[64:67], s87 offen offset:128 sc1\ns_nop 5\n" \
    "buffer_load_dwordx4 v[100:103], v0, s[64:67], s87 offen offset:0 sc1\ns_nop 10\nbuffer_load_dwordx4 v[104:107], v0, s[64:67], s87 offen offset:1024 sc1\ns_nop 10\n" \
    "buffer_load_dwordx4 v[108:111], v0, s[64:67], s87 offen offset:2048 sc1\ns_nop 10\nbuffer_load_dwordx4 v[112:115], v0, s[64:67], s87 offen offset:3072 sc1\n" \
    "s_branch .Lr55_%=\n.Lr77_%=:\ns_sleep 3\ns_add_u32 s68, s68, 1\ns_and_b32 s69, s68, 63\ns_cbranch_scc1 .Lr75_%=\ns_cmp_gt_u32 s68, 0x200000\ns_cbranch_scc1 .Lr1_%=\n" \
    "buffer_load_dword v21, off, s[64:67], 0 sc1\ns_waitcnt vmcnt(0)\nv_readfirstlane_b32 s69, v21\ns_cmp_eq_u32 s69, 1\ns_cbranch_scc0 .Lr75_%=\ns_endpgm\n.Lr80_%=:\n" \
    "s_sleep 3\ns_add_u32 s68, s68, 1\ns_and_b32 s69, s68, 63\ns_cbranch_scc1 .Lr78_%=\ns_cmp_gt_u32 s68, 0x200000\ns_cbranch_scc1 .Lr1_%=\n" \
    "buffer_load_dword v21, off, s[64:67], 0 sc1\ns_waitcnt vmcnt(0)\nv_readfirstlane_b32 s69, v21\ns_cmp_eq_u32 s69, 1\ns_cbranch_scc0 .Lr78_%=\ns_endpgm\n.Lr73_%=:\n" \
    "s_waitcnt vmcnt(0)\ns_sleep 4\ns_add_u32 s68, s68, 1\ns_and_b32 s69, s68, 63\ns_cbranch_scc1 .Lr74_%=\ns_cmp_gt_u32 s68, 0x200000\ns_cbranch_scc1 .Lr1_%=\n" \
    "buffer_load_dword v21, off, s[64:67], 0 sc1\ns_waitcnt vmcnt(0)\nv_readfirstlane_b32 s69, v21\ns_cmp_eq_u32 s69, 1\ns_cbranch_scc0 .Lr74_%=\ns_endpgm\n.Lr86_%=:\n" \
    "s_waitcnt lgkmcnt(0)\n.Lr87_%=:\ns_add_u32 s51, s51, s53\ns_sub_u32 s70, s51, s72\ns_cmp_lt_i32 s70, 52\ns_cbranch_scc1 .Lr87_%=\ns_add_u32 s68, s68, 1\n" \
    "s_and_b32 s69, s68, 63\ns_cbranch_scc1 .Lr85_%=\ns_cmp_gt_u32 s68, 0x200000\ns_cbranch_scc1 .Lr1_%=\nbuffer_load_dword v21, off, s[64:67], 0 sc1\ns_waitcnt vmcnt(0)\n" \
    "v_readfirstlane_b32 s69, v21\ns_cmp_eq_u32 s69, 1\ns_cbranch_scc0 .Lr85_%=\ns_endpgm\n.Lr98_%=:\ns_xor_b32 s90, s90, 0x1000\ns_sub_u32 s71, s71, s53\ns_cmp_ge_i32 s71, 0\n" \
    "s_cbranch_scc1 .Lr98_%=\ns_add_u32 s71, s71, s53\ns_cmp_lt_i32 s71, 36\ns_cselect_b32 s71, s71, -1\ns_branch .Lr99_%=\n.Lr101_%=:\ns_sub_u32 s70, s72, s71\n" \
    "s_add_u32 s70, s70, 36\n.Lr102_%=:\ns_memrealtime s[72:73]\ns_waitcnt lgkmcnt(0)\ns_sub_u32 s72, s72, s70\ns_cmp_lt_i32 s72, 0\ns_cbranch_scc1 .Lr102_%=\n" \
    "s_branch .Lr100_%=\n.Lr90_%=:\nv_max3_u32 v22, v40, v41, v42\nv_max3_u32 v22, v22, v43, v44\nv_max3_u32 v22, v22, v45, v46\nv_max3_u32 v22, v22, v47, v48\n" \
    "v_max3_u32 v22, v22, v49, v50\nv_max3_u32 v22, v22, v51, v52\nv_max3_u32 v22, v22, v53, v54\nv_max_u32 v22, v22, v55\nv_cmp_eq_u32 vcc, -1, v22\ns_add_u32 s51, s51, s53\n" \
    "s_xor_b32 s87, s87, 0x1000\ns_add_u32 s68, s68, 1\ns_and_b32 s69, s68, 63\ns_cbranch_scc1 .Lr91_%=\ns_cmp_gt_u32 s68, 0x200000\ns_cbranch_scc1 .Lr1_%=\n" \
    "buffer_load_dword v21, off, s[64:67], 0 sc1\ns_waitcnt vmcnt(0)\nv_readfirstlane_b32 s69, v21\ns_cmp_eq_u32 s69, 1\ns_cbranch_scc0 .Lr91_%=\ns_endpgm\n.Lr91_%=:\n" \
    "s_cbranch_vccz .Lr92_%=\ns_sub_u32 s70, s51, s54\n.Lr103_%=:\ns_memrealtime s[72:73]\ns_waitcnt lgkmcnt(0)\ns_sub_u32 s72, s72, s70\ns_cmp_lt_i32 s72, 0\n" \
    "s_cbranch_scc1 .Lr103_%=\nbuffer_load_dwordx4 v[40:43], v0, s[64:67], s74 offen offset:0 sc1\nbuffer_load_dwordx4 v[44:47], v0, s[64:67], s74 offen offset:1024 sc1\n" \
    "s_add_u32 s69, s74, 0x4000\nbuffer_load_dwordx4 v[48:51], v0, s[64:67], s69 offen offset:0 sc1\nbuffer_load_dwordx4 v[52:55], v0, s[64:67], s69 offen offset:1024 sc1\n" \
    ".Lr92_%=:\n.Lr104_%=:\ns_memrealtime s[72:73]\ns_waitcnt lgkmcnt(0)\ns_sub_u32 s72, s72, s51\ns_cmp_lt_i32 s72, -3\ns_cbranch_scc1 .Lr104_%=\ns_cmp_ge_i32 s72, 0\n" \
    "s_cbranch_scc1 .Lr105_%=\ns_nop 2\ns_cmp_ge_i32 s72, -1\ns_cbranch_scc1 .Lr105_%=\ns_nop 2\ns_cmp_ge_i32 s72, -2\ns_cbranch_scc1 .Lr105_%=\ns_nop 2\n.Lr105_%=:\n" \
    "buffer_load_dwordx2 v[116:117], v5, s[64:67], s87 offen offset:0 sc1\ns_nop 5\nbuffer_load_dwordx2 v[118:119], v5, s[64:67], s87 offen offset:128 sc1\ns_nop 5\n" \
    "buffer_load_dwordx4 v[100:103], v0, s[64:67], s87 offen offset:0 sc1\ns_nop 10\nbuffer_load_dwordx4 v[104:107], v0, s[64:67], s87 offen offset:1024 sc1\ns_nop 10\n" \
    "buffer_load_dwordx4 v[108:111], v0, s[64:67], s87 offen offset:2048 sc1\ns_nop 10\nbuffer_load_dwordx4 v[112:115], v0, s[64:67], s87 offen offset:3072 sc1\n" \
    "s_branch .Lr89_%=\n.Lr110_%=:\ns_sleep 3\ns_add_u32 s68, s68, 1\ns_and_b32 s69, s68, 63\ns_cbranch_scc1 .Lr108_%=\ns_cmp_gt_u32 s68, 0x200000\ns_cbranch_scc1 .Lr1_%=\n" \
    "buffer_load_dword v21, off, s[64:67], 0 sc1\ns_waitcnt vmcnt(0)\nv_readfirstlane_b32 s69, v21\ns_cmp_eq_u32 s69, 1\ns_cbranch_scc0 .Lr108_%=\ns_endpgm\n.Lr113_%=:\n" \
    "s_sleep 3\ns_add_u32 s68, s68, 1\ns_and_b32 s69, s68, 63\ns_cbranch_scc1 .Lr111_%=\ns_cmp_gt_u32 s68, 0x200000\ns_cbranch_scc1 .Lr1_%=\n" \
    "buffer_load_dword v21, off, s[64:67], 0 sc1\ns_waitcnt vmcnt(0)\nv_readfirstlane_b32 s69, v21\ns_cmp_eq_u32 s69, 1\ns_cbranch_scc0 .Lr111_%=\ns_endpgm\n.Lr107_%=:\n" \
    "s_waitcnt vmcnt(0)\ns_sleep 4\ns_add_u32 s68, s68, 1\ns_and_b32 s69, s68, 63\ns_cbranch_scc1 .Lr48_%=\ns_cmp_gt_u32 s68, 0x200000\ns_cbranch_scc1 .Lr1_%=\n" \
    "buffer_load_dword v21, off, s[64:67], 0 sc1\ns_waitcnt vmcnt(0)\nv_readfirstlane_b32 s69, v21\ns_cmp_eq_u32 s69, 1\ns_cbranch_scc0 .Lr48_%=\ns_endpgm\n.Lr6_%=:\ns_sleep 3\n" \
    "s_add_u32 s68, s68, 1\ns_and_b32 s69, s68, 63\ns_cbranch_scc1 .Lr5_%=\ns_cmp_gt_u32 s68, 0x200000\ns_cbranch_scc1 .Lr1_%=\nbuffer_load_dword v21, off, s[64:67], 0 sc1\n" \
    "s_waitcnt vmcnt(0)\nv_readfirstlane_b32 s69, v21\ns_cmp_eq_u32 s69, 1\ns_cbranch_scc0 .Lr5_%=\ns_endpgm\n.Lr50_%=:\ns_sleep 3\ns_add_u32 s68, s68, 1\n" \
    "s_and_b32 s69, s68, 63\ns_cbranch_scc1 .Lr49_%=\ns_cmp_gt_u32 s68, 0x200000\ns_cbranch_scc1 .Lr1_%=\nbuffer_load_dword v21, off, s[64:67], 0 sc1\ns_waitcnt vmcnt(0)\n" \
    "v_readfirstlane_b32 s69, v21\ns_cmp_eq_u32 s69, 1\ns_cbranch_scc0 .Lr49_%=\ns_endpgm\n.Lr1_%=:\nv_mov_b32 v21, 1\nbuffer_store_dword v21, off, s[64:67], 0 sc1\ns_endpgm\n" \
    ".Lr2_%=:\n"
#define ROLE_F1 \
    "s_mov_b32 s80, 0\ns_lshl_b32 s79, s54, 12\n.Lf7_%=:\ns_mov_b32 s68, 0\n.Lf6_%=:\ns_mov_b32 s71, s50\ns_and_b32 s69, s71, 1\ns_lshl_b32 s69, s69, 2\n" \
    "buffer_load_dword v9, v7, s[64:67], s69 offen sc1\ns_waitcnt vmcnt(0)\nv_readfirstlane_b32 s78, v9\ns_cmp_gt_i32 s78, s50\ns_cbranch_scc0 .Lf8_%=\n.Lf3_%=:\n.Lf13_%=:\n" \
    "s_add_u32 s72, s52, s51\nbuffer_load_dwordx2 v[104:105], v2, s[64:67], s53 offen offset:0 sc1\ns_add_u32 m0, s72, 0\ns_add_u32 s70, s53, s42\n" \
    "buffer_load_dwordx4 v0, s[64:67], s70 offen offset:0 sc1 lds\nbuffer_load_dwordx4 v0, s[64:67], s70 offen offset:1024 sc1 lds\n" \
    "buffer_load_dwordx4 v0, s[64:67], s70 offen offset:2048 sc1 lds\nbuffer_load_dwordx4 v0, s[64:67], s70 offen offset:3072 sc1 lds\n" \
    "buffer_load_dwordx2 v[106:107], v2, s[64:67], s53 offen offset:256 sc1\nbuffer_load_dwordx2 v[108:109], v2, s[64:67], s53 offen offset:512 sc1\n" \
    "buffer_load_dwordx2 v[110:111], v2, s[64:67], s53 offen offset:768 sc1\ns_add_u32 m0, s72, 16384\ns_add_u32 s70, s53, s43\n" \
    "buffer_load_dwordx4 v0, s[64:67], s70 offen offset:0 sc1 lds\nbuffer_load_dwordx4 v0, s[64:67], s70 offen offset:1024 sc1 lds\n" \
    "buffer_load_dwordx4 v0, s[64:67], s70 offen offset:2048 sc1 lds\nbuffer_load_dwordx4 v0, s[64:67], s70 offen offset:3072 sc1 lds\n.Lf14_%=:\ns_waitcnt vmcnt(11)\n" \
    "v_cmp_eq_u32 vcc, -1, v104\ns_cbranch_vccnz .Lf12_%=\nv_add_u32 v8, s51, v1\ns_waitcnt vmcnt(7)\ns_barrier\nds_read_b128 v[24:27], v8 offset:0\n" \
    "ds_read_b128 v[28:31], v8 offset:512\nds_read_b128 v[32:35], v8 offset:2048\nds_read_b128 v[36:39], v8 offset:2560\nds_read_b128 v[40:43], v8 offset:4096\n" \
    "ds_read_b128 v[44:47], v8 offset:4608\nds_read_b128 v[48:51], v8 offset:6144\nds_read_b128 v[52:55], v8 offset:6656\ns_waitcnt lgkmcnt(6)\n" \
    "v_mfma_f32_32x32x16_bf16 v[56:71], a[0:3], v[24:27], 0\nds_read_b128 v[24:27], v8 offset:8192\ns_add_u32 m0, s72, 32768\ns_add_u32 s70, s53, s44\n" \
    "buffer_load_dwordx4 v0, s[64:67], s70 offen offset:0 sc1 lds\nv_mfma_f32_32x32x16_bf16 v[56:71], a[4:7], v[28:31], v[56:71]\ns_waitcnt lgkmcnt(5)\n" \
    "ds_read_b128 v[28:31], v8 offset:8704\nv_mfma_f32_32x32x16_bf16 v[56:71], a[8:11], v[32:35], v[56:71]\nds_read_b128 v[32:35], v8 offset:10240\n" \
    "buffer_load_dwordx4 v0, s[64:67], s70 offen offset:1024 sc1 lds\ns_add_u32 s71, s50, 1\ns_cmp_ge_u32 s71, s56\ns_addc_u32 s71, s71, 0\n" \
    "v_mfma_f32_32x32x16_bf16 v[56:71], a[12:15], v[36:39], v[56:71]\ns_waitcnt lgkmcnt(5)\nds_read_b128 v[36:39], v8 offset:10752\ns_min_u32 s73, s71, 63\n" \
    "s_lshl_b32 s73, s73, 20\ns_add_u32 s73, s73, s40\ns_add_u32 s74, s48, s73\nv_mfma_f32_32x32x16_bf16 v[56:71], a[16:19], v[40:43], v[56:71]\n" \
    "ds_read_b128 v[40:43], v8 offset:12288\nbuffer_load_dwordx4 v0, s[64:67], s70 offen offset:2048 sc1 lds\ns_lshr_b32 s74, s74, 8\ns_xor_b32 s74, s74, s49\n" \
    "s_and_b32 s74, s74, 0x2000\nv_mfma_f32_32x32x16_bf16 v[56:71], a[20:23], v[44:47], v[56:71]\ns_waitcnt lgkmcnt(5)\nds_read_b128 v[44:47], v8 offset:12800\n" \
    "s_add_u32 s74, s74, s73\ns_xor_b32 s75, s51, 0x10000\ns_add_u32 s75, s75, s52\nv_mfma_f32_32x32x16_bf16 v[56:71], a[24:27], v[48:51], v[56:71]\n" \
    "ds_read_b128 v[48:51], v8 offset:14336\nbuffer_load_dwordx4 v0, s[64:67], s70 offen offset:3072 sc1 lds\nv_mfma_f32_32x32x16_bf16 v[56:71], a[28:31], v[52:55], v[56:71]\n" \
    "s_waitcnt lgkmcnt(5)\ns_waitcnt vmcnt(4)\ns_barrier\nds_read_b128 v[52:55], v8 offset:14848\nv_mfma_f32_32x32x16_bf16 v[56:71], a[32:35], v[24:27], v[56:71]\n" \
    "ds_read_b128 v[24:27], v8 offset:16384\ns_add_u32 m0, s72, 49152\ns_add_u32 s70, s53, s45\nv_mfma_f32_32x32x16_bf16 v[56:71], a[36:39], v[28:31], v[56:71]\n" \
    "s_waitcnt lgkmcnt(5)\nds_read_b128 v[28:31], v8 offset:16896\nbuffer_load_dwordx4 v0, s[64:67], s70 offen offset:0 sc1 lds\n" \
    "v_mfma_f32_32x32x16_bf16 v[56:71], a[40:43], v[32:35], v[56:71]\nds_read_b128 v[32:35], v8 offset:18432\nv_mfma_f32_32x32x16_bf16 v[56:71], a[44:47], v[36:39], v[56:71]\n" \
    "s_waitcnt lgkmcnt(5)\nds_read_b128 v[36:39], v8 offset:18944\nbuffer_load_dwordx4 v0, s[64:67], s70 offen offset:1024 sc1 lds\n" \
    "v_mfma_f32_32x32x16_bf16 v[56:71], a[48:51], v[40:43], v[56:71]\nds_read_b128 v[40:43], v8 offset:20480\ns_and_b32 s69, s71, 1\ns_lshl_b32 s69, s69, 2\n" \
    "buffer_load_dword v9, v7, s[64:67], s69 offen sc1\nv_mfma_f32_32x32x16_bf16 v[56:71], a[52:55], v[44:47], v[56:71]\ns_waitcnt lgkmcnt(5)\n" \
    "ds_read_b128 v[44:47], v8 offset:20992\nbuffer_load_dwordx4 v0, s[64:67], s70 offen offset:2048 sc1 lds\nv_mfma_f32_32x32x16_bf16 v[56:71], a[56:59], v[48:51], v[56:71]\n" \
    "ds_read_b128 v[48:51], v8 offset:22528\nv_mfma_f32_32x32x16_bf16 v[56:71], a[60:63], v[52:55], v[56:71]\ns_waitcnt lgkmcnt(5)\nds_read_b128 v[52:55], v8 offset:23040\n" \
    "buffer_load_dwordx4 v0, s[64:67], s70 offen offset:3072 sc1 lds\nv_mfma_f32_32x32x16_bf16 v[56:71], a[64:67], v[24:27], v[56:71]\nds_read_b128 v[24:27], v8 offset:24576\n" \
    "v_mov_b32 v10, 1\nv_mfma_f32_32x32x16_bf16 v[56:71], a[68:71], v[28:31], v[56:71]\ns_waitcnt lgkmcnt(5)\nds_read_b128 v[28:31], v8 offset:25088\ns_lshl_b32 s81, s50, 2\n" \
    "s_add_u32 s81, s81, s55\nv_mfma_f32_32x32x16_bf16 v[56:71], a[72:75], v[32:35], v[56:71]\nds_read_b128 v[32:35], v8 offset:26624\n" \
    "v_mfma_f32_32x32x16_bf16 v[56:71], a[76:79], v[36:39], v[56:71]\ns_waitcnt lgkmcnt(5)\nds_read_b128 v[36:39], v8 offset:27136\n" \
    "v_mfma_f32_32x32x16_bf16 v[56:71], a[80:83], v[40:43], v[56:71]\nds_read_b128 v[40:43], v8 offset:28672\nv_mfma_f32_32x32x16_bf16 v[56:71], a[84:87], v[44:47], v[56:71]\n" \
    "s_waitcnt lgkmcnt(5)\nds_read_b128 v[44:47], v8 offset:29184\nv_mfma_f32_32x32x16_bf16 v[56:71], a[88:91], v[48:51], v[56:71]\nds_read_b128 v[48:51], v8 offset:30720\n" \
    "v_max3_u32 v12, v104, v105, v106\nv_max3_u32 v12, v12, v107, v108\nv_max3_u32 v12, v12, v109, v110\nv_max_u32 v12, v12, v111\n" \
    "v_mfma_f32_32x32x16_bf16 v[56:71], a[92:95], v[52:55], v[56:71]\ns_waitcnt lgkmcnt(5)\ns_waitcnt vmcnt(5)\ns_barrier\nds_read_b128 v[52:55], v8 offset:31232\n" \
    "v_mfma_f32_32x32x16_bf16 v[56:71], a[96:99], v[24:27], v[56:71]\nds_read_b128 v[24:27], v8 offset:32768\nv_cmp_eq_u32 vcc, -1, v12\n" \
    "v_mfma_f32_32x32x16_bf16 v[56:71], a[100:103], v[28:31], v[56:71]\ns_waitcnt lgkmcnt(5)\nds_read_b128 v[28:31], v8 offset:33280\ns_cbranch_vccnz .Lf16_%=\n.Lf17_%=:\n" \
    "v_add_f32 v12, v104, v106\nv_add_f32 v13, v108, v110\nv_mfma_f32_32x32x16_bf16 v[56:71], a[104:107], v[32:35], v[56:71]\nds_read_b128 v[32:35], v8 offset:34816\n" \
    "s_waitcnt vmcnt(2)\nv_add_f32 v12, v12, v13\nv_add_f32 v105, v105, v107\nv_add_f32 v109, v109, v111\nv_readfirstlane_b32 s76, v9\n" \
    "v_mfma_f32_32x32x16_bf16 v[56:71], a[108:111], v[36:39], v[56:71]\ns_waitcnt lgkmcnt(5)\nds_read_b128 v[36:39], v8 offset:35328\ns_cmp_gt_i32 s76, s71\n" \
    "s_cselect_b32 s76, 0, 0x7f000000\nv_add_f32_dpp v13, v12, v12 row_ror:8 row_mask:0xf bank_mask:0xf\nv_add_f32_dpp v14, v12, v12 row_ror:8 row_mask:0xf bank_mask:0xf\n" \
    "v_mfma_f32_32x32x16_bf16 v[56:71], a[112:115], v[40:43], v[56:71]\nds_read_b128 v[40:43], v8 offset:36864\nv_permlane16_swap_b32 v13, v14\nv_add_f32 v12, v13, v14\n" \
    "v_add_f32 v13, v13, v14\nv_or_b32 v11, s76, v0\nv_mfma_f32_32x32x16_bf16 v[56:71], a[116:119], v[44:47], v[56:71]\ns_waitcnt lgkmcnt(5)\n" \
    "ds_read_b128 v[44:47], v8 offset:37376\nv_permlane32_swap_b32 v12, v13\nv_add_f32 v12, v12, v13\nv_mul_f32 v12, 0x3d000000, v12\n" \
    "v_mfma_f32_32x32x16_bf16 v[56:71], a[120:123], v[48:51], v[56:71]\nds_read_b128 v[48:51], v8 offset:38912\nv_sub_f32 v104, v104, v12\nv_sub_f32 v106, v106, v12\n" \
    "v_sub_f32 v108, v108, v12\nv_sub_f32 v110, v110, v12\nv_mul_f32 v13, v104, v104\nv_mfma_f32_32x32x16_bf16 v[56:71], a[124:127], v[52:55], v[56:71]\ns_waitcnt lgkmcnt(5)\n" \
    "ds_read_b128 v[52:55], v8 offset:39424\nv_fmac_f32 v13, v106, v106\nv_fmac_f32 v13, v108, v108\nv_fmac_f32 v13, v110, v110\nv_add_f32 v14, v105, v109\n" \
    "v_mfma_f32_32x32x16_bf16 v[56:71], a[128:131], v[24:27], v[56:71]\nds_read_b128 v[24:27], v8 offset:40960\ns_add_u32 m0, s75, 0\ns_add_u32 s70, s74, s42\n" \
    "buffer_load_dwordx4 v11, s[64:67], s70 offen offset:0 sc1 lds\nv_fmamk_f32 v13, v13, 0x42000000, v14\nv_mfma_f32_32x32x16_bf16 v[56:71], a[132:135], v[28:31], v[56:71]\n" \
    "s_waitcnt lgkmcnt(5)\nds_read_b128 v[28:31], v8 offset:41472\nbuffer_load_dwordx4 v11, s[64:67], s70 offen offset:1024 sc1 lds\n" \
    "v_add_f32_dpp v14, v13, v13 row_ror:8 row_mask:0xf bank_mask:0xf\nv_add_f32_dpp v15, v13, v13 row_ror:8 row_mask:0xf bank_mask:0xf\n" \
    "v_mfma_f32_32x32x16_bf16 v[56:71], a[136:139], v[32:35], v[56:71]\nds_read_b128 v[32:35], v8 offset:43008\nv_permlane16_swap_b32 v14, v15\nv_add_f32 v13, v14, v15\n" \
    "v_add_f32 v14, v14, v15\nv_mfma_f32_32x32x16_bf16 v[56:71], a[140:143], v[36:39], v[56:71]\ns_waitcnt lgkmcnt(5)\nds_read_b128 v[36:39], v8 offset:43520\n" \
    "v_permlane32_swap_b32 v13, v14\nv_add_f32 v13, v13, v14\nv_mov_b32 v14, 0x3727c5ac\nv_mfma_f32_32x32x16_bf16 v[56:71], a[144:147], v[40:43], v[56:71]\n" \
    "ds_read_b128 v[40:43], v8 offset:45056\nbuffer_load_dwordx4 v11, s[64:67], s70 offen offset:2048 sc1 lds\nv_fmamk_f32 v13, v13, 0x3a800000, v14\nv_rsq_f32 v13, v13\n" \
    "v_mfma_f32_32x32x16_bf16 v[56:71], a[148:151], v[44:47], v[56:71]\ns_waitcnt lgkmcnt(5)\nds_read_b128 v[44:47], v8 offset:45568\nds_write_b64 v3, v[12:13]\n" \
    "v_mfma_f32_32x32x16_bf16 v[56:71], a[152:155], v[48:51], v[56:71]\nds_read_b128 v[48:51], v8 offset:47104\n" \
    "buffer_load_dwordx4 v11, s[64:67], s70 offen offset:3072 sc1 lds\ns_add_u32 m0, s75, 16384\ns_add_u32 s70, s74, s43\n" \
    "v_mfma_f32_32x32x16_bf16 v[56:71], a[156:159], v[52:55], v[56:71]\ns_waitcnt lgkmcnt(6)\ns_waitcnt vmcnt(4) lgkmcnt(1)\ns_barrier\nds_read_b128 v[52:55], v8 offset:47616\n" \
    "v_mfma_f32_32x32x16_bf16 v[56:71], a[160:163], v[24:27], v[56:71]\nds_read_b128 v[24:27], v8 offset:49152\nbuffer_load_dwordx4 v11, s[64:67], s70 offen offset:0 sc1 lds\n" \
    "v_mfma_f32_32x32x16_bf16 v[56:71], a[164:167], v[28:31], v[56:71]\nds_read_b128 v[28:31], v8 offset:49664\ns_cmp_eq_u32 s54, 3\ns_cselect_b64 exec, 1, 0\n" \
    "buffer_atomic_add v10, off, s[64:67], s81 sc0\ns_mov_b64 exec, -1\nv_mfma_f32_32x32x16_bf16 v[56:71], a[168:171], v[32:35], v[56:71]\n" \
    "ds_read_b128 v[32:35], v8 offset:51200\nbuffer_load_dwordx4 v11, s[64:67], s70 offen offset:1024 sc1 lds\n" \
    "v_mfma_f32_32x32x16_bf16 v[56:71], a[172:175], v[36:39], v[56:71]\nds_read_b128 v[36:39], v8 offset:51712\n" \
    "buffer_load_dwordx4 v11, s[64:67], s70 offen offset:2048 sc1 lds\nv_mfma_f32_32x32x16_bf16 v[56:71], a[176:179], v[40:43], v[56:71]\n" \
    "ds_read_b128 v[40:43], v8 offset:53248\nds_read_b64 v[14:15], v4\nbuffer_load_dwordx4 v11, s[64:67], s70 offen offset:3072 sc1 lds\n" \
    "v_mfma_f32_32x32x16_bf16 v[56:71], a[180:183], v[44:47], v[56:71]\ns_waitcnt lgkmcnt(6)\nds_read_b128 v[44:47], v8 offset:53760\n" \
    "v_mfma_f32_32x32x16_bf16 v[56:71], a[184:187], v[48:51], v[56:71]\nds_read_b128 v[48:51], v8 offset:55296\ns_add_u32 m0, s75, 32768\ns_add_u32 s70, s74, s44\n" \
    "buffer_load_dwordx4 v11, s[64:67], s70 offen offset:0 sc1 lds\nv_mfma_f32_32x32x16_bf16 v[56:71], a[188:191], v[52:55], v[56:71]\ns_waitcnt lgkmcnt(6)\n" \
    "ds_read_b128 v[52:55], v8 offset:55808\ns_and_b32 s69, s71, 1\ns_lshl_b32 s69, s69, 2\nbuffer_load_dword v9, v7, s[64:67], s69 offen sc1\n" \
    "v_mfma_f32_32x32x16_bf16 v[56:71], a[192:195], v[24:27], v[56:71]\nds_read_b128 v[24:27], v8 offset:57344\n" \
    "buffer_load_dwordx4 v11, s[64:67], s70 offen offset:1024 sc1 lds\nv_mfma_f32_32x32x16_bf16 v[56:71], a[196:199], v[28:31], v[56:71]\ns_waitcnt lgkmcnt(6)\n" \
    "ds_read_b128 v[28:31], v8 offset:57856\nv_mfma_f32_32x32x16_bf16 v[56:71], a[200:203], v[32:35], v[56:71]\nds_read_b128 v[32:35], v8 offset:59392\n" \
    "buffer_load_dwordx4 v11, s[64:67], s70 offen offset:2048 sc1 lds\nv_mfma_f32_32x32x16_bf16 v[56:71], a[204:207], v[36:39], v[56:71]\ns_waitcnt lgkmcnt(5)\n" \
    "ds_read_b128 v[36:39], v8 offset:59904\nv_mfma_f32_32x32x16_bf16 v[56:71], a[208:211], v[40:43], v[56:71]\nds_read_b128 v[40:43], v8 offset:61440\n" \
    "buffer_load_dwordx4 v11, s[64:67], s70 offen offset:3072 sc1 lds\nv_mfma_f32_32x32x16_bf16 v[56:71], a[212:215], v[44:47], v[56:71]\ns_waitcnt lgkmcnt(5)\n" \
    "ds_read_b128 v[44:47], v8 offset:61952\nv_mfma_f32_32x32x16_bf16 v[56:71], a[216:219], v[48:51], v[56:71]\nds_read_b128 v[48:51], v8 offset:63488\n" \
    "s_add_u32 m0, s75, 49152\ns_add_u32 s70, s74, s45\nbuffer_load_dwordx4 v11, s[64:67], s70 offen offset:0 sc1 lds\n" \
    "v_mfma_f32_32x32x16_bf16 v[56:71], a[220:223], v[52:55], v[56:71]\ns_waitcnt lgkmcnt(5)\nds_read_b128 v[52:55], v8 offset:64000\n" \
    "v_mfma_f32_32x32x16_bf16 v[56:71], a[224:227], v[24:27], v[56:71]\nbuffer_load_dwordx4 v11, s[64:67], s70 offen offset:1024 sc1 lds\n" \
    "v_mfma_f32_32x32x16_bf16 v[56:71], a[228:231], v[28:31], v[56:71]\ns_waitcnt lgkmcnt(4)\nv_mfma_f32_32x32x16_bf16 v[56:71], a[232:235], v[32:35], v[56:71]\n" \
    "buffer_load_dwordx4 v11, s[64:67], s70 offen offset:2048 sc1 lds\nv_mfma_f32_32x32x16_bf16 v[56:71], a[236:239], v[36:39], v[56:71]\ns_waitcnt lgkmcnt(2)\n" \
    "v_mfma_f32_32x32x16_bf16 v[56:71], a[240:243], v[40:43], v[56:71]\nbuffer_load_dwordx4 v11, s[64:67], s70 offen offset:3072 sc1 lds\n" \
    "v_mfma_f32_32x32x16_bf16 v[56:71], a[244:247], v[44:47], v[56:71]\ns_waitcnt lgkmcnt(0)\ns_waitcnt vmcnt(9)\nv_readfirstlane_b32 s81, v10\n" \
    "v_mfma_f32_32x32x16_bf16 v[56:71], a[248:251], v[48:51], v[56:71]\nv_mfma_f32_32x32x16_bf16 v[56:71], a[252:255], v[52:55], v[56:71]\ns_waitcnt vmcnt(7)\n" \
    "v_readfirstlane_b32 s78, v9\ns_and_b32 s69, s71, 1\ns_lshl_b32 s69, s69, 2\ns_branch .Lf5_%=\n.Lf4_%=:\ns_add_u32 s72, s52, s51\nv_add_u32 v8, s51, v1\n" \
    "s_waitcnt vmcnt(16)\ns_barrier\nds_read_b128 v[24:27], v8 offset:0\nds_read_b128 v[28:31], v8 offset:512\nds_read_b128 v[32:35], v8 offset:2048\n" \
    "ds_read_b128 v[36:39], v8 offset:2560\nds_read_b128 v[40:43], v8 offset:4096\nds_read_b128 v[44:47], v8 offset:4608\nds_read_b128 v[48:51], v8 offset:6144\n" \
    "ds_read_b128 v[52:55], v8 offset:6656\ns_waitcnt lgkmcnt(6)\nv_mfma_f32_32x32x16_bf16 v[56:71], a[0:3], v[24:27], 0\nds_read_b128 v[24:27], v8 offset:8192\n" \
    "buffer_load_dwordx2 v[104:105], v2, s[64:67], s53 offen offset:0 sc1\nv_mfma_f32_32x32x16_bf16 v[56:71], a[4:7], v[28:31], v[56:71]\ns_waitcnt lgkmcnt(5)\n" \
    "ds_read_b128 v[28:31], v8 offset:8704\nbuffer_load_dwordx2 v[106:107], v2, s[64:67], s53 offen offset:256 sc1\n" \
    "v_mfma_f32_32x32x16_bf16 v[56:71], a[8:11], v[32:35], v[56:71]\nds_read_b128 v[32:35], v8 offset:10240\n" \
    "buffer_load_dwordx2 v[108:109], v2, s[64:67], s53 offen offset:512 sc1\ns_add_u32 s71, s50, 1\ns_cmp_ge_u32 s71, s56\ns_addc_u32 s71, s71, 0\n" \
    "v_mfma_f32_32x32x16_bf16 v[56:71], a[12:15], v[36:39], v[56:71]\ns_waitcnt lgkmcnt(5)\nds_read_b128 v[36:39], v8 offset:10752\n" \
    "buffer_load_dwordx2 v[110:111], v2, s[64:67], s53 offen offset:768 sc1\ns_min_u32 s73, s71, 63\ns_lshl_b32 s73, s73, 20\n" \
    "v_mfma_f32_32x32x16_bf16 v[56:71], a[16:19], v[40:43], v[56:71]\nds_read_b128 v[40:43], v8 offset:12288\ns_add_u32 s73, s73, s40\ns_add_u32 s74, s48, s73\n" \
    "s_lshr_b32 s74, s74, 8\ns_xor_b32 s74, s74, s49\ns_and_b32 s74, s74, 0x2000\nv_mfma_f32_32x32x16_bf16 v[56:71], a[20:23], v[44:47], v[56:71]\ns_waitcnt lgkmcnt(5)\n" \
    "ds_read_b128 v[44:47], v8 offset:12800\ns_add_u32 s74, s74, s73\ns_xor_b32 s75, s51, 0x10000\ns_add_u32 s75, s75, s52\n" \
    "v_mfma_f32_32x32x16_bf16 v[56:71], a[24:27], v[48:51], v[56:71]\nds_read_b128 v[48:51], v8 offset:14336\nv_mfma_f32_32x32x16_bf16 v[56:71], a[28:31], v[52:55], v[56:71]\n" \
    "s_waitcnt lgkmcnt(5)\ns_waitcnt vmcnt(16)\ns_barrier\nds_read_b128 v[52:55], v8 offset:14848\nv_mfma_f32_32x32x16_bf16 v[56:71], a[32:35], v[24:27], v[56:71]\n" \
    "ds_read_b128 v[24:27], v8 offset:16384\nv_mfma_f32_32x32x16_bf16 v[56:71], a[36:39], v[28:31], v[56:71]\ns_waitcnt lgkmcnt(5)\nds_read_b128 v[28:31], v8 offset:16896\n" \
    "v_mfma_f32_32x32x16_bf16 v[56:71], a[40:43], v[32:35], v[56:71]\nds_read_b128 v[32:35], v8 offset:18432\nv_mfma_f32_32x32x16_bf16 v[56:71], a[44:47], v[36:39], v[56:71]\n" \
    "s_waitcnt lgkmcnt(5)\nds_read_b128 v[36:39], v8 offset:18944\nv_mfma_f32_32x32x16_bf16 v[56:71], a[48:51], v[40:43], v[56:71]\nds_read_b128 v[40:43], v8 offset:20480\n" \
    "s_and_b32 s69, s71, 1\ns_lshl_b32 s69, s69, 2\nbuffer_load_dword v9, v7, s[64:67], s69 offen sc1\nv_mfma_f32_32x32x16_bf16 v[56:71], a[52:55], v[44:47], v[56:71]\n" \
    "s_waitcnt lgkmcnt(5)\nds_read_b128 v[44:47], v8 offset:20992\nv_mfma_f32_32x32x16_bf16 v[56:71], a[56:59], v[48:51], v[56:71]\nds_read_b128 v[48:51], v8 offset:22528\n" \
    "v_mfma_f32_32x32x16_bf16 v[56:71], a[60:63], v[52:55], v[56:71]\ns_waitcnt lgkmcnt(5)\nds_read_b128 v[52:55], v8 offset:23040\n" \
    "v_mfma_f32_32x32x16_bf16 v[56:71], a[64:67], v[24:27], v[56:71]\nds_read_b128 v[24:27], v8 offset:24576\nv_mov_b32 v10, 1\n" \
    "v_mfma_f32_32x32x16_bf16 v[56:71], a[68:71], v[28:31], v[56:71]\ns_waitcnt lgkmcnt(5)\nds_read_b128 v[28:31], v8 offset:25088\ns_lshl_b32 s81, s50, 2\n" \
    "s_add_u32 s81, s81, s55\nv_mfma_f32_32x32x16_bf16 v[56:71], a[72:75], v[32:35], v[56:71]\nds_read_b128 v[32:35], v8 offset:26624\n" \
    "v_mfma_f32_32x32x16_bf16 v[56:71], a[76:79], v[36:39], v[56:71]\ns_waitcnt lgkmcnt(5)\nds_read_b128 v[36:39], v8 offset:27136\n" \
    "v_mfma_f32_32x32x16_bf16 v[56:71], a[80:83], v[40:43], v[56:71]\nds_read_b128 v[40:43], v8 offset:28672\nv_mfma_f32_32x32x16_bf16 v[56:71], a[84:87], v[44:47], v[56:71]\n" \
    "s_waitcnt lgkmcnt(5)\nds_read_b128 v[44:47], v8 offset:29184\nv_mfma_f32_32x32x16_bf16 v[56:71], a[88:91], v[48:51], v[56:71]\nds_read_b128 v[48:51], v8 offset:30720\n" \
    "s_waitcnt vmcnt(1)\nv_max3_u32 v12, v104, v105, v106\nv_max3_u32 v12, v12, v107, v108\nv_max3_u32 v12, v12, v109, v110\nv_max_u32 v12, v12, v111\n" \
    "v_mfma_f32_32x32x16_bf16 v[56:71], a[92:95], v[52:55], v[56:71]\ns_waitcnt lgkmcnt(5)\ns_barrier\nds_read_b128 v[52:55], v8 offset:31232\n" \
    "v_mfma_f32_32x32x16_bf16 v[56:71], a[96:99], v[24:27], v[56:71]\nds_read_b128 v[24:27], v8 offset:32768\nv_cmp_eq_u32 vcc, -1, v12\n" \
    "v_mfma_f32_32x32x16_bf16 v[56:71], a[100:103], v[28:31], v[56:71]\ns_waitcnt lgkmcnt(5)\nds_read_b128 v[28:31], v8 offset:33280\ns_cbranch_vccnz .Lf19_%=\n.Lf20_%=:\n" \
    "v_add_f32 v12, v104, v106\nv_add_f32 v13, v108, v110\nv_mfma_f32_32x32x16_bf16 v[56:71], a[104:107], v[32:35], v[56:71]\nds_read_b128 v[32:35], v8 offset:34816\n" \
    "s_waitcnt vmcnt(0)\nv_add_f32 v12, v12, v13\nv_add_f32 v105, v105, v107\nv_add_f32 v109, v109, v111\nv_readfirstlane_b32 s76, v9\n" \
    "v_mfma_f32_32x32x16_bf16 v[56:71], a[108:111], v[36:39], v[56:71]\ns_waitcnt lgkmcnt(5)\nds_read_b128 v[36:39], v8 offset:35328\ns_cmp_gt_i32 s76, s71\n" \
    "s_cselect_b32 s76, 0, 0x7f000000\nv_add_f32_dpp v13, v12, v12 row_ror:8 row_mask:0xf bank_mask:0xf\nv_add_f32_dpp v14, v12, v12 row_ror:8 row_mask:0xf bank_mask:0xf\n" \
    "v_mfma_f32_32x32x16_bf16 v[56:71], a[112:115], v[40:43], v[56:71]\nds_read_b128 v[40:43], v8 offset:36864\nv_permlane16_swap_b32 v13, v14\nv_add_f32 v12, v13, v14\n" \
    "v_add_f32 v13, v13, v14\nv_or_b32 v11, s76, v0\nv_mfma_f32_32x32x16_bf16 v[56:71], a[116:119], v[44:47], v[56:71]\ns_waitcnt lgkmcnt(5)\n" \
    "ds_read_b128 v[44:47], v8 offset:37376\nv_permlane32_swap_b32 v12, v13\nv_add_f32 v12, v12, v13\nv_mul_f32 v12, 0x3d000000, v12\n" \
    "v_mfma_f32_32x32x16_bf16 v[56:71], a[120:123], v[48:51], v[56:71]\nds_read_b128 v[48:51], v8 offset:38912\nv_sub_f32 v104, v104, v12\nv_sub_f32 v106, v106, v12\n" \
    "v_sub_f32 v108, v108, v12\nv_sub_f32 v110, v110, v12\nv_mul_f32 v13, v104, v104\nv_mfma_f32_32x32x16_bf16 v[56:71], a[124:127], v[52:55], v[56:71]\ns_waitcnt lgkmcnt(5)\n" \
    "ds_read_b128 v[52:55], v8 offset:39424\nv_fmac_f32 v13, v106, v106\nv_fmac_f32 v13, v108, v108\nv_fmac_f32 v13, v110, v110\nv_add_f32 v14, v105, v109\n" \
    "v_mfma_f32_32x32x16_bf16 v[56:71], a[128:131], v[24:27], v[56:71]\nds_read_b128 v[24:27], v8 offset:40960\ns_add_u32 m0, s75, 0\ns_add_u32 s70, s74, s42\n" \
    "buffer_load_dwordx4 v11, s[64:67], s70 offen offset:0 sc1 lds\nv_fmamk_f32 v13, v13, 0x42000000, v14\nv_mfma_f32_32x32x16_bf16 v[56:71], a[132:135], v[28:31], v[56:71]\n" \
    "s_waitcnt lgkmcnt(5)\nds_read_b128 v[28:31], v8 offset:41472\nbuffer_load_dwordx4 v11, s[64:67], s70 offen offset:1024 sc1 lds\n" \
    "v_add_f32_dpp v14, v13, v13 row_ror:8 row_mask:0xf bank_mask:0xf\nv_add_f32_dpp v15, v13, v13 row_ror:8 row_mask:0xf bank_mask:0xf\n" \
    "v_mfma_f32_32x32x16_bf16 v[56:71], a[136:139], v[32:35], v[56:71]\nds_read_b128 v[32:35], v8 offset:43008\nv_permlane16_swap_b32 v14, v15\nv_add_f32 v13, v14, v15\n" \
    "v_add_f32 v14, v14, v15\nv_mfma_f32_32x32x16_bf16 v[56:71], a[140:143], v[36:39], v[56:71]\ns_waitcnt lgkmcnt(5)\nds_read_b128 v[36:39], v8 offset:43520\n" \
    "v_permlane32_swap_b32 v13, v14\nv_add_f32 v13, v13, v14\nv_mov_b32 v14, 0x3727c5ac\nv_mfma_f32_32x32x16_bf16 v[56:71], a[144:147], v[40:43], v[56:71]\n" \
    "ds_read_b128 v[40:43], v8 offset:45056\nbuffer_load_dwordx4 v11, s[64:67], s70 offen offset:2048 sc1 lds\nv_fmamk_f32 v13, v13, 0x3a800000, v14\nv_rsq_f32 v13, v13\n" \
    "v_mfma_f32_32x32x16_bf16 v[56:71], a[148:151], v[44:47], v[56:71]\ns_waitcnt lgkmcnt(5)\nds_read_b128 v[44:47], v8 offset:45568\nds_write_b64 v3, v[12:13]\n" \
    "v_mfma_f32_32x32x16_bf16 v[56:71], a[152:155], v[48:51], v[56:71]\nds_read_b128 v[48:51], v8 offset:47104\n" \
    "buffer_load_dwordx4 v11, s[64:67], s70 offen offset:3072 sc1 lds\ns_add_u32 m0, s75, 16384\ns_add_u32 s70, s74, s43\n" \
    "v_mfma_f32_32x32x16_bf16 v[56:71], a[156:159], v[52:55], v[56:71]\ns_waitcnt lgkmcnt(6)\ns_waitcnt lgkmcnt(1)\ns_barrier\nds_read_b128 v[52:55], v8 offset:47616\n" \
    "v_mfma_f32_32x32x16_bf16 v[56:71], a[160:163], v[24:27], v[56:71]\nds_read_b128 v[24:27], v8 offset:49152\nbuffer_load_dwordx4 v11, s[64:67], s70 offen offset:0 sc1 lds\n" \
    "v_mfma_f32_32x32x16_bf16 v[56:71], a[164:167], v[28:31], v[56:71]\nds_read_b128 v[28:31], v8 offset:49664\ns_cmp_eq_u32 s54, 3\ns_cselect_b64 exec, 1, 0\n" \
    "buffer_atomic_add v10, off, s[64:67], s81 sc0\ns_mov_b64 exec, -1\nv_mfma_f32_32x32x16_bf16 v[56:71], a[168:171], v[32:35], v[56:71]\n" \
    "ds_read_b128 v[32:35], v8 offset:51200\nbuffer_load_dwordx4 v11, s[64:67], s70 offen offset:1024 sc1 lds\n" \
    "v_mfma_f32_32x32x16_bf16 v[56:71], a[172:175], v[36:39], v[56:71]\nds_read_b128 v[36:39], v8 offset:51712\n" \
    "buffer_load_dwordx4 v11, s[64:67], s70 offen offset:2048 sc1 lds\nv_mfma_f32_32x32x16_bf16 v[56:71], a[176:179], v[40:43], v[56:71]\n" \
    "ds_read_b128 v[40:43], v8 offset:53248\nds_read_b64 v[14:15], v4\nbuffer_load_dwordx4 v11, s[64:67], s70 offen offset:3072 sc1 lds\n" \
    "v_mfma_f32_32x32x16_bf16 v[56:71], a[180:183], v[44:47], v[56:71]\ns_waitcnt lgkmcnt(6)\nds_read_b128 v[44:47], v8 offset:53760\n" \
    "v_mfma_f32_32x32x16_bf16 v[56:71], a[184:187], v[48:51], v[56:71]\nds_read_b128 v[48:51], v8 offset:55296\ns_add_u32 m0, s75, 32768\ns_add_u32 s70, s74, s44\n" \
    "buffer_load_dwordx4 v11, s[64:67], s70 offen offset:0 sc1 lds\nv_mfma_f32_32x32x16_bf16 v[56:71], a[188:191], v[52:55], v[56:71]\ns_waitcnt lgkmcnt(6)\n" \
    "ds_read_b128 v[52:55], v8 offset:55808\ns_and_b32 s69, s71, 1\ns_lshl_b32 s69, s69, 2\nbuffer_load_dword v9, v7, s[64:67], s69 offen sc1\n" \
    "v_mfma_f32_32x32x16_bf16 v[56:71], a[192:195], v[24:27], v[56:71]\nds_read_b128 v[24:27], v8 offset:57344\n" \
    "buffer_load_dwordx4 v11, s[64:67], s70 offen offset:1024 sc1 lds\nv_mfma_f32_32x32x16_bf16 v[56:71], a[196:199], v[28:31], v[56:71]\ns_waitcnt lgkmcnt(6)\n" \
    "ds_read_b128 v[28:31], v8 offset:57856\nv_mfma_f32_32x32x16_bf16 v[56:71], a[200:203], v[32:35], v[56:71]\nds_read_b128 v[32:35], v8 offset:59392\n" \
    "buffer_load_dwordx4 v11, s[64:67], s70 offen offset:2048 sc1 lds\nv_mfma_f32_32x32x16_bf16 v[56:71], a[204:207], v[36:39], v[56:71]\ns_waitcnt lgkmcnt(5)\n" \
    "ds_read_b128 v[36:39], v8 offset:59904\nv_mfma_f32_32x32x16_bf16 v[56:71], a[208:211], v[40:43], v[56:71]\nds_read_b128 v[40:43], v8 offset:61440\n" \
    "buffer_load_dwordx4 v11, s[64:67], s70 offen offset:3072 sc1 lds\nv_mfma_f32_32x32x16_bf16 v[56:71], a[212:215], v[44:47], v[56:71]\ns_waitcnt lgkmcnt(5)\n" \
    "ds_read_b128 v[44:47], v8 offset:61952\nv_mfma_f32_32x32x16_bf16 v[56:71], a[216:219], v[48:51], v[56:71]\nds_read_b128 v[48:51], v8 offset:63488\n" \
    "s_add_u32 m0, s75, 49152\ns_add_u32 s70, s74, s45\nbuffer_load_dwordx4 v11, s[64:67], s70 offen offset:0 sc1 lds\n" \
    "v_mfma_f32_32x32x16_bf16 v[56:71], a[220:223], v[52:55], v[56:71]\ns_waitcnt lgkmcnt(5)\nds_read_b128 v[52:55], v8 offset:64000\n" \
    "v_mfma_f32_32x32x16_bf16 v[56:71], a[224:227], v[24:27], v[56:71]\nbuffer_load_dwordx4 v11, s[64:67], s70 offen offset:1024 sc1 lds\n" \
    "v_mfma_f32_32x32x16_bf16 v[56:71], a[228:231], v[28:31], v[56:71]\ns_waitcnt lgkmcnt(4)\nv_mfma_f32_32x32x16_bf16 v[56:71], a[232:235], v[32:35], v[56:71]\n" \
    "buffer_load_dwordx4 v11, s[64:67], s70 offen offset:2048 sc1 lds\nv_mfma_f32_32x32x16_bf16 v[56:71], a[236:239], v[36:39], v[56:71]\ns_waitcnt lgkmcnt(2)\n" \
    "v_mfma_f32_32x32x16_bf16 v[56:71], a[240:243], v[40:43], v[56:71]\nbuffer_load_dwordx4 v11, s[64:67], s70 offen offset:3072 sc1 lds\n" \
    "v_mfma_f32_32x32x16_bf16 v[56:71], a[244:247], v[44:47], v[56:71]\ns_waitcnt lgkmcnt(0)\ns_waitcnt vmcnt(9)\nv_readfirstlane_b32 s81, v10\n" \
    "v_mfma_f32_32x32x16_bf16 v[56:71], a[248:251], v[48:51], v[56:71]\nv_mfma_f32_32x32x16_bf16 v[56:71], a[252:255], v[52:55], v[56:71]\ns_waitcnt vmcnt(7)\n" \
    "v_readfirstlane_b32 s78, v9\ns_and_b32 s69, s71, 1\ns_lshl_b32 s69, s69, 2\n.Lf5_%=:\nbuffer_load_dword v9, v7, s[64:67], s69 offen sc1\ns_nop 6\n" \
    "v_cmp_u_f32 vcc, v56, v56\ns_add_u32 s77, s53, s41\ns_cbranch_vccnz .Lf9_%=\n.Lf11_%=:\ns_cmp_gt_i32 s78, s71\ns_cselect_b32 s78, s76, 0\ns_cmp_lg_u32 s78, 0\n" \
    "s_cbranch_scc1 .Lf15_%=\nv_pk_fma_f32 v[56:57], v[72:73], v[14:15], v[56:57] op_sel_hi:[1,0,1] neg_lo:[1,0,0] neg_hi:[1,0,0]\n" \
    "v_pk_fma_f32 v[58:59], v[74:75], v[14:15], v[58:59] op_sel_hi:[1,0,1] neg_lo:[1,0,0] neg_hi:[1,0,0]\n" \
    "v_pk_fma_f32 v[60:61], v[76:77], v[14:15], v[60:61] op_sel_hi:[1,0,1] neg_lo:[1,0,0] neg_hi:[1,0,0]\n" \
    "v_pk_fma_f32 v[62:63], v[78:79], v[14:15], v[62:63] op_sel_hi:[1,0,1] neg_lo:[1,0,0] neg_hi:[1,0,0]\nv_pk_fma_f32 v[56:57], v[56:57], v[14:15], v[88:89] op_sel:[0,1,0]\n" \
    "v_pk_fma_f32 v[58:59], v[58:59], v[14:15], v[90:91] op_sel:[0,1,0]\nv_pk_fma_f32 v[60:61], v[60:61], v[14:15], v[92:93] op_sel:[0,1,0]\n" \
    "v_pk_fma_f32 v[62:63], v[62:63], v[14:15], v[94:95] op_sel:[0,1,0]\nv_pk_mul_f32 v[112:113], v[56:57], v[56:57]\nv_pk_mul_f32 v[114:115], v[58:59], v[58:59]\n" \
    "v_pk_mul_f32 v[116:117], v[60:61], v[60:61]\nv_pk_mul_f32 v[118:119], v[62:63], v[62:63]\n" \
    "v_pk_fma_f32 v[112:113], v[112:113], s[46:47], s[46:47] op_sel:[0,0,1] op_sel_hi:[1,0,1]\n" \
    "v_pk_fma_f32 v[114:115], v[114:115], s[46:47], s[46:47] op_sel:[0,0,1] op_sel_hi:[1,0,1]\n" \
    "v_pk_fma_f32 v[116:117], v[116:117], s[46:47], s[46:47] op_sel:[0,0,1] op_sel_hi:[1,0,1]\n" \
    "v_pk_fma_f32 v[118:119], v[118:119], s[46:47], s[46:47] op_sel:[0,0,1] op_sel_hi:[1,0,1]\nv_pk_mul_f32 v[112:113], v[112:113], v[56:57]\n" \
    "v_pk_mul_f32 v[114:115], v[114:115], v[58:59]\nv_pk_mul_f32 v[116:117], v[116:117], v[60:61]\nv_pk_mul_f32 v[118:119], v[118:119], v[62:63]\nv_exp_f32 v112, v112\n" \
    "v_exp_f32 v113, v113\nv_exp_f32 v114, v114\nv_exp_f32 v115, v115\nv_exp_f32 v116, v116\ns_cmp_ge_u32 s81, 13\ns_cbranch_scc1 .Lf22_%=\n.Lf23_%=:\nv_exp_f32 v117, v117\n" \
    "v_exp_f32 v118, v118\nv_exp_f32 v119, v119\nv_pk_add_f32 v[112:113], v[112:113], 1.0 op_sel_hi:[1,0]\nv_pk_add_f32 v[114:115], v[114:115], 1.0 op_sel_hi:[1,0]\n" \
    "v_pk_add_f32 v[116:117], v[116:117], 1.0 op_sel_hi:[1,0]\nv_pk_add_f32 v[118:119], v[118:119], 1.0 op_sel_hi:[1,0]\nv_rcp_f32 v112, v112\nv_rcp_f32 v113, v113\n" \
    "v_rcp_f32 v114, v114\nv_rcp_f32 v115, v115\nv_rcp_f32 v116, v116\nv_rcp_f32 v117, v117\nv_rcp_f32 v118, v118\nv_rcp_f32 v119, v119\n" \
    "v_pk_mul_f32 v[56:57], v[56:57], v[112:113]\nv_pk_mul_f32 v[58:59], v[58:59], v[114:115]\nv_pk_mul_f32 v[60:61], v[60:61], v[116:117]\n" \
    "v_pk_mul_f32 v[62:63], v[62:63], v[118:119]\nv_cvt_pk_bf16_f32 v56, v56, v57\nv_cvt_pk_bf16_f32 v57, v58, v59\nv_cvt_pk_bf16_f32 v58, v60, v61\n" \
    "v_cvt_pk_bf16_f32 v59, v62, v63\nv_min_u32 v56, -2, v56\nv_min_u32 v57, -2, v57\nv_min_u32 v58, -2, v58\nv_min_u32 v59, -2, v59\ns_nop 0\nv_permlane32_swap_b32 v56, v58\n" \
    "v_permlane32_swap_b32 v57, v59\nbuffer_store_dwordx4 v[56:59], v5, s[64:67], s77 offen offset:0 sc1\n" \
    "v_pk_fma_f32 v[64:65], v[80:81], v[14:15], v[64:65] op_sel_hi:[1,0,1] neg_lo:[1,0,0] neg_hi:[1,0,0]\n" \
    "v_pk_fma_f32 v[66:67], v[82:83], v[14:15], v[66:67] op_sel_hi:[1,0,1] neg_lo:[1,0,0] neg_hi:[1,0,0]\n" \
    "v_pk_fma_f32 v[68:69], v[84:85], v[14:15], v[68:69] op_sel_hi:[1,0,1] neg_lo:[1,0,0] neg_hi:[1,0,0]\n" \
    "v_pk_fma_f32 v[70:71], v[86:87], v[14:15], v[70:71] op_sel_hi:[1,0,1] neg_lo:[1,0,0] neg_hi:[1,0,0]\nv_pk_fma_f32 v[64:65], v[64:65], v[14:15], v[96:97] op_sel:[0,1,0]\n" \
    "v_pk_fma_f32 v[66:67], v[66:67], v[14:15], v[98:99] op_sel:[0,1,0]\nv_pk_fma_f32 v[68:69], v[68:69], v[14:15], v[100:101] op_sel:[0,1,0]\n" \
    "v_pk_fma_f32 v[70:71], v[70:71], v[14:15], v[102:103] op_sel:[0,1,0]\nv_pk_mul_f32 v[112:113], v[64:65], v[64:65]\nv_pk_mul_f32 v[114:115], v[66:67], v[66:67]\n" \
    "v_pk_mul_f32 v[116:117], v[68:69], v[68:69]\nv_pk_mul_f32 v[118:119], v[70:71], v[70:71]\n" \
    "v_pk_fma_f32 v[112:113], v[112:113], s[46:47], s[46:47] op_sel:[0,0,1] op_sel_hi:[1,0,1]\n" \
    "v_pk_fma_f32 v[114:115], v[114:115], s[46:47], s[46:47] op_sel:[0,0,1] op_sel_hi:[1,0,1]\n" \
    "v_pk_fma_f32 v[116:117], v[116:117], s[46:47], s[46:47] op_sel:[0,0,1] op_sel_hi:[1,0,1]\n" \
    "v_pk_fma_f32 v[118:119], v[118:119], s[46:47], s[46:47] op_sel:[0,0,1] op_sel_hi:[1,0,1]\nv_pk_mul_f32 v[112:113], v[112:113], v[64:65]\n" \
    "v_pk_mul_f32 v[114:115], v[114:115], v[66:67]\nv_pk_mul_f32 v[116:117], v[116:117], v[68:69]\nv_pk_mul_f32 v[118:119], v[118:119], v[70:71]\nv_exp_f32 v112, v112\n" \
    "v_exp_f32 v113, v113\nv_exp_f32 v114, v114\nv_exp_f32 v115, v115\nv_exp_f32 v116, v116\nv_exp_f32 v117, v117\nv_exp_f32 v118, v118\nv_exp_f32 v119, v119\n" \
    "v_pk_add_f32 v[112:113], v[112:113], 1.0 op_sel_hi:[1,0]\nv_pk_add_f32 v[114:115], v[114:115], 1.0 op_sel_hi:[1,0]\n" \
    "v_pk_add_f32 v[116:117], v[116:117], 1.0 op_sel_hi:[1,0]\nv_pk_add_f32 v[118:119], v[118:119], 1.0 op_sel_hi:[1,0]\nv_rcp_f32 v112, v112\nv_rcp_f32 v113, v113\n" \
    "v_rcp_f32 v114, v114\nv_rcp_f32 v115, v115\nv_rcp_f32 v116, v116\nv_rcp_f32 v117, v117\nv_rcp_f32 v118, v118\nv_rcp_f32 v119, v119\n" \
    "v_pk_mul_f32 v[64:65], v[64:65], v[112:113]\nv_pk_mul_f32 v[66:67], v[66:67], v[114:115]\nv_pk_mul_f32 v[68:69], v[68:69], v[116:117]\n" \
    "v_pk_mul_f32 v[70:71], v[70:71], v[118:119]\nv_cvt_pk_bf16_f32 v64, v64, v65\nv_cvt_pk_bf16_f32 v65, v66, v67\nv_cvt_pk_bf16_f32 v66, v68, v69\n" \
    "v_cvt_pk_bf16_f32 v67, v70, v71\nv_min_u32 v64, -2, v64\nv_min_u32 v65, -2, v65\nv_min_u32 v66, -2, v66\nv_min_u32 v67, -2, v67\ns_nop 0\nv_permlane32_swap_b32 v64, v66\n" \
    "v_permlane32_swap_b32 v65, v67\nbuffer_store_dwordx4 v[64:67], v5, s[64:67], s77 offen offset:512 sc1\ns_waitcnt vmcnt(2)\nv_readfirstlane_b32 s78, v9\ns_mov_b32 s50, s71\n" \
    "s_mov_b32 s53, s74\ns_xor_b32 s51, s51, 0x10000\ns_cmp_ge_u32 s50, 64\ns_cbranch_scc1 .Lf2_%=\ns_cmp_eq_u32 s76, 0\ns_cbranch_scc1 .Lf4_%=\ns_cmp_gt_i32 s78, s50\n" \
    "s_cbranch_scc1 .Lf3_%=\ns_branch .Lf7_%=\n.Lf8_%=:\ns_sleep 2\ns_add_u32 s68, s68, 1\ns_and_b32 s69, s68, 63\ns_cbranch_scc1 .Lf6_%=\ns_cmp_gt_u32 s68, 0x200000\n" \
    "s_cbranch_scc1 .Lf1_%=\nbuffer_load_dword v12, off, s[64:67], 0 sc1\ns_waitcnt vmcnt(0)\nv_readfirstlane_b32 s69, v12\ns_cmp_eq_u32 s69, 1\ns_cbranch_scc0 .Lf6_%=\n" \
    "s_endpgm\n.Lf15_%=:\nbuffer_load_dwordx2 v[104:105], v2, s[64:67], s74 offen offset:0 sc1\n" \
    "v_pk_fma_f32 v[56:57], v[72:73], v[14:15], v[56:57] op_sel_hi:[1,0,1] neg_lo:[1,0,0] neg_hi:[1,0,0]\n" \
    "v_pk_fma_f32 v[58:59], v[74:75], v[14:15], v[58:59] op_sel_hi:[1,0,1] neg_lo:[1,0,0] neg_hi:[1,0,0]\n" \
    "v_pk_fma_f32 v[60:61], v[76:77], v[14:15], v[60:61] op_sel_hi:[1,0,1] neg_lo:[1,0,0] neg_hi:[1,0,0]\n" \
    "v_pk_fma_f32 v[62:63], v[78:79], v[14:15], v[62:63] op_sel_hi:[1,0,1] neg_lo:[1,0,0] neg_hi:[1,0,0]\nv_pk_fma_f32 v[56:57], v[56:57], v[14:15], v[88:89] op_sel:[0,1,0]\n" \
    "v_pk_fma_f32 v[58:59], v[58:59], v[14:15], v[90:91] op_sel:[0,1,0]\nv_pk_fma_f32 v[60:61], v[60:61], v[14:15], v[92:93] op_sel:[0,1,0]\ns_add_u32 m0, s75, 0\n" \
    "s_add_u32 s70, s74, s42\nbuffer_load_dwordx4 v0, s[64:67], s70 offen offset:0 sc1 lds\nv_pk_fma_f32 v[62:63], v[62:63], v[14:15], v[94:95] op_sel:[0,1,0]\n" \
    "v_pk_mul_f32 v[112:113], v[56:57], v[56:57]\nv_pk_mul_f32 v[114:115], v[58:59], v[58:59]\nv_pk_mul_f32 v[116:117], v[60:61], v[60:61]\n" \
    "v_pk_mul_f32 v[118:119], v[62:63], v[62:63]\nv_pk_fma_f32 v[112:113], v[112:113], s[46:47], s[46:47] op_sel:[0,0,1] op_sel_hi:[1,0,1]\n" \
    "v_pk_fma_f32 v[114:115], v[114:115], s[46:47], s[46:47] op_sel:[0,0,1] op_sel_hi:[1,0,1]\nbuffer_load_dwordx4 v0, s[64:67], s70 offen offset:1024 sc1 lds\n" \
    "v_pk_fma_f32 v[116:117], v[116:117], s[46:47], s[46:47] op_sel:[0,0,1] op_sel_hi:[1,0,1]\n" \
    "v_pk_fma_f32 v[118:119], v[118:119], s[46:47], s[46:47] op_sel:[0,0,1] op_sel_hi:[1,0,1]\nv_pk_mul_f32 v[112:113], v[112:113], v[56:57]\n" \
    "v_pk_mul_f32 v[114:115], v[114:115], v[58:59]\nv_pk_mul_f32 v[116:117], v[116:117], v[60:61]\nv_pk_mul_f32 v[118:119], v[118:119], v[62:63]\nv_exp_f32 v112, v112\n" \
    "buffer_load_dwordx4 v0, s[64:67], s70 offen offset:2048 sc1 lds\nv_exp_f32 v113, v113\nv_exp_f32 v114, v114\nv_exp_f32 v115, v115\nv_exp_f32 v116, v116\n" \
    "s_cmp_ge_u32 s81, 13\ns_cbranch_scc1 .Lf24_%=\n.Lf25_%=:\nv_exp_f32 v117, v117\nv_exp_f32 v118, v118\nbuffer_load_dwordx4 v0, s[64:67], s70 offen offset:3072 sc1 lds\n" \
    "v_exp_f32 v119, v119\nv_pk_add_f32 v[112:113], v[112:113], 1.0 op_sel_hi:[1,0]\nv_pk_add_f32 v[114:115], v[114:115], 1.0 op_sel_hi:[1,0]\n" \
    "v_pk_add_f32 v[116:117], v[116:117], 1.0 op_sel_hi:[1,0]\nv_pk_add_f32 v[118:119], v[118:119], 1.0 op_sel_hi:[1,0]\nv_rcp_f32 v112, v112\nv_rcp_f32 v113, v113\n" \
    "buffer_load_dwordx2 v[106:107], v2, s[64:67], s74 offen offset:256 sc1\nv_rcp_f32 v114, v114\nv_rcp_f32 v115, v115\nv_rcp_f32 v116, v116\nv_rcp_f32 v117, v117\n" \
    "v_rcp_f32 v118, v118\nv_rcp_f32 v119, v119\nv_pk_mul_f32 v[56:57], v[56:57], v[112:113]\nbuffer_load_dwordx2 v[108:109], v2, s[64:67], s74 offen offset:512 sc1\n" \
    "v_pk_mul_f32 v[58:59], v[58:59], v[114:115]\nv_pk_mul_f32 v[60:61], v[60:61], v[116:117]\nv_pk_mul_f32 v[62:63], v[62:63], v[118:119]\nv_cvt_pk_bf16_f32 v56, v56, v57\n" \
    "v_cvt_pk_bf16_f32 v57, v58, v59\nv_cvt_pk_bf16_f32 v58, v60, v61\nv_cvt_pk_bf16_f32 v59, v62, v63\nbuffer_load_dwordx2 v[110:111], v2, s[64:67], s74 offen offset:768 sc1\n" \
    "v_min_u32 v56, -2, v56\nv_min_u32 v57, -2, v57\nv_min_u32 v58, -2, v58\nv_min_u32 v59, -2, v59\ns_nop 0\nv_permlane32_swap_b32 v56, v58\nv_permlane32_swap_b32 v57, v59\n" \
    "buffer_store_dwordx4 v[56:59], v5, s[64:67], s77 offen offset:0 sc1\ns_add_u32 m0, s75, 16384\ns_add_u32 s70, s74, s43\n" \
    "buffer_load_dwordx4 v0, s[64:67], s70 offen offset:0 sc1 lds\nv_pk_fma_f32 v[64:65], v[80:81], v[14:15], v[64:65] op_sel_hi:[1,0,1] neg_lo:[1,0,0] neg_hi:[1,0,0]\n" \
    "v_pk_fma_f32 v[66:67], v[82:83], v[14:15], v[66:67] op_sel_hi:[1,0,1] neg_lo:[1,0,0] neg_hi:[1,0,0]\n" \
    "v_pk_fma_f32 v[68:69], v[84:85], v[14:15], v[68:69] op_sel_hi:[1,0,1] neg_lo:[1,0,0] neg_hi:[1,0,0]\n" \
    "v_pk_fma_f32 v[70:71], v[86:87], v[14:15], v[70:71] op_sel_hi:[1,0,1] neg_lo:[1,0,0] neg_hi:[1,0,0]\nv_pk_fma_f32 v[64:65], v[64:65], v[14:15], v[96:97] op_sel:[0,1,0]\n" \
    "v_pk_fma_f32 v[66:67], v[66:67], v[14:15], v[98:99] op_sel:[0,1,0]\nv_pk_fma_f32 v[68:69], v[68:69], v[14:15], v[100:101] op_sel:[0,1,0]\n" \
    "buffer_load_dwordx4 v0, s[64:67], s70 offen offset:1024 sc1 lds\nv_pk_fma_f32 v[70:71], v[70:71], v[14:15], v[102:103] op_sel:[0,1,0]\n" \
    "v_pk_mul_f32 v[112:113], v[64:65], v[64:65]\nv_pk_mul_f32 v[114:115], v[66:67], v[66:67]\nv_pk_mul_f32 v[116:117], v[68:69], v[68:69]\n" \
    "v_pk_mul_f32 v[118:119], v[70:71], v[70:71]\nv_pk_fma_f32 v[112:113], v[112:113], s[46:47], s[46:47] op_sel:[0,0,1] op_sel_hi:[1,0,1]\n" \
    "v_pk_fma_f32 v[114:115], v[114:115], s[46:47], s[46:47] op_sel:[0,0,1] op_sel_hi:[1,0,1]\nbuffer_load_dwordx4 v0, s[64:67], s70 offen offset:2048 sc1 lds\n" \
    "v_pk_fma_f32 v[116:117], v[116:117], s[46:47], s[46:47] op_sel:[0,0,1] op_sel_hi:[1,0,1]\n" \
    "v_pk_fma_f32 v[118:119], v[118:119], s[46:47], s[46:47] op_sel:[0,0,1] op_sel_hi:[1,0,1]\nv_pk_mul_f32 v[112:113], v[112:113], v[64:65]\n" \
    "v_pk_mul_f32 v[114:115], v[114:115], v[66:67]\nv_pk_mul_f32 v[116:117], v[116:117], v[68:69]\nv_pk_mul_f32 v[118:119], v[118:119], v[70:71]\nv_exp_f32 v112, v112\n" \
    "buffer_load_dwordx4 v0, s[64:67], s70 offen offset:3072 sc1 lds\nv_exp_f32 v113, v113\nv_exp_f32 v114, v114\nv_exp_f32 v115, v115\nv_exp_f32 v116, v116\n" \
    "v_exp_f32 v117, v117\nv_exp_f32 v118, v118\nv_exp_f32 v119, v119\nv_pk_add_f32 v[112:113], v[112:113], 1.0 op_sel_hi:[1,0]\n" \
    "v_pk_add_f32 v[114:115], v[114:115], 1.0 op_sel_hi:[1,0]\nv_pk_add_f32 v[116:117], v[116:117], 1.0 op_sel_hi:[1,0]\n" \
    "v_pk_add_f32 v[118:119], v[118:119], 1.0 op_sel_hi:[1,0]\nv_rcp_f32 v112, v112\nv_rcp_f32 v113, v113\nv_rcp_f32 v114, v114\nv_rcp_f32 v115, v115\nv_rcp_f32 v116, v116\n" \
    "v_rcp_f32 v117, v117\nv_rcp_f32 v118, v118\nv_rcp_f32 v119, v119\nv_pk_mul_f32 v[64:65], v[64:65], v[112:113]\nv_pk_mul_f32 v[66:67], v[66:67], v[114:115]\n" \
    "v_pk_mul_f32 v[68:69], v[68:69], v[116:117]\nv_pk_mul_f32 v[70:71], v[70:71], v[118:119]\nv_cvt_pk_bf16_f32 v64, v64, v65\nv_cvt_pk_bf16_f32 v65, v66, v67\n" \
    "v_cvt_pk_bf16_f32 v66, v68, v69\nv_cvt_pk_bf16_f32 v67, v70, v71\nv_min_u32 v64, -2, v64\nv_min_u32 v65, -2, v65\nv_min_u32 v66, -2, v66\nv_min_u32 v67, -2, v67\ns_nop 0\n" \
    "v_permlane32_swap_b32 v64, v66\nv_permlane32_swap_b32 v65, v67\nbuffer_store_dwordx4 v[64:67], v5, s[64:67], s77 offen offset:512 sc1\ns_mov_b32 s50, s71\n" \
    "s_mov_b32 s53, s74\ns_xor_b32 s51, s51, 0x10000\ns_mov_b32 s72, s75\ns_branch .Lf14_%=\n.Lf9_%=:\nv_mov_b32 v12, 0\ns_sub_u32 s69, s52, s79\ns_add_u32 s69, s69, s51\n" \
    "v_add_u32 v13, s69, v0\ns_mov_b32 s69, 64\n.Lf10_%=:\nds_read_b128 v[112:115], v13\nv_add_u32 v13, 0x400, v13\ns_waitcnt lgkmcnt(0)\nv_max3_u32 v12, v12, v112, v113\n" \
    "v_max3_u32 v12, v12, v114, v115\ns_sub_u32 s69, s69, 1\ns_cmp_lg_u32 s69, 0\ns_cbranch_scc1 .Lf10_%=\nv_cmp_eq_u32 vcc, -1, v12\ns_cbranch_vccz .Lf11_%=\n" \
    "s_waitcnt vmcnt(0)\ns_barrier\n.Lf12_%=:\ns_sleep 2\ns_mov_b32 s68, s80\ns_add_u32 s80, s80, 1\ns_add_u32 s68, s68, 1\ns_and_b32 s69, s68, 63\ns_cbranch_scc1 .Lf13_%=\n" \
    "s_cmp_gt_u32 s68, 0x200000\ns_cbranch_scc1 .Lf1_%=\nbuffer_load_dword v12, off, s[64:67], 0 sc1\ns_waitcnt vmcnt(0)\nv_readfirstlane_b32 s69, v12\ns_cmp_eq_u32 s69, 1\n" \
    "s_cbranch_scc0 .Lf13_%=\ns_endpgm\n.Lf16_%=:\ns_mov_b32 s68, 0\n.Lf18_%=:\ns_sleep 1\nbuffer_load_dwordx2 v[104:105], v2, s[64:67], s53 offen offset:0 sc1\n" \
    "buffer_load_dwordx2 v[106:107], v2, s[64:67], s53 offen offset:256 sc1\nbuffer_load_dwordx2 v[108:109], v2, s[64:67], s53 offen offset:512 sc1\n" \
    "buffer_load_dwordx2 v[110:111], v2, s[64:67], s53 offen offset:768 sc1\ns_waitcnt vmcnt(0)\nv_max3_u32 v12, v104, v105, v106\nv_max3_u32 v12, v12, v107, v108\n" \
    "v_max3_u32 v12, v12, v109, v110\nv_max_u32 v12, v12, v111\nv_cmp_eq_u32 vcc, -1, v12\ns_cbranch_vccz .Lf17_%=\ns_add_u32 s68, s68, 1\ns_and_b32 s69, s68, 63\n" \
    "s_cbranch_scc1 .Lf18_%=\ns_cmp_gt_u32 s68, 0x200000\ns_cbranch_scc1 .Lf1_%=\nbuffer_load_dword v12, off, s[64:67], 0 sc1\ns_waitcnt vmcnt(0)\nv_readfirstlane_b32 s69, v12\n" \
    "s_cmp_eq_u32 s69, 1\ns_cbranch_scc0 .Lf18_%=\ns_endpgm\n.Lf19_%=:\ns_mov_b32 s68, 0\n.Lf21_%=:\ns_sleep 1\n" \
    "buffer_load_dwordx2 v[104:105], v2, s[64:67], s53 offen offset:0 sc1\nbuffer_load_dwordx2 v[106:107], v2, s[64:67], s53 offen offset:256 sc1\n" \
    "buffer_load_dwordx2 v[108:109], v2, s[64:67], s53 offen offset:512 sc1\nbuffer_load_dwordx2 v[110:111], v2, s[64:67], s53 offen offset:768 sc1\ns_waitcnt vmcnt(0)\n" \
    "v_max3_u32 v12, v104, v105, v106\nv_max3_u32 v12, v12, v107, v108\nv_max3_u32 v12, v12, v109, v110\nv_max_u32 v12, v12, v111\nv_cmp_eq_u32 vcc, -1, v12\n" \
    "s_cbranch_vccz .Lf20_%=\ns_add_u32 s68, s68, 1\ns_and_b32 s69, s68, 63\ns_cbranch_scc1 .Lf21_%=\ns_cmp_gt_u32 s68, 0x200000\ns_cbranch_scc1 .Lf1_%=\n" \
    "buffer_load_dword v12, off, s[64:67], 0 sc1\ns_waitcnt vmcnt(0)\nv_readfirstlane_b32 s69, v12\ns_cmp_eq_u32 s69, 1\ns_cbranch_scc0 .Lf21_%=\ns_endpgm\n.Lf22_%=:\n" \
    "s_add_u32 s81, s50, 1\nv_mov_b32 v16, s81\ns_and_b32 s81, s50, 1\ns_lshl_b32 s81, s81, 2\nbuffer_atomic_umax v16, v6, s[64:67], s81 offen sc1\ns_branch .Lf23_%=\n" \
    ".Lf24_%=:\ns_add_u32 s81, s50, 1\nv_mov_b32 v16, s81\ns_and_b32 s81, s50, 1\ns_lshl_b32 s81, s81, 2\nbuffer_atomic_umax v16, v6, s[64:67], s81 offen sc1\n" \
    "s_branch .Lf25_%=\n.Lf1_%=:\nv_mov_b32 v12, 1\nbuffer_store_dword v12, off, s[64:67], 0 sc1\ns_endpgm\n.Lf2_%=:\n"
#define ROLE_F2 \
    "s_mov_b32 s80, 0\ns_mov_b32 s81, 0\nv_mov_b32 v18, 0x7f000000\nv_mov_b32 v19, 0x7f000000\nv_mov_b32 v20, 0x7f000000\ns_branch .Lg7_%=\n.Lg4_%=:\ns_waitcnt vmcnt(6)\n" \
    "v_mfma_f32_16x16x32_bf16 v[136:139], a[0:3], v[24:27], 0\ns_add_u32 s70, s51, s58\nbuffer_load_dwordx4 v[56:59], v0, s[64:67], s70 offen offset:0 sc1\n" \
    "v_mfma_f32_16x16x32_bf16 v[140:143], a[0:3], v[28:31], 0\ns_add_u32 s71, s50, 1\ns_cmp_ge_u32 s71, s55\ns_addc_u32 s71, s71, 0\n" \
    "v_mfma_f32_16x16x32_bf16 v[144:147], a[4:7], v[24:27], 0\ns_waitcnt lgkmcnt(0)\ns_barrier\nv_mfma_f32_16x16x32_bf16 v[148:151], a[4:7], v[28:31], 0\n" \
    "ds_read_b128 v[200:203], v6 offset:0\ns_min_u32 s72, s71, 63\nv_mfma_f32_16x16x32_bf16 v[152:155], a[8:11], v[24:27], 0\n" \
    "buffer_load_dwordx4 v[60:63], v0, s[64:67], s70 offen offset:1024 sc1\ns_lshl_b32 s72, s72, 20\nv_mfma_f32_16x16x32_bf16 v[156:159], a[8:11], v[28:31], 0\n" \
    "ds_read_b128 v[204:207], v6 offset:8192\ns_add_u32 s72, s72, s40\nv_mfma_f32_16x16x32_bf16 v[160:163], a[12:15], v[24:27], 0\nds_read_b128 v[208:211], v6 offset:16384\n" \
    "s_add_u32 s74, s48, s72\nv_mfma_f32_16x16x32_bf16 v[164:167], a[12:15], v[28:31], 0\ns_waitcnt vmcnt(6)\nds_read_b128 v[212:215], v6 offset:24576\n" \
    "v_mfma_f32_16x16x32_bf16 v[136:139], a[16:19], v[32:35], v[136:139]\nbuffer_load_dwordx4 v[64:67], v0, s[64:67], s70 offen offset:2048 sc1\ns_lshr_b32 s74, s74, 8\n" \
    "v_mfma_f32_16x16x32_bf16 v[140:143], a[16:19], v[36:39], v[140:143]\nds_read_b128 v[216:219], v2 offset:0\ns_xor_b32 s74, s74, s49\n" \
    "v_mfma_f32_16x16x32_bf16 v[144:147], a[20:23], v[32:35], v[144:147]\nds_read_b128 v[220:223], v2 offset:8192\ns_and_b32 s74, s74, 0x2000\n" \
    "v_mfma_f32_16x16x32_bf16 v[148:151], a[20:23], v[36:39], v[148:151]\nds_read_b128 v[224:227], v2 offset:16384\ns_add_u32 s74, s74, s72\n" \
    "v_mfma_f32_16x16x32_bf16 v[152:155], a[24:27], v[32:35], v[152:155]\nbuffer_load_dwordx4 v[68:71], v0, s[64:67], s70 offen offset:3072 sc1\ns_and_b32 s75, s71, 1\n" \
    "v_mfma_f32_16x16x32_bf16 v[156:159], a[24:27], v[36:39], v[156:159]\nds_read_b128 v[228:231], v2 offset:24576\nv_xor_b32 v1, 0x8000, v1\n" \
    "v_mfma_f32_16x16x32_bf16 v[160:163], a[28:31], v[32:35], v[160:163]\nbuffer_load_dword v17, v5, s[64:67], 0 offen sc1\nv_xor_b32 v2, 0x8000, v2\nv_xor_b32 v6, 0x8000, v6\n" \
    "v_mfma_f32_16x16x32_bf16 v[164:167], a[28:31], v[36:39], v[164:167]\ns_waitcnt vmcnt(7)\ns_waitcnt lgkmcnt(4)\nv_add_f32 v200, v200, v204\n" \
    "v_mfma_f32_16x16x32_bf16 v[136:139], a[32:35], v[40:43], v[136:139]\ns_add_u32 s70, s51, s59\nbuffer_load_dwordx4 v[72:75], v0, s[64:67], s70 offen offset:0 sc1\n" \
    "v_mfma_f32_16x16x32_bf16 v[140:143], a[32:35], v[44:47], v[140:143]\nv_add_f32 v201, v201, v205\nv_add_f32 v202, v202, v206\n" \
    "v_mfma_f32_16x16x32_bf16 v[144:147], a[36:39], v[40:43], v[144:147]\nv_add_f32 v203, v203, v207\nv_add_f32 v208, v208, v212\n" \
    "v_mfma_f32_16x16x32_bf16 v[148:151], a[36:39], v[44:47], v[148:151]\nv_add_f32 v209, v209, v213\nv_add_f32 v210, v210, v214\n" \
    "v_mfma_f32_16x16x32_bf16 v[152:155], a[40:43], v[40:43], v[152:155]\nbuffer_load_dwordx4 v[76:79], v0, s[64:67], s70 offen offset:1024 sc1\nv_add_f32 v211, v211, v215\n" \
    "v_mfma_f32_16x16x32_bf16 v[156:159], a[40:43], v[44:47], v[156:159]\nv_add_f32 v200, v200, v208\nv_add_f32 v201, v201, v209\n" \
    "v_mfma_f32_16x16x32_bf16 v[160:163], a[44:47], v[40:43], v[160:163]\nv_add_f32 v202, v202, v210\nv_add_f32 v203, v203, v211\n" \
    "v_mfma_f32_16x16x32_bf16 v[164:167], a[44:47], v[44:47], v[164:167]\ns_waitcnt vmcnt(7)\ns_add_u32 s73, s52, s41\ns_xor_b32 s73, s73, 0x1000\n" \
    "v_mfma_f32_16x16x32_bf16 v[136:139], a[48:51], v[48:51], v[136:139]\nbuffer_load_dwordx4 v[80:83], v0, s[64:67], s70 offen offset:2048 sc1\n" \
    "v_mfma_f32_16x16x32_bf16 v[140:143], a[48:51], v[52:55], v[140:143]\nbuffer_store_dwordx4 v[200:203], v0, s[64:67], s73 offen sc1\n" \
    "v_mfma_f32_16x16x32_bf16 v[144:147], a[52:55], v[48:51], v[144:147]\ns_waitcnt lgkmcnt(0)\nv_add_f32 v216, v216, v220\nv_add_f32 v217, v217, v221\n" \
    "v_mfma_f32_16x16x32_bf16 v[148:151], a[52:55], v[52:55], v[148:151]\nv_add_f32 v218, v218, v222\nv_add_f32 v219, v219, v223\n" \
    "v_mfma_f32_16x16x32_bf16 v[152:155], a[56:59], v[48:51], v[152:155]\nbuffer_load_dwordx4 v[84:87], v0, s[64:67], s70 offen offset:3072 sc1\nv_add_f32 v224, v224, v228\n" \
    "v_mfma_f32_16x16x32_bf16 v[156:159], a[56:59], v[52:55], v[156:159]\nv_add_f32 v225, v225, v229\nv_add_f32 v226, v226, v230\n" \
    "v_mfma_f32_16x16x32_bf16 v[160:163], a[60:63], v[48:51], v[160:163]\nv_add_f32 v227, v227, v231\nv_add_f32 v216, v216, v12\n" \
    "v_mfma_f32_16x16x32_bf16 v[164:167], a[60:63], v[52:55], v[164:167]\ns_waitcnt vmcnt(8)\nv_add_f32 v217, v217, v13\nv_add_f32 v218, v218, v14\n" \
    "v_mfma_f32_16x16x32_bf16 v[136:139], a[64:67], v[56:59], v[136:139]\ns_add_u32 s70, s51, s60\nbuffer_load_dwordx4 v[88:91], v0, s[64:67], s70 offen offset:0 sc1\n" \
    "v_mfma_f32_16x16x32_bf16 v[140:143], a[64:67], v[60:63], v[140:143]\nv_add_f32 v219, v219, v15\nv_add_f32 v168, v216, v224\n" \
    "v_mfma_f32_16x16x32_bf16 v[144:147], a[68:71], v[56:59], v[144:147]\nv_add_f32 v169, v217, v225\nv_add_f32 v170, v218, v226\n" \
    "v_mfma_f32_16x16x32_bf16 v[148:151], a[68:71], v[60:63], v[148:151]\nv_add_f32 v171, v219, v227\nv_mfma_f32_16x16x32_bf16 v[152:155], a[72:75], v[56:59], v[152:155]\n" \
    "buffer_load_dwordx4 v[92:95], v0, s[64:67], s70 offen offset:1024 sc1\nv_mfma_f32_16x16x32_bf16 v[156:159], a[72:75], v[60:63], v[156:159]\n" \
    "v_mfma_f32_16x16x32_bf16 v[160:163], a[76:79], v[56:59], v[160:163]\nv_mfma_f32_16x16x32_bf16 v[164:167], a[76:79], v[60:63], v[164:167]\ns_waitcnt vmcnt(8)\n" \
    "v_mfma_f32_16x16x32_bf16 v[136:139], a[80:83], v[64:67], v[136:139]\nbuffer_load_dwordx4 v[96:99], v0, s[64:67], s70 offen offset:2048 sc1\n" \
    "v_mfma_f32_16x16x32_bf16 v[140:143], a[80:83], v[68:71], v[140:143]\nv_mfma_f32_16x16x32_bf16 v[144:147], a[84:87], v[64:67], v[144:147]\ns_waitcnt vmcnt(8)\n" \
    "v_readlane_b32 s76, v17, s75\nv_mfma_f32_16x16x32_bf16 v[148:151], a[84:87], v[68:71], v[148:151]\nv_mfma_f32_16x16x32_bf16 v[152:155], a[88:91], v[64:67], v[152:155]\n" \
    "buffer_load_dwordx4 v[100:103], v0, s[64:67], s70 offen offset:3072 sc1\ns_cmp_gt_i32 s76, s71\ns_cselect_b32 s77, 0, 0x7f000000\nv_or_b32 v16, s77, v7\n" \
    "v_mfma_f32_16x16x32_bf16 v[156:159], a[88:91], v[68:71], v[156:159]\nbuffer_load_dword v16, v16, s[64:67], s74 offen sc1\n" \
    "v_mfma_f32_16x16x32_bf16 v[160:163], a[92:95], v[64:67], v[160:163]\nv_mfma_f32_16x16x32_bf16 v[164:167], a[92:95], v[68:71], v[164:167]\ns_waitcnt vmcnt(8)\n" \
    "v_mfma_f32_16x16x32_bf16 v[136:139], a[96:99], v[72:75], v[136:139]\ns_add_u32 s70, s51, s61\nbuffer_load_dwordx4 v[104:107], v0, s[64:67], s70 offen offset:0 sc1\n" \
    "v_mfma_f32_16x16x32_bf16 v[140:143], a[96:99], v[76:79], v[140:143]\ns_add_u32 s73, s52, s41\nbuffer_load_dwordx4 v[172:175], v18, s[64:67], s73 offen sc1\n" \
    "v_mfma_f32_16x16x32_bf16 v[144:147], a[100:103], v[72:75], v[144:147]\nbuffer_load_dwordx2 v[176:177], v19, s[64:67], s52 offen sc1\n" \
    "v_mfma_f32_16x16x32_bf16 v[148:151], a[100:103], v[76:79], v[148:151]\nv_mfma_f32_16x16x32_bf16 v[152:155], a[104:107], v[72:75], v[152:155]\n" \
    "buffer_load_dwordx4 v[108:111], v0, s[64:67], s70 offen offset:1024 sc1\nv_mfma_f32_16x16x32_bf16 v[156:159], a[104:107], v[76:79], v[156:159]\n" \
    "v_mfma_f32_16x16x32_bf16 v[160:163], a[108:111], v[72:75], v[160:163]\nv_mfma_f32_16x16x32_bf16 v[164:167], a[108:111], v[76:79], v[164:167]\ns_waitcnt vmcnt(9)\n" \
    "v_mfma_f32_16x16x32_bf16 v[136:139], a[112:115], v[80:83], v[136:139]\nbuffer_load_dwordx4 v[112:115], v0, s[64:67], s70 offen offset:2048 sc1\n" \
    "v_mfma_f32_16x16x32_bf16 v[140:143], a[112:115], v[84:87], v[140:143]\nv_mfma_f32_16x16x32_bf16 v[144:147], a[116:119], v[80:83], v[144:147]\n" \
    "v_mfma_f32_16x16x32_bf16 v[148:151], a[116:119], v[84:87], v[148:151]\nv_mfma_f32_16x16x32_bf16 v[152:155], a[120:123], v[80:83], v[152:155]\n" \
    "buffer_load_dwordx4 v[116:119], v0, s[64:67], s70 offen offset:3072 sc1\nv_mfma_f32_16x16x32_bf16 v[156:159], a[120:123], v[84:87], v[156:159]\n" \
    "v_mfma_f32_16x16x32_bf16 v[160:163], a[124:127], v[80:83], v[160:163]\nv_mfma_f32_16x16x32_bf16 v[164:167], a[124:127], v[84:87], v[164:167]\ns_waitcnt vmcnt(9)\n" \
    "v_mfma_f32_16x16x32_bf16 v[136:139], a[128:131], v[88:91], v[136:139]\ns_add_u32 s70, s51, s62\nbuffer_load_dwordx4 v[120:123], v0, s[64:67], s70 offen offset:0 sc1\n" \
    "v_mfma_f32_16x16x32_bf16 v[140:143], a[128:131], v[92:95], v[140:143]\nv_mfma_f32_16x16x32_bf16 v[144:147], a[132:135], v[88:91], v[144:147]\n" \
    "v_mfma_f32_16x16x32_bf16 v[148:151], a[132:135], v[92:95], v[148:151]\nv_mfma_f32_16x16x32_bf16 v[152:155], a[136:139], v[88:91], v[152:155]\n" \
    "buffer_load_dwordx4 v[124:127], v0, s[64:67], s70 offen offset:1024 sc1\nv_mfma_f32_16x16x32_bf16 v[156:159], a[136:139], v[92:95], v[156:159]\n" \
    "v_mfma_f32_16x16x32_bf16 v[160:163], a[140:143], v[88:91], v[160:163]\nv_mfma_f32_16x16x32_bf16 v[164:167], a[140:143], v[92:95], v[164:167]\ns_waitcnt vmcnt(9)\n" \
    "v_mfma_f32_16x16x32_bf16 v[136:139], a[144:147], v[96:99], v[136:139]\nbuffer_load_dwordx4 v[128:131], v0, s[64:67], s70 offen offset:2048 sc1\n" \
    "v_mfma_f32_16x16x32_bf16 v[140:143], a[144:147], v[100:103], v[140:143]\nv_mfma_f32_16x16x32_bf16 v[144:147], a[148:151], v[96:99], v[144:147]\n" \
    "v_mfma_f32_16x16x32_bf16 v[148:151], a[148:151], v[100:103], v[148:151]\ns_waitcnt vmcnt(9)\nv_cmp_eq_u32 vcc, -1, v16\n" \
    "v_mfma_f32_16x16x32_bf16 v[152:155], a[152:155], v[96:99], v[152:155]\nbuffer_load_dwordx4 v[132:135], v0, s[64:67], s70 offen offset:3072 sc1\n" \
    "v_mfma_f32_16x16x32_bf16 v[156:159], a[152:155], v[100:103], v[156:159]\ns_cmp_eq_u64 vcc, 0\ns_cselect_b32 s77, s77, 0x7f000000\nv_or_b32 v16, s77, v0\n" \
    "v_mfma_f32_16x16x32_bf16 v[160:163], a[156:159], v[96:99], v[160:163]\nv_mfma_f32_16x16x32_bf16 v[164:167], a[156:159], v[100:103], v[164:167]\ns_waitcnt vmcnt(6)\n" \
    "v_mfma_f32_16x16x32_bf16 v[136:139], a[160:163], v[104:107], v[136:139]\nv_max3_u32 v21, v172, v173, v174\nv_max_u32 v21, v21, v175\n" \
    "v_mfma_f32_16x16x32_bf16 v[140:143], a[160:163], v[108:111], v[140:143]\ns_add_u32 s70, s74, s56\nbuffer_load_dwordx4 v[24:27], v16, s[64:67], s70 offen offset:0 sc1\n" \
    "v_mfma_f32_16x16x32_bf16 v[144:147], a[164:167], v[104:107], v[144:147]\nv_cmp_eq_u32 vcc, -1, v21\n" \
    "v_mfma_f32_16x16x32_bf16 v[148:151], a[164:167], v[108:111], v[148:151]\ns_cbranch_vccnz .Lg19_%=\n.Lg20_%=:\nv_add_f32 v172, v172, v168\n" \
    "v_mfma_f32_16x16x32_bf16 v[152:155], a[168:171], v[104:107], v[152:155]\nv_add_f32 v173, v173, v169\nv_add_f32 v174, v174, v170\n" \
    "v_mfma_f32_16x16x32_bf16 v[156:159], a[168:171], v[108:111], v[156:159]\nbuffer_load_dwordx4 v[28:31], v16, s[64:67], s70 offen offset:1024 sc1\n" \
    "v_add_f32 v175, v175, v171\nv_mfma_f32_16x16x32_bf16 v[160:163], a[172:175], v[104:107], v[160:163]\nv_lshlrev_b32 v168, 16, v176\nv_and_b32 v169, 0xffff0000, v176\n" \
    "v_mfma_f32_16x16x32_bf16 v[164:167], a[172:175], v[108:111], v[164:167]\ns_waitcnt vmcnt(6)\nv_lshlrev_b32 v170, 16, v177\nv_and_b32 v171, 0xffff0000, v177\n" \
    "v_mfma_f32_16x16x32_bf16 v[136:139], a[176:179], v[112:115], v[136:139]\nv_fma_f32 v172, v172, v8, v168\nv_fma_f32 v173, v173, v9, v169\n" \
    "v_mfma_f32_16x16x32_bf16 v[140:143], a[176:179], v[116:119], v[140:143]\nbuffer_load_dwordx4 v[32:35], v16, s[64:67], s70 offen offset:2048 sc1\n" \
    "v_fma_f32 v174, v174, v10, v170\nv_mfma_f32_16x16x32_bf16 v[144:147], a[180:183], v[112:115], v[144:147]\nv_fma_f32 v175, v175, v11, v171\n" \
    "v_cvt_pk_bf16_f32 v176, v172, v173\nv_mfma_f32_16x16x32_bf16 v[148:151], a[180:183], v[116:119], v[148:151]\nv_cvt_pk_bf16_f32 v177, v174, v175\n" \
    "v_mfma_f32_16x16x32_bf16 v[152:155], a[184:187], v[112:115], v[152:155]\ns_lshl_b32 s73, s53, 16\nv_mfma_f32_16x16x32_bf16 v[156:159], a[184:187], v[116:119], v[156:159]\n" \
    "buffer_load_dwordx4 v[36:39], v16, s[64:67], s70 offen offset:3072 sc1\nv_mfma_f32_16x16x32_bf16 v[160:163], a[188:191], v[112:115], v[160:163]\n" \
    "buffer_store_dwordx2 v[176:177], v20, s[44:47], s73 offen\nv_mfma_f32_16x16x32_bf16 v[164:167], a[188:191], v[116:119], v[164:167]\ns_waitcnt vmcnt(7)\n" \
    "v_mfma_f32_16x16x32_bf16 v[136:139], a[192:195], v[120:123], v[136:139]\ns_waitcnt vmcnt(5)\nv_mfma_f32_16x16x32_bf16 v[136:139], a[208:211], v[128:131], v[136:139]\n" \
    "s_add_u32 s70, s74, s57\nbuffer_load_dwordx4 v[40:43], v16, s[64:67], s70 offen offset:0 sc1\nv_mfma_f32_16x16x32_bf16 v[140:143], a[192:195], v[124:127], v[140:143]\n" \
    "v_mfma_f32_16x16x32_bf16 v[140:143], a[208:211], v[132:135], v[140:143]\nv_mfma_f32_16x16x32_bf16 v[144:147], a[196:199], v[120:123], v[144:147]\n" \
    "v_mfma_f32_16x16x32_bf16 v[144:147], a[212:215], v[128:131], v[144:147]\nbuffer_load_dwordx4 v[44:47], v16, s[64:67], s70 offen offset:1024 sc1\n" \
    "v_mfma_f32_16x16x32_bf16 v[148:151], a[196:199], v[124:127], v[148:151]\nds_write_b128 v1, v[136:139] offset:0\n" \
    "v_mfma_f32_16x16x32_bf16 v[148:151], a[212:215], v[132:135], v[148:151]\nv_mfma_f32_16x16x32_bf16 v[152:155], a[200:203], v[120:123], v[152:155]\ns_nop 0\n" \
    "ds_write_b128 v1, v[140:143] offset:1024\nv_mfma_f32_16x16x32_bf16 v[152:155], a[216:219], v[128:131], v[152:155]\n" \
    "buffer_load_dwordx4 v[48:51], v16, s[64:67], s70 offen offset:2048 sc1\nv_mfma_f32_16x16x32_bf16 v[156:159], a[200:203], v[124:127], v[156:159]\n" \
    "ds_write_b128 v1, v[144:147] offset:2048\nv_mfma_f32_16x16x32_bf16 v[156:159], a[216:219], v[132:135], v[156:159]\n" \
    "v_mfma_f32_16x16x32_bf16 v[160:163], a[204:207], v[120:123], v[160:163]\nds_write_b128 v1, v[148:151] offset:3072\n" \
    "v_mfma_f32_16x16x32_bf16 v[160:163], a[220:223], v[128:131], v[160:163]\nbuffer_load_dwordx4 v[52:55], v16, s[64:67], s70 offen offset:3072 sc1\n" \
    "v_mfma_f32_16x16x32_bf16 v[164:167], a[204:207], v[124:127], v[164:167]\nds_write_b128 v1, v[152:155] offset:4096\n" \
    "v_mfma_f32_16x16x32_bf16 v[164:167], a[220:223], v[132:135], v[164:167]\nbuffer_load_dword v17, v5, s[64:67], 0 offen sc1\nds_write_b128 v1, v[156:159] offset:5120\n" \
    "s_nop 1\nds_write_b128 v1, v[160:163] offset:6144\ns_nop 2\nds_write_b128 v1, v[164:167] offset:7168\ns_branch .Lg5_%=\n.Lg7_%=:\ns_mov_b32 s68, 0\n.Lg6_%=:\n" \
    "buffer_load_dword v17, v5, s[64:67], 0 offen sc1\ns_waitcnt vmcnt(0)\ns_and_b32 s69, s50, 1\ns_nop 3\nv_readlane_b32 s76, v17, s69\ns_cmp_gt_i32 s76, s50\n" \
    "s_cbranch_scc0 .Lg8_%=\n.Lg3_%=:\n.Lg15_%=:\nbuffer_load_dword v21, v7, s[64:67], s51 offen sc1\ns_add_u32 s70, s51, s56\n" \
    "buffer_load_dwordx4 v[24:27], v0, s[64:67], s70 offen offset:0 sc1\nbuffer_load_dwordx4 v[28:31], v0, s[64:67], s70 offen offset:1024 sc1\n" \
    "buffer_load_dwordx4 v[32:35], v0, s[64:67], s70 offen offset:2048 sc1\nbuffer_load_dwordx4 v[36:39], v0, s[64:67], s70 offen offset:3072 sc1\ns_add_u32 s70, s51, s57\n" \
    "buffer_load_dwordx4 v[40:43], v0, s[64:67], s70 offen offset:0 sc1\nbuffer_load_dwordx4 v[44:47], v0, s[64:67], s70 offen offset:1024 sc1\n" \
    "buffer_load_dwordx4 v[48:51], v0, s[64:67], s70 offen offset:2048 sc1\nbuffer_load_dwordx4 v[52:55], v0, s[64:67], s70 offen offset:3072 sc1\ns_waitcnt vmcnt(8)\n" \
    "v_cmp_eq_u32 vcc, -1, v21\ns_cbranch_vccnz .Lg12_%=\n.Lg13_%=:\ns_waitcnt vmcnt(6)\nv_mfma_f32_16x16x32_bf16 v[136:139], a[0:3], v[24:27], 0\ns_add_u32 s70, s51, s58\n" \
    "buffer_load_dwordx4 v[56:59], v0, s[64:67], s70 offen offset:0 sc1\nv_mfma_f32_16x16x32_bf16 v[140:143], a[0:3], v[28:31], 0\ns_add_u32 s71, s50, 1\ns_cmp_ge_u32 s71, s55\n" \
    "s_addc_u32 s71, s71, 0\nv_mfma_f32_16x16x32_bf16 v[144:147], a[4:7], v[24:27], 0\ns_min_u32 s72, s71, 63\ns_lshl_b32 s72, s72, 20\ns_add_u32 s72, s72, s40\n" \
    "v_mfma_f32_16x16x32_bf16 v[148:151], a[4:7], v[28:31], 0\ns_add_u32 s74, s48, s72\ns_lshr_b32 s74, s74, 8\ns_xor_b32 s74, s74, s49\n" \
    "v_mfma_f32_16x16x32_bf16 v[152:155], a[8:11], v[24:27], 0\nbuffer_load_dwordx4 v[60:63], v0, s[64:67], s70 offen offset:1024 sc1\ns_and_b32 s74, s74, 0x2000\n" \
    "v_mfma_f32_16x16x32_bf16 v[156:159], a[8:11], v[28:31], 0\ns_add_u32 s74, s74, s72\ns_and_b32 s75, s71, 1\nv_mfma_f32_16x16x32_bf16 v[160:163], a[12:15], v[24:27], 0\n" \
    "v_mfma_f32_16x16x32_bf16 v[164:167], a[12:15], v[28:31], 0\ns_waitcnt vmcnt(6)\nv_mfma_f32_16x16x32_bf16 v[136:139], a[16:19], v[32:35], v[136:139]\n" \
    "buffer_load_dwordx4 v[64:67], v0, s[64:67], s70 offen offset:2048 sc1\nv_mfma_f32_16x16x32_bf16 v[140:143], a[16:19], v[36:39], v[140:143]\n" \
    "v_mfma_f32_16x16x32_bf16 v[144:147], a[20:23], v[32:35], v[144:147]\nv_mfma_f32_16x16x32_bf16 v[148:151], a[20:23], v[36:39], v[148:151]\n" \
    "v_mfma_f32_16x16x32_bf16 v[152:155], a[24:27], v[32:35], v[152:155]\nbuffer_load_dwordx4 v[68:71], v0, s[64:67], s70 offen offset:3072 sc1\n" \
    "v_mfma_f32_16x16x32_bf16 v[156:159], a[24:27], v[36:39], v[156:159]\nv_mfma_f32_16x16x32_bf16 v[160:163], a[28:31], v[32:35], v[160:163]\n" \
    "buffer_load_dword v17, v5, s[64:67], 0 offen sc1\nv_mfma_f32_16x16x32_bf16 v[164:167], a[28:31], v[36:39], v[164:167]\ns_waitcnt vmcnt(7)\n" \
    "v_mfma_f32_16x16x32_bf16 v[136:139], a[32:35], v[40:43], v[136:139]\ns_add_u32 s70, s51, s59\nbuffer_load_dwordx4 v[72:75], v0, s[64:67], s70 offen offset:0 sc1\n" \
    "v_mfma_f32_16x16x32_bf16 v[140:143], a[32:35], v[44:47], v[140:143]\nv_mfma_f32_16x16x32_bf16 v[144:147], a[36:39], v[40:43], v[144:147]\ns_add_u32 s73, s52, s41\n" \
    "buffer_load_dwordx4 v[172:175], v18, s[64:67], s73 offen sc1\nv_mfma_f32_16x16x32_bf16 v[148:151], a[36:39], v[44:47], v[148:151]\n" \
    "buffer_load_dwordx2 v[176:177], v19, s[64:67], s52 offen sc1\nv_mfma_f32_16x16x32_bf16 v[152:155], a[40:43], v[40:43], v[152:155]\n" \
    "buffer_load_dwordx4 v[76:79], v0, s[64:67], s70 offen offset:1024 sc1\nv_mfma_f32_16x16x32_bf16 v[156:159], a[40:43], v[44:47], v[156:159]\n" \
    "v_mfma_f32_16x16x32_bf16 v[160:163], a[44:47], v[40:43], v[160:163]\nv_mfma_f32_16x16x32_bf16 v[164:167], a[44:47], v[44:47], v[164:167]\ns_waitcnt vmcnt(9)\n" \
    "v_mfma_f32_16x16x32_bf16 v[136:139], a[48:51], v[48:51], v[136:139]\nbuffer_load_dwordx4 v[80:83], v0, s[64:67], s70 offen offset:2048 sc1\n" \
    "v_mfma_f32_16x16x32_bf16 v[140:143], a[48:51], v[52:55], v[140:143]\nv_mfma_f32_16x16x32_bf16 v[144:147], a[52:55], v[48:51], v[144:147]\n" \
    "v_mfma_f32_16x16x32_bf16 v[148:151], a[52:55], v[52:55], v[148:151]\nv_mfma_f32_16x16x32_bf16 v[152:155], a[56:59], v[48:51], v[152:155]\n" \
    "buffer_load_dwordx4 v[84:87], v0, s[64:67], s70 offen offset:3072 sc1\nv_mfma_f32_16x16x32_bf16 v[156:159], a[56:59], v[52:55], v[156:159]\n" \
    "v_mfma_f32_16x16x32_bf16 v[160:163], a[60:63], v[48:51], v[160:163]\nv_mfma_f32_16x16x32_bf16 v[164:167], a[60:63], v[52:55], v[164:167]\ns_waitcnt vmcnt(9)\n" \
    "v_mfma_f32_16x16x32_bf16 v[136:139], a[64:67], v[56:59], v[136:139]\ns_add_u32 s70, s51, s60\nbuffer_load_dwordx4 v[88:91], v0, s[64:67], s70 offen offset:0 sc1\n" \
    "v_mfma_f32_16x16x32_bf16 v[140:143], a[64:67], v[60:63], v[140:143]\nv_mfma_f32_16x16x32_bf16 v[144:147], a[68:71], v[56:59], v[144:147]\n" \
    "v_mfma_f32_16x16x32_bf16 v[148:151], a[68:71], v[60:63], v[148:151]\nv_mfma_f32_16x16x32_bf16 v[152:155], a[72:75], v[56:59], v[152:155]\n" \
    "buffer_load_dwordx4 v[92:95], v0, s[64:67], s70 offen offset:1024 sc1\nv_mfma_f32_16x16x32_bf16 v[156:159], a[72:75], v[60:63], v[156:159]\n" \
    "v_mfma_f32_16x16x32_bf16 v[160:163], a[76:79], v[56:59], v[160:163]\nv_mfma_f32_16x16x32_bf16 v[164:167], a[76:79], v[60:63], v[164:167]\ns_waitcnt vmcnt(9)\n" \
    "v_mfma_f32_16x16x32_bf16 v[136:139], a[80:83], v[64:67], v[136:139]\nbuffer_load_dwordx4 v[96:99], v0, s[64:67], s70 offen offset:2048 sc1\n" \
    "v_mfma_f32_16x16x32_bf16 v[140:143], a[80:83], v[68:71], v[140:143]\nv_mfma_f32_16x16x32_bf16 v[144:147], a[84:87], v[64:67], v[144:147]\ns_waitcnt vmcnt(9)\n" \
    "v_readlane_b32 s76, v17, s75\nv_mfma_f32_16x16x32_bf16 v[148:151], a[84:87], v[68:71], v[148:151]\nv_mfma_f32_16x16x32_bf16 v[152:155], a[88:91], v[64:67], v[152:155]\n" \
    "buffer_load_dwordx4 v[100:103], v0, s[64:67], s70 offen offset:3072 sc1\ns_cmp_gt_i32 s76, s71\ns_cselect_b32 s77, 0, 0x7f000000\nv_or_b32 v16, s77, v7\n" \
    "v_mfma_f32_16x16x32_bf16 v[156:159], a[88:91], v[68:71], v[156:159]\nbuffer_load_dword v16, v16, s[64:67], s74 offen sc1\n" \
    "v_mfma_f32_16x16x32_bf16 v[160:163], a[92:95], v[64:67], v[160:163]\nv_mfma_f32_16x16x32_bf16 v[164:167], a[92:95], v[68:71], v[164:167]\ns_waitcnt vmcnt(7)\n" \
    "v_mfma_f32_16x16x32_bf16 v[136:139], a[96:99], v[72:75], v[136:139]\ns_add_u32 s70, s51, s61\nbuffer_load_dwordx4 v[104:107], v0, s[64:67], s70 offen offset:0 sc1\n" \
    "v_mfma_f32_16x16x32_bf16 v[140:143], a[96:99], v[76:79], v[140:143]\nv_max3_u32 v21, v172, v173, v174\nv_max_u32 v21, v21, v175\n" \
    "v_mfma_f32_16x16x32_bf16 v[144:147], a[100:103], v[72:75], v[144:147]\nv_cmp_eq_u32 vcc, -1, v21\nv_mfma_f32_16x16x32_bf16 v[148:151], a[100:103], v[76:79], v[148:151]\n" \
    "s_cbranch_vccnz .Lg16_%=\n.Lg17_%=:\nv_add_f32 v172, v172, v168\nv_mfma_f32_16x16x32_bf16 v[152:155], a[104:107], v[72:75], v[152:155]\n" \
    "buffer_load_dwordx4 v[108:111], v0, s[64:67], s70 offen offset:1024 sc1\nv_add_f32 v173, v173, v169\nv_mfma_f32_16x16x32_bf16 v[156:159], a[104:107], v[76:79], v[156:159]\n" \
    "v_add_f32 v174, v174, v170\nv_add_f32 v175, v175, v171\nv_mfma_f32_16x16x32_bf16 v[160:163], a[108:111], v[72:75], v[160:163]\nv_lshlrev_b32 v168, 16, v176\n" \
    "v_and_b32 v169, 0xffff0000, v176\nv_mfma_f32_16x16x32_bf16 v[164:167], a[108:111], v[76:79], v[164:167]\ns_waitcnt vmcnt(7)\nv_lshlrev_b32 v170, 16, v177\n" \
    "v_and_b32 v171, 0xffff0000, v177\nv_mfma_f32_16x16x32_bf16 v[136:139], a[112:115], v[80:83], v[136:139]\n" \
    "buffer_load_dwordx4 v[112:115], v0, s[64:67], s70 offen offset:2048 sc1\nv_fma_f32 v172, v172, v8, v168\n" \
    "v_mfma_f32_16x16x32_bf16 v[140:143], a[112:115], v[84:87], v[140:143]\nv_fma_f32 v173, v173, v9, v169\nv_fma_f32 v174, v174, v10, v170\n" \
    "v_mfma_f32_16x16x32_bf16 v[144:147], a[116:119], v[80:83], v[144:147]\nv_fma_f32 v175, v175, v11, v171\nv_cvt_pk_bf16_f32 v176, v172, v173\n" \
    "v_mfma_f32_16x16x32_bf16 v[148:151], a[116:119], v[84:87], v[148:151]\nv_cvt_pk_bf16_f32 v177, v174, v175\n" \
    "v_mfma_f32_16x16x32_bf16 v[152:155], a[120:123], v[80:83], v[152:155]\nbuffer_load_dwordx4 v[116:119], v0, s[64:67], s70 offen offset:3072 sc1\ns_lshl_b32 s73, s53, 16\n" \
    "v_mfma_f32_16x16x32_bf16 v[156:159], a[120:123], v[84:87], v[156:159]\nbuffer_store_dwordx2 v[176:177], v20, s[44:47], s73 offen\n" \
    "v_mfma_f32_16x16x32_bf16 v[160:163], a[124:127], v[80:83], v[160:163]\nv_mfma_f32_16x16x32_bf16 v[164:167], a[124:127], v[84:87], v[164:167]\ns_waitcnt vmcnt(8)\n" \
    "v_mfma_f32_16x16x32_bf16 v[136:139], a[128:131], v[88:91], v[136:139]\ns_add_u32 s70, s51, s62\nbuffer_load_dwordx4 v[120:123], v0, s[64:67], s70 offen offset:0 sc1\n" \
    "v_mfma_f32_16x16x32_bf16 v[140:143], a[128:131], v[92:95], v[140:143]\nv_mfma_f32_16x16x32_bf16 v[144:147], a[132:135], v[88:91], v[144:147]\n" \
    "v_mfma_f32_16x16x32_bf16 v[148:151], a[132:135], v[92:95], v[148:151]\nv_mfma_f32_16x16x32_bf16 v[152:155], a[136:139], v[88:91], v[152:155]\n" \
    "buffer_load_dwordx4 v[124:127], v0, s[64:67], s70 offen offset:1024 sc1\nv_mfma_f32_16x16x32_bf16 v[156:159], a[136:139], v[92:95], v[156:159]\n" \
    "v_mfma_f32_16x16x32_bf16 v[160:163], a[140:143], v[88:91], v[160:163]\nv_mfma_f32_16x16x32_bf16 v[164:167], a[140:143], v[92:95], v[164:167]\ns_waitcnt vmcnt(8)\n" \
    "v_mfma_f32_16x16x32_bf16 v[136:139], a[144:147], v[96:99], v[136:139]\nbuffer_load_dwordx4 v[128:131], v0, s[64:67], s70 offen offset:2048 sc1\n" \
    "v_mfma_f32_16x16x32_bf16 v[140:143], a[144:147], v[100:103], v[140:143]\nv_mfma_f32_16x16x32_bf16 v[144:147], a[148:151], v[96:99], v[144:147]\n" \
    "v_mfma_f32_16x16x32_bf16 v[148:151], a[148:151], v[100:103], v[148:151]\ns_waitcnt vmcnt(8)\nv_cmp_eq_u32 vcc, -1, v16\n" \
    "v_mfma_f32_16x16x32_bf16 v[152:155], a[152:155], v[96:99], v[152:155]\nbuffer_load_dwordx4 v[132:135], v0, s[64:67], s70 offen offset:3072 sc1\n" \
    "v_mfma_f32_16x16x32_bf16 v[156:159], a[152:155], v[100:103], v[156:159]\ns_cmp_eq_u64 vcc, 0\ns_cselect_b32 s77, s77, 0x7f000000\nv_or_b32 v16, s77, v0\n" \
    "v_mfma_f32_16x16x32_bf16 v[160:163], a[156:159], v[96:99], v[160:163]\nv_mfma_f32_16x16x32_bf16 v[164:167], a[156:159], v[100:103], v[164:167]\ns_waitcnt vmcnt(7)\n" \
    "v_mfma_f32_16x16x32_bf16 v[136:139], a[160:163], v[104:107], v[136:139]\nv_mfma_f32_16x16x32_bf16 v[140:143], a[160:163], v[108:111], v[140:143]\ns_add_u32 s70, s74, s56\n" \
    "buffer_load_dwordx4 v[24:27], v16, s[64:67], s70 offen offset:0 sc1\nv_mfma_f32_16x16x32_bf16 v[144:147], a[164:167], v[104:107], v[144:147]\n" \
    "v_mfma_f32_16x16x32_bf16 v[148:151], a[164:167], v[108:111], v[148:151]\nv_mfma_f32_16x16x32_bf16 v[152:155], a[168:171], v[104:107], v[152:155]\n" \
    "v_mfma_f32_16x16x32_bf16 v[156:159], a[168:171], v[108:111], v[156:159]\nbuffer_load_dwordx4 v[28:31], v16, s[64:67], s70 offen offset:1024 sc1\n" \
    "v_mfma_f32_16x16x32_bf16 v[160:163], a[172:175], v[104:107], v[160:163]\nv_mfma_f32_16x16x32_bf16 v[164:167], a[172:175], v[108:111], v[164:167]\ns_waitcnt vmcnt(7)\n" \
    "v_mfma_f32_16x16x32_bf16 v[136:139], a[176:179], v[112:115], v[136:139]\nv_mfma_f32_16x16x32_bf16 v[140:143], a[176:179], v[116:119], v[140:143]\n" \
    "buffer_load_dwordx4 v[32:35], v16, s[64:67], s70 offen offset:2048 sc1\nv_mfma_f32_16x16x32_bf16 v[144:147], a[180:183], v[112:115], v[144:147]\n" \
    "v_mfma_f32_16x16x32_bf16 v[148:151], a[180:183], v[116:119], v[148:151]\nv_mfma_f32_16x16x32_bf16 v[152:155], a[184:187], v[112:115], v[152:155]\n" \
    "v_mfma_f32_16x16x32_bf16 v[156:159], a[184:187], v[116:119], v[156:159]\nbuffer_load_dwordx4 v[36:39], v16, s[64:67], s70 offen offset:3072 sc1\n" \
    "v_mfma_f32_16x16x32_bf16 v[160:163], a[188:191], v[112:115], v[160:163]\nv_mfma_f32_16x16x32_bf16 v[164:167], a[188:191], v[116:119], v[164:167]\ns_waitcnt vmcnt(6)\n" \
    "v_mfma_f32_16x16x32_bf16 v[136:139], a[192:195], v[120:123], v[136:139]\ns_waitcnt vmcnt(4)\nv_mfma_f32_16x16x32_bf16 v[136:139], a[208:211], v[128:131], v[136:139]\n" \
    "s_add_u32 s70, s74, s57\nbuffer_load_dwordx4 v[40:43], v16, s[64:67], s70 offen offset:0 sc1\nv_mfma_f32_16x16x32_bf16 v[140:143], a[192:195], v[124:127], v[140:143]\n" \
    "v_mfma_f32_16x16x32_bf16 v[140:143], a[208:211], v[132:135], v[140:143]\nv_mfma_f32_16x16x32_bf16 v[144:147], a[196:199], v[120:123], v[144:147]\n" \
    "v_mfma_f32_16x16x32_bf16 v[144:147], a[212:215], v[128:131], v[144:147]\nbuffer_load_dwordx4 v[44:47], v16, s[64:67], s70 offen offset:1024 sc1\n" \
    "v_mfma_f32_16x16x32_bf16 v[148:151], a[196:199], v[124:127], v[148:151]\nds_write_b128 v1, v[136:139] offset:0\n" \
    "v_mfma_f32_16x16x32_bf16 v[148:151], a[212:215], v[132:135], v[148:151]\nv_mfma_f32_16x16x32_bf16 v[152:155], a[200:203], v[120:123], v[152:155]\ns_nop 0\n" \
    "ds_write_b128 v1, v[140:143] offset:1024\nv_mfma_f32_16x16x32_bf16 v[152:155], a[216:219], v[128:131], v[152:155]\n" \
    "buffer_load_dwordx4 v[48:51], v16, s[64:67], s70 offen offset:2048 sc1\nv_mfma_f32_16x16x32_bf16 v[156:159], a[200:203], v[124:127], v[156:159]\n" \
    "ds_write_b128 v1, v[144:147] offset:2048\nv_mfma_f32_16x16x32_bf16 v[156:159], a[216:219], v[132:135], v[156:159]\n" \
    "v_mfma_f32_16x16x32_bf16 v[160:163], a[204:207], v[120:123], v[160:163]\nds_write_b128 v1, v[148:151] offset:3072\n" \
    "v_mfma_f32_16x16x32_bf16 v[160:163], a[220:223], v[128:131], v[160:163]\nbuffer_load_dwordx4 v[52:55], v16, s[64:67], s70 offen offset:3072 sc1\n" \
    "v_mfma_f32_16x16x32_bf16 v[164:167], a[204:207], v[124:127], v[164:167]\nds_write_b128 v1, v[152:155] offset:4096\n" \
    "v_mfma_f32_16x16x32_bf16 v[164:167], a[220:223], v[132:135], v[164:167]\nbuffer_load_dword v17, v5, s[64:67], 0 offen sc1\nds_write_b128 v1, v[156:159] offset:5120\n" \
    "s_nop 1\nds_write_b128 v1, v[160:163] offset:6144\ns_nop 2\nds_write_b128 v1, v[164:167] offset:7168\n.Lg5_%=:\nv_cmp_u_f32 vcc, v136, v140\ns_cbranch_vccnz .Lg9_%=\n" \
    ".Lg11_%=:\nv_mov_b32 v18, v0\nv_mov_b32 v19, v3\nv_mov_b32 v20, v4\ns_mov_b32 s52, s51\ns_mov_b32 s53, s50\ns_mov_b32 s51, s74\ns_mov_b32 s50, s71\ns_mov_b32 s80, 0\n" \
    "s_cmp_eq_u32 s77, 0\ns_cbranch_scc1 .Lg4_%=\ns_waitcnt lgkmcnt(0)\ns_barrier\nds_read_b128 v[136:139], v6 offset:0\nds_read_b128 v[140:143], v6 offset:8192\n" \
    "ds_read_b128 v[144:147], v6 offset:16384\nds_read_b128 v[148:151], v6 offset:24576\nds_read_b128 v[152:155], v2 offset:0\nds_read_b128 v[156:159], v2 offset:8192\n" \
    "ds_read_b128 v[160:163], v2 offset:16384\nds_read_b128 v[164:167], v2 offset:24576\ns_add_u32 s73, s52, s41\ns_xor_b32 s73, s73, 0x1000\ns_waitcnt lgkmcnt(4)\n" \
    "v_pk_add_f32 v[136:137], v[136:137], v[140:141]\nv_pk_add_f32 v[138:139], v[138:139], v[142:143]\nv_pk_add_f32 v[144:145], v[144:145], v[148:149]\n" \
    "v_pk_add_f32 v[146:147], v[146:147], v[150:151]\nv_pk_add_f32 v[136:137], v[136:137], v[144:145]\nv_pk_add_f32 v[138:139], v[138:139], v[146:147]\n" \
    "buffer_store_dwordx4 v[136:139], v0, s[64:67], s73 offen sc1\ns_waitcnt lgkmcnt(0)\nv_pk_add_f32 v[152:153], v[152:153], v[156:157]\n" \
    "v_pk_add_f32 v[154:155], v[154:155], v[158:159]\nv_pk_add_f32 v[160:161], v[160:161], v[164:165]\nv_pk_add_f32 v[162:163], v[162:163], v[166:167]\n" \
    "v_pk_add_f32 v[152:153], v[152:153], v[12:13]\nv_pk_add_f32 v[154:155], v[154:155], v[14:15]\nv_pk_add_f32 v[168:169], v[152:153], v[160:161]\n" \
    "v_pk_add_f32 v[170:171], v[154:155], v[162:163]\ns_memrealtime s[78:79]\nv_xor_b32 v1, 0x8000, v1\nv_xor_b32 v2, 0x8000, v2\nv_xor_b32 v6, 0x8000, v6\ns_waitcnt vmcnt(1)\n" \
    "s_and_b32 s69, s50, 1\ns_nop 3\nv_readlane_b32 s76, v17, s69\ns_cmp_gt_i32 s76, s50\ns_cbranch_scc1 .Lg3_%=\n.Lg14_%=:\ns_waitcnt lgkmcnt(0)\ns_add_u32 s78, s78, s42\n" \
    ".Lg22_%=:\ns_memrealtime s[72:73]\ns_waitcnt lgkmcnt(0)\ns_sub_u32 s72, s72, s78\ns_cmp_lt_i32 s72, 0\ns_cbranch_scc1 .Lg22_%=\ns_mov_b32 s68, 0\n.Lg23_%=:\n" \
    "s_add_u32 s73, s52, s41\nbuffer_load_dwordx4 v[172:175], v0, s[64:67], s73 offen sc1\nbuffer_load_dwordx2 v[176:177], v3, s[64:67], s52 offen sc1\n" \
    "buffer_load_dword v17, v5, s[64:67], 0 offen sc1\ns_waitcnt vmcnt(1)\nv_max3_u32 v21, v172, v173, v174\nv_max_u32 v21, v21, v175\nv_cmp_eq_u32 vcc, -1, v21\n" \
    "s_cbranch_vccnz .Lg24_%=\n.Lg25_%=:\nv_add_f32 v172, v172, v168\nv_add_f32 v173, v173, v169\nv_add_f32 v174, v174, v170\nv_add_f32 v175, v175, v171\n" \
    "v_lshlrev_b32 v168, 16, v176\nv_and_b32 v169, 0xffff0000, v176\nv_lshlrev_b32 v170, 16, v177\nv_and_b32 v171, 0xffff0000, v177\nv_fma_f32 v172, v172, v8, v168\n" \
    "v_fma_f32 v173, v173, v9, v169\nv_fma_f32 v174, v174, v10, v170\nv_fma_f32 v175, v175, v11, v171\nv_cvt_pk_bf16_f32 v176, v172, v173\nv_cvt_pk_bf16_f32 v177, v174, v175\n" \
    "s_lshl_b32 s73, s53, 16\nbuffer_store_dwordx2 v[176:177], v4, s[44:47], s73 offen\nv_mov_b32 v18, 0x7f000000\nv_mov_b32 v19, 0x7f000000\nv_mov_b32 v20, 0x7f000000\n" \
    "s_cmp_ge_u32 s50, 64\ns_cbranch_scc1 .Lg2_%=\ns_waitcnt vmcnt(1)\ns_and_b32 s69, s50, 1\ns_nop 3\nv_readlane_b32 s76, v17, s69\ns_cmp_gt_i32 s76, s50\n" \
    "s_cbranch_scc1 .Lg3_%=\ns_branch .Lg7_%=\n.Lg8_%=:\ns_sleep 2\ns_add_u32 s68, s68, 1\ns_and_b32 s69, s68, 63\ns_cbranch_scc1 .Lg6_%=\ns_cmp_gt_u32 s68, 0x200000\n" \
    "s_cbranch_scc1 .Lg1_%=\nbuffer_load_dword v21, off, s[64:67], 0 sc1\ns_waitcnt vmcnt(0)\nv_readfirstlane_b32 s69, v21\ns_cmp_eq_u32 s69, 1\ns_cbranch_scc0 .Lg6_%=\n" \
    "s_endpgm\n.Lg12_%=:\ns_sleep 2\ns_mov_b32 s68, s81\ns_add_u32 s81, s81, 1\ns_add_u32 s68, s68, 1\ns_and_b32 s69, s68, 63\ns_cbranch_scc1 .Lg15_%=\n" \
    "s_cmp_gt_u32 s68, 0x200000\ns_cbranch_scc1 .Lg1_%=\nbuffer_load_dword v21, off, s[64:67], 0 sc1\ns_waitcnt vmcnt(0)\nv_readfirstlane_b32 s69, v21\ns_cmp_eq_u32 s69, 1\n" \
    "s_cbranch_scc0 .Lg15_%=\ns_endpgm\n.Lg9_%=:\ns_cmp_eq_u32 s80, 1\ns_cbranch_scc1 .Lg11_%=\ns_mov_b32 s68, 0\nv_mov_b32 v18, 0x7f000000\nv_mov_b32 v19, 0x7f000000\n" \
    "v_mov_b32 v20, 0x7f000000\n.Lg10_%=:\ns_add_u32 s70, s51, s56\nbuffer_load_dwordx4 v[24:27], v0, s[64:67], s70 offen offset:0 sc1\n" \
    "buffer_load_dwordx4 v[28:31], v0, s[64:67], s70 offen offset:1024 sc1\nbuffer_load_dwordx4 v[32:35], v0, s[64:67], s70 offen offset:2048 sc1\n" \
    "buffer_load_dwordx4 v[36:39], v0, s[64:67], s70 offen offset:3072 sc1\ns_add_u32 s70, s51, s57\nbuffer_load_dwordx4 v[40:43], v0, s[64:67], s70 offen offset:0 sc1\n" \
    "buffer_load_dwordx4 v[44:47], v0, s[64:67], s70 offen offset:1024 sc1\nbuffer_load_dwordx4 v[48:51], v0, s[64:67], s70 offen offset:2048 sc1\n" \
    "buffer_load_dwordx4 v[52:55], v0, s[64:67], s70 offen offset:3072 sc1\ns_add_u32 s70, s51, s58\nbuffer_load_dwordx4 v[56:59], v0, s[64:67], s70 offen offset:0 sc1\n" \
    "buffer_load_dwordx4 v[60:63], v0, s[64:67], s70 offen offset:1024 sc1\nbuffer_load_dwordx4 v[64:67], v0, s[64:67], s70 offen offset:2048 sc1\n" \
    "buffer_load_dwordx4 v[68:71], v0, s[64:67], s70 offen offset:3072 sc1\ns_add_u32 s70, s51, s59\nbuffer_load_dwordx4 v[72:75], v0, s[64:67], s70 offen offset:0 sc1\n" \
    "buffer_load_dwordx4 v[76:79], v0, s[64:67], s70 offen offset:1024 sc1\nbuffer_load_dwordx4 v[80:83], v0, s[64:67], s70 offen offset:2048 sc1\n" \
    "buffer_load_dwordx4 v[84:87], v0, s[64:67], s70 offen offset:3072 sc1\ns_add_u32 s70, s51, s60\nbuffer_load_dwordx4 v[88:91], v0, s[64:67], s70 offen offset:0 sc1\n" \
    "buffer_load_dwordx4 v[92:95], v0, s[64:67], s70 offen offset:1024 sc1\nbuffer_load_dwordx4 v[96:99], v0, s[64:67], s70 offen offset:2048 sc1\n" \
    "buffer_load_dwordx4 v[100:103], v0, s[64:67], s70 offen offset:3072 sc1\ns_add_u32 s70, s51, s61\nbuffer_load_dwordx4 v[104:107], v0, s[64:67], s70 offen offset:0 sc1\n" \
    "buffer_load_dwordx4 v[108:111], v0, s[64:67], s70 offen offset:1024 sc1\nbuffer_load_dwordx4 v[112:115], v0, s[64:67], s70 offen offset:2048 sc1\n" \
    "buffer_load_dwordx4 v[116:119], v0, s[64:67], s70 offen offset:3072 sc1\ns_add_u32 s70, s51, s62\nbuffer_load_dwordx4 v[120:123], v0, s[64:67], s70 offen offset:0 sc1\n" \
    "buffer_load_dwordx4 v[124:127], v0, s[64:67], s70 offen offset:1024 sc1\nbuffer_load_dwordx4 v[128:131], v0, s[64:67], s70 offen offset:2048 sc1\n" \
    "buffer_load_dwordx4 v[132:135], v0, s[64:67], s70 offen offset:3072 sc1\ns_waitcnt vmcnt(0) lgkmcnt(0)\nv_max3_u32 v21, v24, v25, v25\nv_max3_u32 v21, v21, v26, v27\n" \
    "v_max3_u32 v21, v21, v28, v29\nv_max3_u32 v21, v21, v30, v31\nv_max3_u32 v21, v21, v32, v33\nv_max3_u32 v21, v21, v34, v35\nv_max3_u32 v21, v21, v36, v37\n" \
    "v_max3_u32 v21, v21, v38, v39\nv_max3_u32 v21, v21, v40, v41\nv_max3_u32 v21, v21, v42, v43\nv_max3_u32 v21, v21, v44, v45\nv_max3_u32 v21, v21, v46, v47\n" \
    "v_max3_u32 v21, v21, v48, v49\nv_max3_u32 v21, v21, v50, v51\nv_max3_u32 v21, v21, v52, v53\nv_max3_u32 v21, v21, v54, v55\nv_max3_u32 v21, v21, v56, v57\n" \
    "v_max3_u32 v21, v21, v58, v59\nv_max3_u32 v21, v21, v60, v61\nv_max3_u32 v21, v21, v62, v63\nv_max3_u32 v21, v21, v64, v65\nv_max3_u32 v21, v21, v66, v67\n" \
    "v_max3_u32 v21, v21, v68, v69\nv_max3_u32 v21, v21, v70, v71\nv_max3_u32 v21, v21, v72, v73\nv_max3_u32 v21, v21, v74, v75\nv_max3_u32 v21, v21, v76, v77\n" \
    "v_max3_u32 v21, v21, v78, v79\nv_max3_u32 v21, v21, v80, v81\nv_max3_u32 v21, v21, v82, v83\nv_max3_u32 v21, v21, v84, v85\nv_max3_u32 v21, v21, v86, v87\n" \
    "v_max3_u32 v21, v21, v88, v89\nv_max3_u32 v21, v21, v90, v91\nv_max3_u32 v21, v21, v92, v93\nv_max3_u32 v21, v21, v94, v95\nv_max3_u32 v21, v21, v96, v97\n" \
    "v_max3_u32 v21, v21, v98, v99\nv_max3_u32 v21, v21, v100, v101\nv_max3_u32 v21, v21, v102, v103\nv_max3_u32 v21, v21, v104, v105\nv_max3_u32 v21, v21, v106, v107\n" \
    "v_max3_u32 v21, v21, v108, v109\nv_max3_u32 v21, v21, v110, v111\nv_max3_u32 v21, v21, v112, v113\nv_max3_u32 v21, v21, v114, v115\nv_max3_u32 v21, v21, v116, v117\n" \
    "v_max3_u32 v21, v21, v118, v119\nv_max3_u32 v21, v21, v120, v121\nv_max3_u32 v21, v21, v122, v123\nv_max3_u32 v21, v21, v124, v125\nv_max3_u32 v21, v21, v126, v127\n" \
    "v_max3_u32 v21, v21, v128, v129\nv_max3_u32 v21, v21, v130, v131\nv_max3_u32 v21, v21, v132, v133\nv_max3_u32 v21, v21, v134, v135\nv_cmp_eq_u32 vcc, -1, v21\n" \
    "s_mov_b32 s80, 1\ns_cbranch_vccz .Lg13_%=\ns_sleep 2\ns_add_u32 s68, s68, 1\ns_and_b32 s69, s68, 63\ns_cbranch_scc1 .Lg10_%=\ns_cmp_gt_u32 s68, 0x200000\n" \
    "s_cbranch_scc1 .Lg1_%=\nbuffer_load_dword v21, off, s[64:67], 0 sc1\ns_waitcnt vmcnt(0)\nv_readfirstlane_b32 s69, v21\ns_cmp_eq_u32 s69, 1\ns_cbranch_scc0 .Lg10_%=\n" \
    "s_endpgm\n.Lg16_%=:\ns_mov_b32 s68, 0\n.Lg18_%=:\ns_sleep 1\nbuffer_load_dwordx4 v[172:175], v0, s[64:67], s73 offen sc1\ns_waitcnt vmcnt(0)\n" \
    "v_max3_u32 v21, v172, v173, v174\nv_max_u32 v21, v21, v175\nv_cmp_eq_u32 vcc, -1, v21\ns_cbranch_vccz .Lg17_%=\ns_add_u32 s68, s68, 1\ns_and_b32 s69, s68, 63\n" \
    "s_cbranch_scc1 .Lg18_%=\ns_cmp_gt_u32 s68, 0x200000\ns_cbranch_scc1 .Lg1_%=\nbuffer_load_dword v21, off, s[64:67], 0 sc1\ns_waitcnt vmcnt(0)\nv_readfirstlane_b32 s69, v21\n" \
    "s_cmp_eq_u32 s69, 1\ns_cbranch_scc0 .Lg18_%=\ns_endpgm\n.Lg19_%=:\ns_mov_b32 s68, 0\n.Lg21_%=:\ns_sleep 1\nbuffer_load_dwordx4 v[172:175], v0, s[64:67], s73 offen sc1\n" \
    "s_waitcnt vmcnt(0)\nv_max3_u32 v21, v172, v173, v174\nv_max_u32 v21, v21, v175\nv_cmp_eq_u32 vcc, -1, v21\ns_cbranch_vccz .Lg20_%=\ns_add_u32 s68, s68, 1\n" \
    "s_and_b32 s69, s68, 63\ns_cbranch_scc1 .Lg21_%=\ns_cmp_gt_u32 s68, 0x200000\ns_cbranch_scc1 .Lg1_%=\nbuffer_load_dword v21, off, s[64:67], 0 sc1\ns_waitcnt vmcnt(0)\n" \
    "v_readfirstlane_b32 s69, v21\ns_cmp_eq_u32 s69, 1\ns_cbranch_scc0 .Lg21_%=\ns_endpgm\n.Lg24_%=:\ns_waitcnt vmcnt(0)\ns_and_b32 s69, s50, 1\ns_nop 3\n" \
    "v_readlane_b32 s76, v17, s69\ns_cmp_gt_i32 s76, s50\ns_cbranch_scc1 .Lg3_%=\ns_add_u32 s68, s68, 1\ns_and_b32 s69, s68, 63\ns_cbranch_scc1 .Lg23_%=\n" \
    "s_cmp_gt_u32 s68, 0x200000\ns_cbranch_scc1 .Lg1_%=\nbuffer_load_dword v21, off, s[64:67], 0 sc1\ns_waitcnt vmcnt(0)\nv_readfirstlane_b32 s69, v21\ns_cmp_eq_u32 s69, 1\n" \
    "s_cbranch_scc0 .Lg23_%=\ns_endpgm\n.Lg1_%=:\nv_mov_b32 v21, 1\nbuffer_store_dword v21, off, s[64:67], 0 sc1\ns_endpgm\n.Lg2_%=:\ns_waitcnt vmcnt(0)\n"
#define TD_F 57
#define TD_G 155
constexpr int DQ = LQ + 1, NR = 4, NQ = 8, L_Q = NR * 16384, L_KP = L_Q + NQ * 8192;
static_assert(DQ < NQ, "q slots");
__device__ __forceinline__ void opaque(v4u& r) { asm volatile("s_nop 4" : "+s"(r)); }
__device__ __forceinline__ int keyof(int gg, int r, int t) { return 16 * (gg & 1) + 8 * (gg >> 1) + (r & 1) + 4 * (r >> 1) + 2 * t; }
__device__ __forceinline__ void smm4(f32x4& sc, const v4u (&k)[4], const v4u (&q)[4]) {
    asm volatile("s_nop 1\n\tv_mfma_f32_16x16x32_bf16 %0, %1, %5, 0\n\tv_mfma_f32_16x16x32_bf16 %0, %2, %6, %0\n\tv_mfma_f32_16x16x32_bf16 %0, %3, %7, %0\n\tv_mfma_f32_16x16x32_bf16 %0, %4, %8, %0\n\ts_nop 7"
                 : "=&v"(sc) : "v"(k[0]), "v"(k[1]), "v"(k[2]), "v"(k[3]), "v"(q[0]), "v"(q[1]), "v"(q[2]), "v"(q[3]));
}
template <class C8> __device__ __forceinline__ void qk_tail(const Args& a, rsrc_t rs, unsigned char* lds, int head, unsigned* ctl, C8 c, f32x2 ms) {
    const int lane = lane_id(), wave = a.pad0, l15 = lane & 15, g = lane >> 4;
    const unsigned lb = (unsigned)(size_t)(LAS unsigned char*)lds, q = l15 >> 2, p = lane & 3, l16 = lane * 16;
    const unsigned pol = __builtin_amdgcn_readfirstlane(*(const unsigned*)(a.ws + O_POL));
    const int qt = wave & 1, mk = (l15 & 1) + 4 * ((l15 >> 1) & 1) + 2 * ((l15 >> 2) & 1);
    const unsigned kxr = g * 512 + keyof(q, p, 0) * 16, kmr = mk * 64 + 16 * g;
    const unsigned po1 = (g >> 1) * 0x7F000000u + (8 + (head >> 4)) * 16384 + (((head >> 2) & 3) * 2 + qt) * 1024 + ((head & 3) * 16 + l15) * 16 + 8 * (g & 1);
    constexpr float SC2 = 0.08838834764831845f * 1.4426950408889634f;
    v4u qf[4], kf[4];
    f32x4 sc;
    float mx = ms[0], sum = ms[1];
    bool vok = false;
    for (int ps = NSEG - DQ + (((NSEG - DQ) ^ (wave >> 1)) & 1); ps < NSEG; ps += 2) {
#pragma unroll
        for (int ks = 0; ks < 4; ++ks) qf[ks] = lds16(lds + L_Q + (ps & (NQ - 1)) * 8192 + (qt * 4 + ks) * 1024 + l16);
        if (ps > NSEG - DQ) {
            if (!vok && !wave_wait<8>(PW_V(head), 1, 1, NSEG, ctl + C_ABORT)) asm volatile("s_endpgm");
            vok = true;
            const unsigned kx = nearg(a.ws, pol, O_KV + RI(ps) * KV_SLOT + head * 16384, 1);
            v4u k2[4];
#pragma unroll
            for (int ks = 0; ks < 4; ++ks) { kf[ks] = sld16(rs, kxr + ks * 2048, kx); k2[ks] = sld16(rs, kxr + 32 + ks * 2048, kx); }
            mx = -3e38f; sum = 0.f;
#pragma unroll
            for (int t = 0; t < 2; ++t) {
                if (t) smm4(sc, k2, qf); else smm4(sc, kf, qf);
#pragma unroll
                for (int r = 0; r < 4; ++r) { c[4 * t + r] = keyof(g, r, t) <= 16 * qt + l15 ? sc[r] : -INFINITY; mx = fmaxf(mx, c[4 * t + r]); }
            }
#pragma unroll
            for (int k = 0; k < 8; ++k) { c[k] = __builtin_amdgcn_exp2f(fmaf(c[k], SC2, -SC2 * mx)); sum += c[k]; }
        }
        const unsigned pb = nearg(a.ws, pol, O_D + (unsigned)ps * D_SLOT, 0), kb = kmof(a.ws, pol, ps, head);
        unsigned spins = 0;
        for (;;) {
            const unsigned cw = __builtin_amdgcn_readfirstlane(__hip_atomic_load((gu32*)(ctl + C_C + 32 * (head >> 2)), RLX_AGENT));
            if ((int)(cw & 127) > ps) { if ((int)(cw & 127) == ps + 1) cp_delay(ps == NSEG - 1 ? TD_P - TD_L : TD_P, cw); break; }
            __builtin_amdgcn_s_sleep(5);
            if (spin_fail(spins, ctl + C_ABORT)) asm volatile("s_endpgm");
        }
        RSTAMP(ps + DQ, 0);
        for (;;) {
#pragma unroll
            for (int ks = 0; ks < 4; ++ks) kf[ks] = sld16(rs, kmr + 512 * ks, kb);
            if (!__any(max(max(umax4(kf[0]), umax4(kf[1])), max(umax4(kf[2]), umax4(kf[3]))) == SENT)) break;
            __builtin_amdgcn_s_sleep(2);
            if (spin_fail(spins, ctl + C_ABORT)) asm volatile("s_endpgm");
        }
        RSTAMP(ps + DQ, 1);
        smm4(sc, kf, qf);
        float t1 = fmaxf(fmaxf(fmaxf(sc[0], sc[1]), fmaxf(sc[2], sc[3])), mx);
        { auto r = __builtin_amdgcn_permlane16_swap(__float_as_uint(t1), __float_as_uint(t1), false, false); t1 = fmaxf(__uint_as_float(r[0]), __uint_as_float(r[1])); }
        { auto r = __builtin_amdgcn_permlane32_swap(__float_as_uint(t1), __float_as_uint(t1), false, false); t1 = -SC2 * fmaxf(__uint_as_float(r[0]), __uint_as_float(r[1])); }
        const float ax = __builtin_amdgcn_exp2f(fmaf(mx, SC2, t1));
#pragma unroll
        for (int r = 0; r < 4; ++r) sc[r] = __builtin_amdgcn_exp2f(fmaf(sc[r], SC2, t1));
        const float rc = __builtin_amdgcn_rcpf(red32_add(red16_add(fmaf(0.5f, (sc[0] + sc[1]) + (sc[2] + sc[3]), sum * ax)))), fx = ax * rc;
        sst16((v4u){pack2(c[0] * fx, c[1] * fx), pack2(c[2] * fx, c[3] * fx), pack2(c[4] * fx, c[5] * fx), pack2(c[6] * fx, c[7] * fx)}, rs, l16, pb + (head >> 2) * 16384 + ((head & 3) * 2 + qt) * 1024, 16);
        __builtin_amdgcn_raw_buffer_store_b64((v2u){pack2(sc[0] * rc, sc[1] * rc), pack2(sc[2] * rc, sc[3] * rc)}, rs, po1, pb, 16);
        tell<0>((unsigned*)(a.ws + nearg(a.ws, pol, C_P * 4, 0)) - GO(0, 0), ps, rdw<0>(arrive(a.ws, 0, ps)), 64);
        RSTAMP(ps + DQ, 2);
    }
}
__device__ __forceinline__ v16u cat(v4u x0, v4u x1, v4u x2, v4u x3) { return __builtin_shufflevector(__builtin_shufflevector(x0, x1, 0, 1, 2, 3, 4, 5, 6, 7), __builtin_shufflevector(x2, x3, 0, 1, 2, 3, 4, 5, 6, 7), 0, 1, 2, 3, 4, 5, 6, 7, 8, 9, 10, 11, 12, 13, 14, 15); }
__device__ __forceinline__ v32u cat2(v16u x, v16u y) { return __builtin_shufflevector(x, y, 0, 1, 2, 3, 4, 5, 6, 7, 8, 9, 10, 11, 12, 13, 14, 15, 16, 17, 18, 19, 20, 21, 22, 23, 24, 25, 26, 27, 28, 29, 30, 31); }
template <bool isV> __device__ __forceinline__ void proj_role(const Args& a, rsrc_t rs, unsigned char* lds, int head, unsigned* ctl) {
    const int tid = otid(), lane = tid & 63, wave = a.pad0, l15 = lane & 15, g = lane >> 4;
    const unsigned lb = (unsigned)(size_t)(LAS unsigned char*)lds, q = l15 >> 2, p = lane & 3;
    const unsigned pol = __builtin_amdgcn_readfirstlane(*(const unsigned*)(a.ws + O_POL));
    v4u rA = mkrs(a.ws, WS_END);
    auto prm = [](unsigned x) { return isV ? ((x & 1) << 1) | (x >> 1) : x; };
    const unsigned ar = 8 * wave + (lane >> 3), ao = (2 * prm((ar & 15) >> 2) + (ar >> 4)) * 16384 + (ar & 3) * 2048 + 16 * ((lane & 7) ^ ((ar >> 1) & 7));
    const unsigned h0 = nearg(a.ws, pol, O_D + D_H1, 1);
    auto stage0 = [&](int t, unsigned oob) { set_m0(lb + wave * 4096 + t * 16384); asm volatile("s_nop 0"); const unsigned so = h0 + t * 512;
        dma1<0>(rA, ao | oob, so); dma1<1024>(rA, ao | oob, so - 896); dma1<2048>(rA, ao | oob, so - 1792); dma1<3072>(rA, ao | oob, so - 2688); };
    int hr = 0;
    v4u w[32][3];
    {
        v4u rX = mkrs(isV ? a.Wv : a.Wq, DM * HD * 2), rK = mkrs(a.Wk, DM * HD * 2);
        unsigned bo[2], ko[2];
#pragma unroll
        for (int i = 0; i < 2; ++i) {
            bo[i] = (g * HD + 16 * ((l15 >> 1) ^ (g | i << 2)) + 8 * (lane & 1)) * 2;
            ko[i] = ((lane >> 3) * HD + 16 * (((lane >> 1) & 3) ^ ((g & 1) | i << 1)) + 8 * (lane & 1)) * 2;
        }
        asm volatile("s_nop 4" : "+s"(rX), "+s"(rK), "+s"(rA));
        unsigned hc;
        auto issue = [&](int G) {
            const unsigned r0 = (4 * GO_(G) + wave) * 32, d = lb + ((G + 1) % 3) * 49152;
#pragma unroll
            for (int j = 0; j < 8; ++j) bdma<(NTW & 1)>(rX, bo[(j >> 1) & 1], ((r0 + 4 * j) * HD + head * DH) * 2, d + wave * 8192 + j * 1024);
#pragma unroll
            for (int j = 0; j < 4; ++j) bdma<(NTW & 1)>(rK, ko[j & 1], ((r0 + 8 * j) * HD + head * DH + (isV ? 64 : 0)) * 2, d + 32768 + wave * 4096 + j * 1024);
        };
        issue(0); issue(1); issue(2);
        const unsigned ox = (8 * g + q) * 256 + 8 * p, sx = q | (g & 1) << 2, ok = 32768 + (8 * g + q) * 128 + 32 * (wave ^ ((q >> 1) | (g & 1) << 1)) + 8 * p;
        sfor<0, 8>([&](auto gg) {
            constexpr int G = decltype(gg)::v;
            asm volatile("s_waitcnt vmcnt(%0)\n\ts_barrier" :: "n"(G < 7 ? 24 : 12) : "memory");
            if constexpr (G == 5) { asm volatile("" : "+v"(hc)); const u64 m = __ballot(hc < 32u); if (wave == 0) *(volatile LAS unsigned*)(lds + L_FLAG) = m ? __builtin_ctzll(m) : NSEG; }
#pragma unroll
            for (int k = 0; k < 4; ++k) {
#pragma unroll
                for (int n = 0; n < 3; ++n) {
                    const unsigned o = ((G + 1) % 3) * 49152 + (n < 2 ? k * 8192 + ox + 32 * ((2 * wave + n) ^ sx) : k * 4096 + ok);
                    const v2u lo = tr_rd(lds, o), hi = tr_rd(lds, o + (n < 2 ? 1024 : 512));
                    w[4 * GO_(G) + k][n] = (v4u){lo.x, lo.y, hi.x, hi.y};
                }
            }
#pragma unroll
            for (int k = 4 * GO_(G); k < 4 * GO_(G) + 4; ++k) asm volatile("" : "+a"(w[k][0]), "+a"(w[k][1]), "+v"(w[k][2]));
            lds_barrier();
            if constexpr (G == 3) asm volatile("buffer_load_dword %0, %1, %2, 0 offen sc1" : "=v"(hc) : "v"((C_H + 32 * lane) * 4), "s"(rA));
            if (G + 3 < 8) issue(G + 3);
            if constexpr (G == 5) hr = __builtin_amdgcn_readfirstlane(*(volatile LAS unsigned*)(lds + L_FLAG));
            if constexpr (G == 5) { for (int t = 0; t < 3; ++t) stage0(t, hr ? 0u : 0x7F000000u); }
        });
    }
    RSTAMP(74, 0);
    unsigned ra[2];
#pragma unroll
    for (int u = 0; u < 2; ++u) ra[u] = (l15 >> 3) * 4096 + (l15 & 7) * 128 + 16 * ((4 * u + g) ^ (l15 >> 1));
    if (!hr) {
        if (!wave_wait<5>(ctl + C_H, 1, 1, 32, ctl + C_ABORT)) asm volatile("s_endpgm");
        for (int t = 0; t < 3; ++t) stage0(t, 0u);
    }
    asm volatile("s_waitcnt vmcnt(0)\n\ts_barrier" ::: "memory");
    const unsigned tok = 8 * prm(q) + p, l16 = lane * 16;
    const int qt = wave & 1, mk = (l15 & 1) + 4 * ((l15 >> 1) & 1) + 2 * ((l15 >> 2) & 1);
    const unsigned kxr = g * 512 + keyof(q, p, 0) * 16, kmr = mk * 64 + 16 * g;
    const unsigned po1 = (g >> 1) * 0x7F000000u + (8 + (head >> 4)) * 16384 + (((head >> 2) & 3) * 2 + qt) * 1024 + ((head & 3) * 16 + l15) * 16 + 8 * (g & 1);
    constexpr float SC2 = 0.08838834764831845f * 1.4426950408889634f;
    constexpr unsigned OOB = 0x7F000000u;
    typedef float f32x8 __attribute__((ext_vector_type(8)));
    f32x8 c = {0.f, 0.f, 0.f, 0.f, 0.f, 0.f, 0.f, 0.f};
    f32x2 ms = {0.f, 0.f};
    {
        auto qwa = [&](int m) { return lb + L_Q + (((tok >> 4) * 4 + wave) * 64 + (2 * (g & 1) + (g >> 1)) * 16 + ((tok + 4 * m) & 15)) * 16; };
        v16u c0 = {l16, ao, lb + ra[0], lb + ra[1], ((isV ? 8 : 0) + 2 * wave + (g >> 1)) * 512 + (tok + 4 * (g & 1)) * 16, tid ? OOB : (C_X + 32 * (head + (isV ? 0 : 64))) * 4u,
                   tid == 3 ? C_HA * 4u : tid > 1 ? OOB : (tid == 0 ? C_C + 32 * (head >> 2) : isV ? GO(1, head >> 2) : C_X + 32 * head) * 4u, lb + L_FLAG + (tid < 4 ? 4 * tid : 16),
                   isV ? tid * 16u : lb + L_KP + kxr, kmr, po1, qwa(0), qwa(1), lb + L_Q + qt * 4096 + l16, isV ? lb + L_FLAG : lane < 8 ? nearg(a.ws, pol, C_P * 4, 0) + 128 * lane : OOB, isV ? (lane == 1) * 4u : lb + L_FLAG};
        auto u = [](unsigned x) { return (unsigned)__builtin_amdgcn_readfirstlane(x); };
        v16u sv = {(unsigned)(size_t)a.ws, pol, 0u, (unsigned)hr, u(lb + wave * 4096), u(O_KV + head * 16384), h0, (unsigned)wave,
                   u(isV ? O_UM + head * 32768 : lb + L_KP + wave * 2048), u(isV ? (head < 27 ? head * 32768 : (head - 27) * 32768 + 8192) : (head >> 2) * 16384 + (head & 3) * 2048),
                   u(isV ? (head - 21u < 8u ? (head - 21) * 16384 + 8192 : head * 16384) : (head >> 2) * 16384 + ((head & 3) * 2 + qt) * 1024), u(isV ? O_E1 + (head >> 1) * 16384 : 0u), u(isV ? (head & 1) * 4096 : wave * 2048), 0u, 0u, 0u};
        const v16u sk = {RG - 1, ROFF & 1 ? 64 : RG, O_D + D_H1, D_H1, O_EP, O_D, C_N * 4, DQ, DQ - 1, (NQ - DQ + 1) * 8192, (NQ - DQ) * 8192, LVS, LV0, LV, TD_F << 8, TD_G << 8};
        v16u mk8;
#pragma unroll
        for (int i = 0; i < 8; ++i) { const u64 m = __ballot(keyof(g, i & 3, i >> 2) <= 16 * qt + l15); mk8[2 * i] = (unsigned)m; mk8[2 * i + 1] = (unsigned)(m >> 32); }
        v32u ww[12];
#pragma unroll
        for (int i = 0; i < 8; ++i) ww[i] = cat2(cat(w[4 * i][0], w[4 * i][1], w[4 * i + 1][0], w[4 * i + 1][1]), cat(w[4 * i + 2][0], w[4 * i + 2][1], w[4 * i + 3][0], w[4 * i + 3][1]));
#pragma unroll
        for (int i = 0; i < 4; ++i) ww[8 + i] = cat2(cat(w[8 * i][2], w[8 * i + 1][2], w[8 * i + 2][2], w[8 * i + 3][2]), cat(w[8 * i + 4][2], w[8 * i + 5][2], w[8 * i + 6][2], w[8 * i + 7][2]));
        v4u sn = sentv();
        const unsigned* dp = nullptr;
#define PROJ_CALL(TXT) asm volatile(TXT : "+{s[48:63]}"(sv), "+{v[0:15]}"(c0), "+{v[16:19]}"(sn), "+{v[112:119]}"(c), "+{v[120:121]}"(ms) \
            : "{s[16:31]}"(sk), "{s[64:67]}"(rA), "{s[80:95]}"(mk8), "{s32}"(SC2), "{s33}"(-3e38f), "{s[34:35]}"(dp), "{s36}"(C_H * 4), "{v22}"(-INFINITY), [sm] "n"(SM), [sv] "n"(SV), \
              "{a[0:31]}"(ww[0]), "{a[32:63]}"(ww[1]), "{a[64:95]}"(ww[2]), "{a[96:127]}"(ww[3]), "{a[128:159]}"(ww[4]), "{a[160:191]}"(ww[5]), "{a[192:223]}"(ww[6]), "{a[224:255]}"(ww[7]), \
              "{v[124:155]}"(ww[8]), "{v[156:187]}"(ww[9]), "{v[188:219]}"(ww[10]), "{v[220:251]}"(ww[11]) \
            : "memory", "vcc", "scc", "m0", "v20", "v21", "v24", "v25", "v26", "v27", "v28", "v29", "v30", "v31", "v32", "v33", "v34", "v35", "v36", "v37", "v38", "v39", "v40", "v41", "v42", "v43", "v44", "v45", "v46", "v47", "v48", "v49", "v50", "v51", "v52", "v53", "v54", "v55", "v56", "v57", "v58", "v59", "v60", "v61", "v62", "v63", "v64", "v65", "v66", "v67", "v68", "v69", "v70", "v71", "v72", "v73", "v74", "v75", "v76", "v77", "v78", "v79", "v80", "v81", "v82", "v83", "v84", "v85", "v86", "v87", "v88", "v89", "v90", "v91", "v92", "v93", "v94", "v95", "v96", "v97", "v98", "v99", "v100", "v101", "v102", "v103", "v104", "v105", "v106", "v107", "v108", "v109", "v110", "v111", "v122", "v123", "s37", "s38", "s39", "s40", "s41", "s42", "s43", "s68", "s69", "s70", "s71", "s72", "s73", "s74", "s75", "s76", "s77", "s78", "s79", "s96", "s97", "s98", "s99")
        if constexpr (isV) PROJ_CALL(PROJ_V); else PROJ_CALL(PROJ_Q);
    }
    auto signal = [&](int n) { if (otid() == 0) __hip_atomic_store((gu32*)(isV ? PW_V(head) : PW_Q(head)), (unsigned)n, RLX_AGENT); };
    asm volatile("s_waitcnt vmcnt(0) lgkmcnt(0)\n\ts_barrier" ::: "memory");
    signal(NSEG + 1);
    if constexpr (!isV) qk_tail(a, rs, lds, head, ctl, c, ms);
}

#define RDM 16
#define TD_T 24
__device__ __forceinline__ void r_role(const Args& a, unsigned char* lds, int xg, int cg, int set, unsigned par, unsigned ev) {
    const int tid = otid(), lane = tid & 63, wave = a.pad0, l15 = lane & 15, g = lane >> 4;
    const unsigned q = l15 >> 2, p = lane & 3, lb0 = (unsigned)(size_t)(LAS unsigned char*)lds;
    const unsigned pol = __builtin_amdgcn_readfirstlane(*(const unsigned*)(a.ws + O_POL));
    f32x4 ga[2];
    mod8(a, 2 * DM + 32 * cg + 8 * ((lane >> 3) & 3), ga);
    asm volatile("" : "+v"(ga[0]), "+v"(ga[1]));
    v4u w[8][8];
    {
        const unsigned lb = lb0 + wave * 32768;
        v4u rW = mkrs(a.Wo, DM * HD * 2);
        const unsigned bo = ((lane >> 2) * DM + 16 * (((lane >> 1) & 1) ^ (g & 1)) + 8 * (lane & 1)) * 2;
        asm volatile("s_nop 4" : "+s"(rW));
        auto issue = [&](int c) {
#pragma unroll
            for (int i = 0; i < 8; ++i) bdma<(NTW & 128)>(rW, bo, (((8 * wave + c) * DH + 16 * i) * DM + 32 * cg) * 2, lb + (c & 3) * 8192 + i * 1024);
        };
#define RHN 4
#define DLR 450
#define DLH 100
        set_m0(lb0 + LDS_BYTES - 1024);
        {
            const unsigned t0 = rt_now();
            const int W = ((cg & 7) * 2 + set) * 4 + wave;
            v4u rQ = mkrs(W & 1 ? a.Wq : a.Wk, DM * HD * 2);
            asm volatile("s_nop 4" : "+s"(rQ));
#define TLT 0
            if (TLT && wave < 2) {
                v4u rT = wave ? mkrs(a.ws, WS_END) : mkrs(a.x, S_TOK * DM * 2);
                asm volatile("s_nop 4\n\tbuffer_load_dword %0, %1, 0 offen lds" :: "v"(wave ? lane * 2 * MiB + 63 * 16384 : lane * 65536 + cg * 128), "s"(rT));
            }
            rt_delay(DLH, t0);
            for (int j = 0; j < RHN; ++j) asm volatile("s_nop 4\n\tbuffer_load_dword %0, %1, %2 offen lds" :: "v"((lane >> 4) * 8192 + (lane & 15) * 128), "s"(rQ), "s"((128 * (8 - RHN + j) + 4 * (W >> 1)) * 8192 + (cg >> 3) * 2048));
            rt_delay(DLR, t0);
            for (int j = 0; j < 4; ++j) asm volatile("s_nop 4\n\tbuffer_load_dword %0, %1, %2 offen lds" :: "v"((lane >> 2) * 2048 + (lane & 3) * 128), "s"(rW), "s"(((8 * wave + 2 * j + set) * DH + 16 * (cg & 7)) * 2048 + (cg >> 3) * 512));
        }
        issue(0); issue(1); issue(2); issue(3);
        sfor<0, 8>([&](auto cc) {
            constexpr int c = decltype(cc)::v;
            asm volatile("s_waitcnt vmcnt(%0)" :: "n"(c < 5 ? 24 : 8 * (7 - c)) : "memory");
#pragma unroll
            for (int n = 0; n < 8; ++n) {
                const unsigned o = wave * 32768 + (c & 3) * 8192 + (n & 3) * 2048 + (4 * g + q) * 64 + 32 * ((n >> 2) ^ (g & 1)) + 8 * p;
                const v2u lo = tr_rd(lds, o), hi = tr_rd(lds, o + 1024);
                w[c][n] = (v4u){lo.x, lo.y, hi.x, hi.y};
            }
#pragma unroll
            for (int n = 0; n < 8; n += 4) asm volatile("" : "+a"(w[c][n]), "+a"(w[c][n + 1]), "+a"(w[c][n + 2]), "+a"(w[c][n + 3]));
            asm volatile("s_waitcnt lgkmcnt(0)" ::: "memory");
            if (c + 4 < 8) issue(c + 4);
        });
    }
    lds_barrier();
    constexpr unsigned OOB = 0x7F000000u, L_R = 0, L_O = 4608, L_M = L_O + 4 * 4608;
    RSTAMP(74, 0);
    const unsigned l16 = lane * 16, r = 8 * wave + (lane & 7), c4 = 8 * ((lane >> 3) & 3) + 4 * (lane >> 5), cr = xg >> 1, cb = 2 * cg;
    v16u c0 = {l16, l16 + 4096, l16, l16 + 4096, (lane & 8) ? 0xEEEEu : 0x4444u, (16 - 3 * cr - wave) * 16384 + wave * 1024 + 2 * g * 128 + (l15 & 7) * 16 + (l15 >> 3) * 8,
                     lb0 + L_R + wave * 1152 + 4 * (g & 1) * 144 + (16 * (g >> 1) + l15) * 4, lb0 + L_R + (l15 >> 1) * 144 + (16 * (g & 1) + 8 * (lane & 1) + 4 * (g >> 1)) * 4,
                     lane < 8 ? (C_X + 32 * (8 * wave + lane)) * 4u : lane - 9u < 2u ? nearg(a.ws, pol, (C_P + 32 * xg) * 4, 0) + 4 * (set ^ (lane & 1) ^ 1) : OOB,
                     lb0 + L_O + wave * 4608 + l15 * 144 + g * 16, lb0 + L_O + r * 144 + c4 * 4, (r * DM + 32 * cg + c4) * 2,
                     lane < 32 ? D_X2 - wave * 32768 + (cg >> 2) * 16384 + ((cg & 3) * 2 + (r >> 4)) * 1024 + ((lane >> 3) * 16 + (r & 15)) * 16 : OOB,
                     lane < 8 ? D_ST - wave * 32768 + cg * 256 + r * 8 : OOB, tid ? OOB : (C_X + 32 * (32 + cg) + 3072 * set) * 4u, cg == 9 && tid < 8 ? (GO(0, lane) + set) * 4u : OOB};
    *(LAS f32x4*)(lds + L_M + wave * 3072 + l16) = ga[lane >> 5];
    auto u = [](unsigned x) { return (unsigned)__builtin_amdgcn_readfirstlane(x); };
    const u64 dp = 0;
    const v16u sk = {u(O_UM + cb * 16384 + wave * 2048), u(O_EP + (cr * 4 + wave) * 16384), u(O_E1 + (wave * 4 + ((cb >> 2) & 3)) * 16384 + (cb >> 4) * 1024 + (cb & 3) * 256), u(O_KV + wave * 131072),
                     u(O_D + wave * 32768), u((8 + (wave >> 1) - 2 * wave) * 16384 + (wave & 1) * 4096), u((20 - cr * 4 - wave) * 16384 + xg * 128), nearg(a.ws, pol, (C_R + 32 * xg + 1) * 4, 0), C_T * 4, RG - 1, CH_S1, TD_O, TD_O - TD_L, 0u, (unsigned)dp, (unsigned)(dp >> 32)};
    v8u sv = {(unsigned)(size_t)a.ws, pol, u(set - 2), 0u, 0u, par >> 26, u(cg & 4 ? (par >> 16) & 31 : ev), (par & 255) + ((par >> 8) & 255)};
    unsigned hold = ((par >> 8) & 255) - RDM;
    v32u ww[8];
#pragma unroll
    for (int i = 0; i < 8; ++i) ww[i] = cat2(cat(w[i][0], w[i][1], w[i][2], w[i][3]), cat(w[i][4], w[i][5], w[i][6], w[i][7]));
    asm volatile("s_nop 4\n" ROLE_R : "+{s[48:55]}"(sv), "+{v[0:15]}"(c0), "+{s57}"(hold)
        : "{s58}"(CH_MA), "{s59}"(CH_MB), "{s94}"(par & 255), "{v16}"(lb0 + L_M + wave * 3072 + l16), "{s[32:47]}"(sk), "{s56}"(wave), "{s[60:63]}"(mkrs(a.x, S_TOK * DM * 2)), "{s[64:67]}"(mkrs(a.ws, WS_END)),
          "{a[0:31]}"(ww[0]), "{a[32:63]}"(ww[1]), "{a[64:95]}"(ww[2]), "{a[96:127]}"(ww[3]), "{a[128:159]}"(ww[4]), "{a[160:191]}"(ww[5]), "{a[192:223]}"(ww[6]), "{a[224:255]}"(ww[7])
        : "memory", "vcc", "scc", "m0", "v17", "v18", "v19", "v20", "v21", "v22", "v24", "v25", "v26", "v27", "v28", "v29", "v30", "v31", "v32", "v33", "v34", "v35", "v36", "v37", "v38", "v39", "v40", "v41", "v42", "v43", "v44", "v45", "v46", "v47", "v48", "v49", "v50", "v51", "v52", "v53", "v54", "v55", "v56", "v57", "v58", "v59", "v60", "v61", "v62", "v63", "v64", "v65", "v66", "v67", "v68", "v69", "v70", "v71", "v72", "v73", "v74", "v75", "v76", "v77", "v78", "v79", "v80", "v81", "v82", "v83", "v84", "v85", "v86", "v87", "v88", "v89", "v90", "v91", "v92", "v93", "v94", "v95", "v96", "v97", "v98", "v99", "v100", "v101", "v102", "v103", "v104", "v105", "v106", "v107", "v108", "v109", "v110", "v111", "v112", "v113", "v114", "v115", "v116", "v117", "v118", "v119", "v120", "v121", "v122", "v123", "v124", "v125", "v126", "v127", "v128", "v129", "v130", "v131", "v132", "v133", "v134", "v135", "v136", "v137", "v138", "v139", "v140", "v141", "v142", "v143", "v144", "v145", "v146", "v147", "v148", "v149", "v150", "v151", "v152", "v153", "v154", "v155", "v156", "v157", "v158", "v159", "v160", "v161", "v162", "v163", "v164", "v165", "v166", "v167", "v168", "v169", "v170", "v171", "v172", "v173", "v174", "v175", "v176", "v177", "v178", "v179", "v180", "v181", "v182", "v183", "v184", "v185", "v186", "v187", "v188", "v189", "v190", "v191", "v192", "v193", "v194", "v195", "v196", "v197", "v198", "v199", "v200", "v201", "v202", "v203", "v204", "v205", "v206", "v207", "v208", "v209", "v210", "v211", "v212", "v213", "v214", "v215", "v216", "v217", "v218", "v219", "v220", "v221", "v222", "v223", "v224", "v225", "v226", "v227", "v228", "v229", "v230", "v231", "v232", "v233", "v234", "v235", "v236", "v237", "v238", "v239", "v240", "v241", "v242", "v243", "v244", "v245", "v246", "v247", "v248", "v249", "v250", "v251", "v252", "v253", "s68", "s69", "s70", "s71", "s72", "s73", "s74", "s75", "s76", "s77", "s78", "s79", "s80", "s81", "s82", "s83", "s84", "s85", "s86", "s87", "s88", "s89", "s90");
}

template <int OFF> __device__ __forceinline__ void ld2(v4u (&d)[2], v4u r, unsigned vo, unsigned so) {
    asm volatile("buffer_load_dwordx4 %0, %2, %3, %4 offen offset:%5 sc1\n\tbuffer_load_dwordx4 %1, %2, %3, %4 offen offset:%6 sc1"
                 : "+v"(d[0]), "+v"(d[1]) : "v"(vo), "s"(r), "s"(so), "n"(OFF), "n"(OFF + 1024));
}
__device__ __forceinline__ v4u ld16w(v4u r, unsigned vo, unsigned so) {
    v4u d; asm volatile("buffer_load_dwordx4 %0, %1, %2, %3 offen sc1\n\ts_waitcnt vmcnt(0)" : "=&v"(d) : "v"(vo), "s"(r), "s"(so)); return d;
}
template <int OFF> __device__ __forceinline__ void ld2n(v4u (&d)[2], v4u r, unsigned vo, unsigned so) {
    asm volatile("buffer_load_dwordx4 %0, %2, %3, %4 offen offset:%5 sc1\n\tbuffer_load_dwordx4 %1, %2, %3, %4 offen offset:%6 sc1"
                 : "=&v"(d[0]), "=&v"(d[1]) : "v"(vo), "s"(r), "s"(so), "n"(OFF), "n"(OFF + 1024));
}
typedef __attribute__((ext_vector_type(16))) float f32x16;
__device__ __forceinline__ unsigned scl2(unsigned x, float a, float b) {
    unsigned lo, hi;
    asm("v_lshlrev_b32 %0, 16, %2\n\tv_and_b32 %1, 0xffff0000, %2\n\tv_mul_f32 %0, %0, %3\n\tv_mul_f32 %1, %1, %4\n\tv_cvt_pk_bf16_f32 %0, %0, %1" : "=&v"(lo), "=&v"(hi) : "v"(x), "v"(a), "v"(b));
    return lo;
}
__device__ __forceinline__ void mm32(f32x16& c, v4u x, v4u y) { asm volatile("s_nop 1\n\tv_mfma_f32_32x32x16_bf16 %0, %1, %2, %0" : "+v"(c) : "v"(x), "v"(y)); }
__device__ __forceinline__ void f1_role(const Args& a, rsrc_t rs, unsigned char* lds, int arg, int s0, unsigned* ctl) {
    const int tid = otid(), lane = tid & 63, wave = a.pad0, l15 = lane & 15, g = lane >> 4;
    const unsigned lb = (unsigned)(size_t)(LAS unsigned char*)lds, q = l15 >> 2, p = lane & 3, l16 = lane * 16;
    const unsigned pol = __builtin_amdgcn_readfirstlane(*(const unsigned*)(a.ws + O_POL));
    const int kh = arg >= 14, ia = arg - 14 * kh, rot = ia & 3;
    lds_barrier();
    v4u w[32][2];
    f32x16 bv, cs = {};
#pragma unroll
    for (int h = 0; h < 4; ++h) {
        const v2u br = *(const v2u*)(a.b1 + 128 * arg + 32 * wave + 8 * h + 4 * (g >> 1));
        bv[4 * h] = lo16f(br.x); bv[4 * h + 1] = hi16f(br.x); bv[4 * h + 2] = lo16f(br.y); bv[4 * h + 3] = hi16f(br.y);
    }
    v4u one = {0x3F803F80u, 0x3F803F80u, 0x3F803F80u, 0x3F803F80u};
    asm volatile("" : "+v"(bv), "+v"(one));
    {
        v4u rW = mkrs(a.w1, DM * FF * 2);
        unsigned bo[2];
#pragma unroll
        for (int i = 0; i < 2; ++i) bo[i] = (g * FF + 16 * ((l15 >> 1) ^ (g | i << 2)) + 8 * (lane & 1)) * 2;
        asm volatile("s_nop 4" : "+s"(rW));
        auto issue = [&](int G) {
            const unsigned r0 = ((4 * G + wave + 8 * rot) & 31) * 32;
#pragma unroll
            for (int j = 0; j < 8; ++j) bdma<(NTW & 8)>(rW, bo[(j >> 1) & 1], ((r0 + 4 * j) * FF + 128 * arg) * 2, lb + (G & 3) * 32768 + wave * 8192 + j * 1024);
        };
        issue(0); issue(1); issue(2); issue(3);
        {
            f32x4 m[2];
            ld_mod<2>(rs, ctl, {4 * DM + 4 * tid, 3 * DM + 4 * tid}, m);
            *(LAS f32x4*)(lds + 131072 + 16 * tid) = m[0]; *(LAS v2u*)(lds + 135168 + 8 * tid) = pack4(m[1]);
            asm volatile("s_waitcnt lgkmcnt(0)" ::: "memory");
        }
        const unsigned ox = (8 * (g >> 1) + q) * 256 + 32 * ((2 * wave + (g & 1)) ^ (q | (g >> 1) << 2)) + 8 * p;
        sfor<0, 8>([&](auto gg) {
            constexpr int G = decltype(gg)::v;
            asm volatile("s_waitcnt vmcnt(%0)\n\ts_barrier" :: "n"(G < 5 ? 24 : 8 * (7 - G)) : "memory");
#pragma unroll
            for (int k = 0; k < 4; ++k)
#pragma unroll
                for (int n = 0; n < 2; ++n) {
                    const unsigned o = (G & 3) * 32768 + k * 8192 + n * 4096 + ox;
                    const v2u lo = tr_rd(lds, o), hi = tr_rd(lds, o + 1024);
                    w[4 * G + k][n] = (v4u){lo.x, lo.y, hi.x, hi.y};
                }
            v4u sbf[8];
            f32x4 c1[8][2];
            const unsigned k0 = ((4 * G + 8 * rot) & 31) * 32 + 8 * (g >> 1);
#pragma unroll
            for (int i = 0; i < 8; ++i) { sbf[i] = lds16(lds + 135168 + 2 * k0 + 32 * i); c1[i][0] = *(const LAS f32x4*)(lds + 131072 + 4 * k0 + 64 * i); c1[i][1] = *(const LAS f32x4*)(lds + 131088 + 4 * k0 + 64 * i); }
            lds_barrier();
            if (G + 4 < 8) issue(G + 4);
            v4u y[8];
#pragma unroll
            for (int i = 0; i <= 8; ++i) {
                SB();
                if (i < 8) mm32(bv, w[4 * G + (i >> 1)][i & 1], sbf[i]);
                SB();
                if (i < 8) for (int j = 0; j < 2; ++j) y[i][j] = scl2(w[4 * G + (i >> 1)][i & 1][j], c1[i][0][2 * j], c1[i][0][2 * j + 1]);
                SB();
                if (i) mm32(cs, y[i - 1], one);
                SB();
                if (i < 8) for (int j = 0; j < 2; ++j) y[i][j + 2] = scl2(w[4 * G + (i >> 1)][i & 1][j + 2], c1[i][1][2 * j], c1[i][1][2 * j + 1]);
                if (i) { v4u& x = w[4 * G + ((i - 1) >> 1)][(i - 1) & 1]; x = y[i - 1]; asm volatile("" : "+a"(x)); }
            }
        });
    }
    asm volatile("s_nop 11" : "+v"(bv), "+v"(cs));
    RSTAMP(74, 0);
    constexpr unsigned OOB = 0x7F000000u, L_S = 139264;
    auto u = [](unsigned x) { return (unsigned)__builtin_amdgcn_readfirstlane(x); };
    auto st = [&](int t) { return u(D_X2 + (((t + rot) & 3) * 2 + (wave >> 1)) * 16384 + (wave & 1) * 4096); };
    const v8u c0 = {l16, lb + (g & 1) * 1024 + (g >> 1) * 256 + l15 * 16, D_ST + (lane >> 3) * 1024 + (8 * wave + (lane & 7)) * 8, lb + L_S + (8 * wave + (lane & 7)) * 8, lb + L_S + (lane & 31) * 8,
                    (g & 1) * 1024u + (g >> 1) * 256 + l15 * 16, lane < 4 ? GO(2, 4 * kh + lane) * 4u : OOB, lane ? OOB : GO(0, 4 * kh + (ia >> 2)) * 4u};
    v8u sv = {(unsigned)(size_t)a.ws, pol ^ 8192, (unsigned)s0, 0u, u(lb + wave * 4096), nearg(a.ws, pol, O_D + (unsigned)s0 * D_SLOT, 1), (unsigned)wave, u((C_N + 64 * (2 + kh)) * 4)};
    const v8u sk = {O_D, u(arg * 16384 + wave * 2048), st(0), st(1), st(2), st(3), __float_as_uint(-0.1029432397f), __float_as_uint(-2.302208198f)};
    const unsigned* dp = nullptr;
    v32u ww[8];
#pragma unroll
    for (int i = 0; i < 8; ++i) ww[i] = cat2(cat(w[4 * i][0], w[4 * i][1], w[4 * i + 1][0], w[4 * i + 1][1]), cat(w[4 * i + 2][0], w[4 * i + 2][1], w[4 * i + 3][0], w[4 * i + 3][1]));
    asm volatile("s_nop 11\n" ROLE_F1 : "+{s[48:55]}"(sv)
        : "{v[0:7]}"(c0), "{v[72:87]}"(cs), "{v[88:103]}"(bv), "{s[40:47]}"(sk), "{s[38:39]}"(dp), "{s56}"(XF1), "{s[64:67]}"(mkrs(a.ws, WS_END)), [sm] "n"(SM), [sv] "n"(SV),
          "{a[0:31]}"(ww[0]), "{a[32:63]}"(ww[1]), "{a[64:95]}"(ww[2]), "{a[96:127]}"(ww[3]), "{a[128:159]}"(ww[4]), "{a[160:191]}"(ww[5]), "{a[192:223]}"(ww[6]), "{a[224:255]}"(ww[7])
        : "memory", "vcc", "scc", "m0", "v8", "v9", "v10", "v11", "v12", "v13", "v14", "v15", "v16", "v17", "v18", "v24", "v25", "v26", "v27", "v28", "v29", "v30", "v31", "v32", "v33", "v34", "v35", "v36", "v37", "v38", "v39", "v40", "v41", "v42", "v43", "v44", "v45", "v46", "v47", "v48", "v49", "v50", "v51", "v52", "v53", "v54", "v55", "v56", "v57", "v58", "v59", "v60", "v61", "v62", "v63", "v64", "v65", "v66", "v67", "v68", "v69", "v70", "v71", "v104", "v105", "v106", "v107", "v108", "v109", "v110", "v111", "v112", "v113", "v114", "v115", "v116", "v117", "v118", "v119", "s68", "s69", "s70", "s71", "s72", "s73", "s74", "s75", "s76", "s77", "s78", "s79", "s80", "s81", "s82", "s83", "s84", "s85", "s86");
}

static_assert(LV < 16 && LV + 2 < RG && DQ + 2 < RG, "rings");
__device__ __forceinline__ void f2_role(const Args& a, rsrc_t rs, unsigned char* lds, int kh, int cg, int s0, unsigned* ctl) {
    constexpr int KS = 14;
    const int tid = otid(), lane = tid & 63, wave = a.pad0, l15 = lane & 15, g = lane >> 4;
    const int ks0 = (kh * 4 + wave) * KS, rot = (cg & 3) * 2;
    auto kp = [&](int k) { const int x = (k >> 1) + rot; return x >= 7 ? x - 7 : x; };
    auto ko = [&](int k) { const int pi = wave * 7 + kp(k); return (pi >> 1) * 16384 + (pi & 1) * 4096; };
    const unsigned pol = __builtin_amdgcn_readfirstlane(*(const unsigned*)(a.ws + O_POL));
    lds_barrier();
    v4u w[KS][4];
    {
        v4u rW = mkrs(a.w2, DM * FF * 2);
        const unsigned lb = (unsigned)(size_t)(LAS unsigned char*)lds + wave * 32768, q = l15 >> 2, p = lane & 3;
        unsigned bo[2];
#pragma unroll
        for (int i = 0; i < 2; ++i) bo[i] = ((lane >> 3) * DM + 16 * (((lane >> 1) & 3) ^ ((g & 1) | i << 1)) + 8 * (lane & 1)) * 2;
        sfor<0, 2>([&](auto hh) {
            constexpr int h0 = 7 * decltype(hh)::v, h1 = h0 + 7;
            opaque(rW);
#pragma unroll
            for (int k = h0; k < h1; ++k)
#pragma unroll
                for (int j = 0; j < 4; ++j) bdma<(NTW & 8)>(rW, bo[j & 1], (unsigned)(((ks0 + 2 * kp(k) + (k & 1)) * 32 + 8 * j) * DM + 64 * cg) * 2, lb + (k - h0) * 4096 + j * 1024);
            wait_vm0();
#pragma unroll
            for (int k = h0; k < h1; ++k)
#pragma unroll
                for (int n = 0; n < 4; ++n) {
                    const unsigned o = wave * 32768 + (k - h0) * 4096 + (8 * g + q) * 128 + 32 * (n ^ ((q >> 1) | (g & 1) << 1)) + 8 * p;
                    const v2u lo = tr_rd(lds, o), hi = tr_rd(lds, o + 512);
                    w[k][n] = (v4u){lo.x, lo.y, hi.x, hi.y};
                }
#pragma unroll
            for (int k = h0; k < h1; ++k) asm volatile("" : "+a"(w[k][0]), "+a"(w[k][1]), "+a"(w[k][2]), "+a"(w[k][3]));
            asm volatile("s_waitcnt lgkmcnt(0)" ::: "memory");
        });
    }
    const int colf = 64 * cg + 32 * kh + 16 * (wave & 1) + 4 * g;
    const v2u br = *(const v2u*)(a.b2 + colf);
    f32x4 bv = {lo16f(br.x), hi16f(br.x), lo16f(br.y), hi16f(br.y)};
    f32x4 gv;
    ld_mod<1>(rs, ctl, {5 * DM + colf}, &gv);
    asm volatile("" : "+v"(gv), "+v"(bv));
    lds_barrier();
    RSTAMP(74, 0);
    const unsigned l16 = lane * 16, lb = (unsigned)(size_t)(LAS unsigned char*)lds, t0 = (wave & 1) * 2 + (wave >> 1);
    auto u = [](unsigned x) { return (unsigned)__builtin_amdgcn_readfirstlane(x); };
    v8u c0 = {l16, lb + wave * 8192 + l16, lb + (4 * kh + t0) * 1024 + l16, D_X2 + (colf >> 7) * 16384 + (((colf >> 5) & 3) * 2 + (wave >> 1)) * 1024 + (((colf >> 3) & 3) * 16 + l15) * 16 + (colf & 4) * 2,
              (unsigned)((16 * (wave >> 1) + l15) * DM + colf) * 2, lane < 2 ? (GO(2, kh * 4 + (cg >> 2)) + lane) * 4u : 0x7F000000u, lb + (4 * (kh ^ 1) + t0) * 1024 + l16,
              lane < KS ? kh * 14 * 16384 + ((wave * KS + lane) >> 2) * 16384 + ((wave * KS + lane) & 3) * 2048 + 2044 : 0x7F000000u};
    v8u sv = {(unsigned)(size_t)a.ws, pol ^ 8192, (unsigned)s0, nearg(a.ws, pol, O_D + (unsigned)s0 * D_SLOT, 1), 0u, 0u, (unsigned)wave, XF2};
    v8u kk;
#pragma unroll
    for (int i = 0; i < 8; ++i) kk[i] = u(kh * 14 * 16384 + ko(2 * (i % 7)));
    const v4u sk = {O_D, u(D_SL + cg * 16384 + (kh * 4 + wave) * 1024), TD_S, 0u};
    const unsigned* dp = nullptr;
    v32u ww[7];
#pragma unroll
    for (int i = 0; i < 7; ++i) ww[i] = cat2(cat(w[2 * i][0], w[2 * i][1], w[2 * i][2], w[2 * i][3]), cat(w[2 * i + 1][0], w[2 * i + 1][1], w[2 * i + 1][2], w[2 * i + 1][3]));
    asm volatile("s_nop 4\n" ROLE_F2 : "+{s[48:55]}"(sv), "+{v[0:7]}"(c0)
        : "{v[8:11]}"(gv), "{v[12:15]}"(bv), "{s[40:43]}"(sk), "{s[44:47]}"(mkrs(a.out, S_TOK * DM * 2)), "{s[56:63]}"(kk), "{s[38:39]}"(dp), "{s[64:67]}"(mkrs(a.ws, WS_END)), [sm] "n"(SM), [sv] "n"(SV),
          "{a[0:31]}"(ww[0]), "{a[32:63]}"(ww[1]), "{a[64:95]}"(ww[2]), "{a[96:127]}"(ww[3]), "{a[128:159]}"(ww[4]), "{a[160:191]}"(ww[5]), "{a[192:223]}"(ww[6])
        : "memory", "vcc", "scc", "v16", "v17", "v18", "v19", "v20", "v21", "v24", "v25", "v26", "v27", "v28", "v29", "v30", "v31", "v32", "v33", "v34", "v35", "v36", "v37", "v38", "v39", "v40", "v41", "v42", "v43", "v44", "v45", "v46", "v47", "v48", "v49", "v50", "v51", "v52", "v53", "v54", "v55", "v56", "v57", "v58", "v59", "v60", "v61", "v62", "v63", "v64", "v65", "v66", "v67", "v68", "v69", "v70", "v71", "v72", "v73", "v74", "v75", "v76", "v77", "v78", "v79", "v80", "v81", "v82", "v83", "v84", "v85", "v86", "v87", "v88", "v89", "v90", "v91", "v92", "v93", "v94", "v95", "v96", "v97", "v98", "v99", "v100", "v101", "v102", "v103", "v104", "v105", "v106", "v107", "v108", "v109", "v110", "v111", "v112", "v113", "v114", "v115", "v116", "v117", "v118", "v119", "v120", "v121", "v122", "v123", "v124", "v125", "v126", "v127", "v128", "v129", "v130", "v131", "v132", "v133", "v134", "v135", "v136", "v137", "v138", "v139", "v140", "v141", "v142", "v143", "v144", "v145", "v146", "v147", "v148", "v149", "v150", "v151", "v152", "v153", "v154", "v155", "v156", "v157", "v158", "v159", "v160", "v161", "v162", "v163", "v164", "v165", "v166", "v167", "v168", "v169", "v170", "v171", "v172", "v173", "v174", "v175", "v176", "v177", "v178", "v179", "v180", "v181", "v182", "v183", "v184", "v185", "v186", "v187", "v188", "v189", "v190", "v191", "v192", "v193", "v194", "v195", "v196", "v197", "v198", "v199", "v200", "v201", "v202", "v203", "v204", "v205", "v206", "v207", "v208", "v209", "v210", "v211", "v212", "v213", "v214", "v215", "v216", "v217", "v218", "v219", "v220", "v221", "v222", "v223", "v224", "v225", "v226", "v227", "v228", "v229", "v230", "v231", "v232", "v233", "v234", "v235", "v236", "v237", "v238", "v239", "s68", "s69", "s70", "s71", "s72", "s73", "s74", "s75", "s76", "s77", "s78", "s79", "s80", "s81", "s82", "s83", "s84", "s85", "s86");
}

#define KPG 408
#define GM 0x76325410u
__global__ void __launch_bounds__(256, 1) k_main(Args a) {
    extern __shared__ __attribute__((aligned(16))) unsigned char lds[];
    const int px = blockIdx.x & 7, by = blockIdx.x >> 3;
    const int xg = 2 * (px & 3) + (by & 1), b2 = by >> 1;
    const int ii = ((GM >> (4 * ((b2 >> 2) + (px & 4)))) & 7) * 4 + (b2 & 3);
    int wv = __builtin_amdgcn_readfirstlane(threadIdx.x >> 6); asm volatile("" : "+s"(wv));
    if (blockIdx.x >= 224) code_touch(lds + LDS_BYTES - 1024, ((blockIdx.x >> 3) - 28) * 4 + wv);
#define DLC 0
#define DLQ 0
    if (DLC && ii < 8) rt_delay(DLC);
    if (DLQ && (ii >> 2) == 3) rt_delay(DLQ);
    if (ii < 8) {
#define XSW 0
        const bool sw = XSW && px == 1 + (ii & 1);
        if ((ii & 1) != sw) chain_main<true>(a, lds, sw ? xg ^ 6 : xg, ii | 1, px, wv); else chain_main<false>(a, lds, sw ? xg ^ 6 : xg, ii & ~1, px, wv);
        return;
    }
    if ((a.mode & 255) == 2) return;
    const unsigned par = a.pad0, ev = a.mode >> 8;
    a.pad0 = wv; a.mode = ii * 8 + xg;
    const rsrc_t rs = __builtin_amdgcn_make_buffer_rsrc((void*)a.ws, (short)0, (int)WS_END, 0x00020000);
    unsigned* const ctl = (unsigned*)(a.ws + O_CTL);
    const int role = (ii >> 2) - 2, i4 = ii & 3;
    RSTAMP(75, 0);
    if (role == 0) proj_role<true>(a, rs, lds, 4 * xg + i4, ctl);
    else if (role == 1) proj_role<false>(a, rs, lds, 4 * xg + i4, ctl);
    else if (role < 4) { r_role(a, lds, xg, 4 * xg + i4, role & 1, par, ev); return; }
    else {
        const unsigned t0 = rt_now();
        ln1_rows(a, rs, lds, (ii - 24) * 8 + xg);
        if (role > 3 && (ii < 30 || (xg & 3) < 3)) rt_delay(TD_W, t0);
        RSTAMP(75, 1);
    }
    if (ROFF & 2) return;
    else if (role == 4) { f2_role(a, rs, lds, xg >> 2, 4 * (xg & 3) + i4, 0, ctl); RSTAMP(73, 0); }
    else if (role < 3 && (role || XF1 >= NSEG)) return;
    else if (4 * (xg & 3) + i4 < 14) f1_role(a, rs, lds, 14 * (xg >> 2) + 4 * (xg & 3) + i4, role ? 0 : XF1, ctl);
}

extern "C" void kernel_launch(void* const* d_in, const int* in_sizes, int n_in,
                              void* d_out, int out_size, void* d_ws, size_t ws_size,
                              hipStream_t stream) {
    Args a;
    a.x = (const u16*)d_in[0]; a.mem0 = (const u16*)d_in[1]; a.Wq = (const u16*)d_in[2]; a.Wk = (const u16*)d_in[3];
    a.Wv = (const u16*)d_in[4]; a.Wo = (const u16*)d_in[5];
    a.w1 = (const u16*)d_in[8]; a.b1 = (const u16*)d_in[9]; a.w2 = (const u16*)d_in[10]; a.b2 = (const u16*)d_in[11];
    a.out = (u16*)d_out; a.ws = (char*)d_ws + (size_t)WSB * MiB; a.mode = CH_EV << 8; a.pad0 = CH_PAR;
    char* ws = a.ws;
    a.c = (const u16*)d_in[12]; a.w_mod = (const u16*)d_in[13];
    k_pre<<<KPG, 256, 0, stream>>>(a.c, a.w_mod, a.mem0, ws);
    hipFuncSetAttribute((const void*)k_main, hipFuncAttributeMaxDynamicSharedMemorySize, LDS_BYTES);
    k_main<<<256, 256, LDS_BYTES, stream>>>(a);
}
```
